```python
import torch

def _softmax(x, axis=-1):
    return torch.softmax(x, dim=axis)

def _gelu(x):
    return torch.nn.functional.gelu(x, approximate='tanh')

def _silu(x):
    return torch.nn.functional.silu(x)

def _sigmoid(x):
    return torch.sigmoid(x)

def _layernorm(x, axis=-1, eps=1e-05):
    m = x.mean(dim=axis, keepdim=True)
    v = x.var(dim=axis, keepdim=True, unbiased=False)
    return (x - m) / torch.sqrt(v + eps)

def _scatter_add(tgt, idx, src, axis=0):
    return tgt.clone().index_add_(axis, idx, src)

def _segment_sum(src, seg, n):
    out = torch.zeros((n,) + src.shape[1:], dtype=src.dtype)
    return out.index_add_(0, seg, src)

def _pad(x, pw):
    flat = tuple((v for pair in reversed(pw) for v in pair))
    return torch.nn.functional.pad(x, flat)
kernel_name = 'adaLN_cond__ssm_conv_s4__dense_activation_silu__prompt'

def setup_inputs():

    _w = lambda a: (lambda t: t.to(torch.bfloat16) if t.is_floating_point() else t)(torch.as_tensor(a))

    def _A_setup():
        B, L, D, N = (1, 2048, 512, 128)
        A_re = -(lambda _x: torch.exp(torch.as_tensor(_x)))((lambda *s: torch.randn(*s, dtype=torch.bfloat16) * 3.2)(D, N))
        A_im = torch.pi * (lambda *s: torch.randn(*s, dtype=torch.bfloat16) * 0.05)(D, N)
        B_re = (lambda *s: torch.randn(*s, dtype=torch.bfloat16) * 0.1)(D, N)
        B_im = (lambda *s: torch.randn(*s, dtype=torch.bfloat16) * 0.1)(D, N)
        C_re = (lambda *s: torch.randn(*s, dtype=torch.bfloat16) * 0.1)(D, N)
        C_im = (lambda *s: torch.randn(*s, dtype=torch.bfloat16) * 0.1)(D, N)
        log_dt = (lambda *s: torch.randn(*s, dtype=torch.bfloat16) * 0.05)(D) * 0.5 - 4.0
        Dskip = (lambda *s: torch.randn(*s, dtype=torch.bfloat16) * 0.05)(D)
        return {'u': _w((lambda *s: torch.randn(*s, dtype=torch.bfloat16) * 0.05)(B, L, D)), 'A_re': _w(A_re), 'A_im': _w(A_im), 'B_re': _w(B_re), 'B_im': _w(B_im), 'C_re': _w(C_re), 'C_im': _w(C_im), 'log_dt': _w(log_dt), 'Dskip': _w(Dskip)}
    _A_in = _A_setup()

    def _B_setup():
        B, S, D, F = (1, 2048, 512, 1792)
        return {'x': _w((lambda *s: torch.randn(*s, dtype=torch.bfloat16) * 0.05)(B, S, D)), 'w1': _w((lambda *s: torch.randn(*s, dtype=torch.bfloat16) * 3.2)(D, F) * D ** (-0.5)), 'w2': _w((lambda *s: torch.randn(*s, dtype=torch.bfloat16) * 0.4)(F, D) * F ** (-0.5))}
    _B_in = _B_setup()
    return {'A_u': _A_in['u'], 'A_A_re': _A_in['A_re'], 'A_A_im': _A_in['A_im'], 'A_B_re': _A_in['B_re'], 'A_B_im': _A_in['B_im'], 'A_C_re': _A_in['C_re'], 'A_C_im': _A_in['C_im'], 'A_log_dt': _A_in['log_dt'], 'A_Dskip': _A_in['Dskip'], 'B_x': _B_in['x'], 'B_w1': _B_in['w1'], 'B_w2': _B_in['w2'], 'c': _w((lambda *s: torch.randn(*s, dtype=torch.bfloat16) * 0.2)(1, 512) * 8.0), 'w_mod': _w((lambda *s: torch.randn(*s, dtype=torch.bfloat16) * 0.2)(512, 6 * 512) * 512 ** (-0.5) * 8.0)}

def reference(A_u, A_A_re, A_A_im, A_B_re, A_B_im, A_C_re, A_C_im, A_log_dt, A_Dskip, B_x, B_w1, B_w2, c, w_mod):

    def _A_ref(u, A_re, A_im, B_re, B_im, C_re, C_im, log_dt, Dskip):
        Bsz, L, D = u.shape
        dt = torch.exp(log_dt)[:, None]
        A = A_re + 1j * A_im
        Bc = B_re + 1j * B_im
        Cc = C_re + 1j * C_im
        logdA = dt * A
        dB = dt * Bc
        l = (lambda x, dt: x.to(dt))(torch.arange(L), A_re.dtype)
        P = torch.exp(logdA[:, :, None] * l[None, None, :])
        K = torch.einsum('dn,dnl,dn->dl', Cc, P, dB).real
        L2 = 2 * L
        Kp = _pad(K, ((0, 0), (0, L)))
        up = (lambda x, *d: x.permute(*d))(u, 0, 2, 1)
        up = _pad(up, ((0, 0), (0, 0), (0, L)))
        Kf = (lambda x, axes=(-2, -1): torch.fft.rfft2(x.float(), dim=axes))(Kp[:, None, :], axes=(-2, -1))
        Uf = (lambda x, axes=(-2, -1): torch.fft.rfft2(x.float(), dim=axes))(up[:, :, None, :], axes=(-2, -1))
        Yf = Uf * Kf[None, :, :, :]
        y = (lambda x, s, axes=(-2, -1): torch.fft.irfft2(x, s=s, dim=axes).to(torch.bfloat16))(Yf, (1, L2), axes=(-2, -1))[:, :, 0, :L]
        y = (lambda x, *d: x.permute(*d))(y, 0, 2, 1) + u * Dskip[None, None, :]
        return y

    def _B_ref(x, w1, w2):
        h = torch.einsum('bsd,df->bsf', x, w1)
        h = _silu(h)
        out = torch.einsum('bsf,fd->bsd', h, w2)
        return out
    x = A_u
    mod = (_silu(c) @ w_mod).reshape(c.shape[0], 6, 1, -1)
    sa, ca, ga, sb, cb, gb = (mod[:, 0].clone(), mod[:, 1].clone(), mod[:, 2].clone(), mod[:, 3].clone(), mod[:, 4].clone(), mod[:, 5].clone())
    h = _layernorm(x, axis=-1) * (1 + ca) + sa
    x = x + ga * _A_ref(h, A_A_re, A_A_im, A_B_re, A_B_im, A_C_re, A_C_im, A_log_dt, A_Dskip)
    h = _layernorm(x, axis=-1) * (1 + cb) + sb
    x = x + gb * _B_ref(h, B_w1, B_w2)
    return x
```

```cpp
#include <hip/hip_runtime.h>

#define GAS __attribute__((address_space(1)))
#define LAS __attribute__((address_space(3)))
#define DI __device__ __forceinline__
typedef unsigned v4u __attribute__((ext_vector_type(4)));
typedef unsigned v2u __attribute__((ext_vector_type(2)));
typedef float f32x2 __attribute__((ext_vector_type(2)));
typedef float f32x4 __attribute__((ext_vector_type(4)));
typedef float f32x16 __attribute__((ext_vector_type(16)));
typedef __bf16 bf16x2 __attribute__((ext_vector_type(2)));
typedef __bf16 bf16x8 __attribute__((ext_vector_type(8)));
typedef short s16x4 __attribute__((ext_vector_type(4)));
typedef unsigned short u16;
typedef unsigned long long u64;

constexpr int OFF_EPOCH = 0;
constexpr int OFF_SG = 4096;
constexpr int OFF_MG = OFF_SG + 16384;
constexpr int OFF_XG = 131072;
constexpr int OFF_P1 = OFF_XG + (4 << 20);
constexpr int OFF_X1 = OFF_P1;
constexpr int OFF_W1P = OFF_X1 + 2097152;
constexpr int OFF_HID = OFF_P1 + (4 << 20);
constexpr int WS_BYTES = OFF_HID + 7340032;

constexpr int L_MODT = 0;
constexpr int L_UVT = 512;
constexpr int L_STAT = 1536;
constexpr int L_ONE = 2560;
constexpr int L_KC = 4096;
constexpr int KC_STRIDE = 4160;
constexpr int L_H = L_KC + 8 * KC_STRIDE;
constexpr int H_STRIDE = 4576;
constexpr int L_XB = L_H + 2 * H_STRIDE;
constexpr int L_R1 = L_XB + 8192;
constexpr int R1_STEPS = 13, R2_STEPS = 11, R3_STEPS = 4;
constexpr int L_UVS = L_R1;
constexpr int UVS_ROW = 528, UVS_CH = 57 * UVS_ROW;
constexpr int L_WM = L_UVS + 2 * UVS_CH;
constexpr int L_W1R = L_WM + 12288;
constexpr int L_SC = L_W1R + 7424;
constexpr int L_KT = L_SC + 256;
constexpr int KT_CH = 1600;
constexpr int L_XR = 4096;
constexpr int L_R2 = L_XR + 65536;
constexpr int LDS_BYTES = 163840;
static_assert(L_R1 + 4 * R1_STEPS * 2048 <= LDS_BYTES && L_KT + 2 * KT_CH <= LDS_BYTES && L_R2 + 4 * R2_STEPS * 2048 <= LDS_BYTES, "LDS map");
static_assert(L_H % 16 == 0 && L_XB % 16 == 0 && L_R1 % 16 == 0 && L_WM % 16 == 0 && L_W1R % 16 == 0 && L_KT % 16 == 0, "alignment");

constexpr int CODE_BYTES = 49152;
constexpr int SC1 = 16;
constexpr unsigned SPIN_CAP = 1u << 15;
constexpr unsigned POISON = 0xFFFFFFFFu;
constexpr float LOG2E = 1.4426950408889634f, LN2 = 0.6931471805599453f, INV2PI = 0.15915494309189535f;

#define SB() __builtin_amdgcn_sched_barrier(0)
#define WAITV(n) asm volatile("s_waitcnt vmcnt(" #n ")" ::: "memory")
#define PIN(x) asm volatile("" : "+v"(x))
#define FRESH() asm volatile("" ::: "memory")
#define BAR() do { asm volatile("s_waitcnt lgkmcnt(0)" ::: "memory"); __builtin_amdgcn_s_barrier(); asm volatile("" ::: "memory"); } while (0)
#define STAMP(i)

DI float u2f(unsigned u) { return __builtin_bit_cast(float, u); }
DI unsigned f2u(float f) { return __builtin_bit_cast(unsigned, f); }
DI float dot2(unsigned u, float c) { const bf16x2 v = __builtin_bit_cast(bf16x2, u); return __builtin_amdgcn_fdot2_f32_bf16(v, v, c, false); }
DI float bf_lo(unsigned u) { return __builtin_bit_cast(float, u << 16); }
DI float bf_hi(unsigned u) { return __builtin_bit_cast(float, u & 0xFFFF0000u); }
DI unsigned pack2(float a, float b) { f32x2 v = {a, b}; return __builtin_bit_cast(unsigned, __builtin_convertvector(v, bf16x2)); }
DI float rbf(float a) { return bf_lo(pack2(a, a)); }
DI unsigned guard(unsigned u) { return u < 0xFFFFFFFEu ? u : 0xFFFFFFFEu; }
DI unsigned max3(unsigned a, unsigned b, unsigned c) { unsigned r; asm("v_max3_u32 %0, %1, %2, %3" : "=v"(r) : "v"(a), "v"(b), "v"(c)); return r; }
DI unsigned max4(unsigned m, v4u v) { return max3(max3(m, v.x, v.y), v.z, v.w); }
DI f32x4 mfma16(v4u a, v4u b, f32x4 c) { return __builtin_amdgcn_mfma_f32_16x16x32_bf16(__builtin_bit_cast(bf16x8, a), __builtin_bit_cast(bf16x8, b), c, 0, 0, 0); }
DI f32x16 mfma32(v4u a, v4u b, f32x16 c) { return __builtin_amdgcn_mfma_f32_32x32x16_bf16(__builtin_bit_cast(bf16x8, a), __builtin_bit_cast(bf16x8, b), c, 0, 0, 0); }
template <bool FIRST> DI void mfma_av(f32x16& acc, const v4u& a, const v4u& b) {
  if (FIRST) asm volatile("v_mfma_f32_32x32x16_bf16 %0, %1, %2, 0" : "=&v"(acc) : "a"(a), "v"(b));
  else asm volatile("v_mfma_f32_32x32x16_bf16 %0, %1, %2, %0" : "+v"(acc) : "a"(a), "v"(b));
}
template <bool FIRST> DI void mfma_va(f32x16& acc, const v4u& a, const v4u& b) {
  if (FIRST) asm volatile("v_mfma_f32_32x32x16_bf16 %0, %1, %2, 0" : "=&v"(acc) : "v"(a), "a"(b));
  else asm volatile("v_mfma_f32_32x32x16_bf16 %0, %1, %2, %0" : "+v"(acc) : "v"(a), "a"(b));
}
DI void mfma_done(f32x16& acc) { asm volatile("s_nop 15" : "+v"(acc)); }
DI v2u trd(LAS unsigned char* p) { return __builtin_bit_cast(v2u, __builtin_amdgcn_ds_read_tr16_b64_v4i16((LAS s16x4*)p)); }
DI v4u trd2(LAS unsigned char* p0, LAS unsigned char* p1) { const v2u a = trd(p0), b = trd(p1); return v4u{a.x, a.y, b.x, b.y}; }
DI v4u lds128(LAS unsigned char* p) { return *(LAS v4u*)p; }
DI v4u ldg(__amdgpu_buffer_rsrc_t r, int voff, int soff) { return __builtin_amdgcn_raw_buffer_load_b128(r, voff, soff, SC1); }
DI void stg(__amdgpu_buffer_rsrc_t r, int voff, int soff, v4u v) { __builtin_amdgcn_raw_buffer_store_b128(v, r, voff, soff, SC1); }
template <int CTRL> DI float dpp_add(float v) {
  return v + __builtin_bit_cast(float, __builtin_amdgcn_update_dpp(0, __builtin_bit_cast(int, v), CTRL, 0xF, 0xF, true));
}
DI float swap16_add(float v) { const unsigned u = __builtin_bit_cast(unsigned, v); const auto r = __builtin_amdgcn_permlane16_swap(u, u, false, false);
  return u2f(r[0]) + u2f(r[1]); }
DI float swap32_add(float v) { const unsigned u = __builtin_bit_cast(unsigned, v); const auto r = __builtin_amdgcn_permlane32_swap(u, u, false, false);
  return u2f(r[0]) + u2f(r[1]); }
DI float quad_sum(float v) { v = dpp_add<0xB1>(v); return dpp_add<0x4E>(v); }
DI float wave_sum(float v) { v = quad_sum(v); v = dpp_add<0x141>(v); v = dpp_add<0x140>(v); v = swap16_add(v); return swap32_add(v); }
DI f32x2 cmul(f32x2 a, f32x2 e, f32x2 ep) { const f32x2 t = f32x2{a.x, a.x} * e; return __builtin_elementwise_fma(f32x2{a.y, a.y}, ep, t); }
DI f32x2 cexp(float re2, float rev) {
  const float m = __builtin_amdgcn_exp2f(re2), f = __builtin_amdgcn_fractf(rev);
  return f32x2{m * __builtin_amdgcn_cosf(f), m * __builtin_amdgcn_sinf(f)};
}
DI void dma16(const unsigned char* sbase, unsigned voff, unsigned lds_dst) {
  unsigned keep;
  asm volatile("s_mov_b32 %0, m0\n\ts_mov_b32 m0, %3\n\ts_nop 0\n\tglobal_load_lds_dwordx4 %1, %2\n\ts_mov_b32 m0, %0"
               : "=&s"(keep) : "v"(voff), "s"(sbase), "s"(lds_dst) : "memory");
}
DI unsigned ktab_off(int j) {
  unsigned u = __builtin_bit_cast(unsigned, (float)j);
  u = (u + 0x7FFFu + ((u >> 16) & 1u)) & 0xFFFF0000u;
  const int z = (int)__builtin_bit_cast(float, u) - 1;
  const int uu = z >> 7;
  const int s = 31 - __builtin_clz((unsigned)uu | 1u);
  const int row = 8 * s + ((8 * uu + (z & 7)) >> s);
  int off = row * 32 + ((z >> 3) & 15) * 2;
  off = j == 0 ? 40 * 32 : off;
  return j < 0 ? 48 * 32 : off;
}

template <int S> DI void conv_tiles(LAS unsigned char* ka, LAS unsigned char* hb, f32x4 (&acc)[4]) {
  constexpr int T[4] = {S ? 6 : 7, S ? 5 : 4, S ? 2 : 3, S ? 1 : 0};
  constexpr int NF = T[0] + 1;
  v4u A[2][NF], B[2][NF];
#pragma unroll
  for (int u = 0; u < NF; ++u) {
    A[0][u] = trd2(ka + 512 * u + 8, ka + 512 * u);
    B[0][NF - 1 - u] = lds128(hb + 64 * (8 * (NF - 1 - u) + 7));
  }
#pragma unroll
  for (int r = 0; r < 8; ++r) {
    int k = 0;
#pragma unroll
    for (int u = 0; u < NF; ++u)
#pragma unroll
      for (int i = 0; i < 4; ++i)
        if (u <= T[i]) {
          if (r < 7 && k < 2 * NF) {
            const int n = k >> 1;
            if (k & 1) B[(r + 1) & 1][NF - 1 - n] = lds128(hb + 64 * (8 * (NF - 1 - n) - r + 6));
            else A[(r + 1) & 1][n] = trd2(ka + 512 * n + 64 * (r + 1) + 8, ka + 512 * n + 64 * (r + 1));
          }
          acc[i] = mfma16(A[r & 1][u], B[r & 1][T[i] - u], acc[i]);
          SB();
          ++k;
        }
  }
}

DI unsigned max8(const v4u (&x)[8]) {
  unsigned m = 0;
#pragma unroll
  for (int i = 0; i < 8; ++i) m = max4(m, x[i]);
  return m;
}
template <int U> DI void settle_unit(__amdgpu_buffer_rsrc_t rs, int l16, int goff, v4u (&xs)[4][8], unsigned m) {
  if (__builtin_expect(__any(m == POISON), 0)) {
    for (unsigned spin = 0; spin < SPIN_CAP; ++spin) {
      FRESH();
#pragma unroll
      for (int i = 0; i < 8; ++i)
        if (__any(max4(0u, xs[U][i]) == POISON)) {
#pragma unroll
          for (int u = U; u < (U == 0 ? 4 : U + 1); ++u) xs[u][i] = ldg(rs, l16, goff + u * 32768 + i * 4096);
        }
      if (!__any(max8(xs[U]) == POISON)) break;
    }
  }
}

struct Epi { f32x2 st; f32x4 uu, vv; float p, e, pp, y[4]; unsigned o[4]; };
template <int NV> DI void epi_step(int s, Epi& E, const f32x16& acc, __amdgpu_buffer_rsrc_t rs, LAS unsigned char* uvp, int l16, int hoff) {
  const int j = s >> 1;
  if ((s & 1) == 0) {
    if (j >= 1 && j <= NV) {
      const int k = j - 1;
      E.y[k & 3] = E.pp * E.e;
      PIN(E.y[k & 3]);
      if ((k & 1) == 1) { E.o[(k >> 1) & 3] = guard(pack2(E.y[(k & 3) - 1], E.y[k & 3])); PIN(E.o[(k >> 1) & 3]); }
      if ((k & 7) == 7) stg(rs, l16 + (k >> 3) * 1024, hoff, v4u{E.o[0], E.o[1], E.o[2], E.o[3]});
    }
    if (j < NV) {
      E.p = __builtin_fmaf(E.st.x, acc[j], __builtin_fmaf(E.st.y, E.uu[j & 3], E.vv[j & 3]));
      E.e = __builtin_amdgcn_exp2f(E.p);
      PIN(E.e);
      if ((j & 3) == 3 && j + 1 < NV) { E.uu = *(LAS f32x4*)(uvp + ((j + 1) >> 2) * 32); E.vv = *(LAS f32x4*)(uvp + 512 + ((j + 1) >> 2) * 32); }
    }
  } else if (j < NV) {
    E.e = __builtin_amdgcn_rcpf(1.0f + E.e);
    PIN(E.e);
    E.pp = E.p;
  }
}
DI void epi_begin(Epi& E, LAS unsigned char* L, int u, LAS unsigned char* uvp, int lane) {
  E.st = *(LAS f32x2*)(L + L_STAT + u * 256 + (lane & 31) * 8);
  E.uu = *(LAS f32x4*)(uvp);
  E.vv = *(LAS f32x4*)(uvp + 512);
}

template <bool STAT, int U>
DI f32x16 gemm1_unit(__amdgpu_buffer_rsrc_t rs, LAS unsigned char* L, LAS unsigned char* xr, LAS unsigned char* xw, const v4u (&w1)[32], v4u (&xs)[4][8],
                     const f32x16& prev, LAS unsigned char* uvp, int xoff, int hoff, int lane) {
  constexpr int NV = STAT ? 8 : 16, PD = 3, CH0 = U == 0 ? 8 : 0, CHS = U == 0 ? 1 : 2;
  LAS unsigned char* xb = xr + (U & 1) * 32768;
  f32x16 acc;
  float sq0 = 0.f, sq1 = 0.f;
  v4u x[PD + 1];
  Epi E;
  unsigned chk = 0;
#pragma unroll
  for (int s = 0; s < PD; ++s) x[s] = trd2(xb + s * 1024, xb + s * 1024 + 256);
  if (U > 0) epi_begin(E, L, U - 1, uvp, lane);
#pragma unroll
  for (int s = 0; s < 32; ++s) {
    if (s == 16 && U < 3) settle_unit<(U < 3 ? U + 1 : 3)>(rs, lane * 16, xoff, xs, chk);
    if (s + PD < 32) x[(s + PD) % (PD + 1)] = trd2(xb + (s + PD) * 1024, xb + (s + PD) * 1024 + 256);
    const v4u c = x[s % (PD + 1)];
    if (s == 0) mfma_av<true>(acc, w1[s], c); else mfma_av<false>(acc, w1[s], c);
    if (STAT) { sq0 = dot2(c.x, sq0); sq1 = dot2(c.y, sq1); sq0 = dot2(c.z, sq0); sq1 = dot2(c.w, sq1); PIN(sq0); PIN(sq1); }
    if (U > 0) epi_step<NV>(s, E, prev, rs, uvp, lane * 16, hoff + (U - 1) * 114688);
    if (U < 3 && s >= CH0 && s < 16 && (s - CH0) % CHS == CHS - 1) { chk = max4(chk, xs[U < 3 ? U + 1 : 3][(s - CH0) / CHS]); PIN(chk); }
    if (U < 3 && s >= 16 && (s & 1)) *(LAS v4u*)(xw + ((U + 1) & 1) * 32768 + ((s - 16) >> 1) * 4096) = xs[U < 3 ? U + 1 : 3][(s - 16) >> 1];
    SB();
  }
  if (U > 0) epi_step<NV>(32, E, prev, rs, uvp, lane * 16, hoff + (U - 1) * 114688);
  mfma_done(acc);
  if (STAT) {
    const float mean = acc[8] * (1.0f / 512.0f);
    const float var = fmaxf(swap32_add(sq0 + sq1) * (1.0f / 512.0f) - mean * mean, 0.f);
    const float a = -LOG2E * __builtin_amdgcn_rsqf(var + 1e-5f);
    *(LAS f32x2*)(L + L_STAT + U * 256 + (lane & 31) * 8) = f32x2{a, -a * mean};
  }
  return acc;
}

template <bool STAT>
DI f32x16 gemm1(__amdgpu_buffer_rsrc_t rs, LAS unsigned char* L, const v4u (&w1)[32], v4u (&xs)[4][8], v4u (&res)[2], const unsigned char* w2w, unsigned w2voff,
                LAS unsigned char* uvp, int xoff, int hoff, int w, int lane, int rg, int dt) {
  const int tra = ((lane >> 5) * 8 + ((lane & 15) >> 2)) * 64 + ((lane >> 4) & 1) * 32 + (lane & 3) * 8;
  LAS unsigned char* xr = L + L_XR + tra;
  LAS unsigned char* xw = L + L_XR + w * 1024 + lane * 16;
  LAS unsigned char* rr = xr + rg * 32768 + (4 * dt + w) * 1024;
  f32x16 a0 = {}, a1;
  a0 = gemm1_unit<STAT, 0>(rs, L, xr, xw, w1, xs, a0, uvp, xoff, hoff, lane);
  if (rg == 0) res[0] = trd2(rr, rr + 256);
  BAR();
  a1 = gemm1_unit<STAT, 1>(rs, L, xr, xw, w1, xs, a0, uvp, xoff, hoff, lane);
  if (rg == 1) res[0] = trd2(rr, rr + 256);
  BAR();
  a0 = gemm1_unit<STAT, 2>(rs, L, xr, xw, w1, xs, a1, uvp, xoff, hoff, lane);
  if (rg == 0) res[1] = trd2(rr, rr + 256);
  BAR();
#pragma unroll
  for (int n = 0; n < 2 * R2_STEPS; ++n) dma16(w2w + (2 * R1_STEPS + n) * 8192, w2voff, L_R2 + w * (R2_STEPS * 2048) + n * 1024);
#pragma unroll
  for (int n = 0; n < 2 * R3_STEPS; ++n) dma16(w2w + (2 * (R1_STEPS + R2_STEPS) + n) * 8192, w2voff, L_XR + w * (R3_STEPS * 2048) + n * 1024);
  a1 = gemm1_unit<STAT, 3>(rs, L, xr, xw, w1, xs, a0, uvp, xoff, hoff, lane);
  if (rg == 1) res[1] = trd2(rr, rr + 256);
  return a1;
}

__global__ void __launch_bounds__(256, 1)
s4_block_kernel(const unsigned char* __restrict__ xin, const u16* __restrict__ pAre, const u16* __restrict__ pAim, const u16* __restrict__ pBre,
                const u16* __restrict__ pBim, const u16* __restrict__ pCre, const u16* __restrict__ pCim, const u16* __restrict__ pldt,
                const u16* __restrict__ pD, const unsigned char* __restrict__ w1, const unsigned char* __restrict__ w2, const u16* __restrict__ pc,
                const unsigned char* __restrict__ wmod, unsigned char* __restrict__ out, unsigned char* ws) {
  extern __shared__ __attribute__((aligned(16))) unsigned char smem[];
  LAS unsigned char* L = (LAS unsigned char*)smem;
  const int tid = threadIdx.x, lane = tid & 63, w = __builtin_amdgcn_readfirstlane(tid >> 6);
  const int b = blockIdx.x, xcd = b & 7, slot = b >> 3, id = xcd * 32 + slot;
  const __amdgpu_buffer_rsrc_t rs = __builtin_amdgcn_make_buffer_rsrc((void*)ws, 0, WS_BYTES, 0x00020000);
  const int ks4 = id >> 6, cc = id & 63;
  const int pks = id >> 4, pct = id & 15;
  const int rp = 4 * (xcd >> 1) + (slot >> 3), ct = 8 * (xcd & 1) + (slot & 7), rg = xcd & 1, dt = slot & 7;
  STAMP(0);

  const unsigned epoch = __builtin_amdgcn_raw_buffer_load_b32(rs, OFF_EPOCH, 0, SC1);
  const GAS unsigned char* xrow = (const GAS unsigned char*)xin + id * 8192;
  const v4u xa = *(const GAS v4u*)(xrow + (tid & 3) * 2048 + (tid >> 2) * 16), xb = *(const GAS v4u*)(xrow + (tid & 3) * 2048 + 1024 + (tid >> 2) * 16);
  const v4u ra = *(const GAS v4u*)(xrow + w * 2048 + lane * 16), rb = *(const GAS v4u*)(xrow + w * 2048 + 1024 + lane * 16);
  const unsigned char* pcode;
  asm volatile("s_getpc_b64 %0" : "=s"(pcode));
  const unsigned warm = *(const GAS unsigned*)((const GAS unsigned char*)pcode + min((slot & 7) * 8192 + tid * 32, CODE_BYTES));
  const unsigned cval = ((const GAS u16*)pc)[ks4 * 128 + (tid & 127)];
  v4u wm[3];
#pragma unroll
  for (int i = 0; i < 3; ++i) {
    const int idx = tid + 256 * i, row = idx / 6, ch = idx - row * 6;
    wm[i] = *(const GAS v4u*)((const GAS unsigned char*)wmod + (ks4 * 128 + row) * 6144 + cc * 96 + ch * 16);
  }
  v4u wr[2];
#pragma unroll
  for (int i = 0; i < 2; ++i) {
    const int idx = min(tid + 256 * i, 447), row = idx / 14, ch = idx - row * 14;
    wr[i] = *(const GAS v4u*)((const GAS unsigned char*)w1 + (pks * 32 + row) * 3584 + pct * 224 + ch * 16);
  }
  const int pidx = id * 256 + tid;
  const unsigned uAre = ((const GAS u16*)pAre)[pidx], uAim = ((const GAS u16*)pAim)[pidx], uBre = ((const GAS u16*)pBre)[pidx];
  const unsigned uBim = ((const GAS u16*)pBim)[pidx], uCre = ((const GAS u16*)pCre)[pidx], uCim = ((const GAS u16*)pCim)[pidx];
  const unsigned uldt = ((const GAS u16*)pldt)[2 * id + (tid >> 7)], uD = ((const GAS u16*)pD)[2 * id + (tid >> 7)];
  SB();
  const v4u pois = {POISON, POISON, POISON, POISON};
#pragma unroll
  for (int i = 0; i < 4; ++i) stg(rs, tid * 16, OFF_P1 + id * 16384 + i * 4096, pois);
  SB();
  unsigned koff[11];
#pragma unroll
  for (int i = 0; i < 11; ++i) koff[i] = ktab_off(8 * tid - 3 + i);
  {
    const v4u z = {0u, 0u, 0u, 0u};
    if (tid < 16) *(LAS v4u*)(L + L_KC + (tid >> 1) * KC_STRIDE + (tid & 1) * 16) = z;
    else if (tid < 76) { const int t = tid - 16, c = t >= 30; *(LAS v4u*)(L + L_H + c * H_STRIDE + (t - 30 * c) * 16) = z; }
    else if (tid < 78) *(LAS v4u*)(L + L_KT + (tid - 76) * KT_CH + 48 * 32) = z;
    else if (tid >= 192) *(LAS v4u*)(L + L_ONE + lane * 16) = v4u{0x3F803F80u, 0x3F803F80u, 0x3F803F80u, 0x3F803F80u};
  }
  SB();
  const unsigned tagA = epoch * 0x9E3779B1u + 0x7F4A7C15u, tagB = ~tagA;
#pragma unroll
  for (int i = 0; i < 4; ++i) stg(rs, (tid >> 2) * 65536 + (tid & 3) * 16, OFF_XG + i * 16384 + id * 64, v4u{xa[i], tagA, xb[i], tagB});
  STAMP(1);
  unsigned stat0, stat1;
  {
    float fa[8], fb[8];
#pragma unroll
    for (int i = 0; i < 4; ++i) { fa[2 * i] = bf_lo(ra[i]); fa[2 * i + 1] = bf_hi(ra[i]); fb[2 * i] = bf_lo(rb[i]); fb[2 * i + 1] = bf_hi(rb[i]); }
    float sa_ = 0.f, sb_ = 0.f;
#pragma unroll
    for (int i = 0; i < 8; ++i) { sa_ += fa[i]; sb_ += fb[i]; }
    const float ma = wave_sum(sa_) * (1.0f / 512.0f), mb = wave_sum(sb_) * (1.0f / 512.0f);
    float qa = 0.f, qb = 0.f;
#pragma unroll
    for (int i = 0; i < 8; ++i) { const float da = fa[i] - ma, db = fb[i] - mb; qa = __builtin_fmaf(da, da, qa); qb = __builtin_fmaf(db, db, qb); }
    const float va = rbf(rbf(wave_sum(qa) * (1.0f / 512.0f)) + 1e-5f), vb = rbf(rbf(wave_sum(qb) * (1.0f / 512.0f)) + 1e-5f);
    stat0 = pack2(ma, __builtin_amdgcn_sqrtf(va));
    stat1 = pack2(mb, __builtin_amdgcn_sqrtf(vb));
  }
  {
    const float c = bf_lo(cval);
    const float s = c * __builtin_amdgcn_rcpf(1.0f + __builtin_amdgcn_exp2f(-LOG2E * c));
    if (tid < 128) *(LAS u16*)(L + L_SC + tid * 2) = (u16)pack2(s, s);
#pragma unroll
    for (int i = 0; i < 3; ++i) *(LAS v4u*)(L + L_WM + (tid + 256 * i) * 16) = wm[i];
#pragma unroll
    for (int i = 0; i < 2; ++i) *(LAS v4u*)(L + L_W1R + min(tid + 256 * i, 447) * 16) = wr[i];
  }
  const float dtv = rbf(__builtin_amdgcn_exp2f(LOG2E * bf_lo(uldt)));
  const float are = dtv * bf_lo(uAre) * LOG2E, aim = dtv * bf_lo(uAim) * INV2PI;
  f32x2 wv;
  {
    const float br = dtv * bf_lo(uBre), bi = dtv * bf_lo(uBim), cr = bf_lo(uCre), ci = bf_lo(uCim);
    wv = f32x2{cr * br - ci * bi, cr * bi + ci * br};
  }
  float dsk = bf_lo(uD);
  asm volatile("" : "+v"(wv), "+v"(dsk) : "v"(warm));
  WAITV(4);
  if (lane == 0) stg(rs, 0, OFF_SG + (id * 4 + w) * 16, v4u{stat0, tagA, stat1, tagB});
  BAR();
  STAMP(2);
  const int g16 = lane >> 4, q4 = (lane & 15) >> 2, p4 = lane & 3, hf = lane >> 5, blk = (lane >> 4) & 1;
  if (w < 3) {
    f32x4 acc = {};
    LAS unsigned char* a = L + L_WM + (8 * g16 + q4) * 96 + w * 32 + p4 * 8;
#pragma unroll
    for (int s = 0; s < 4; ++s) acc = mfma16(trd2(a + s * 3072, a + s * 3072 + 384), lds128(L + L_SC + s * 64 + g16 * 16), acc);
    const int n = lane & 15;
    const float v0 = n ? acc[2] : acc[0], v1 = n ? acc[3] : acc[1];
    if (n < 2) stg(rs, (g16 * 2 + n) * 64, OFF_MG + (cc * 24 + w * 8) * 64 + ks4 * 16, v4u{f2u(v0), tagA, f2u(v1), tagB});
  }
  v4u w1raw[2];
  {
    LAS unsigned char* a = L + L_W1R + (8 * hf + q4) * 224 + w * 64 + blk * 32 + p4 * 8;
    w1raw[0] = trd2(a, a + 896);
    w1raw[1] = trd2(a + 3584, a + 3584 + 896);
  }
#pragma unroll
  for (int i = 0; i < 7; ++i) stg(rs, tid * 16, OFF_HID + id * 28672 + i * 4096, pois);
  SB();

  {
    LAS unsigned char* row = L + L_UVS + (tid >> 7) * UVS_CH + (tid & 127) * 4;
#define PUT(r, z) *(LAS unsigned*)(row + (r) * UVS_ROW) = pack2((z).x, (z).y)
    const f32x2 e1 = cexp(are, aim), e8 = cexp(8.f * are, -8.f * aim), e128 = cexp(128.f * are, 128.f * aim);
    const f32x2 e1p = {-e1.y, e1.x}, e8p = {-e8.y, e8.x}, e128p = {-e128.y, e128.x};
    f32x2 z[8], v = e8;
    PUT(40, wv);
    PUT(41, (f32x2{1.f, 0.f}));
    PUT(42, v);
    int vr = 43;
#pragma unroll
    for (int i = 0; i < 8; ++i) {
      z[i] = cmul(i ? z[i - 1] : wv, e1, e1p);
      PUT(i, z[i]);
      if (i < 6) { v = cmul(v, e8, e8p); PUT(vr, v); ++vr; }
    }
#pragma unroll
    for (int i = 0; i < 8; ++i) { z[i] = cmul(z[i], e128, e128p); PUT(8 + i, z[i]); }
#pragma unroll
    for (int u = 0; u < 2; ++u)
#pragma unroll
      for (int i = 0; i < 4; ++i) { z[2 * i + 1] = cmul(z[2 * i + 1], e128, e128p); PUT(16 + 4 * u + i, z[2 * i + 1]); }
#pragma unroll
    for (int u = 0; u < 4; ++u)
#pragma unroll
      for (int i = 0; i < 2; ++i) { z[4 * i + 3] = cmul(z[4 * i + 3], e128, e128p); PUT(24 + 2 * u + i, z[4 * i + 3]); }
#pragma unroll
    for (int u = 0; u < 8; ++u) {
      z[7] = cmul(z[7], e128, e128p); PUT(32 + u, z[7]);
      v = cmul(v, e8, e8p); PUT(vr, v); ++vr;
    }
#undef PUT
  }
  BAR();
  STAMP(3);
  {
    const int ch = w >> 1;
    LAS unsigned char* base = L + L_UVS + ch * UVS_CH + (lane & 15) * UVS_ROW + g16 * 16;
    LAS unsigned char* t0 = base + (w & 1) * 32 * UVS_ROW;
    f32x4 a0 = {}, a1 = {};
#pragma unroll
    for (int s = 0; s < 8; ++s) {
      const v4u vv = lds128(base + 41 * UVS_ROW + s * 64);
      a0 = mfma16(vv, lds128(t0 + s * 64), a0);
      a1 = mfma16(vv, lds128(base + 16 * UVS_ROW + s * 64), a1);
    }
    if ((w & 1) && lane == 8) a0[0] += dsk;
    LAS unsigned char* kt = L + L_KT + ch * KT_CH + (lane & 15) * 32 + g16 * 8;
    *(LAS v2u*)(kt + (w & 1) * 1024) = v2u{pack2(a0[0], a0[1]), pack2(a0[2], a0[3])};
    if (!(w & 1)) *(LAS v2u*)(kt + 512) = v2u{pack2(a1[0], a1[1]), pack2(a1[2], a1[3])};
  }
  v4u gx[4], gs[4], gm, gt;
  const int xgo = OFF_XG + id * 16384;
  const int gmo = OFF_MG + (min((lane >> 2) & 3, 2) * 256 + id) * 64 + p4 * 16;
  const int gto = OFF_MG + (tid < 128 ? (768 + 256 * (tid >> 6) + pks * 16 + ((tid >> 2) & 15)) : (1280 + dt * 32 + ((tid - 128) >> 2))) * 64 + p4 * 16;
  SB();
#pragma unroll
  for (int i = 0; i < 4; ++i) gx[i] = ldg(rs, tid * 16, xgo + i * 4096);
#pragma unroll
  for (int i = 0; i < 4; ++i) gs[i] = ldg(rs, tid * 16, OFF_SG + i * 4096);
  gm = ldg(rs, gmo, 0);
  gt = ldg(rs, gto, 0);
  SB();
  BAR();
#pragma unroll
  for (int c = 0; c < 2; ++c) {
    unsigned k[11];
#pragma unroll
    for (int i = 0; i < 11; ++i) k[i] = *(LAS u16*)(L + L_KT + c * KT_CH + koff[i]);
    unsigned e[5], o[5];
#pragma unroll
    for (int i = 0; i < 5; ++i) { o[i] = k[2 * i] | (k[2 * i + 1] << 16); e[i] = k[2 * i + 1] | (k[2 * i + 2] << 16); }
    LAS unsigned char* d = L + L_KC + c * 4 * KC_STRIDE + 32 + tid * 16;
    *(LAS v4u*)(d) = v4u{e[1], e[2], e[3], e[4]};
    *(LAS v4u*)(d + KC_STRIDE) = v4u{o[1], o[2], o[3], o[4]};
    *(LAS v4u*)(d + 2 * KC_STRIDE) = v4u{e[0], e[1], e[2], e[3]};
    *(LAS v4u*)(d + 3 * KC_STRIDE) = v4u{o[0], o[1], o[2], o[3]};
  }
  SB();
  STAMP(4);
  for (unsigned spin = 0; spin < SPIN_CAP; ++spin) {
    unsigned bx = 0, bs = 0;
#pragma unroll
    for (int i = 0; i < 4; ++i) { bx |= (gx[i].y ^ tagA) | (gx[i].w ^ tagB); bs |= (gs[i].y ^ tagA) | (gs[i].w ^ tagB); }
    const unsigned bm = (gm.y ^ tagA) | (gm.w ^ tagB) | (gt.y ^ tagA) | (gt.w ^ tagB);
    if (!__any((bx | bs | bm) != 0u)) break;
    FRESH();
    if (__any(bx != 0u)) {
#pragma unroll
      for (int i = 0; i < 4; ++i) gx[i] = ldg(rs, tid * 16, xgo + i * 4096);
    }
    if (__any(bs != 0u)) {
#pragma unroll
      for (int i = 0; i < 4; ++i) gs[i] = ldg(rs, tid * 16, OFF_SG + i * 4096);
    }
    gm = ldg(rs, gmo, 0);
    gt = ldg(rs, gto, 0);
  }
  WAITV(0);
  STAMP(5);
  {
    const float m0 = rbf(quad_sum(u2f(gm.x))), m1 = rbf(quad_sum(u2f(gm.z)));
    const int i0 = __builtin_bit_cast(int, m0), i1 = __builtin_bit_cast(int, m1);
    const float sa0 = __builtin_bit_cast(float, __builtin_amdgcn_readlane(i0, 0)), sa1 = __builtin_bit_cast(float, __builtin_amdgcn_readlane(i1, 0));
    const float ca0 = __builtin_bit_cast(float, __builtin_amdgcn_readlane(i0, 4)), ca1 = __builtin_bit_cast(float, __builtin_amdgcn_readlane(i1, 4));
    const float ga0 = __builtin_bit_cast(float, __builtin_amdgcn_readlane(i0, 8)), ga1 = __builtin_bit_cast(float, __builtin_amdgcn_readlane(i1, 8));
    const float A0 = rbf(1.0f + ca0) * ga0, A1 = rbf(1.0f + ca1) * ga1, B0 = sa0 * ga0, B1 = sa1 * ga1;
#pragma unroll
    for (int i = 0; i < 4; ++i) {
      const float mA = bf_lo(gs[i].x), rA = __builtin_amdgcn_rcpf(bf_hi(gs[i].x)), mB = bf_lo(gs[i].z), rB = __builtin_amdgcn_rcpf(bf_hi(gs[i].z));
      const float n00 = (bf_lo(gx[i].x) - mA) * rA, n01 = (bf_hi(gx[i].x) - mA) * rA, n10 = (bf_lo(gx[i].z) - mB) * rB, n11 = (bf_hi(gx[i].z) - mB) * rB;
      const int o = (tid + 256 * i) * 4;
      *(LAS unsigned*)(L + L_H + 480 + o) = pack2(__builtin_fmaf(n00, A0, B0), __builtin_fmaf(n10, A0, B0));
      *(LAS unsigned*)(L + L_H + H_STRIDE + 480 + o) = pack2(__builtin_fmaf(n01, A1, B1), __builtin_fmaf(n11, A1, B1));
      *(LAS unsigned*)(L + L_XB + o) = (gx[i].x & 0xFFFFu) | (gx[i].z << 16);
      *(LAS unsigned*)(L + L_XB + 4096 + o) = (gx[i].x >> 16) | (gx[i].z & 0xFFFF0000u);
    }
    float t0 = rbf(quad_sum(u2f(gt.x))), t1 = rbf(quad_sum(u2f(gt.z)));
    if (w == 0) { t0 *= -LOG2E; t1 *= -LOG2E; }
    if (w == 1) { t0 = rbf(1.0f + t0); t1 = rbf(1.0f + t1); t0 = t0 == 0.f ? 0x1p-30f : t0; t1 = t1 == 0.f ? 0x1p-30f : t1; }
    if (w >= 2) { t0 *= -LN2; t1 *= -LN2; }
    if (p4 == 0) *(LAS f32x2*)(L + L_MODT + (tid >> 2) * 8) = f32x2{t0, t1};
  }
  BAR();
  STAMP(6);
  if (b == 0 && tid == 0) __builtin_amdgcn_raw_buffer_store_b32(epoch + 1u, rs, OFF_EPOCH, 0, SC1);

  const unsigned char* w2w = w2 + (448 * w) * 1024 + dt * 128;
  const unsigned w2voff = (lane >> 3) * 1024 + (((lane & 7) ^ (((lane >> 4) & 1) << 2)) * 16);
#pragma unroll
  for (int n = 0; n < 2 * R1_STEPS; ++n) dma16(w2w + n * 8192, w2voff, L_R1 + w * (R1_STEPS * 2048) + n * 1024);
  {
    const int wo = OFF_W1P + pct * 131072 + w * 32768 + 2 * pks * 1024;
    const bool fill = w == 3 && blk;
    const unsigned one = (lane & 11) == 0 ? 0x3F803F80u : 0u, qm = (lane & 15) == 1 ? 0xFFFFFFFFu : 0u;
#pragma unroll
    for (int s = 0; s < 2; ++s) {
      const f32x4 s0 = *(LAS f32x4*)(L + L_MODT + (16 * s + 8 * hf) * 4), s1 = *(LAS f32x4*)(L + L_MODT + (16 * s + 8 * hf + 4) * 4);
      const f32x4 c0 = *(LAS f32x4*)(L + L_MODT + 128 + (16 * s + 8 * hf) * 4), c1 = *(LAS f32x4*)(L + L_MODT + 128 + (16 * s + 8 * hf + 4) * 4);
      const v4u r = w1raw[s];
      v4u sc;
      sc.x = pack2(bf_lo(r.x) * c0[0], bf_hi(r.x) * c0[1]);
      sc.y = pack2(bf_lo(r.y) * c0[2], bf_hi(r.y) * c0[3]);
      sc.z = pack2(bf_lo(r.z) * c1[0], bf_hi(r.z) * c1[1]);
      sc.w = pack2(bf_lo(r.w) * c1[2], bf_hi(r.w) * c1[3]);
      if (w == 3) {
        v4u q;
        q.x = pack2(s0[0] * __builtin_amdgcn_rcpf(c0[0]), s0[1] * __builtin_amdgcn_rcpf(c0[1]));
        q.y = pack2(s0[2] * __builtin_amdgcn_rcpf(c0[2]), s0[3] * __builtin_amdgcn_rcpf(c0[3]));
        q.z = pack2(s1[0] * __builtin_amdgcn_rcpf(c1[0]), s1[1] * __builtin_amdgcn_rcpf(c1[1]));
        q.w = pack2(s1[2] * __builtin_amdgcn_rcpf(c1[2]), s1[3] * __builtin_amdgcn_rcpf(c1[3]));
#pragma unroll
        for (int j = 0; j < 4; ++j) sc[j] = fill ? ((q[j] & qm) | one) : sc[j];
      }
      stg(rs, lane * 16 + s * 1024, wo, sc);
    }
  }
  STAMP(7);
  {
    const int ch = w >> 1, S = w & 1;
    const int t0 = S ? 6 : 7, t1 = S ? 5 : 4, t2 = S ? 2 : 3, t3 = S ? 1 : 0;
    const int tt[4] = {t0, t1, t2, t3};
    f32x4 acc[4];
#pragma unroll
    for (int i = 0; i < 4; ++i) {
      const v2u xv = *(LAS v2u*)(L + L_XB + ch * 4096 + tt[i] * 512 + (lane & 15) * 32 + g16 * 8);
      acc[i] = f32x4{bf_lo(xv.x), bf_hi(xv.x), bf_lo(xv.y), bf_hi(xv.y)};
    }
    LAS unsigned char* ka = L + L_KC + (ch * 4 + q4) * KC_STRIDE + 2 * (32 - 8 * g16 + 4 * p4) - 8;
    LAS unsigned char* hb = L + L_H + ch * H_STRIDE + (lane & 15) * 32 + g16 * 16;
    if (S) conv_tiles<1>(ka, hb, acc); else conv_tiles<0>(ka, hb, acc);
    STAMP(8);
#pragma unroll
    for (int i = 0; i < 4; i += 2) {
      const auto s0 = __builtin_amdgcn_permlane16_swap(pack2(acc[i][0], acc[i][1]), pack2(acc[i + 1][0], acc[i + 1][1]), false, false);
      const auto s1 = __builtin_amdgcn_permlane16_swap(pack2(acc[i][2], acc[i][3]), pack2(acc[i + 1][2], acc[i + 1][3]), false, false);
      const int t = i == 0 ? ((g16 & 1) ? t1 : t0) : ((g16 & 1) ? t3 : t2);
      const int unit = (t * 4 + q4) * 2 + (g16 >> 1);
      stg(rs, unit * 32768 + p4 * 16, OFF_X1 + (2 * id + ch) * 64, v4u{guard(s0[0]), guard(s1[0]), guard(s0[1]), guard(s1[1])});
    }
  }
  SB();
  STAMP(9);

  v4u w1f[32];
  const int l16 = lane * 16;
  const int w1o = OFF_W1P + ct * 131072 + w * 32768;
  const int qo = (lane >> 1) * 1024 + ((lane & 1) * 32 + 17) * 16, qs = OFF_W1P + ct * 131072 + 98304;
  v4u xs[4][8];
  const int xoff = OFF_X1 + rp * 131072 + w * 1024;
  {
    LAS unsigned char* qw = L + L_XB + (w >> 1) * 4096 + ((w & 1) ? 512 : 1536);
    LAS unsigned char* qr = ((lane & 31) == 0 ? L + L_ONE : qw) + hf * 16;
    f32x16 uv;
    for (unsigned spin = 0;; ++spin) {
      FRESH();
      const v4u qv = ldg(rs, qo, qs);
#pragma unroll
      for (int s = 0; s < 32; ++s) w1f[s] = ldg(rs, l16 + (s & 3) * 1024, w1o + (s >> 2) * 4096);
      SB();
#pragma unroll
      for (int u = 0; u < 3; ++u)
#pragma unroll
        for (int i = 0; i < 8; ++i) xs[u][i] = ldg(rs, l16, xoff + u * 32768 + i * 4096);
      SB();
      *(LAS v4u*)(qw + l16) = qv;
      v4u qa[3];
      qa[0] = lds128(qr);
      qa[1] = lds128(qr + 32);
#pragma unroll
      for (int s = 0; s < 32; ++s) {
        if (s + 2 < 32) qa[(s + 2) % 3] = lds128(qr + (s + 2) * 32);
        if (s == 0) mfma_va<true>(uv, qa[s % 3], w1f[s]); else mfma_va<false>(uv, qa[s % 3], w1f[s]);
        SB();
      }
      mfma_done(uv);
      if (__builtin_expect(!__any(lane < 32 && !(uv[0] + uv[1] == uv[0] + uv[1])) || spin >= SPIN_CAP / 8, 1)) break;
    }
    if (lane < 32) {
      *(LAS float*)(L + L_UVT + (32 * w + lane) * 4) = uv[0];
      *(LAS float*)(L + L_UVT + 512 + (32 * w + lane) * 4) = uv[1];
    }
  }
  SB();
#pragma unroll
  for (int i = 0; i < 8; ++i) xs[3][i] = ldg(rs, l16, xoff + 3 * 32768 + i * 4096);
  SB();
  v4u w2f[28][2];
  const int w2t = (4 * hf + q4) * 128 + blk * 32 + (((p4 & 1) << 1) | (p4 >> 1)) * 8;
  const int w2e = (q4 >> 1) * 64;
  {
    LAS unsigned char* a = L + L_R1 + w * (R1_STEPS * 2048) + w2t;
#pragma unroll
    for (int s = 0; s < R1_STEPS; ++s) {
      w2f[s][0] = trd2(a + w2e + s * 2048, a + w2e + s * 2048 + 1024);
      w2f[s][1] = trd2(a + (w2e ^ 64) + s * 2048, a + (w2e ^ 64) + s * 2048 + 1024);
    }
  }
  SB();
  BAR();
  STAMP(10);
  settle_unit<0>(rs, l16, xoff, xs, max8(xs[0]));
#pragma unroll
  for (int i = 0; i < 8; ++i) *(LAS v4u*)(L + L_XR + w * 1024 + lane * 16 + i * 4096) = xs[0][i];
  BAR();
  STAMP(11);

  v4u res[2];
  const int hoff = OFF_HID + (rp * 4 * 112 + ct * 7 + 2 * w) * 1024;
  LAS unsigned char* uvp = L + L_UVT + (32 * w + 4 * hf) * 4;
  f32x16 last = {};
  res[0] = res[1] = v4u{0u, 0u, 0u, 0u};
  int w_ = w;
  asm volatile("" : "+s"(w_));
  if (w == 3) {
    last = gemm1<true>(rs, L, w1f, xs, res, w2w, w2voff, uvp, xoff, hoff, w, lane, rg, dt);
#pragma unroll
    for (int u = 1; u < 4; ++u)
#pragma unroll
      for (int i = 0; i < 8; ++i) asm volatile("" : "=v"(xs[u][i]));
  }
  if (w_ < 3) last = gemm1<false>(rs, L, w1f, xs, res, w2w, w2voff, uvp, xoff, hoff, w, lane, rg, dt);
  SB();
  STAMP(12);

  constexpr int HW = 42;
  v4u hf_[HW];
  const int ho = OFF_HID + ((rp * 4 + rg) * 112 + 28 * w) * 1024;
#define HOFF(i) l16 + ((i) & 3) * 1024, ho + ((i) / 28) * (2 * 114688) + (((i) % 28) >> 2) * 4096
#pragma unroll
  for (int i = 0; i < 28; ++i) hf_[i] = ldg(rs, HOFF(i));
  SB();
  BAR();
  {
    Epi E;
    epi_begin(E, L, 3, uvp, lane);
    if (w < 3) {
#pragma unroll
      for (int s = 0; s <= 32; ++s) epi_step<16>(s, E, last, rs, uvp, l16, hoff + 3 * 114688);
    } else {
#pragma unroll
      for (int s = 0; s <= 16; ++s) epi_step<8>(s, E, last, rs, uvp, l16, hoff + 3 * 114688);
    }
  }
  SB();
  STAMP(13);
  WAITV(29);
  {
    LAS unsigned char* a = L + L_R2 + w * (R2_STEPS * 2048) + w2t;
#pragma unroll
    for (int s = 0; s < R2_STEPS; ++s) {
      w2f[R1_STEPS + s][0] = trd2(a + w2e + s * 2048, a + w2e + s * 2048 + 1024);
      w2f[R1_STEPS + s][1] = trd2(a + (w2e ^ 64) + s * 2048, a + (w2e ^ 64) + s * 2048 + 1024);
    }
    a = L + L_XR + w * (R3_STEPS * 2048) + w2t;
#pragma unroll
    for (int s = 0; s < R3_STEPS; ++s) {
      w2f[R1_STEPS + R2_STEPS + s][0] = trd2(a + w2e + s * 2048, a + w2e + s * 2048 + 1024);
      w2f[R1_STEPS + R2_STEPS + s][1] = trd2(a + (w2e ^ 64) + s * 2048, a + (w2e ^ 64) + s * 2048 + 1024);
    }
  }
  const f32x4 gb0 = *(LAS f32x4*)(L + L_MODT + 256 + (16 * w + 8 * hf) * 4), gb1 = *(LAS f32x4*)(L + L_MODT + 256 + (16 * w + 8 * hf + 4) * 4);
  SB();
#pragma unroll
  for (int un = 0; un < 2; ++un) {
    f32x16 a0, a1;
#pragma unroll
    for (int gq = 0; gq < 4; ++gq) {
      const int i0 = un * 28 + gq * 7;
      {
        unsigned m = 0;
#pragma unroll
        for (int i = 0; i < 7; ++i) m = max4(m, hf_[(i0 + i) % HW]);
        if (__builtin_expect(__any(m == POISON), 0)) {
          for (unsigned spin = 0; spin < SPIN_CAP; ++spin) {
            FRESH();
#pragma unroll
            for (int i = 0; i < 7; ++i) hf_[(i0 + i) % HW] = ldg(rs, HOFF(i0 + i));
            m = 0;
#pragma unroll
            for (int i = 0; i < 7; ++i) m = max4(m, hf_[(i0 + i) % HW]);
            if (!__any(m == POISON)) break;
          }
        }
      }
#pragma unroll
      for (int i = 0; i < 7; ++i) {
        if (gq == 0 && i == 0) { mfma_av<true>(a0, w2f[0][0], hf_[i0 % HW]); mfma_av<true>(a1, w2f[0][1], hf_[i0 % HW]); }
        else { mfma_av<false>(a0, w2f[gq * 7 + i][0], hf_[(i0 + i) % HW]); mfma_av<false>(a1, w2f[gq * 7 + i][1], hf_[(i0 + i) % HW]); }
        SB();
        if (un == 0 && gq >= 1) {
          const int n = 28 + (gq - 1) * 10 + 2 * i;
          if (i < 5 && n < 56) hf_[n % HW] = ldg(rs, HOFF(n));
          if (i < 5 && n + 1 < 56) hf_[(n + 1) % HW] = ldg(rs, HOFF(n + 1));
          SB();
        }
      }
    }
    mfma_done(a0);
    asm volatile("" : "+v"(a1));
    LAS unsigned char* red = L + (un ? L_R2 : L_XR + 32768);
#pragma unroll
    for (int q = 0; q < 4; ++q) {
      *(LAS f32x4*)(red + w * 8192 + q * 1024 + lane * 16) = f32x4{a0[4 * q], a0[4 * q + 1], a0[4 * q + 2], a0[4 * q + 3]};
      *(LAS f32x4*)(red + w * 8192 + 4096 + q * 1024 + lane * 16) = f32x4{a1[4 * q], a1[4 * q + 1], a1[4 * q + 2], a1[4 * q + 3]};
    }
    BAR();
    f32x4 s0 = {}, s1 = {};
#pragma unroll
    for (int k = 0; k < 4; ++k) {
      s0 += *(LAS f32x4*)(red + k * 8192 + w * 2048 + lane * 16);
      s1 += *(LAS f32x4*)(red + k * 8192 + w * 2048 + 1024 + lane * 16);
    }
    const v4u r = res[un];
    v4u o;
    o.x = pack2(__builtin_fmaf(gb0[0], s0[0], bf_lo(r.x)), __builtin_fmaf(gb0[1], s0[1], bf_hi(r.x)));
    o.y = pack2(__builtin_fmaf(gb0[2], s0[2], bf_lo(r.y)), __builtin_fmaf(gb0[3], s0[3], bf_hi(r.y)));
    o.z = pack2(__builtin_fmaf(gb1[0], s1[0], bf_lo(r.z)), __builtin_fmaf(gb1[1], s1[1], bf_hi(r.z)));
    o.w = pack2(__builtin_fmaf(gb1[2], s1[2], bf_lo(r.w)), __builtin_fmaf(gb1[3], s1[3], bf_hi(r.w)));
    const int pos = lane & 31;
    const int row = rp * 128 + un * 64 + (pos >> 3) * 16 + rg * 8 + (pos & 7);
    *(GAS v4u*)((GAS unsigned char*)out + row * 1024 + dt * 128 + w * 32 + hf * 16) = o;
    if (un == 0) STAMP(14);
  }
#undef HOFF
  STAMP(15);
}

extern "C" void kernel_launch(void* const* d_in, const int* in_sizes, int n_in, void* d_out, int out_size, void* d_ws, size_t ws_size,
                              hipStream_t stream) {
  (void)in_sizes; (void)n_in; (void)out_size; (void)ws_size;
  (void)hipFuncSetAttribute((const void*)s4_block_kernel, hipFuncAttributeMaxDynamicSharedMemorySize, LDS_BYTES);
  s4_block_kernel<<<256, 256, LDS_BYTES, stream>>>(
      (const unsigned char*)d_in[0], (const u16*)d_in[1], (const u16*)d_in[2], (const u16*)d_in[3], (const u16*)d_in[4], (const u16*)d_in[5],
      (const u16*)d_in[6], (const u16*)d_in[7], (const u16*)d_in[8], (const unsigned char*)d_in[10], (const unsigned char*)d_in[11],
      (const u16*)d_in[12], (const unsigned char*)d_in[13], (unsigned char*)d_out, (unsigned char*)d_ws);
}
```

```cpp
#include <hip/hip_runtime.h>

#define GAS __attribute__((address_space(1)))
#define LAS __attribute__((address_space(3)))
#define DI __device__ __forceinline__
typedef unsigned v4u __attribute__((ext_vector_type(4)));
typedef unsigned v2u __attribute__((ext_vector_type(2)));
typedef float f32x2 __attribute__((ext_vector_type(2)));
typedef float f32x4 __attribute__((ext_vector_type(4)));
typedef float f32x16 __attribute__((ext_vector_type(16)));
typedef __bf16 bf16x2 __attribute__((ext_vector_type(2)));
typedef __bf16 bf16x8 __attribute__((ext_vector_type(8)));
typedef short s16x4 __attribute__((ext_vector_type(4)));
typedef unsigned short u16;
typedef unsigned long long u64;

constexpr int SET_BASE = 4096, SET_STRIDE = 16 << 20;
constexpr int OFF_PD = 0;
constexpr int OFF_SG = 4096;
constexpr int OFF_MG = OFF_SG + 16384;
constexpr int OFF_XG = 131072;
constexpr int OFF_X1 = OFF_XG + (4 << 20);
constexpr int OFF_W1P = OFF_X1 + 2097152;
constexpr int OFF_HID = OFF_W1P + 2097152;
constexpr int SET_BYTES = OFF_HID + 7340032;
static_assert(SET_BYTES <= SET_STRIDE, "set size");

constexpr int L_MODT = 0;
constexpr int L_UVT = 512;
constexpr int L_ONE = 1536;
constexpr int L_M = 4096;
constexpr int L_WM = L_M;
constexpr int L_W1R = L_WM + 12288;
constexpr int L_SC = L_W1R + 7424;
constexpr int L_UVS = L_M;
constexpr int UVS_ROW = 528, UVS_CH = 57 * UVS_ROW;
constexpr int L_KC = L_M;
constexpr int KC_STRIDE = 4160;
constexpr int L_H = L_KC + 8 * KC_STRIDE;
constexpr int H_STRIDE = 4576;
constexpr int L_XB = L_H + 2 * H_STRIDE;
constexpr int L_XR = L_M;
constexpr int L_R = L_M + 65536;
constexpr int RS = 11, R_WAVE = RS * 2048;
constexpr int L_TOP = L_R + 4 * R_WAVE;
constexpr int L_KT = L_TOP;
constexpr int KT_CH = 1600;
constexpr int L_STAT = L_TOP;
constexpr int LDS_BYTES = 163840;
static_assert(L_KT + 2 * KT_CH <= LDS_BYTES && L_XB + 8192 <= L_R && L_UVS + 2 * UVS_CH <= L_R, "LDS map");
static_assert(L_H % 16 == 0 && L_XB % 16 == 0 && L_W1R % 16 == 0 && L_SC % 16 == 0 && L_KT % 16 == 0, "alignment");

constexpr int CODE_BYTES = 49152;
constexpr int SC1 = 16;
constexpr int OOB = 0x7FFFFF00;
constexpr unsigned SPIN_CAP = 1u << 15;
constexpr unsigned POISON = 0xFFFFFFFFu;
constexpr float LOG2E = 1.4426950408889634f, LN2 = 0.6931471805599453f, INV2PI = 0.15915494309189535f;

#define SB() __builtin_amdgcn_sched_barrier(0)
#define WAITV(n) asm volatile("s_waitcnt vmcnt(" #n ")" ::: "memory")
#define PIN(x) asm volatile("" : "+v"(x))
#define FRESH() asm volatile("" ::: "memory")
#define BAR() do { asm volatile("s_waitcnt lgkmcnt(0)" ::: "memory"); __builtin_amdgcn_s_barrier(); asm volatile("" ::: "memory"); } while (0)
#define STAMP(i)
#define SARG
#define SPASS
#define COUNT(i)

DI float u2f(unsigned u) { return __builtin_bit_cast(float, u); }
DI unsigned f2u(float f) { return __builtin_bit_cast(unsigned, f); }
DI float dot2(unsigned u, float c) { const bf16x2 v = __builtin_bit_cast(bf16x2, u); return __builtin_amdgcn_fdot2_f32_bf16(v, v, c, false); }
DI float bf_lo(unsigned u) { return __builtin_bit_cast(float, u << 16); }
DI float bf_hi(unsigned u) { return __builtin_bit_cast(float, u & 0xFFFF0000u); }
DI unsigned pack2(float a, float b) { f32x2 v = {a, b}; return __builtin_bit_cast(unsigned, __builtin_convertvector(v, bf16x2)); }
DI float rbf(float a) { return bf_lo(pack2(a, a)); }
DI unsigned guard(unsigned u) { return u < 0xFFFFFFFEu ? u : 0xFFFFFFFEu; }
DI unsigned max3(unsigned a, unsigned b, unsigned c) { unsigned r; asm("v_max3_u32 %0, %1, %2, %3" : "=v"(r) : "v"(a), "v"(b), "v"(c)); return r; }
DI unsigned max4(unsigned m, v4u v) { return max3(max3(m, v.x, v.y), v.z, v.w); }
DI f32x4 mfma16(v4u a, v4u b, f32x4 c) { return __builtin_amdgcn_mfma_f32_16x16x32_bf16(__builtin_bit_cast(bf16x8, a), __builtin_bit_cast(bf16x8, b), c, 0, 0, 0); }
DI f32x16 mfma32(v4u a, v4u b, f32x16 c) { return __builtin_amdgcn_mfma_f32_32x32x16_bf16(__builtin_bit_cast(bf16x8, a), __builtin_bit_cast(bf16x8, b), c, 0, 0, 0); }
template <bool FIRST> DI void mfma_av(f32x16& acc, const v4u& a, const v4u& b) {
  if (FIRST) asm volatile("v_mfma_f32_32x32x16_bf16 %0, %1, %2, 0" : "=&v"(acc) : "a"(a), "v"(b));
  else asm volatile("v_mfma_f32_32x32x16_bf16 %0, %1, %2, %0" : "+v"(acc) : "a"(a), "v"(b));
}
template <bool FIRST> DI void mfma_va(f32x16& acc, const v4u& a, const v4u& b) {
  if (FIRST) asm volatile("v_mfma_f32_32x32x16_bf16 %0, %1, %2, 0" : "=&v"(acc) : "v"(a), "a"(b));
  else asm volatile("v_mfma_f32_32x32x16_bf16 %0, %1, %2, %0" : "+v"(acc) : "v"(a), "a"(b));
}
DI void mfma_done(f32x16& acc) { asm volatile("s_nop 15" : "+v"(acc)); }
DI v2u trd(LAS unsigned char* p) { return __builtin_bit_cast(v2u, __builtin_amdgcn_ds_read_tr16_b64_v4i16((LAS s16x4*)p)); }
DI v4u trd2(LAS unsigned char* p0, LAS unsigned char* p1) { const v2u a = trd(p0), b = trd(p1); return v4u{a.x, a.y, b.x, b.y}; }
DI v4u lds128(LAS unsigned char* p) { return *(LAS v4u*)p; }
DI v4u ldg(__amdgpu_buffer_rsrc_t r, int voff, int soff) { return __builtin_amdgcn_raw_buffer_load_b128(r, voff, soff, SC1); }
DI void stg(__amdgpu_buffer_rsrc_t r, int voff, int soff, v4u v) { __builtin_amdgcn_raw_buffer_store_b128(v, r, voff, soff, SC1); }
template <int CTRL> DI float dpp_add(float v) {
  return v + __builtin_bit_cast(float, __builtin_amdgcn_update_dpp(0, __builtin_bit_cast(int, v), CTRL, 0xF, 0xF, true));
}
DI float swap16_add(float v) { const unsigned u = __builtin_bit_cast(unsigned, v); const auto r = __builtin_amdgcn_permlane16_swap(u, u, false, false);
  return u2f(r[0]) + u2f(r[1]); }
DI float swap32_add(float v) { const unsigned u = __builtin_bit_cast(unsigned, v); const auto r = __builtin_amdgcn_permlane32_swap(u, u, false, false);
  return u2f(r[0]) + u2f(r[1]); }
DI float quad_sum(float v) { v = dpp_add<0xB1>(v); return dpp_add<0x4E>(v); }
DI float wave_sum(float v) { v = quad_sum(v); v = dpp_add<0x141>(v); v = dpp_add<0x140>(v); v = swap16_add(v); return swap32_add(v); }
DI f32x2 cmul(f32x2 a, f32x2 e, f32x2 ep) { const f32x2 t = f32x2{a.x, a.x} * e; return __builtin_elementwise_fma(f32x2{a.y, a.y}, ep, t); }
DI f32x2 cexp(float re2, float rev) {
  const float m = __builtin_amdgcn_exp2f(re2), f = __builtin_amdgcn_fractf(rev);
  return f32x2{m * __builtin_amdgcn_cosf(f), m * __builtin_amdgcn_sinf(f)};
}
DI void dma16(const unsigned char* sbase, unsigned voff, unsigned lds_dst) {
  unsigned keep;
  asm volatile("s_mov_b32 %0, m0\n\ts_mov_b32 m0, %3\n\ts_nop 0\n\tglobal_load_lds_dwordx4 %1, %2\n\ts_mov_b32 m0, %0"
               : "=&s"(keep) : "v"(voff), "s"(sbase), "s"(lds_dst) : "memory");
}
DI unsigned ktab_off(int j) {
  unsigned u = __builtin_bit_cast(unsigned, (float)j);
  u = (u + 0x7FFFu + ((u >> 16) & 1u)) & 0xFFFF0000u;
  const int z = (int)__builtin_bit_cast(float, u) - 1;
  const int uu = z >> 7;
  const int s = 31 - __builtin_clz((unsigned)uu | 1u);
  const int row = 8 * s + ((8 * uu + (z & 7)) >> s);
  int off = row * 32 + ((z >> 3) & 15) * 2;
  off = j == 0 ? 40 * 32 : off;
  return j < 0 ? 48 * 32 : off;
}

struct ConvOrder { int tile[18], u[18]; };
template <int S> constexpr ConvOrder conv_order() {
  const int T[4] = {S ? 6 : 7, S ? 5 : 4, S ? 2 : 3, S ? 1 : 0};
  ConvOrder o = {};
  int done[4] = {0, 0, 0, 0}, last = -1;
  for (int k = 0; k < 18; ++k) {
    int best = -1;
    for (int i = 0; i < 4; ++i)
      if (i != last && done[i] <= T[i] && (best < 0 || T[i] - done[i] > T[best] - done[best])) best = i;
    o.tile[k] = best; o.u[k] = done[best]++; last = best;
  }
  return o;
}
template <int S> DI void conv_tiles(LAS unsigned char* ka, LAS unsigned char* hb, f32x4 (&acc)[4], v4u& pd, __amdgpu_buffer_rsrc_t rs, int pdo) {
  constexpr int T[4] = {S ? 6 : 7, S ? 5 : 4, S ? 2 : 3, S ? 1 : 0};
  constexpr int NF = T[0] + 1;
  constexpr ConvOrder O = conv_order<S>();
  v2u A[2][NF][2];
  v4u B[2][NF];
#pragma unroll
  for (int u = 0; u < NF; ++u) {
    A[0][u][0] = trd(ka + 512 * u + 8);
    A[0][u][1] = trd(ka + 512 * u);
    B[0][u] = lds128(hb + 64 * (8 * u + 7));
  }
#pragma unroll
  for (int r = 0; r < 8; ++r) {
    asm volatile("" :: "v"(B[r & 1][NF - 1]));
    if (r == 4) pd = ldg(rs, pdo, OFF_PD);
#pragma unroll
    for (int k = 0; k < 18; ++k) {
      const int i = O.tile[k], u = O.u[k];
      acc[i] = mfma16(v4u{A[r & 1][u][0].x, A[r & 1][u][0].y, A[r & 1][u][1].x, A[r & 1][u][1].y}, B[r & 1][T[i] - u], acc[i]);
      SB();
      if (r < 7) {
#pragma unroll
        for (int n = (k * 5 + 2) / 3; n < ((k + 1) * 5 + 2) / 3; ++n)
          if (n < 3 * NF) {
            const int f = n / 3;
            if (n % 3 == 2) B[(r + 1) & 1][f] = lds128(hb + 64 * (8 * f - r + 6));
            else A[(r + 1) & 1][f][n % 3] = trd(ka + 512 * f + 64 * (r + 1) + 8 - 8 * (n % 3));
          }
        SB();
      }
    }
  }
}

DI unsigned max8(const v4u (&x)[8]) {
  unsigned m = 0;
#pragma unroll
  for (int i = 0; i < 8; ++i) m = max4(m, x[i]);
  return m;
}
template <int U0, int U1> DI void settle(__amdgpu_buffer_rsrc_t rs, int l16, int goff, v4u (&xs)[4][8], unsigned m SARG) {
  if (__builtin_expect(__any(m == POISON), 0)) {
    for (unsigned spin = 0; spin < SPIN_CAP; ++spin) {
      FRESH();
      COUNT(U0 == 0 ? 22 : 23);
#pragma unroll
      for (int i = 0; i < 8; ++i) {
        unsigned c = 0;
#pragma unroll
        for (int u = U0; u < U1; ++u) c = max4(c, xs[u][i]);
        if (__any(c == POISON)) {
#pragma unroll
          for (int u = U0; u < (U0 == 0 ? 4 : U1); ++u) xs[u][i] = ldg(rs, l16, goff + u * 32768 + i * 4096);
        }
      }
      m = 0;
#pragma unroll
      for (int u = U0; u < U1; ++u) m = max(m, max8(xs[u]));
      if (!__any(m == POISON)) break;
    }
  }
}

struct Epi { float a, c; f32x4 uu, vv; float p1, p2, e, r, y; v4u o; };
DI void epi_begin(Epi& E, LAS unsigned char* st, LAS unsigned char* uvp) {
  const f32x4 q = *(LAS f32x4*)st;
  const float mean = *(LAS float*)(st + 28) * (1.0f / 512.0f);
  E.uu = *(LAS f32x4*)(uvp);
  E.vv = *(LAS f32x4*)(uvp + 512);
  const float var = fmaxf(__builtin_fmaf(-mean, mean, ((q[0] + q[1]) + (q[2] + q[3])) * (1.0f / 512.0f)), 0.f);
  E.a = -LOG2E * __builtin_amdgcn_rsqf(var + 1e-5f);
  E.c = -E.a * mean;
}
DI void epi_step(int j, Epi& E, const f32x16& acc, __amdgpu_buffer_rsrc_t rs, LAS unsigned char* uvp, int v0, int v1, int hoff) {
  if (j >= 2) {
    const int k = j - 2;
    float y = E.p2 * E.r;
    if (k & 1) { unsigned o = guard(pack2(E.y, y)); PIN(o); E.o[(k >> 1) & 3] = o; }
    else { PIN(y); E.y = y; }
    if ((k & 7) == 7) stg(rs, k >> 3 ? v1 : v0, hoff, E.o);
  }
  if (j >= 1 && j <= 16) { float r = __builtin_amdgcn_rcpf(1.0f + E.e); PIN(r); E.r = r; E.p2 = E.p1; }
  if (j < 16) {
    E.p1 = __builtin_fmaf(E.a, acc[j], __builtin_fmaf(E.c, E.uu[j & 3], E.vv[j & 3]));
    float e = __builtin_amdgcn_exp2f(E.p1);
    PIN(e);
    E.e = e;
  }
}
DI void epi_uv(int j, Epi& E, LAS unsigned char* uvp) {
  if ((j & 3) == 3 && j < 15) { E.uu = *(LAS f32x4*)(uvp + ((j + 1) >> 2) * 32); E.vv = *(LAS f32x4*)(uvp + 512 + ((j + 1) >> 2) * 32); }
}
DI f32x4 gram(unsigned lo, unsigned hi, f32x4 g) {
  const s16x4 v = __builtin_bit_cast(s16x4, v2u{lo, hi});
  return __builtin_amdgcn_mfma_f32_4x4x4bf16_1k(v, v, g, 0, 0, 0);
}

struct G1 {
  LAS unsigned char *L, *xr[4], *xw, *uvp, *stw, *str;
  const unsigned char* w2w;
  unsigned w2voff;
  int l16, v1, xoff, hoff, w, lane;
};
template <int U> DI f32x16 gemm1_unit(__amdgpu_buffer_rsrc_t rs, const G1& c, const v4u (&w1)[32], v4u (&xs)[4][8], const f32x16& prev SARG) {
  constexpr int XB = (U & 1) * 32768;
  f32x16 acc;
  f32x4 g = {};
  v4u x[6];
  Epi E;
  unsigned chk = 0;
#define XF(s) trd2(c.xr[(s) & 3] + XB + ((s) & ~3) * 1024, c.xr[(s) & 3] + XB + ((s) & ~3) * 1024 + 256)
#pragma unroll
  for (int s = 0; s < 4; ++s) x[s] = XF(s);
  if (U > 0) epi_begin(E, c.str + ((U - 1) & 1) * 1024, c.uvp);
  SB();
#pragma unroll
  for (int j = 0; j < 16; ++j) {
    const v4u x0 = x[(2 * j) % 6], x1 = x[(2 * j + 1) % 6];
    asm volatile("" :: "v"(x1));
    if (j == 0) mfma_av<true>(acc, w1[0], x0); else mfma_av<false>(acc, w1[2 * j], x0);
    if (j < 14) { x[(2 * j + 4) % 6] = XF(2 * j + 4); x[(2 * j + 5) % 6] = XF(2 * j + 5); }
    SB();
    mfma_av<false>(acc, w1[2 * j + 1], x1);
    SB();
    if (U > 0) epi_step(j, E, prev, rs, c.uvp, c.l16, c.v1, c.hoff + (U - 1) * 114688);
    if (U == 0) {
      if (j >= 2 && j < 6) { chk = max4(max4(chk, xs[1][2 * (j - 2)]), xs[1][2 * (j - 2) + 1]); PIN(chk); }
      if (j == 6) { settle<1, 2>(rs, c.l16, c.xoff, xs, chk SPASS); chk = 0; }
      if (j >= 8) { chk = max4(max4(chk, xs[2 + ((j - 8) >> 2)][2 * (j & 3)]), xs[2 + ((j - 8) >> 2)][2 * (j & 3) + 1]); PIN(chk); }
    }
    SB();
    if ((j & 1) == 0) { g = gram(x0.x, x0.y, g); SB(); }
    if (U > 0) epi_uv(j, E, c.uvp);
    if (U < 3 && j >= 7 && j < 15) *(LAS v4u*)(c.xw + (32768 - XB) + (j - 7) * 4096) = xs[U < 3 ? U + 1 : 3][j - 7];
    if (U == 1 && j < 11) {
      dma16(c.w2w + (2 * RS + 2 * j) * 8192, c.w2voff, L_R + c.w * R_WAVE + 2 * j * 1024);
      dma16(c.w2w + (2 * RS + 2 * j + 1) * 8192, c.w2voff, L_R + c.w * R_WAVE + (2 * j + 1) * 1024);
    }
    if (U == 3 && j < 8) dma16(c.w2w + (4 * RS + j) * 8192, c.w2voff, L_XR + c.w * 8192 + j * 1024);
    SB();
    if ((j & 1) == 0) { g = gram(x0.z, x0.w, g); SB(); }
  }
#undef XF
  if (U > 0) { epi_step(16, E, prev, rs, c.uvp, c.l16, c.v1, c.hoff + (U - 1) * 114688); epi_step(17, E, prev, rs, c.uvp, c.l16, c.v1, c.hoff + (U - 1) * 114688); }
  if (U == 0) settle<2, 4>(rs, c.l16, c.xoff, xs, chk SPASS);
  const float d = (c.lane & 2) ? ((c.lane & 1) ? g[3] : g[2]) : ((c.lane & 1) ? g[1] : g[0]);
  *(LAS float*)(c.stw + (U & 1) * 1024) = swap32_add(d);
  SB();
  mfma_done(acc);
  *(LAS float*)(c.stw + (U & 1) * 1024 + 16) = acc[8];
  return acc;
}

__global__ void __launch_bounds__(256, 1)
s4_block_kernel(const unsigned char* __restrict__ xin, const u16* __restrict__ pAre, const u16* __restrict__ pAim, const u16* __restrict__ pBre,
                const u16* __restrict__ pBim, const u16* __restrict__ pCre, const u16* __restrict__ pCim, const u16* __restrict__ pldt,
                const u16* __restrict__ pD, const unsigned char* __restrict__ w1, const unsigned char* __restrict__ w2, const u16* __restrict__ pc,
                const unsigned char* __restrict__ wmod, unsigned char* __restrict__ out, unsigned char* ws) {
  extern __shared__ __attribute__((aligned(16))) unsigned char smem[];
  LAS unsigned char* L = (LAS unsigned char*)smem;
  const int tid = threadIdx.x, lane = tid & 63, w = __builtin_amdgcn_readfirstlane(tid >> 6);
  const int b = blockIdx.x, xcd = b & 7, slot = b >> 3, id = xcd * 32 + slot;
  const int ks4 = id >> 6, cc = id & 63;
  const int pks = id >> 4, pct = id & 15;
  const int rp = 4 * (xcd >> 1) + (slot >> 3), ct = 8 * (xcd & 1) + (slot & 7), rg = xcd & 1, dt = slot & 7;
  STAMP(0);

  const unsigned epoch_v = __hip_atomic_load((GAS unsigned*)ws, __ATOMIC_RELAXED, __HIP_MEMORY_SCOPE_AGENT);
  const GAS unsigned char* xrow = (const GAS unsigned char*)xin + id * 8192;
  const v4u xa = *(const GAS v4u*)(xrow + (tid & 3) * 2048 + (tid >> 2) * 16), xb = *(const GAS v4u*)(xrow + (tid & 3) * 2048 + 1024 + (tid >> 2) * 16);
  const v4u ra = *(const GAS v4u*)(xrow + w * 2048 + lane * 16), rb = *(const GAS v4u*)(xrow + w * 2048 + 1024 + lane * 16);
  const unsigned char* pcode;
  asm volatile("s_getpc_b64 %0" : "=s"(pcode));
  const unsigned warm = *(const GAS unsigned*)((const GAS unsigned char*)pcode + min((slot & 7) * 8192 + tid * 32, CODE_BYTES));
  const unsigned cval = ((const GAS u16*)pc)[ks4 * 128 + (tid & 127)];
  v4u wm[3];
#pragma unroll
  for (int i = 0; i < 3; ++i) {
    const int idx = tid + 256 * i, row = idx / 6, ch = idx - row * 6;
    wm[i] = *(const GAS v4u*)((const GAS unsigned char*)wmod + (ks4 * 128 + row) * 6144 + cc * 96 + ch * 16);
  }
  v4u wr[2];
#pragma unroll
  for (int i = 0; i < 2; ++i) {
    const int idx = min(tid + 256 * i, 447), row = idx / 14, ch = idx - row * 14;
    wr[i] = *(const GAS v4u*)((const GAS unsigned char*)w1 + (pks * 32 + row) * 3584 + pct * 224 + ch * 16);
  }
  const int pidx = id * 256 + tid;
  const unsigned uAre = ((const GAS u16*)pAre)[pidx], uAim = ((const GAS u16*)pAim)[pidx], uBre = ((const GAS u16*)pBre)[pidx];
  const unsigned uBim = ((const GAS u16*)pBim)[pidx], uCre = ((const GAS u16*)pCre)[pidx], uCim = ((const GAS u16*)pCim)[pidx];
  const unsigned uldt = ((const GAS u16*)pldt)[2 * id + (tid >> 7)], uD = ((const GAS u16*)pD)[2 * id + (tid >> 7)];
  SB();
  unsigned koff[11];
#pragma unroll
  for (int i = 0; i < 11; ++i) { koff[i] = ktab_off(8 * tid - 3 + i); PIN(koff[i]); }
  if (w == 3) *(LAS v4u*)(L + L_ONE + lane * 16) = v4u{0x3F803F80u, 0x3F803F80u, 0x3F803F80u, 0x3F803F80u};
  if (w == 2) *(LAS unsigned*)(L + L_KT + (lane & 1) * KT_CH + 48 * 32) = 0u;
  SB();
  const unsigned epoch = __builtin_amdgcn_readfirstlane(epoch_v);
  const __amdgpu_buffer_rsrc_t rs = __builtin_amdgcn_make_buffer_rsrc((void*)(ws + SET_BASE + (size_t)(epoch & 3u) * SET_STRIDE), 0, SET_BYTES, 0x00020000);
  const unsigned tagA = epoch * 0x9E3779B1u + 0x7F4A7C15u, tagB = ~tagA;
  {
    v4u gq[4];
#pragma unroll
    for (int i = 0; i < 4; ++i) { gq[i] = v4u{xa[i], tagA, xb[i], tagB}; PIN(gq[i]); }
#pragma unroll
    for (int i = 0; i < 4; ++i) stg(rs, (tid >> 2) * 65536 + (tid & 3) * 16, OFF_XG + i * 16384 + id * 64, gq[i]);
    SB();
  }
  STAMP(1);
  {
    float fa[8], fb[8];
#pragma unroll
    for (int i = 0; i < 4; ++i) { fa[2 * i] = bf_lo(ra[i]); fa[2 * i + 1] = bf_hi(ra[i]); fb[2 * i] = bf_lo(rb[i]); fb[2 * i + 1] = bf_hi(rb[i]); }
    float sa_ = 0.f, sb_ = 0.f;
#pragma unroll
    for (int i = 0; i < 8; ++i) { sa_ += fa[i]; sb_ += fb[i]; }
    const float ma = wave_sum(sa_) * (1.0f / 512.0f), mb = wave_sum(sb_) * (1.0f / 512.0f);
    float qa = 0.f, qb = 0.f;
#pragma unroll
    for (int i = 0; i < 8; ++i) { const float da = fa[i] - ma, db = fb[i] - mb; qa = __builtin_fmaf(da, da, qa); qb = __builtin_fmaf(db, db, qb); }
    const float va = rbf(rbf(wave_sum(qa) * (1.0f / 512.0f)) + 1e-5f), vb = rbf(rbf(wave_sum(qb) * (1.0f / 512.0f)) + 1e-5f);
    stg(rs, lane ? OOB : 0, OFF_SG + (id * 4 + w) * 16, v4u{pack2(ma, __builtin_amdgcn_sqrtf(va)), tagA, pack2(mb, __builtin_amdgcn_sqrtf(vb)), tagB});
    SB();
  }
  {
    const float c = bf_lo(cval);
    const float s = c * __builtin_amdgcn_rcpf(1.0f + __builtin_amdgcn_exp2f(-LOG2E * c));
    *(LAS u16*)(L + L_SC + (tid & 127) * 2) = (u16)pack2(s, s);
#pragma unroll
    for (int i = 0; i < 3; ++i) *(LAS v4u*)(L + L_WM + (tid + 256 * i) * 16) = wm[i];
#pragma unroll
    for (int i = 0; i < 2; ++i) *(LAS v4u*)(L + L_W1R + min(tid + 256 * i, 447) * 16) = wr[i];
  }
  asm volatile("" :: "v"(warm));
  BAR();
  STAMP(2);
  const int g16 = lane >> 4, q4 = (lane & 15) >> 2, p4 = lane & 3, hf = lane >> 5, blk = (lane >> 4) & 1, l16 = lane * 16;
  v4u w1raw[2];
  {
    LAS unsigned char* a = L + L_W1R + (8 * hf + q4) * 224 + w * 64 + blk * 32 + p4 * 8;
    w1raw[0] = trd2(a, a + 896);
    w1raw[1] = trd2(a + 3584, a + 3584 + 896);
  }
  if (w < 3) {
    f32x4 acc = {};
    LAS unsigned char* a = L + L_WM + (8 * g16 + q4) * 96 + w * 32 + p4 * 8;
#pragma unroll
    for (int s = 0; s < 4; ++s) acc = mfma16(trd2(a + s * 3072, a + s * 3072 + 384), lds128(L + L_SC + s * 64 + g16 * 16), acc);
    const int n = lane & 15;
    const float v0 = n ? acc[2] : acc[0], v1 = n ? acc[3] : acc[1];
    stg(rs, n < 2 ? (g16 * 2 + n) * 64 : OOB, OFF_MG + (cc * 24 + w * 8) * 64 + ks4 * 16, v4u{f2u(v0), tagA, f2u(v1), tagB});
  }
  SB();
  const v4u pois = {POISON, POISON, POISON, POISON};
  const int x1s = OFF_X1 + (2 * id + (w >> 1)) * 64, w1ps = OFF_W1P + pct * 131072 + w * 32768 + 2 * pks * 1024;
  const int x1a = (((g16 & 1) ? 4 + (w & 1) : 7 - (w & 1)) * 8 + q4 * 2 + (g16 >> 1)) * 32768 + p4 * 16, x1b = x1a - 32 * 32768;
  stg(rs, x1a, x1s, pois);
  stg(rs, x1b, x1s, pois);
  stg(rs, l16, w1ps, pois);
  stg(rs, l16 + 1024, w1ps, pois);
  SB();
  const float dtv = rbf(__builtin_amdgcn_exp2f(LOG2E * bf_lo(uldt)));
  const float are = dtv * bf_lo(uAre) * LOG2E, aim = dtv * bf_lo(uAim) * INV2PI;
  f32x2 wv;
  {
    const float br = dtv * bf_lo(uBre), bi = dtv * bf_lo(uBim), cr = bf_lo(uCre), ci = bf_lo(uCim);
    wv = f32x2{cr * br - ci * bi, cr * bi + ci * br};
  }
  float dsk = bf_lo(uD);
  PIN(dsk);
  const unsigned char* w2w = w2 + (448 * w) * 1024 + dt * 128;
  const unsigned w2voff = (lane >> 3) * 1024 + (((lane & 7) ^ (((lane >> 4) & 1) << 2)) * 16);

  {
    LAS unsigned char* row = L + L_UVS + (tid >> 7) * UVS_CH + (tid & 127) * 4;
    int nput = 0;
#define PUT(r, z) do { *(LAS unsigned*)(row + (r) * UVS_ROW) = pack2((z).x, (z).y); \
      if (nput % 5 == 1 || nput % 5 == 3) { const int n_ = nput / 5 * 2 + nput % 5 / 3; if (n_ < 2 * RS) dma16(w2w + n_ * 8192, w2voff, L_R + w * R_WAVE + n_ * 1024); } \
      ++nput; } while (0)
    f32x2 e1 = cexp(are, aim), e8 = cexp(8.f * are, -8.f * aim), e128 = cexp(128.f * are, 128.f * aim);
    PIN(e1); PIN(e8); PIN(e128); PIN(wv);
    BAR();
    const f32x2 e1p = {-e1.y, e1.x}, e8p = {-e8.y, e8.x}, e128p = {-e128.y, e128.x};
    f32x2 z[8], v = e8;
    PUT(40, wv);
    PUT(41, (f32x2{1.f, 0.f}));
    PUT(42, v);
    int vr = 43;
#pragma unroll
    for (int i = 0; i < 8; ++i) {
      z[i] = cmul(i ? z[i - 1] : wv, e1, e1p);
      PUT(i, z[i]);
      if (i < 6) { v = cmul(v, e8, e8p); PUT(vr, v); ++vr; }
    }
#pragma unroll
    for (int i = 0; i < 8; ++i) { z[i] = cmul(z[i], e128, e128p); PUT(8 + i, z[i]); }
#pragma unroll
    for (int u = 0; u < 2; ++u)
#pragma unroll
      for (int i = 0; i < 4; ++i) { z[2 * i + 1] = cmul(z[2 * i + 1], e128, e128p); PUT(16 + 4 * u + i, z[2 * i + 1]); }
#pragma unroll
    for (int u = 0; u < 4; ++u)
#pragma unroll
      for (int i = 0; i < 2; ++i) { z[4 * i + 3] = cmul(z[4 * i + 3], e128, e128p); PUT(24 + 2 * u + i, z[4 * i + 3]); }
#pragma unroll
    for (int u = 0; u < 8; ++u) {
      z[7] = cmul(z[7], e128, e128p); PUT(32 + u, z[7]);
      v = cmul(v, e8, e8p); PUT(vr, v); ++vr;
    }
#undef PUT
  }
  BAR();
  STAMP(3);
  {
    const int ch = w >> 1;
    LAS unsigned char* base = L + L_UVS + ch * UVS_CH + (lane & 15) * UVS_ROW + g16 * 16;
    LAS unsigned char* t0 = base + (w & 1) * 32 * UVS_ROW;
    f32x4 a0 = {}, a1 = {};
#pragma unroll
    for (int s = 0; s < 8; ++s) {
      const v4u vv = lds128(base + 41 * UVS_ROW + s * 64);
      a0 = mfma16(vv, lds128(t0 + s * 64), a0);
      a1 = mfma16(vv, lds128(base + 16 * UVS_ROW + s * 64), a1);
    }
    if ((w & 1) && lane == 8) a0[0] += dsk;
    LAS unsigned char* kt = L + L_KT + ch * KT_CH + (lane & 15) * 32 + g16 * 8;
    *(LAS v2u*)(kt + (w & 1) * 1024) = v2u{pack2(a0[0], a0[1]), pack2(a0[2], a0[3])};
    if (!(w & 1)) *(LAS v2u*)(kt + 512) = v2u{pack2(a1[0], a1[1]), pack2(a1[2], a1[3])};
  }
  v4u gx[4], gs[4], gm, gt;
  const int xgo = OFF_XG + id * 16384;
  const int gmo = OFF_MG + (min((lane >> 2) & 3, 2) * 256 + id) * 64 + p4 * 16;
  const int gto = OFF_MG + (tid < 128 ? (768 + 256 * (tid >> 6) + pks * 16 + ((tid >> 2) & 15)) : (1280 + dt * 32 + ((tid - 128) >> 2))) * 64 + p4 * 16;
  SB();
#pragma unroll
  for (int i = 0; i < 4; ++i) gx[i] = ldg(rs, tid * 16, xgo + i * 4096);
#pragma unroll
  for (int i = 0; i < 4; ++i) gs[i] = ldg(rs, tid * 16, OFF_SG + i * 4096);
  gm = ldg(rs, gmo, 0);
  gt = ldg(rs, gto, 0);
  SB();
  BAR();
  {
    const v4u z = {0u, 0u, 0u, 0u};
    if (w == 0) *(LAS v4u*)(L + L_KC + ((lane >> 1) & 7) * KC_STRIDE + (lane & 1) * 16) = z;
    if (w == 1) *(LAS v4u*)(L + L_H + hf * H_STRIDE + min(lane & 31, 29) * 16) = z;
  }
#pragma unroll
  for (int c = 0; c < 2; ++c) {
    unsigned k[11];
#pragma unroll
    for (int i = 0; i < 11; ++i) k[i] = *(LAS u16*)(L + L_KT + c * KT_CH + koff[i]);
    unsigned e[5], o[5];
#pragma unroll
    for (int i = 0; i < 5; ++i) { o[i] = k[2 * i] | (k[2 * i + 1] << 16); e[i] = k[2 * i + 1] | (k[2 * i + 2] << 16); }
    LAS unsigned char* d = L + L_KC + c * 4 * KC_STRIDE + 32 + tid * 16;
    *(LAS v4u*)(d) = v4u{e[1], e[2], e[3], e[4]};
    *(LAS v4u*)(d + KC_STRIDE) = v4u{o[1], o[2], o[3], o[4]};
    *(LAS v4u*)(d + 2 * KC_STRIDE) = v4u{e[0], e[1], e[2], e[3]};
    *(LAS v4u*)(d + 3 * KC_STRIDE) = v4u{o[0], o[1], o[2], o[3]};
  }
  SB();
  STAMP(4);
  for (unsigned spin = 0; spin < SPIN_CAP; ++spin) {
    unsigned bx = 0, bs = 0;
#pragma unroll
    for (int i = 0; i < 4; ++i) { bx |= (gx[i].y ^ tagA) | (gx[i].w ^ tagB); bs |= (gs[i].y ^ tagA) | (gs[i].w ^ tagB); }
    const unsigned bm = (gm.y ^ tagA) | (gm.w ^ tagB) | (gt.y ^ tagA) | (gt.w ^ tagB);
    if (!__any((bx | bs | bm) != 0u)) break;
    FRESH();
    COUNT(20);
    if (__any(bx != 0u)) {
#pragma unroll
      for (int i = 0; i < 4; ++i) gx[i] = ldg(rs, tid * 16, xgo + i * 4096);
    }
    if (__any(bs != 0u)) {
#pragma unroll
      for (int i = 0; i < 4; ++i) gs[i] = ldg(rs, tid * 16, OFF_SG + i * 4096);
    }
    gm = ldg(rs, gmo, 0);
    gt = ldg(rs, gto, 0);
  }
  WAITV(0);
  STAMP(5);
  {
    const float m0 = rbf(quad_sum(u2f(gm.x))), m1 = rbf(quad_sum(u2f(gm.z)));
    const int i0 = __builtin_bit_cast(int, m0), i1 = __builtin_bit_cast(int, m1);
    const float sa0 = __builtin_bit_cast(float, __builtin_amdgcn_readlane(i0, 0)), sa1 = __builtin_bit_cast(float, __builtin_amdgcn_readlane(i1, 0));
    const float ca0 = __builtin_bit_cast(float, __builtin_amdgcn_readlane(i0, 4)), ca1 = __builtin_bit_cast(float, __builtin_amdgcn_readlane(i1, 4));
    const float ga0 = __builtin_bit_cast(float, __builtin_amdgcn_readlane(i0, 8)), ga1 = __builtin_bit_cast(float, __builtin_amdgcn_readlane(i1, 8));
    const float A0 = rbf(1.0f + ca0) * ga0, A1 = rbf(1.0f + ca1) * ga1, B0 = sa0 * ga0, B1 = sa1 * ga1;
#pragma unroll
    for (int i = 0; i < 4; ++i) {
      const float mA = bf_lo(gs[i].x), rA = __builtin_amdgcn_rcpf(bf_hi(gs[i].x)), mB = bf_lo(gs[i].z), rB = __builtin_amdgcn_rcpf(bf_hi(gs[i].z));
      const float n00 = (bf_lo(gx[i].x) - mA) * rA, n01 = (bf_hi(gx[i].x) - mA) * rA, n10 = (bf_lo(gx[i].z) - mB) * rB, n11 = (bf_hi(gx[i].z) - mB) * rB;
      const int o = (tid + 256 * i) * 4;
      *(LAS unsigned*)(L + L_H + 480 + o) = pack2(__builtin_fmaf(n00, A0, B0), __builtin_fmaf(n10, A0, B0));
      *(LAS unsigned*)(L + L_H + H_STRIDE + 480 + o) = pack2(__builtin_fmaf(n01, A1, B1), __builtin_fmaf(n11, A1, B1));
      *(LAS unsigned*)(L + L_XB + o) = (gx[i].x & 0xFFFFu) | (gx[i].z << 16);
      *(LAS unsigned*)(L + L_XB + 4096 + o) = (gx[i].x >> 16) | (gx[i].z & 0xFFFF0000u);
    }
    float t0 = rbf(quad_sum(u2f(gt.x))), t1 = rbf(quad_sum(u2f(gt.z)));
    if (w == 0) { t0 *= -LOG2E; t1 *= -LOG2E; }
    if (w == 1) { t0 = rbf(1.0f + t0); t1 = rbf(1.0f + t1); t0 = t0 == 0.f ? 0x1p-30f : t0; t1 = t1 == 0.f ? 0x1p-30f : t1; }
    if (w >= 2) { t0 *= -LN2; t1 *= -LN2; }
    *(LAS f32x2*)(L + L_MODT + (tid >> 2) * 8) = f32x2{t0, t1};
  }
  BAR();
  STAMP(6);
  if (w == 0) stg(rs, lane ? OOB : 0, OFF_PD + id * 16, v4u{tagA, tagB, tagA, tagB});
  if (b == 0 && tid == 0) __hip_atomic_store((GAS unsigned*)ws, epoch + 1u, __ATOMIC_RELAXED, __HIP_MEMORY_SCOPE_AGENT);

  {
    const bool fill = w == 3 && blk;
    const unsigned one = (lane & 11) == 0 ? 0x3F803F80u : 0u, qm = (lane & 15) == 1 ? 0xFFFFFFFFu : 0u;
#pragma unroll
    for (int s = 0; s < 2; ++s) {
      const f32x4 s0 = *(LAS f32x4*)(L + L_MODT + (16 * s + 8 * hf) * 4), s1 = *(LAS f32x4*)(L + L_MODT + (16 * s + 8 * hf + 4) * 4);
      const f32x4 c0 = *(LAS f32x4*)(L + L_MODT + 128 + (16 * s + 8 * hf) * 4), c1 = *(LAS f32x4*)(L + L_MODT + 128 + (16 * s + 8 * hf + 4) * 4);
      const v4u r = w1raw[s];
      v4u sc;
      sc.x = pack2(bf_lo(r.x) * c0[0], bf_hi(r.x) * c0[1]);
      sc.y = pack2(bf_lo(r.y) * c0[2], bf_hi(r.y) * c0[3]);
      sc.z = pack2(bf_lo(r.z) * c1[0], bf_hi(r.z) * c1[1]);
      sc.w = pack2(bf_lo(r.w) * c1[2], bf_hi(r.w) * c1[3]);
      if (w == 3) {
        v4u q;
        q.x = pack2(s0[0] * __builtin_amdgcn_rcpf(c0[0]), s0[1] * __builtin_amdgcn_rcpf(c0[1]));
        q.y = pack2(s0[2] * __builtin_amdgcn_rcpf(c0[2]), s0[3] * __builtin_amdgcn_rcpf(c0[3]));
        q.z = pack2(s1[0] * __builtin_amdgcn_rcpf(c1[0]), s1[1] * __builtin_amdgcn_rcpf(c1[1]));
        q.w = pack2(s1[2] * __builtin_amdgcn_rcpf(c1[2]), s1[3] * __builtin_amdgcn_rcpf(c1[3]));
#pragma unroll
        for (int j = 0; j < 4; ++j) sc[j] = fill ? ((q[j] & qm) | one) : sc[j];
      }
      stg(rs, l16 + s * 1024, w1ps, sc);
      SB();
    }
  }
#pragma unroll
  for (int i = 0; i < 7; ++i) stg(rs, tid * 16, OFF_HID + id * 28672 + i * 4096, pois);
  SB();
  STAMP(7);
  {
    const int ch = w >> 1, S = w & 1;
    const int t0 = S ? 6 : 7, t1 = S ? 5 : 4, t2 = S ? 2 : 3, t3 = S ? 1 : 0;
    const int tt[4] = {t0, t1, t2, t3};
    f32x4 acc[4];
#pragma unroll
    for (int i = 0; i < 4; ++i) {
      const v2u xv = *(LAS v2u*)(L + L_XB + ch * 4096 + tt[i] * 512 + (lane & 15) * 32 + g16 * 8);
      acc[i] = f32x4{bf_lo(xv.x), bf_hi(xv.x), bf_lo(xv.y), bf_hi(xv.y)};
    }
    LAS unsigned char* ka = L + L_KC + (ch * 4 + q4) * KC_STRIDE + 2 * (32 - 8 * g16 + 4 * p4) - 8;
    LAS unsigned char* hb = L + L_H + ch * H_STRIDE + (lane & 15) * 32 + g16 * 16;
    v4u pd;
    if (S) conv_tiles<1>(ka, hb, acc, pd, rs, tid * 16); else conv_tiles<0>(ka, hb, acc, pd, rs, tid * 16);
    STAMP(8);
    v4u o[2];
#pragma unroll
    for (int i = 0; i < 2; ++i) {
      const auto s0 = __builtin_amdgcn_permlane16_swap(pack2(acc[2 * i][0], acc[2 * i][1]), pack2(acc[2 * i + 1][0], acc[2 * i + 1][1]), false, false);
      const auto s1 = __builtin_amdgcn_permlane16_swap(pack2(acc[2 * i][2], acc[2 * i][3]), pack2(acc[2 * i + 1][2], acc[2 * i + 1][3]), false, false);
      o[i] = v4u{guard(s0[0]), guard(s1[0]), guard(s0[1]), guard(s1[1])};
      PIN(o[i]);
    }
    WAITV(0);
    stg(rs, x1a, x1s, o[0]);
    stg(rs, x1b, x1s, o[1]);
    SB();
    for (unsigned spin = 0; spin < SPIN_CAP && __any(((pd.x ^ tagA) | (pd.y ^ tagB)) != 0u); ++spin) {
      FRESH();
      pd = ldg(rs, tid * 16, OFF_PD);
    }
  }
  BAR();
  STAMP(9);

  v4u w1f[32];
  const int w1o = OFF_W1P + ct * 131072 + w * 32768;
  const int qo = ((lane >> 1) ^ w) * 1024 + ((lane & 1) * 32 + 17) * 16, qs = OFF_W1P + ct * 131072 + 98304;
  v4u xs[4][8];
  const int xoff = OFF_X1 + rp * 131072 + w * 1024;
  {
    LAS unsigned char* qw = L + L_XB + w * 1024;
    LAS unsigned char* qr = ((lane & 31) == 0 ? L + L_ONE : qw) + hf * 16;
    f32x16 uv;
    for (unsigned spin = 0;; ++spin) {
      FRESH();
      COUNT(21);
      const v4u qv = ldg(rs, qo, qs);
#pragma unroll
      for (int s = 0; s < 32; ++s) w1f[s] = ldg(rs, l16 + ((s & 3) ^ w) * 1024, w1o + (s >> 2) * 4096);
      SB();
#pragma unroll
      for (int u = 0; u < 3; ++u)
#pragma unroll
        for (int i = 0; i < 8; ++i) xs[u][i] = ldg(rs, l16, xoff + u * 32768 + i * 4096);
      SB();
      *(LAS v4u*)(qw + l16) = qv;
      v4u qa[3];
      qa[0] = lds128(qr);
      qa[1] = lds128(qr + 32);
#pragma unroll
      for (int s = 0; s < 32; ++s) {
        if (s + 2 < 32) qa[(s + 2) % 3] = lds128(qr + (s + 2) * 32);
        if (s == 0) mfma_va<true>(uv, qa[s % 3], w1f[s]); else mfma_va<false>(uv, qa[s % 3], w1f[s]);
        SB();
      }
      mfma_done(uv);
      if (__builtin_expect(!__any(lane < 32 && !(uv[0] + uv[1] == uv[0] + uv[1])) || spin >= SPIN_CAP / 8, 1)) break;
    }
    if (lane < 32) {
      *(LAS float*)(L + L_UVT + (32 * w + lane) * 4) = uv[0];
      *(LAS float*)(L + L_UVT + 512 + (32 * w + lane) * 4) = uv[1];
    }
  }
  SB();
#pragma unroll
  for (int i = 0; i < 8; ++i) xs[3][i] = ldg(rs, l16, xoff + 3 * 32768 + i * 4096);
  SB();
  v4u w2f[28][2];
  const int w2t = (4 * hf + q4) * 128 + blk * 32 + (((p4 & 1) << 1) | (p4 >> 1)) * 8;
  const int w2e = (q4 >> 1) * 64;
#define PULL(dst, n, base) do { LAS unsigned char* a_ = L + (base) + w2t; \
    _Pragma("unroll") for (int s = 0; s < (n); ++s) { \
      w2f[(dst) + s][0] = trd2(a_ + w2e + s * 2048, a_ + w2e + s * 2048 + 1024); \
      w2f[(dst) + s][1] = trd2(a_ + (w2e ^ 64) + s * 2048, a_ + (w2e ^ 64) + s * 2048 + 1024); } } while (0)
  PULL(0, RS, L_R + w * R_WAVE);
  SB();
  STAMP(10);
  settle<0, 1>(rs, l16, xoff, xs, max8(xs[0]) SPASS);
#pragma unroll
  for (int i = 0; i < 8; ++i) *(LAS v4u*)(L + L_XR + w * 1024 + l16 + i * 4096) = xs[0][i];
  BAR();
  STAMP(11);

  v4u res[2];
  G1 c;
  {
    const int tra = L_XR + (hf * 8 + q4) * 64 + blk * 32 + p4 * 8;
    c.L = L; c.xw = L + L_XR + w * 1024 + l16; c.uvp = L + L_UVT + (32 * w + 4 * hf) * 4;
#pragma unroll
    for (int i = 0; i < 4; ++i) c.xr[i] = L + tra + (i ^ w) * 1024;
    c.str = L + L_STAT + (lane & 31) * 32; c.stw = c.str + w * 4;
    c.w2w = w2w; c.w2voff = w2voff; c.l16 = l16; c.v1 = w == 3 ? OOB : l16 + 1024; c.xoff = xoff;
    c.hoff = OFF_HID + (rp * 4 * 112 + ct * 7 + 2 * w) * 1024; c.w = w; c.lane = lane;
  }
  LAS unsigned char* rr = c.xr[0] + rg * 32768 + dt * 4096;
  f32x16 a0 = {}, a1;
  a0 = gemm1_unit<0>(rs, c, w1f, xs, a0 SPASS);
  STAMP(16);
  if (rg == 0) res[0] = trd2(rr, rr + 256);
  BAR();
  a1 = gemm1_unit<1>(rs, c, w1f, xs, a0 SPASS);
  STAMP(17);
  if (rg == 1) res[0] = trd2(rr, rr + 256);
  BAR();
  a0 = gemm1_unit<2>(rs, c, w1f, xs, a1 SPASS);
  STAMP(18);
  if (rg == 0) res[1] = trd2(rr, rr + 256);
  BAR();
  a1 = gemm1_unit<3>(rs, c, w1f, xs, a0 SPASS);
  if (rg == 1) res[1] = trd2(rr, rr + 256);
  BAR();
  STAMP(12);

  constexpr int HW = 35;
  v4u hq[HW];
  const int ho = OFF_HID + ((rp * 4 + rg) * 112 + 28 * w) * 1024;
#define HOFF(i) l16 + ((i) & 3) * 1024, ho + ((i) / 28) * (2 * 114688) + (((i) % 28) >> 2) * 4096
#pragma unroll
  for (int n = 0; n < 4; ++n) dma16(w2w + (4 * RS + 8 + n) * 8192, w2voff, L_XR + 32768 + w * 4096 + n * 1024);
  {
    Epi E;
    epi_begin(E, c.str + 1024, c.uvp);
#pragma unroll
    for (int j = 0; j < 18; ++j) {
      epi_step(j, E, a1, rs, c.uvp, l16, c.v1, c.hoff + 3 * 114688);
      epi_uv(j, E, c.uvp);
      if (j < 10) hq[j] = ldg(rs, HOFF(j));
      SB();
    }
  }
#pragma unroll
  for (int i = 10; i < 28; ++i) hq[i] = ldg(rs, HOFF(i));
  SB();
  STAMP(13);
  WAITV(30);
  PULL(RS, RS, L_R + w * R_WAVE);
  PULL(2 * RS, 4, L_XR + w * 8192);
  PULL(2 * RS + 4, 2, L_XR + 32768 + w * 4096);
  const f32x4 gb0 = *(LAS f32x4*)(L + L_MODT + 256 + (16 * w + 8 * hf) * 4), gb1 = *(LAS f32x4*)(L + L_MODT + 256 + (16 * w + 8 * hf + 4) * 4);
  SB();
  STAMP(19);
  f32x16 d[2][2];
  LAS unsigned char* redw = L + L_R + w * R_WAVE + l16;
  LAS unsigned char* redr = L + L_R + w * 2048 + l16;
  const int orow = (rp * 128 + ((lane & 31) >> 3) * 16 + rg * 8 + (lane & 7)) * 1024 + dt * 128 + w * 32 + hf * 16;
#define REDW(un, q) *(LAS f32x4*)(redw + (un) * 8192 + (q) * 1024) = \
    f32x4{d[un][(q) >> 2][4 * ((q) & 3)], d[un][(q) >> 2][4 * ((q) & 3) + 1], d[un][(q) >> 2][4 * ((q) & 3) + 2], d[un][(q) >> 2][4 * ((q) & 3) + 3]}
#define FINISH(un) do { f32x4 s0 = {}, s1 = {}; \
    _Pragma("unroll") for (int k = 0; k < 4; ++k) { \
      s0 += *(LAS f32x4*)(redr + k * R_WAVE + (un) * 8192); s1 += *(LAS f32x4*)(redr + k * R_WAVE + (un) * 8192 + 1024); } \
    const v4u r = res[un]; v4u o; \
    o.x = pack2(__builtin_fmaf(gb0[0], s0[0], bf_lo(r.x)), __builtin_fmaf(gb0[1], s0[1], bf_hi(r.x))); \
    o.y = pack2(__builtin_fmaf(gb0[2], s0[2], bf_lo(r.y)), __builtin_fmaf(gb0[3], s0[3], bf_hi(r.y))); \
    o.z = pack2(__builtin_fmaf(gb1[0], s1[0], bf_lo(r.z)), __builtin_fmaf(gb1[1], s1[1], bf_hi(r.z))); \
    o.w = pack2(__builtin_fmaf(gb1[2], s1[2], bf_lo(r.w)), __builtin_fmaf(gb1[3], s1[3], bf_hi(r.w))); \
    *(GAS v4u*)((GAS unsigned char*)out + orow + (un) * 65536) = o; } while (0)
  unsigned m = 0;
#pragma unroll
  for (int i = 0; i < 7; ++i) m = max4(m, hq[i]);
#pragma unroll
  for (int G = 0; G < 8; ++G) {
    const int un = G >> 2, gq = G & 3, i0 = G * 7;
    if (__builtin_expect(__any(m == POISON), 0)) {
      for (unsigned spin = 0; spin < SPIN_CAP; ++spin) {
        FRESH();
        COUNT(24 + un);
#pragma unroll
        for (int i = 0; i < 7; ++i) hq[(i0 + i) % HW] = ldg(rs, HOFF(i0 + i));
        m = 0;
#pragma unroll
        for (int i = 0; i < 7; ++i) m = max4(m, hq[(i0 + i) % HW]);
        if (!__any(m == POISON)) break;
      }
    }
    m = 0;
#pragma unroll
    for (int i = 0; i < 7; ++i) {
      if (gq == 0 && i == 0) mfma_av<true>(d[un][0], w2f[0][0], hq[i0 % HW]); else mfma_av<false>(d[un][0], w2f[gq * 7 + i][0], hq[(i0 + i) % HW]);
      SB();
      if (G < 4) { const int n = 28 + i0 + i; hq[n % HW] = ldg(rs, HOFF(n)); }
      if (G == 4 && i >= 1) REDW(0, i - 1);
      if (G == 5 && i < 2) REDW(0, 6 + i);
      SB();
      if (gq == 0 && i == 0) mfma_av<true>(d[un][1], w2f[0][1], hq[i0 % HW]); else mfma_av<false>(d[un][1], w2f[gq * 7 + i][1], hq[(i0 + i) % HW]);
      SB();
      if (G < 7) { m = max4(m, hq[(i0 + 7 + i) % HW]); PIN(m); }
      SB();
    }
    if (G == 3) STAMP(14);
  }
  mfma_done(d[1][0]);
  PIN(d[1][1]);
#pragma unroll
  for (int q = 0; q < 8; ++q) REDW(1, q);
  BAR();
  FINISH(0);
  FINISH(1);
#undef HOFF
#undef PULL
#undef REDW
#undef FINISH
  STAMP(15);
}

extern "C" void kernel_launch(void* const* d_in, const int* in_sizes, int n_in, void* d_out, int out_size, void* d_ws, size_t ws_size,
                              hipStream_t stream) {
  (void)in_sizes; (void)n_in; (void)out_size; (void)ws_size;
  (void)hipFuncSetAttribute((const void*)s4_block_kernel, hipFuncAttributeMaxDynamicSharedMemorySize, LDS_BYTES);
  s4_block_kernel<<<256, 256, LDS_BYTES, stream>>>(
      (const unsigned char*)d_in[0], (const u16*)d_in[1], (const u16*)d_in[2], (const u16*)d_in[3], (const u16*)d_in[4], (const u16*)d_in[5],
      (const u16*)d_in[6], (const u16*)d_in[7], (const u16*)d_in[8], (const unsigned char*)d_in[10], (const unsigned char*)d_in[11],
      (const u16*)d_in[12], (const unsigned char*)d_in[13], (unsigned char*)d_out, (unsigned char*)d_ws);
}
```

```cpp
#include <hip/hip_runtime.h>

#define GAS __attribute__((address_space(1)))
#define LAS __attribute__((address_space(3)))
#define DI __device__ __forceinline__
typedef unsigned v4u __attribute__((ext_vector_type(4)));
typedef unsigned v2u __attribute__((ext_vector_type(2)));
typedef float f32x2 __attribute__((ext_vector_type(2)));
typedef float f32x4 __attribute__((ext_vector_type(4)));
typedef float f32x16 __attribute__((ext_vector_type(16)));
typedef __bf16 bf16x2 __attribute__((ext_vector_type(2)));
typedef __bf16 bf16x8 __attribute__((ext_vector_type(8)));
typedef short s16x4 __attribute__((ext_vector_type(4)));
typedef unsigned short u16;
typedef unsigned long long u64;

constexpr int SET_BASE = 4096, SET_STRIDE = 16 << 20;
constexpr int OFF_PD = 0;
constexpr int OFF_SG = 4096;
constexpr int OFF_MG = OFF_SG + 16384;
constexpr int OFF_XG = 131072;
constexpr int OFF_X1 = OFF_XG + (4 << 20);
constexpr int OFF_W1P = OFF_X1 + 2097152;
constexpr int OFF_HID = OFF_W1P + 2097152;
constexpr int SET_BYTES = OFF_HID + 7340032;
static_assert(SET_BYTES <= SET_STRIDE, "set size");

constexpr int L_MODT = 0;
constexpr int L_UVT = 512;
constexpr int L_ONE = 1536;
constexpr int L_M = 4096;
constexpr int L_WM = L_M;
constexpr int L_W1R = L_WM + 12288;
constexpr int L_SC = L_W1R + 7424;
constexpr int L_UVS = L_M;
constexpr int UVS_ROW = 528, UVS_CH = 57 * UVS_ROW;
constexpr int L_KC = L_M;
constexpr int KC_STRIDE = 4160;
constexpr int L_H = L_KC + 8 * KC_STRIDE;
constexpr int H_STRIDE = 4576;
constexpr int L_XB = L_H + 2 * H_STRIDE;
constexpr int L_XR = L_M;
constexpr int L_R = L_M + 65536;
constexpr int RS = 11, R_WAVE = RS * 2048;
constexpr int L_TOP = L_R + 4 * R_WAVE;
constexpr int L_KT = L_TOP;
constexpr int KT_CH = 1600;
constexpr int L_STAT = L_TOP;
constexpr int LDS_BYTES = 163840;
static_assert(L_KT + 2 * KT_CH <= LDS_BYTES && L_XB + 8192 <= L_R && L_UVS + 2 * UVS_CH <= L_R, "LDS map");
static_assert(L_H % 16 == 0 && L_XB % 16 == 0 && L_W1R % 16 == 0 && L_SC % 16 == 0 && L_KT % 16 == 0, "alignment");

constexpr int CODE_BYTES = 49152;
constexpr int SC1 = 16;
constexpr int OOB = 0x7FFFFF00;
constexpr unsigned SPIN_CAP = 1u << 15;
constexpr unsigned POISON = 0xFFFFFFFFu;
constexpr float LOG2E = 1.4426950408889634f, LN2 = 0.6931471805599453f, INV2PI = 0.15915494309189535f;

#define SB() __builtin_amdgcn_sched_barrier(0)
#define WAITV(n) asm volatile("s_waitcnt vmcnt(" #n ")" ::: "memory")
#define PIN(x) asm volatile("" : "+v"(x))
#define FRESH() asm volatile("" ::: "memory")
#define BAR() do { asm volatile("s_waitcnt lgkmcnt(0)" ::: "memory"); __builtin_amdgcn_s_barrier(); asm volatile("" ::: "memory"); } while (0)
#define STAMP(i)
#define SARG
#define SPASS
#define COUNT(i)

DI float u2f(unsigned u) { return __builtin_bit_cast(float, u); }
DI unsigned f2u(float f) { return __builtin_bit_cast(unsigned, f); }
DI float dot2(unsigned u, float c) { const bf16x2 v = __builtin_bit_cast(bf16x2, u); return __builtin_amdgcn_fdot2_f32_bf16(v, v, c, false); }
DI float bf_lo(unsigned u) { return __builtin_bit_cast(float, u << 16); }
DI float bf_hi(unsigned u) { return __builtin_bit_cast(float, u & 0xFFFF0000u); }
DI unsigned pack2(float a, float b) { f32x2 v = {a, b}; return __builtin_bit_cast(unsigned, __builtin_convertvector(v, bf16x2)); }
DI float rbf(float a) { return bf_lo(pack2(a, a)); }
DI unsigned guard(unsigned u) { return u < 0xFFFFFFFEu ? u : 0xFFFFFFFEu; }
DI unsigned max3(unsigned a, unsigned b, unsigned c) { unsigned r; asm("v_max3_u32 %0, %1, %2, %3" : "=v"(r) : "v"(a), "v"(b), "v"(c)); return r; }
DI unsigned max4(unsigned m, v4u v) { return max3(max3(m, v.x, v.y), v.z, v.w); }
DI f32x4 mfma16(v4u a, v4u b, f32x4 c) { return __builtin_amdgcn_mfma_f32_16x16x32_bf16(__builtin_bit_cast(bf16x8, a), __builtin_bit_cast(bf16x8, b), c, 0, 0, 0); }
DI f32x16 mfma32(v4u a, v4u b, f32x16 c) { return __builtin_amdgcn_mfma_f32_32x32x16_bf16(__builtin_bit_cast(bf16x8, a), __builtin_bit_cast(bf16x8, b), c, 0, 0, 0); }
template <bool FIRST> DI void mfma_av(f32x16& acc, const v4u& a, const v4u& b) {
  if (FIRST) asm volatile("v_mfma_f32_32x32x16_bf16 %0, %1, %2, 0" : "=&v"(acc) : "a"(a), "v"(b));
  else asm volatile("v_mfma_f32_32x32x16_bf16 %0, %1, %2, %0" : "+v"(acc) : "a"(a), "v"(b));
}
template <bool FIRST> DI void mfma_va(f32x16& acc, const v4u& a, const v4u& b) {
  if (FIRST) asm volatile("v_mfma_f32_32x32x16_bf16 %0, %1, %2, 0" : "=&v"(acc) : "v"(a), "a"(b));
  else asm volatile("v_mfma_f32_32x32x16_bf16 %0, %1, %2, %0" : "+v"(acc) : "v"(a), "a"(b));
}
DI void mfma_done(f32x16& acc) { asm volatile("s_nop 15" : "+v"(acc)); }
DI v2u trd(LAS unsigned char* p) { return __builtin_bit_cast(v2u, __builtin_amdgcn_ds_read_tr16_b64_v4i16((LAS s16x4*)p)); }
DI v4u trd2(LAS unsigned char* p0, LAS unsigned char* p1) { const v2u a = trd(p0), b = trd(p1); return v4u{a.x, a.y, b.x, b.y}; }
DI v4u lds128(LAS unsigned char* p) { return *(LAS v4u*)p; }
DI v4u ldg(__amdgpu_buffer_rsrc_t r, int voff, int soff) { return __builtin_amdgcn_raw_buffer_load_b128(r, voff, soff, SC1); }
DI void stg(__amdgpu_buffer_rsrc_t r, int voff, int soff, v4u v) { __builtin_amdgcn_raw_buffer_store_b128(v, r, voff, soff, SC1); }
template <int CTRL> DI float dpp_add(float v) {
  return v + __builtin_bit_cast(float, __builtin_amdgcn_update_dpp(0, __builtin_bit_cast(int, v), CTRL, 0xF, 0xF, true));
}
DI float swap16_add(float v) { const unsigned u = __builtin_bit_cast(unsigned, v); const auto r = __builtin_amdgcn_permlane16_swap(u, u, false, false);
  return u2f(r[0]) + u2f(r[1]); }
DI float swap32_add(float v) { const unsigned u = __builtin_bit_cast(unsigned, v); const auto r = __builtin_amdgcn_permlane32_swap(u, u, false, false);
  return u2f(r[0]) + u2f(r[1]); }
DI float quad_sum(float v) { v = dpp_add<0xB1>(v); return dpp_add<0x4E>(v); }
DI float wave_sum(float v) { v = quad_sum(v); v = dpp_add<0x141>(v); v = dpp_add<0x140>(v); v = swap16_add(v); return swap32_add(v); }
DI f32x2 cmul(f32x2 a, f32x2 e, f32x2 ep) { const f32x2 t = f32x2{a.x, a.x} * e; return __builtin_elementwise_fma(f32x2{a.y, a.y}, ep, t); }
DI f32x2 cexp(float re2, float rev) {
  const float m = __builtin_amdgcn_exp2f(re2), f = __builtin_amdgcn_fractf(rev);
  return f32x2{m * __builtin_amdgcn_cosf(f), m * __builtin_amdgcn_sinf(f)};
}
DI void dma16(const unsigned char* sbase, unsigned voff, unsigned lds_dst) {
  unsigned keep;
  asm volatile("s_mov_b32 %0, m0\n\ts_mov_b32 m0, %3\n\ts_nop 0\n\tglobal_load_lds_dwordx4 %1, %2\n\ts_mov_b32 m0, %0"
               : "=&s"(keep) : "v"(voff), "s"(sbase), "s"(lds_dst) : "memory");
}
DI unsigned ktab_off(int j) {
  unsigned u = __builtin_bit_cast(unsigned, (float)j);
  u = (u + 0x7FFFu + ((u >> 16) & 1u)) & 0xFFFF0000u;
  const int z = (int)__builtin_bit_cast(float, u) - 1;
  const int uu = z >> 7;
  const int s = 31 - __builtin_clz((unsigned)uu | 1u);
  const int row = 8 * s + ((8 * uu + (z & 7)) >> s);
  int off = row * 32 + ((z >> 3) & 15) * 2;
  off = j == 0 ? 40 * 32 : off;
  return j < 0 ? 48 * 32 : off;
}

struct ConvOrder { int tile[18], u[18]; };
template <int S> constexpr ConvOrder conv_order() {
  const int T[4] = {S ? 6 : 7, S ? 5 : 4, S ? 2 : 3, S ? 1 : 0};
  ConvOrder o = {};
  int done[4] = {0, 0, 0, 0}, last = -1;
  for (int k = 0; k < 18; ++k) {
    int best = -1;
    for (int i = 0; i < 4; ++i)
      if (i != last && done[i] <= T[i] && (best < 0 || T[i] - done[i] > T[best] - done[best])) best = i;
    o.tile[k] = best; o.u[k] = done[best]++; last = best;
  }
  return o;
}
template <int S> DI void conv_tiles(LAS unsigned char* ka, LAS unsigned char* hb, f32x4 (&acc)[4], v4u& pd, __amdgpu_buffer_rsrc_t rs, int pdo) {
  constexpr int T[4] = {S ? 6 : 7, S ? 5 : 4, S ? 2 : 3, S ? 1 : 0};
  constexpr int NF = T[0] + 1;
  constexpr ConvOrder O = conv_order<S>();
  v2u A[2][NF][2];
  v4u B[2][NF];
#pragma unroll
  for (int u = 0; u < NF; ++u) {
    A[0][u][0] = trd(ka + 512 * u + 8);
    A[0][u][1] = trd(ka + 512 * u);
    B[0][u] = lds128(hb + 64 * (8 * u + 7));
  }
#pragma unroll
  for (int r = 0; r < 8; ++r) {
    asm volatile("" :: "v"(B[r & 1][NF - 1]));
    if (r == 4) pd = ldg(rs, pdo, OFF_PD);
#pragma unroll
    for (int k = 0; k < 18; ++k) {
      const int i = O.tile[k], u = O.u[k];
      acc[i] = mfma16(v4u{A[r & 1][u][0].x, A[r & 1][u][0].y, A[r & 1][u][1].x, A[r & 1][u][1].y}, B[r & 1][T[i] - u], acc[i]);
      SB();
      if (r < 7) {
#pragma unroll
        for (int n = (k * 5 + 2) / 3; n < ((k + 1) * 5 + 2) / 3; ++n)
          if (n < 3 * NF) {
            const int f = n / 3;
            if (n % 3 == 2) B[(r + 1) & 1][f] = lds128(hb + 64 * (8 * f - r + 6));
            else A[(r + 1) & 1][f][n % 3] = trd(ka + 512 * f + 64 * (r + 1) + 8 - 8 * (n % 3));
          }
        SB();
      }
    }
  }
}

DI unsigned max8(const v4u (&x)[8]) {
  unsigned m = 0;
#pragma unroll
  for (int i = 0; i < 8; ++i) m = max4(m, x[i]);
  return m;
}
template <int U0, int U1> DI void settle(__amdgpu_buffer_rsrc_t rs, int l16, int goff, v4u (&xs)[4][8], unsigned m SARG) {
  if (__builtin_expect(__any(m == POISON), 0)) {
    for (unsigned spin = 0; spin < SPIN_CAP; ++spin) {
      FRESH();
      COUNT(U0 == 0 ? 22 : 23);
#pragma unroll
      for (int i = 0; i < 8; ++i) {
        unsigned c = 0;
#pragma unroll
        for (int u = U0; u < U1; ++u) c = max4(c, xs[u][i]);
        if (__any(c == POISON)) {
#pragma unroll
          for (int u = U0; u < (U0 == 0 ? 4 : U1); ++u) xs[u][i] = ldg(rs, l16, goff + u * 32768 + i * 4096);
        }
      }
      m = 0;
#pragma unroll
      for (int u = U0; u < U1; ++u) m = max(m, max8(xs[u]));
      if (!__any(m == POISON)) break;
    }
  }
}

struct Epi { float a, c; f32x4 uu, vv; float p1, p2, e, r, y; v4u o; };
DI void epi_begin(Epi& E, LAS unsigned char* st, LAS unsigned char* uvp) {
  const f32x4 q = *(LAS f32x4*)st;
  const float mean = *(LAS float*)(st + 28) * (1.0f / 512.0f);
  E.uu = *(LAS f32x4*)(uvp);
  E.vv = *(LAS f32x4*)(uvp + 512);
  const float var = fmaxf(__builtin_fmaf(-mean, mean, ((q[0] + q[1]) + (q[2] + q[3])) * (1.0f / 512.0f)), 0.f);
  E.a = -LOG2E * __builtin_amdgcn_rsqf(var + 1e-5f);
  E.c = -E.a * mean;
}
DI void epi_step(int j, Epi& E, const f32x16& acc, __amdgpu_buffer_rsrc_t rs, LAS unsigned char* uvp, int v0, int v1, int hoff) {
  if (j >= 2) {
    const int k = j - 2;
    float y = E.p2 * E.r;
    if (k & 1) { unsigned o = guard(pack2(E.y, y)); PIN(o); E.o[(k >> 1) & 3] = o; }
    else { PIN(y); E.y = y; }
    if ((k & 7) == 7) stg(rs, k >> 3 ? v1 : v0, hoff, E.o);
  }
  if (j >= 1 && j <= 16) { float r = __builtin_amdgcn_rcpf(1.0f + E.e); PIN(r); E.r = r; E.p2 = E.p1; }
  if (j < 16) {
    E.p1 = __builtin_fmaf(E.a, acc[j], __builtin_fmaf(E.c, E.uu[j & 3], E.vv[j & 3]));
    float e = __builtin_amdgcn_exp2f(E.p1);
    PIN(e);
    E.e = e;
  }
}
DI void epi_uv(int j, Epi& E, LAS unsigned char* uvp) {
  if ((j & 3) == 3 && j < 15) { E.uu = *(LAS f32x4*)(uvp + ((j + 1) >> 2) * 32); E.vv = *(LAS f32x4*)(uvp + 512 + ((j + 1) >> 2) * 32); }
}
DI f32x4 gram(unsigned lo, unsigned hi, f32x4 g) {
  const s16x4 v = __builtin_bit_cast(s16x4, v2u{lo, hi});
  return __builtin_amdgcn_mfma_f32_4x4x4bf16_1k(v, v, g, 0, 0, 0);
}

struct G1 {
  LAS unsigned char *L, *xr[4], *xw, *uvp, *stw, *str;
  const unsigned char* w2w;
  unsigned w2voff;
  int l16, v1, xoff, hoff, w, lane;
};
template <int U> DI f32x16 gemm1_unit(__amdgpu_buffer_rsrc_t rs, const G1& c, const v4u (&w1)[32], v4u (&xs)[4][8], const f32x16& prev SARG) {
  constexpr int XB = (U & 1) * 32768;
  f32x16 acc;
  f32x4 g = {};
  v4u x[6];
  Epi E;
  unsigned chk = 0;
#define XF(s) trd2(c.xr[(s) & 3] + XB + ((s) & ~3) * 1024, c.xr[(s) & 3] + XB + ((s) & ~3) * 1024 + 256)
#pragma unroll
  for (int s = 0; s < 4; ++s) x[s] = XF(s);
  if (U > 0) epi_begin(E, c.str + ((U - 1) & 1) * 1024, c.uvp);
  SB();
#pragma unroll
  for (int j = 0; j < 16; ++j) {
    const v4u x0 = x[(2 * j) % 6], x1 = x[(2 * j + 1) % 6];
    asm volatile("" :: "v"(x1));
    if (j == 0) mfma_av<true>(acc, w1[0], x0); else mfma_av<false>(acc, w1[2 * j], x0);
    if (j < 14) { x[(2 * j + 4) % 6] = XF(2 * j + 4); x[(2 * j + 5) % 6] = XF(2 * j + 5); }
    SB();
    mfma_av<false>(acc, w1[2 * j + 1], x1);
    SB();
    if (U > 0) epi_step(j, E, prev, rs, c.uvp, c.l16, c.v1, c.hoff + (U - 1) * 114688);
    if (U == 0) {
      if (j >= 2 && j < 6) { chk = max4(max4(chk, xs[1][2 * (j - 2)]), xs[1][2 * (j - 2) + 1]); PIN(chk); }
      if (j == 6) { settle<1, 2>(rs, c.l16, c.xoff, xs, chk SPASS); chk = 0; }
      if (j >= 8) { chk = max4(max4(chk, xs[2 + ((j - 8) >> 2)][2 * (j & 3)]), xs[2 + ((j - 8) >> 2)][2 * (j & 3) + 1]); PIN(chk); }
    }
    SB();
    if ((j & 1) == 0) { g = gram(x0.x, x0.y, g); SB(); }
    if (U > 0) epi_uv(j, E, c.uvp);
    if (U < 3 && j >= 7 && j < 15) *(LAS v4u*)(c.xw + (32768 - XB) + (j - 7) * 4096) = xs[U < 3 ? U + 1 : 3][j - 7];
    if (U == 1 && j < 11) {
      dma16(c.w2w + (2 * RS + 2 * j) * 8192, c.w2voff, L_R + c.w * R_WAVE + 2 * j * 1024);
      dma16(c.w2w + (2 * RS + 2 * j + 1) * 8192, c.w2voff, L_R + c.w * R_WAVE + (2 * j + 1) * 1024);
    }
    if (U == 3 && j < 8) dma16(c.w2w + (4 * RS + j) * 8192, c.w2voff, L_XR + c.w * 8192 + j * 1024);
    SB();
    if ((j & 1) == 0) { g = gram(x0.z, x0.w, g); SB(); }
  }
#undef XF
  if (U > 0) { epi_step(16, E, prev, rs, c.uvp, c.l16, c.v1, c.hoff + (U - 1) * 114688); epi_step(17, E, prev, rs, c.uvp, c.l16, c.v1, c.hoff + (U - 1) * 114688); }
  if (U == 0) settle<2, 4>(rs, c.l16, c.xoff, xs, chk SPASS);
  const float d = (c.lane & 2) ? ((c.lane & 1) ? g[3] : g[2]) : ((c.lane & 1) ? g[1] : g[0]);
  *(LAS float*)(c.stw + (U & 1) * 1024) = swap32_add(d);
  SB();
  mfma_done(acc);
  *(LAS float*)(c.stw + (U & 1) * 1024 + 16) = acc[8];
  return acc;
}

__global__ void __launch_bounds__(256, 1)
s4_block_kernel(const unsigned char* __restrict__ xin, const u16* __restrict__ pAre, const u16* __restrict__ pAim, const u16* __restrict__ pBre,
                const u16* __restrict__ pBim, const u16* __restrict__ pCre, const u16* __restrict__ pCim, const u16* __restrict__ pldt,
                const u16* __restrict__ pD, const unsigned char* __restrict__ w1, const unsigned char* __restrict__ w2, const u16* __restrict__ pc,
                const unsigned char* __restrict__ wmod, unsigned char* __restrict__ out, unsigned char* ws) {
  extern __shared__ __attribute__((aligned(16))) unsigned char smem[];
  LAS unsigned char* L = (LAS unsigned char*)smem;
  const int tid = threadIdx.x, lane = tid & 63, w = __builtin_amdgcn_readfirstlane(tid >> 6);
  const int b = blockIdx.x, xcd = b & 7, slot = b >> 3, id = xcd * 32 + slot;
  const int ks4 = id >> 6, cc = id & 63;
  const int pks = id >> 4, pct = id & 15;
  const int rp = 4 * (xcd >> 1) + (slot >> 3), ct = 8 * (xcd & 1) + (slot & 7), rg = xcd & 1, dt = slot & 7;
  STAMP(0);

  const unsigned epoch_v = __hip_atomic_load((GAS unsigned*)ws, __ATOMIC_RELAXED, __HIP_MEMORY_SCOPE_AGENT);
  const GAS unsigned char* xrow = (const GAS unsigned char*)xin + id * 8192;
  const v4u xa = *(const GAS v4u*)(xrow + (tid & 3) * 2048 + (tid >> 2) * 16), xb = *(const GAS v4u*)(xrow + (tid & 3) * 2048 + 1024 + (tid >> 2) * 16);
  const v4u ra = *(const GAS v4u*)(xrow + w * 2048 + lane * 16), rb = *(const GAS v4u*)(xrow + w * 2048 + 1024 + lane * 16);
  const unsigned char* pcode;
  asm volatile("s_getpc_b64 %0" : "=s"(pcode));
  const unsigned warm = *(const GAS unsigned*)((const GAS unsigned char*)pcode + min((slot & 7) * 8192 + tid * 32, CODE_BYTES));
  const unsigned cval = ((const GAS u16*)pc)[ks4 * 128 + (tid & 127)];
  v4u wm[3];
#pragma unroll
  for (int i = 0; i < 3; ++i) {
    const int idx = tid + 256 * i, row = idx / 6, ch = idx - row * 6;
    wm[i] = *(const GAS v4u*)((const GAS unsigned char*)wmod + (ks4 * 128 + row) * 6144 + cc * 96 + ch * 16);
  }
  v4u wr[2];
#pragma unroll
  for (int i = 0; i < 2; ++i) {
    const int idx = min(tid + 256 * i, 447), row = idx / 14, ch = idx - row * 14;
    wr[i] = *(const GAS v4u*)((const GAS unsigned char*)w1 + (pks * 32 + row) * 3584 + pct * 224 + ch * 16);
  }
  const int pidx = id * 256 + tid;
  const unsigned uAre = ((const GAS u16*)pAre)[pidx], uAim = ((const GAS u16*)pAim)[pidx], uBre = ((const GAS u16*)pBre)[pidx];
  const unsigned uBim = ((const GAS u16*)pBim)[pidx], uCre = ((const GAS u16*)pCre)[pidx], uCim = ((const GAS u16*)pCim)[pidx];
  const unsigned uldt = ((const GAS u16*)pldt)[2 * id + (tid >> 7)], uD = ((const GAS u16*)pD)[2 * id + (tid >> 7)];
  SB();
  unsigned koff[11];
#pragma unroll
  for (int i = 0; i < 11; ++i) { koff[i] = ktab_off(8 * tid - 3 + i); PIN(koff[i]); }
  if (w == 3) *(LAS v4u*)(L + L_ONE + lane * 16) = v4u{0x3F803F80u, 0x3F803F80u, 0x3F803F80u, 0x3F803F80u};
  if (w == 2) *(LAS unsigned*)(L + L_KT + (lane & 1) * KT_CH + 48 * 32) = 0u;
  SB();
  const unsigned epoch = __builtin_amdgcn_readfirstlane(epoch_v);
  const __amdgpu_buffer_rsrc_t rs = __builtin_amdgcn_make_buffer_rsrc((void*)(ws + SET_BASE + (size_t)(epoch & 3u) * SET_STRIDE), 0, SET_BYTES, 0x00020000);
  const unsigned tagA = epoch * 0x9E3779B1u + 0x7F4A7C15u, tagB = ~tagA;
  {
    v4u gq[4];
#pragma unroll
    for (int i = 0; i < 4; ++i) { gq[i] = v4u{xa[i], tagA, xb[i], tagB}; PIN(gq[i]); }
#pragma unroll
    for (int i = 0; i < 4; ++i) stg(rs, (tid >> 2) * 65536 + (tid & 3) * 16, OFF_XG + i * 16384 + id * 64, gq[i]);
    SB();
  }
  STAMP(1);
  {
    float fa[8], fb[8];
#pragma unroll
    for (int i = 0; i < 4; ++i) { fa[2 * i] = bf_lo(ra[i]); fa[2 * i + 1] = bf_hi(ra[i]); fb[2 * i] = bf_lo(rb[i]); fb[2 * i + 1] = bf_hi(rb[i]); }
    float sa_ = 0.f, sb_ = 0.f;
#pragma unroll
    for (int i = 0; i < 8; ++i) { sa_ += fa[i]; sb_ += fb[i]; }
    const float ma = wave_sum(sa_) * (1.0f / 512.0f), mb = wave_sum(sb_) * (1.0f / 512.0f);
    float qa = 0.f, qb = 0.f;
#pragma unroll
    for (int i = 0; i < 8; ++i) { const float da = fa[i] - ma, db = fb[i] - mb; qa = __builtin_fmaf(da, da, qa); qb = __builtin_fmaf(db, db, qb); }
    const float va = rbf(rbf(wave_sum(qa) * (1.0f / 512.0f)) + 1e-5f), vb = rbf(rbf(wave_sum(qb) * (1.0f / 512.0f)) + 1e-5f);
    stg(rs, lane ? OOB : 0, OFF_SG + (id * 4 + w) * 16, v4u{pack2(ma, __builtin_amdgcn_sqrtf(va)), tagA, pack2(mb, __builtin_amdgcn_sqrtf(vb)), tagB});
    SB();
  }
  {
    const float c = bf_lo(cval);
    const float s = c * __builtin_amdgcn_rcpf(1.0f + __builtin_amdgcn_exp2f(-LOG2E * c));
    *(LAS u16*)(L + L_SC + (tid & 127) * 2) = (u16)pack2(s, s);
#pragma unroll
    for (int i = 0; i < 3; ++i) *(LAS v4u*)(L + L_WM + (tid + 256 * i) * 16) = wm[i];
#pragma unroll
    for (int i = 0; i < 2; ++i) *(LAS v4u*)(L + L_W1R + min(tid + 256 * i, 447) * 16) = wr[i];
  }
  asm volatile("" :: "v"(warm));
  BAR();
  STAMP(2);
  const int g16 = lane >> 4, q4 = (lane & 15) >> 2, p4 = lane & 3, hf = lane >> 5, blk = (lane >> 4) & 1, l16 = lane * 16;
  v4u w1raw[2];
  {
    LAS unsigned char* a = L + L_W1R + (8 * hf + q4) * 224 + w * 64 + blk * 32 + p4 * 8;
    w1raw[0] = trd2(a, a + 896);
    w1raw[1] = trd2(a + 3584, a + 3584 + 896);
  }
  if (w < 3) {
    f32x4 acc = {};
    LAS unsigned char* a = L + L_WM + (8 * g16 + q4) * 96 + w * 32 + p4 * 8;
#pragma unroll
    for (int s = 0; s < 4; ++s) acc = mfma16(trd2(a + s * 3072, a + s * 3072 + 384), lds128(L + L_SC + s * 64 + g16 * 16), acc);
    const int n = lane & 15;
    const float v0 = n ? acc[2] : acc[0], v1 = n ? acc[3] : acc[1];
    stg(rs, n < 2 ? (g16 * 2 + n) * 64 : OOB, OFF_MG + (cc * 24 + w * 8) * 64 + ks4 * 16, v4u{f2u(v0), tagA, f2u(v1), tagB});
  }
  SB();
  const v4u pois = {POISON, POISON, POISON, POISON};
  const int x1s = OFF_X1 + (2 * id + (w >> 1)) * 64, w1ps = OFF_W1P + pct * 131072 + w * 32768 + 2 * pks * 1024;
  const int x1a = (((g16 & 1) ? 4 + (w & 1) : 7 - (w & 1)) * 8 + q4 * 2 + (g16 >> 1)) * 32768 + p4 * 16, x1b = x1a - 32 * 32768;
  stg(rs, x1a, x1s, pois);
  stg(rs, x1b, x1s, pois);
  stg(rs, l16, w1ps, pois);
  stg(rs, l16 + 1024, w1ps, pois);
  SB();
  const float dtv = rbf(__builtin_amdgcn_exp2f(LOG2E * bf_lo(uldt)));
  const float are = dtv * bf_lo(uAre) * LOG2E, aim = dtv * bf_lo(uAim) * INV2PI;
  f32x2 wv;
  {
    const float br = dtv * bf_lo(uBre), bi = dtv * bf_lo(uBim), cr = bf_lo(uCre), ci = bf_lo(uCim);
    wv = f32x2{cr * br - ci * bi, cr * bi + ci * br};
  }
  float dsk = bf_lo(uD);
  PIN(dsk);
  const unsigned char* w2w = w2 + (448 * w) * 1024 + dt * 128;
  const unsigned w2voff = (lane >> 3) * 1024 + (((lane & 7) ^ (((lane >> 4) & 1) << 2)) * 16);

  {
    LAS unsigned char* row = L + L_UVS + (tid >> 7) * UVS_CH + (tid & 127) * 4;
    int nput = 0;
#define PUT(r, z) do { *(LAS unsigned*)(row + (r) * UVS_ROW) = pack2((z).x, (z).y); \
      if (nput % 5 == 1 || nput % 5 == 3) { const int n_ = nput / 5 * 2 + nput % 5 / 3; if (n_ < 2 * RS) dma16(w2w + n_ * 8192, w2voff, L_R + w * R_WAVE + n_ * 1024); } \
      ++nput; } while (0)
    f32x2 e1 = cexp(are, aim), e8 = cexp(8.f * are, -8.f * aim), e128 = cexp(128.f * are, 128.f * aim);
    PIN(e1); PIN(e8); PIN(e128); PIN(wv);
    BAR();
    const f32x2 e1p = {-e1.y, e1.x}, e8p = {-e8.y, e8.x}, e128p = {-e128.y, e128.x};
    f32x2 z[8], v = e8;
    PUT(40, wv);
    PUT(41, (f32x2{1.f, 0.f}));
    PUT(42, v);
    int vr = 43;
#pragma unroll
    for (int i = 0; i < 8; ++i) {
      z[i] = cmul(i ? z[i - 1] : wv, e1, e1p);
      PUT(i, z[i]);
      if (i < 6) { v = cmul(v, e8, e8p); PUT(vr, v); ++vr; }
    }
#pragma unroll
    for (int i = 0; i < 8; ++i) { z[i] = cmul(z[i], e128, e128p); PUT(8 + i, z[i]); }
#pragma unroll
    for (int u = 0; u < 2; ++u)
#pragma unroll
      for (int i = 0; i < 4; ++i) { z[2 * i + 1] = cmul(z[2 * i + 1], e128, e128p); PUT(16 + 4 * u + i, z[2 * i + 1]); }
#pragma unroll
    for (int u = 0; u < 4; ++u)
#pragma unroll
      for (int i = 0; i < 2; ++i) { z[4 * i + 3] = cmul(z[4 * i + 3], e128, e128p); PUT(24 + 2 * u + i, z[4 * i + 3]); }
#pragma unroll
    for (int u = 0; u < 8; ++u) {
      z[7] = cmul(z[7], e128, e128p); PUT(32 + u, z[7]);
      v = cmul(v, e8, e8p); PUT(vr, v); ++vr;
    }
#undef PUT
  }
  BAR();
  STAMP(3);
  {
    const int ch = w >> 1;
    LAS unsigned char* base = L + L_UVS + ch * UVS_CH + (lane & 15) * UVS_ROW + g16 * 16;
    LAS unsigned char* t0 = base + (w & 1) * 32 * UVS_ROW;
    f32x4 a0 = {}, a1 = {};
#pragma unroll
    for (int s = 0; s < 8; ++s) {
      const v4u vv = lds128(base + 41 * UVS_ROW + s * 64);
      a0 = mfma16(vv, lds128(t0 + s * 64), a0);
      a1 = mfma16(vv, lds128(base + 16 * UVS_ROW + s * 64), a1);
    }
    if ((w & 1) && lane == 8) a0[0] += dsk;
    LAS unsigned char* kt = L + L_KT + ch * KT_CH + (lane & 15) * 32 + g16 * 8;
    *(LAS v2u*)(kt + (w & 1) * 1024) = v2u{pack2(a0[0], a0[1]), pack2(a0[2], a0[3])};
    if (!(w & 1)) *(LAS v2u*)(kt + 512) = v2u{pack2(a1[0], a1[1]), pack2(a1[2], a1[3])};
  }
  v4u gx[4], gs[4], gm, gt;
  const int xgo = OFF_XG + id * 16384;
  const int gmo = OFF_MG + (min((lane >> 2) & 3, 2) * 256 + id) * 64 + p4 * 16;
  const int gto = OFF_MG + (tid < 128 ? (768 + 256 * (tid >> 6) + pks * 16 + ((tid >> 2) & 15)) : (1280 + dt * 32 + ((tid - 128) >> 2))) * 64 + p4 * 16;
  SB();
#pragma unroll
  for (int i = 0; i < 4; ++i) gx[i] = ldg(rs, tid * 16, xgo + i * 4096);
#pragma unroll
  for (int i = 0; i < 4; ++i) gs[i] = ldg(rs, tid * 16, OFF_SG + i * 4096);
  gm = ldg(rs, gmo, 0);
  gt = ldg(rs, gto, 0);
  SB();
  BAR();
  {
    const v4u z = {0u, 0u, 0u, 0u};
    if (w == 0) *(LAS v4u*)(L + L_KC + ((lane >> 1) & 7) * KC_STRIDE + (lane & 1) * 16) = z;
    if (w == 1) *(LAS v4u*)(L + L_H + hf * H_STRIDE + min(lane & 31, 29) * 16) = z;
  }
#pragma unroll
  for (int c = 0; c < 2; ++c) {
    unsigned k[11];
#pragma unroll
    for (int i = 0; i < 11; ++i) k[i] = *(LAS u16*)(L + L_KT + c * KT_CH + koff[i]);
    unsigned e[5], o[5];
#pragma unroll
    for (int i = 0; i < 5; ++i) { o[i] = k[2 * i] | (k[2 * i + 1] << 16); e[i] = k[2 * i + 1] | (k[2 * i + 2] << 16); }
    LAS unsigned char* d = L + L_KC + c * 4 * KC_STRIDE + 32 + tid * 16;
    *(LAS v4u*)(d) = v4u{e[1], e[2], e[3], e[4]};
    *(LAS v4u*)(d + KC_STRIDE) = v4u{o[1], o[2], o[3], o[4]};
    *(LAS v4u*)(d + 2 * KC_STRIDE) = v4u{e[0], e[1], e[2], e[3]};
    *(LAS v4u*)(d + 3 * KC_STRIDE) = v4u{o[0], o[1], o[2], o[3]};
  }
  SB();
  STAMP(4);
  for (unsigned spin = 0; spin < SPIN_CAP; ++spin) {
    unsigned bx = 0, bs = 0;
#pragma unroll
    for (int i = 0; i < 4; ++i) { bx |= (gx[i].y ^ tagA) | (gx[i].w ^ tagB); bs |= (gs[i].y ^ tagA) | (gs[i].w ^ tagB); }
    const unsigned bm = (gm.y ^ tagA) | (gm.w ^ tagB) | (gt.y ^ tagA) | (gt.w ^ tagB);
    if (!__any((bx | bs | bm) != 0u)) break;
    FRESH();
    COUNT(20);
    if (__any(bx != 0u)) {
#pragma unroll
      for (int i = 0; i < 4; ++i) gx[i] = ldg(rs, tid * 16, xgo + i * 4096);
    }
    if (__any(bs != 0u)) {
#pragma unroll
      for (int i = 0; i < 4; ++i) gs[i] = ldg(rs, tid * 16, OFF_SG + i * 4096);
    }
    gm = ldg(rs, gmo, 0);
    gt = ldg(rs, gto, 0);
  }
  WAITV(0);
  STAMP(5);
  {
    const float m0 = rbf(quad_sum(u2f(gm.x))), m1 = rbf(quad_sum(u2f(gm.z)));
    const int i0 = __builtin_bit_cast(int, m0), i1 = __builtin_bit_cast(int, m1);
    const float sa0 = __builtin_bit_cast(float, __builtin_amdgcn_readlane(i0, 0)), sa1 = __builtin_bit_cast(float, __builtin_amdgcn_readlane(i1, 0));
    const float ca0 = __builtin_bit_cast(float, __builtin_amdgcn_readlane(i0, 4)), ca1 = __builtin_bit_cast(float, __builtin_amdgcn_readlane(i1, 4));
    const float ga0 = __builtin_bit_cast(float, __builtin_amdgcn_readlane(i0, 8)), ga1 = __builtin_bit_cast(float, __builtin_amdgcn_readlane(i1, 8));
    const float A0 = rbf(1.0f + ca0) * ga0, A1 = rbf(1.0f + ca1) * ga1, B0 = sa0 * ga0, B1 = sa1 * ga1;
#pragma unroll
    for (int i = 0; i < 4; ++i) {
      const float mA = bf_lo(gs[i].x), rA = __builtin_amdgcn_rcpf(bf_hi(gs[i].x)), mB = bf_lo(gs[i].z), rB = __builtin_amdgcn_rcpf(bf_hi(gs[i].z));
      const float n00 = (bf_lo(gx[i].x) - mA) * rA, n01 = (bf_hi(gx[i].x) - mA) * rA, n10 = (bf_lo(gx[i].z) - mB) * rB, n11 = (bf_hi(gx[i].z) - mB) * rB;
      const int o = (tid + 256 * i) * 4;
      *(LAS unsigned*)(L + L_H + 480 + o) = pack2(__builtin_fmaf(n00, A0, B0), __builtin_fmaf(n10, A0, B0));
      *(LAS unsigned*)(L + L_H + H_STRIDE + 480 + o) = pack2(__builtin_fmaf(n01, A1, B1), __builtin_fmaf(n11, A1, B1));
      *(LAS unsigned*)(L + L_XB + o) = (gx[i].x & 0xFFFFu) | (gx[i].z << 16);
      *(LAS unsigned*)(L + L_XB + 4096 + o) = (gx[i].x >> 16) | (gx[i].z & 0xFFFF0000u);
    }
    float t0 = rbf(quad_sum(u2f(gt.x))), t1 = rbf(quad_sum(u2f(gt.z)));
    if (w == 0) { t0 *= -LOG2E; t1 *= -LOG2E; }
    if (w == 1) { t0 = rbf(1.0f + t0); t1 = rbf(1.0f + t1); t0 = t0 == 0.f ? 0x1p-30f : t0; t1 = t1 == 0.f ? 0x1p-30f : t1; }
    if (w >= 2) { t0 *= -LN2; t1 *= -LN2; }
    *(LAS f32x2*)(L + L_MODT + (tid >> 2) * 8) = f32x2{t0, t1};
  }
  BAR();
  STAMP(6);
  if (w == 0) stg(rs, lane ? OOB : 0, OFF_PD + id * 16, v4u{tagA, tagB, tagA, tagB});
  if (b == 0 && tid == 0) __hip_atomic_store((GAS unsigned*)ws, epoch + 1u, __ATOMIC_RELAXED, __HIP_MEMORY_SCOPE_AGENT);

  {
    const bool fill = w == 3 && blk;
    const unsigned one = (lane & 11) == 0 ? 0x3F803F80u : 0u, qm = (lane & 15) == 1 ? 0xFFFFFFFFu : 0u;
#pragma unroll
    for (int s = 0; s < 2; ++s) {
      const f32x4 s0 = *(LAS f32x4*)(L + L_MODT + (16 * s + 8 * hf) * 4), s1 = *(LAS f32x4*)(L + L_MODT + (16 * s + 8 * hf + 4) * 4);
      const f32x4 c0 = *(LAS f32x4*)(L + L_MODT + 128 + (16 * s + 8 * hf) * 4), c1 = *(LAS f32x4*)(L + L_MODT + 128 + (16 * s + 8 * hf + 4) * 4);
      const v4u r = w1raw[s];
      v4u sc;
      sc.x = pack2(bf_lo(r.x) * c0[0], bf_hi(r.x) * c0[1]);
      sc.y = pack2(bf_lo(r.y) * c0[2], bf_hi(r.y) * c0[3]);
      sc.z = pack2(bf_lo(r.z) * c1[0], bf_hi(r.z) * c1[1]);
      sc.w = pack2(bf_lo(r.w) * c1[2], bf_hi(r.w) * c1[3]);
      if (w == 3) {
        v4u q;
        q.x = pack2(s0[0] * __builtin_amdgcn_rcpf(c0[0]), s0[1] * __builtin_amdgcn_rcpf(c0[1]));
        q.y = pack2(s0[2] * __builtin_amdgcn_rcpf(c0[2]), s0[3] * __builtin_amdgcn_rcpf(c0[3]));
        q.z = pack2(s1[0] * __builtin_amdgcn_rcpf(c1[0]), s1[1] * __builtin_amdgcn_rcpf(c1[1]));
        q.w = pack2(s1[2] * __builtin_amdgcn_rcpf(c1[2]), s1[3] * __builtin_amdgcn_rcpf(c1[3]));
#pragma unroll
        for (int j = 0; j < 4; ++j) sc[j] = fill ? ((q[j] & qm) | one) : sc[j];
      }
      stg(rs, l16 + s * 1024, w1ps, sc);
      SB();
    }
  }
#pragma unroll
  for (int i = 0; i < 7; ++i) stg(rs, tid * 16, OFF_HID + id * 28672 + i * 4096, pois);
  SB();
  STAMP(7);
  {
    const int ch = w >> 1, S = w & 1;
    const int t0 = S ? 6 : 7, t1 = S ? 5 : 4, t2 = S ? 2 : 3, t3 = S ? 1 : 0;
    const int tt[4] = {t0, t1, t2, t3};
    f32x4 acc[4];
#pragma unroll
    for (int i = 0; i < 4; ++i) {
      const v2u xv = *(LAS v2u*)(L + L_XB + ch * 4096 + tt[i] * 512 + (lane & 15) * 32 + g16 * 8);
      acc[i] = f32x4{bf_lo(xv.x), bf_hi(xv.x), bf_lo(xv.y), bf_hi(xv.y)};
    }
    LAS unsigned char* ka = L + L_KC + (ch * 4 + q4) * KC_STRIDE + 2 * (32 - 8 * g16 + 4 * p4) - 8;
    LAS unsigned char* hb = L + L_H + ch * H_STRIDE + (lane & 15) * 32 + g16 * 16;
    v4u pd;
    if (S) conv_tiles<1>(ka, hb, acc, pd, rs, tid * 16); else conv_tiles<0>(ka, hb, acc, pd, rs, tid * 16);
    STAMP(8);
    v4u o[2];
#pragma unroll
    for (int i = 0; i < 2; ++i) {
      const auto s0 = __builtin_amdgcn_permlane16_swap(pack2(acc[2 * i][0], acc[2 * i][1]), pack2(acc[2 * i + 1][0], acc[2 * i + 1][1]), false, false);
      const auto s1 = __builtin_amdgcn_permlane16_swap(pack2(acc[2 * i][2], acc[2 * i][3]), pack2(acc[2 * i + 1][2], acc[2 * i + 1][3]), false, false);
      o[i] = v4u{guard(s0[0]), guard(s1[0]), guard(s0[1]), guard(s1[1])};
      PIN(o[i]);
    }
    WAITV(0);
    stg(rs, x1a, x1s, o[0]);
    stg(rs, x1b, x1s, o[1]);
    SB();
    for (unsigned spin = 0; spin < SPIN_CAP && __any(((pd.x ^ tagA) | (pd.y ^ tagB)) != 0u); ++spin) {
      FRESH();
      pd = ldg(rs, tid * 16, OFF_PD);
    }
  }
  BAR();
  STAMP(9);

  v4u w1f[32];
  const int w1o = OFF_W1P + ct * 131072 + w * 32768;
  const int qo = ((lane >> 1) ^ w) * 1024 + ((lane & 1) * 32 + 17) * 16, qs = OFF_W1P + ct * 131072 + 98304;
  v4u xs[4][8];
  const int xoff = OFF_X1 + rp * 131072 + w * 1024;
  {
    LAS unsigned char* qw = L + L_XB + w * 1024;
    LAS unsigned char* qr = ((lane & 31) == 0 ? L + L_ONE : qw) + hf * 16;
    f32x16 uv;
    for (unsigned spin = 0;; ++spin) {
      FRESH();
      COUNT(21);
      const v4u qv = ldg(rs, qo, qs);
#pragma unroll
      for (int s = 0; s < 32; ++s) w1f[s] = ldg(rs, l16 + ((s & 3) ^ w) * 1024, w1o + (s >> 2) * 4096);
      SB();
#pragma unroll
      for (int u = 0; u < 3; ++u)
#pragma unroll
        for (int i = 0; i < 8; ++i) xs[u][i] = ldg(rs, l16, xoff + u * 32768 + i * 4096);
      SB();
      *(LAS v4u*)(qw + l16) = qv;
      v4u qa[3];
      qa[0] = lds128(qr);
      qa[1] = lds128(qr + 32);
#pragma unroll
      for (int s = 0; s < 32; ++s) {
        if (s + 2 < 32) qa[(s + 2) % 3] = lds128(qr + (s + 2) * 32);
        if (s == 0) mfma_va<true>(uv, qa[s % 3], w1f[s]); else mfma_va<false>(uv, qa[s % 3], w1f[s]);
        SB();
      }
      mfma_done(uv);
      if (__builtin_expect(!__any(lane < 32 && !(uv[0] + uv[1] == uv[0] + uv[1])) || spin >= SPIN_CAP / 8, 1)) break;
    }
    if (lane < 32) {
      *(LAS float*)(L + L_UVT + (32 * w + lane) * 4) = uv[0];
      *(LAS float*)(L + L_UVT + 512 + (32 * w + lane) * 4) = uv[1];
    }
  }
  SB();
#pragma unroll
  for (int i = 0; i < 8; ++i) xs[3][i] = ldg(rs, l16, xoff + 3 * 32768 + i * 4096);
  SB();
  v4u w2f[28][2];
  const int w2t = (4 * hf + q4) * 128 + blk * 32 + (((p4 & 1) << 1) | (p4 >> 1)) * 8;
  const int w2e = (q4 >> 1) * 64;
#define PULL(dst, n, base) do { LAS unsigned char* a_ = L + (base) + w2t; \
    _Pragma("unroll") for (int s = 0; s < (n); ++s) { \
      w2f[(dst) + s][0] = trd2(a_ + w2e + s * 2048, a_ + w2e + s * 2048 + 1024); \
      w2f[(dst) + s][1] = trd2(a_ + (w2e ^ 64) + s * 2048, a_ + (w2e ^ 64) + s * 2048 + 1024); } } while (0)
  PULL(0, RS, L_R + w * R_WAVE);
  SB();
  STAMP(10);
  settle<0, 1>(rs, l16, xoff, xs, max8(xs[0]) SPASS);
#pragma unroll
  for (int i = 0; i < 8; ++i) *(LAS v4u*)(L + L_XR + w * 1024 + l16 + i * 4096) = xs[0][i];
  BAR();
  STAMP(11);

  v4u res[2];
  G1 c;
  {
    const int tra = L_XR + (hf * 8 + q4) * 64 + blk * 32 + p4 * 8;
    c.L = L; c.xw = L + L_XR + w * 1024 + l16; c.uvp = L + L_UVT + (32 * w + 4 * hf) * 4;
#pragma unroll
    for (int i = 0; i < 4; ++i) c.xr[i] = L + tra + (i ^ w) * 1024;
    c.str = L + L_STAT + (lane & 31) * 32; c.stw = c.str + w * 4;
    c.w2w = w2w; c.w2voff = w2voff; c.l16 = l16; c.v1 = w == 3 ? OOB : l16 + 1024; c.xoff = xoff;
    c.hoff = OFF_HID + (rp * 4 * 112 + ct * 7 + 2 * w) * 1024; c.w = w; c.lane = lane;
  }
  LAS unsigned char* rr = c.xr[0] + rg * 32768 + dt * 4096;
  f32x16 a0 = {}, a1;
  a0 = gemm1_unit<0>(rs, c, w1f, xs, a0 SPASS);
  STAMP(16);
  if (rg == 0) res[0] = trd2(rr, rr + 256);
  BAR();
  a1 = gemm1_unit<1>(rs, c, w1f, xs, a0 SPASS);
  STAMP(17);
  if (rg == 1) res[0] = trd2(rr, rr + 256);
  BAR();
  a0 = gemm1_unit<2>(rs, c, w1f, xs, a1 SPASS);
  STAMP(18);
  if (rg == 0) res[1] = trd2(rr, rr + 256);
  BAR();
  a1 = gemm1_unit<3>(rs, c, w1f, xs, a0 SPASS);
  if (rg == 1) res[1] = trd2(rr, rr + 256);
  BAR();
  STAMP(12);

  constexpr int HW = 35;
  v4u hq[HW];
  const int ho = OFF_HID + ((rp * 4 + rg) * 112 + 28 * w) * 1024;
#define HOFF(i) l16 + ((i) & 3) * 1024, ho + ((i) / 28) * (2 * 114688) + (((i) % 28) >> 2) * 4096
#pragma unroll
  for (int n = 0; n < 4; ++n) dma16(w2w + (4 * RS + 8 + n) * 8192, w2voff, L_XR + 32768 + w * 4096 + n * 1024);
  {
    Epi E;
    epi_begin(E, c.str + 1024, c.uvp);
#pragma unroll
    for (int j = 0; j < 18; ++j) {
      epi_step(j, E, a1, rs, c.uvp, l16, c.v1, c.hoff + 3 * 114688);
      epi_uv(j, E, c.uvp);
      if (j < 10) hq[j] = ldg(rs, HOFF(j));
      SB();
    }
  }
#pragma unroll
  for (int i = 10; i < 28; ++i) hq[i] = ldg(rs, HOFF(i));
  SB();
  STAMP(13);
  WAITV(30);
  PULL(RS, RS, L_R + w * R_WAVE);
  PULL(2 * RS, 4, L_XR + w * 8192);
  PULL(2 * RS + 4, 2, L_XR + 32768 + w * 4096);
  const f32x4 gb0 = *(LAS f32x4*)(L + L_MODT + 256 + (16 * w + 8 * hf) * 4), gb1 = *(LAS f32x4*)(L + L_MODT + 256 + (16 * w + 8 * hf + 4) * 4);
  SB();
  STAMP(19);
  f32x16 d[2][2];
  LAS unsigned char* redw = L + L_R + w * R_WAVE + l16;
  LAS unsigned char* redr = L + L_R + w * 2048 + l16;
  const int orow = (rp * 128 + ((lane & 31) >> 3) * 16 + rg * 8 + (lane & 7)) * 1024 + dt * 128 + w * 32 + hf * 16;
#define REDW(un, q) *(LAS f32x4*)(redw + (un) * 8192 + (q) * 1024) = \
    f32x4{d[un][(q) >> 2][4 * ((q) & 3)], d[un][(q) >> 2][4 * ((q) & 3) + 1], d[un][(q) >> 2][4 * ((q) & 3) + 2], d[un][(q) >> 2][4 * ((q) & 3) + 3]}
#define FINISH(un) do { f32x4 s0 = {}, s1 = {}; \
    _Pragma("unroll") for (int k = 0; k < 4; ++k) { \
      s0 += *(LAS f32x4*)(redr + k * R_WAVE + (un) * 8192); s1 += *(LAS f32x4*)(redr + k * R_WAVE + (un) * 8192 + 1024); } \
    const v4u r = res[un]; v4u o; \
    o.x = pack2(__builtin_fmaf(gb0[0], s0[0], bf_lo(r.x)), __builtin_fmaf(gb0[1], s0[1], bf_hi(r.x))); \
    o.y = pack2(__builtin_fmaf(gb0[2], s0[2], bf_lo(r.y)), __builtin_fmaf(gb0[3], s0[3], bf_hi(r.y))); \
    o.z = pack2(__builtin_fmaf(gb1[0], s1[0], bf_lo(r.z)), __builtin_fmaf(gb1[1], s1[1], bf_hi(r.z))); \
    o.w = pack2(__builtin_fmaf(gb1[2], s1[2], bf_lo(r.w)), __builtin_fmaf(gb1[3], s1[3], bf_hi(r.w))); \
    __builtin_amdgcn_raw_buffer_store_b128(o, __builtin_amdgcn_make_buffer_rsrc((void*)out, 0, 2097152, 0x00020000), orow, (un) * 65536, SC1); } while (0)
  unsigned m = 0;
#pragma unroll
  for (int i = 0; i < 7; ++i) m = max4(m, hq[i]);
#pragma unroll
  for (int G = 0; G < 8; ++G) {
    const int un = G >> 2, gq = G & 3, i0 = G * 7;
    if (__builtin_expect(__any(m == POISON), 0)) {
      for (unsigned spin = 0; spin < SPIN_CAP; ++spin) {
        FRESH();
        COUNT(24 + un);
#pragma unroll
        for (int i = 0; i < 7; ++i) hq[(i0 + i) % HW] = ldg(rs, HOFF(i0 + i));
        m = 0;
#pragma unroll
        for (int i = 0; i < 7; ++i) m = max4(m, hq[(i0 + i) % HW]);
        if (!__any(m == POISON)) break;
      }
    }
    m = 0;
#pragma unroll
    for (int i = 0; i < 7; ++i) {
      if (gq == 0 && i == 0) mfma_av<true>(d[un][0], w2f[0][0], hq[i0 % HW]); else mfma_av<false>(d[un][0], w2f[gq * 7 + i][0], hq[(i0 + i) % HW]);
      SB();
      if (G < 4) { const int n = 28 + i0 + i; hq[n % HW] = ldg(rs, HOFF(n)); }
      if (G == 4 && i >= 1) REDW(0, i - 1);
      if (G == 5 && i < 2) REDW(0, 6 + i);
      SB();
      if (gq == 0 && i == 0) mfma_av<true>(d[un][1], w2f[0][1], hq[i0 % HW]); else mfma_av<false>(d[un][1], w2f[gq * 7 + i][1], hq[(i0 + i) % HW]);
      SB();
      if (G < 7) { m = max4(m, hq[(i0 + 7 + i) % HW]); PIN(m); }
      SB();
    }
    if (G == 3) STAMP(14);
  }
  mfma_done(d[1][0]);
  PIN(d[1][1]);
#pragma unroll
  for (int q = 0; q < 8; ++q) REDW(1, q);
  BAR();
  FINISH(0);
  FINISH(1);
#undef HOFF
#undef PULL
#undef REDW
#undef FINISH
  STAMP(15);
}

__global__ void __launch_bounds__(256, 1) tiny_small(unsigned char* ws) { if (ws == nullptr) __builtin_trap(); }
__global__ void __launch_bounds__(256, 1) tiny_big(unsigned char* ws) { asm volatile("v_mov_b32 v255, 0\n v_accvgpr_write_b32 a255, v255" ::: "v255", "a255"); }
extern "C" void kernel_launch(void* const* d_in, const int* in_sizes, int n_in, void* d_out, int out_size, void* d_ws, size_t ws_size,
                              hipStream_t stream) {
  (void)in_sizes; (void)n_in; (void)out_size; (void)ws_size;
  (void)hipFuncSetAttribute((const void*)s4_block_kernel, hipFuncAttributeMaxDynamicSharedMemorySize, LDS_BYTES);
  (void)hipFuncSetAttribute((const void*)tiny_big, hipFuncAttributeMaxDynamicSharedMemorySize, LDS_BYTES);
  s4_block_kernel<<<256, 256, LDS_BYTES, stream>>>(
      (const unsigned char*)d_in[0], (const u16*)d_in[1], (const u16*)d_in[2], (const u16*)d_in[3], (const u16*)d_in[4], (const u16*)d_in[5],
      (const u16*)d_in[6], (const u16*)d_in[7], (const u16*)d_in[8], (const unsigned char*)d_in[10], (const unsigned char*)d_in[11],
      (const u16*)d_in[12], (const unsigned char*)d_in[13], (unsigned char*)d_out, (unsigned char*)d_ws);
}
```

```cpp
#include <hip/hip_runtime.h>

#define GAS __attribute__((address_space(1)))
#define LAS __attribute__((address_space(3)))
#define DI __device__ __forceinline__
typedef unsigned v4u __attribute__((ext_vector_type(4)));
typedef unsigned v2u __attribute__((ext_vector_type(2)));
typedef float f32x2 __attribute__((ext_vector_type(2)));
typedef float f32x4 __attribute__((ext_vector_type(4)));
typedef float f32x16 __attribute__((ext_vector_type(16)));
typedef __bf16 bf16x2 __attribute__((ext_vector_type(2)));
typedef __bf16 bf16x8 __attribute__((ext_vector_type(8)));
typedef short s16x4 __attribute__((ext_vector_type(4)));
typedef unsigned short u16;
typedef unsigned long long u64;

constexpr int SET_BASE = 4096, SET_STRIDE = 16 << 20;
constexpr int OFF_MT = 0;
constexpr int OFF_SG = 8192;
constexpr int OFF_MG = 65536;
constexpr int OFF_SX = 524288;
constexpr int OFF_XG = 1 << 20;
constexpr int OFF_Z = OFF_XG + (4 << 20);
constexpr int OFF_HID = OFF_Z + 2097152;
constexpr int OFF_CK = OFF_HID + 7340032;
constexpr int SET_BYTES = OFF_CK + 131072;
static_assert(SET_BYTES <= SET_STRIDE && OFF_MG + 393216 <= OFF_SX, "set size");

constexpr int L_TS = 0, L_TB = 1024, L_TG = 2048;
constexpr int L_UVT = 3072;
constexpr int L_M = 4096;
constexpr int L_UVS = L_M;
constexpr int UVS_ROW = 544, UVS_CH = 57 * UVS_ROW;
constexpr int L_KC = L_M;
constexpr int KC_STRIDE = 4160;
constexpr int L_H = L_KC + 8 * KC_STRIDE;
constexpr int H_STRIDE = 4576;
constexpr int L_XB = L_H + 2 * H_STRIDE;
constexpr int L_RX = L_XB + 8192;
constexpr int L_R = L_M + 65536;
constexpr int R_WAVE = 22528;
constexpr int L_TOP = L_R + 4 * R_WAVE;
constexpr int L_KT = L_TOP;
constexpr int L_STAT = L_TOP;
constexpr int L_XR = L_M;
constexpr int L_SP = L_M + 131072;
constexpr int LDS_BYTES = 163840;
static_assert(L_TOP + 4096 <= LDS_BYTES && L_RX + 12288 <= L_R && L_UVS + 2 * UVS_CH <= L_R && L_SP + 24576 <= L_TOP, "LDS map");
static_assert(L_H % 16 == 0 && L_XB % 16 == 0 && L_RX % 16 == 0 && L_KT % 16 == 0, "alignment");

constexpr int CODE_BYTES = 49152;
constexpr int XS0 = 20, SXL = 12, EO = 26;
constexpr int SC1 = 16;
constexpr int OOB = 0x7FFFFF00;
constexpr unsigned SPIN_CAP = 1u << 15;
constexpr unsigned POISON = 0xFFFFFFFFu;
constexpr float LOG2E = 1.4426950408889634f, LN2 = 0.6931471805599453f, INV2PI = 0.15915494309189535f;

#define SB() __builtin_amdgcn_sched_barrier(0)
#define WAITV(n) asm volatile("s_waitcnt vmcnt(" #n ")" ::: "memory")
#define PIN(x) asm volatile("" : "+v"(x))
#define FRESH() asm volatile("" ::: "memory")
#define BAR() do { asm volatile("s_waitcnt lgkmcnt(0)" ::: "memory"); __builtin_amdgcn_s_barrier(); asm volatile("" ::: "memory"); } while (0)
#define STAMP(i)
#define SARG
#define SPASS
#define COUNT(i)

DI float u2f(unsigned u) { return __builtin_bit_cast(float, u); }
DI unsigned f2u(float f) { return __builtin_bit_cast(unsigned, f); }
DI float dot2(unsigned u, float c) { const bf16x2 v = __builtin_bit_cast(bf16x2, u); return __builtin_amdgcn_fdot2_f32_bf16(v, v, c, false); }
DI float bf_lo(unsigned u) { return __builtin_bit_cast(float, u << 16); }
DI float bf_hi(unsigned u) { return __builtin_bit_cast(float, u & 0xFFFF0000u); }
DI unsigned pack2(float a, float b) { f32x2 v = {a, b}; return __builtin_bit_cast(unsigned, __builtin_convertvector(v, bf16x2)); }
DI float rbf(float a) { return bf_lo(pack2(a, a)); }
DI unsigned guard(unsigned u) { return u < 0xFFFFFFFEu ? u : 0xFFFFFFFEu; }
DI unsigned max3(unsigned a, unsigned b, unsigned c) { unsigned r; asm("v_max3_u32 %0, %1, %2, %3" : "=v"(r) : "v"(a), "v"(b), "v"(c)); return r; }
DI unsigned max4(unsigned m, v4u v) { return max3(max3(m, v.x, v.y), v.z, v.w); }
DI f32x4 mfma16(v4u a, v4u b, f32x4 c) { return __builtin_amdgcn_mfma_f32_16x16x32_bf16(__builtin_bit_cast(bf16x8, a), __builtin_bit_cast(bf16x8, b), c, 0, 0, 0); }
DI f32x16 mfma32(v4u a, v4u b, f32x16 c) { return __builtin_amdgcn_mfma_f32_32x32x16_bf16(__builtin_bit_cast(bf16x8, a), __builtin_bit_cast(bf16x8, b), c, 0, 0, 0); }
template <bool FIRST> DI void mfma_av(f32x16& acc, const v4u& a, const v4u& b) {
  if (FIRST) asm volatile("v_mfma_f32_32x32x16_bf16 %0, %1, %2, 0" : "=&v"(acc) : "a"(a), "v"(b));
  else asm volatile("v_mfma_f32_32x32x16_bf16 %0, %1, %2, %0" : "+v"(acc) : "a"(a), "v"(b));
}
template <bool FIRST> DI void mfma_va(f32x16& acc, const v4u& a, const v4u& b) {
  if (FIRST) asm volatile("v_mfma_f32_32x32x16_bf16 %0, %1, %2, 0" : "=&v"(acc) : "v"(a), "a"(b));
  else asm volatile("v_mfma_f32_32x32x16_bf16 %0, %1, %2, %0" : "+v"(acc) : "v"(a), "a"(b));
}
DI void mfma_done(f32x16& acc) { asm volatile("s_nop 15" : "+v"(acc)); }
DI v2u trd(LAS unsigned char* p) { return __builtin_bit_cast(v2u, __builtin_amdgcn_ds_read_tr16_b64_v4i16((LAS s16x4*)p)); }
DI v4u trd2(LAS unsigned char* p0, LAS unsigned char* p1) { const v2u a = trd(p0), b = trd(p1); return v4u{a.x, a.y, b.x, b.y}; }
DI v4u lds128(LAS unsigned char* p) { return *(LAS v4u*)p; }
DI v4u ldg(__amdgpu_buffer_rsrc_t r, int voff, int soff) { return __builtin_amdgcn_raw_buffer_load_b128(r, voff, soff, SC1); }
DI void stg(__amdgpu_buffer_rsrc_t r, int voff, int soff, v4u v) {
  __builtin_amdgcn_raw_buffer_store_b128(v, r, voff, soff, SC1);
  asm volatile("s_nop 1" :: "v"(v) : "memory");
}
template <int CTRL> DI float dpp_add(float v) {
  return v + __builtin_bit_cast(float, __builtin_amdgcn_update_dpp(0, __builtin_bit_cast(int, v), CTRL, 0xF, 0xF, true));
}
DI float swap16_add(float v) { const unsigned u = __builtin_bit_cast(unsigned, v); const auto r = __builtin_amdgcn_permlane16_swap(u, u, false, false);
  return u2f(r[0]) + u2f(r[1]); }
DI float swap32_add(float v) { const unsigned u = __builtin_bit_cast(unsigned, v); const auto r = __builtin_amdgcn_permlane32_swap(u, u, false, false);
  return u2f(r[0]) + u2f(r[1]); }
DI float quad_sum(float v) { v = dpp_add<0xB1>(v); return dpp_add<0x4E>(v); }
DI float row_sum(float v) { return dpp_add<0x140>(dpp_add<0x141>(quad_sum(v))); }
DI float wave_sum(float v) { return swap32_add(swap16_add(row_sum(v))); }
DI f32x2 cmul(f32x2 a, f32x2 e, f32x2 ep) { const f32x2 t = f32x2{a.x, a.x} * e; return __builtin_elementwise_fma(f32x2{a.y, a.y}, ep, t); }
DI f32x2 cexp(float re2, float rev) {
  const float m = __builtin_amdgcn_exp2f(re2), f = __builtin_amdgcn_fractf(rev);
  return f32x2{m * __builtin_amdgcn_cosf(f), m * __builtin_amdgcn_sinf(f)};
}
DI void dma_w2(const unsigned char* sbase, unsigned v0, unsigned v1, unsigned lds_dst) {
  asm volatile("s_mov_b32 m0, %3\n\ts_nop 0\n\tglobal_load_lds_dwordx4 %0, %2\n\tglobal_load_lds_dwordx4 %1, %2 offset:1024"
               :: "v"(v0), "v"(v1), "s"(sbase), "s"(lds_dst) : "memory");
}
DI void dma_w1(const unsigned char* sbase, unsigned v, unsigned lds_dst) {
  asm volatile("s_mov_b32 m0, %2\n\ts_nop 0\n\tglobal_load_lds_dwordx4 %0, %1" :: "v"(v), "s"(sbase), "s"(lds_dst) : "memory");
}
DI void dma_x1(const unsigned char* sbase, unsigned v, unsigned lds_dst) {
  asm volatile("s_mov_b32 m0, %2\n\ts_nop 0\n\tglobal_load_lds_dwordx4 %0, %1 sc1" :: "v"(v), "s"(sbase), "s"(lds_dst) : "memory");
}
DI void dma_x2(const unsigned char* sbase, unsigned v, unsigned lds_dst) {
  asm volatile("s_mov_b32 m0, %2\n\ts_nop 0\n\tglobal_load_lds_dwordx4 %0, %1 sc1\n\tglobal_load_lds_dwordx4 %0, %1 offset:1024 sc1"
               :: "v"(v), "s"(sbase), "s"(lds_dst) : "memory");
}
DI void dma_x4(const unsigned char* sbase, unsigned v, unsigned lds_dst) {
  asm volatile("s_mov_b32 m0, %2\n\ts_nop 0\n\tglobal_load_lds_dwordx4 %0, %1 sc1\n\tglobal_load_lds_dwordx4 %0, %1 offset:1024 sc1\n\t"
               "global_load_lds_dwordx4 %0, %1 offset:2048 sc1\n\tglobal_load_lds_dwordx4 %0, %1 offset:3072 sc1"
               :: "v"(v), "s"(sbase), "s"(lds_dst) : "memory");
}
template <int I> struct IC { static constexpr int v = I; };
template <int I, int N, class F> DI void static_for(F&& f) { if constexpr (I < N) { f(IC<I>{}); static_for<I + 1, N>(f); } }
template <int N> DI void waitv() { asm volatile("s_waitcnt vmcnt(%0)" :: "n"(N) : "memory"); }
DI unsigned ktab_off(int j) {
  unsigned u = __builtin_bit_cast(unsigned, (float)j);
  u = (u + 0x7FFFu + ((u >> 16) & 1u)) & 0xFFFF0000u;
  const int z = (int)__builtin_bit_cast(float, u) - 1;
  const int uu = z >> 7;
  const int s = 31 - __builtin_clz((unsigned)uu | 1u);
  const int row = 8 * s + ((8 * uu + (z & 7)) >> s);
  int off = row * 64 + ((z >> 3) & 15) * 4;
  off = j == 0 ? 40 * 64 : off;
  return L_KT + (j < 0 ? 48 * 64 : off);
}

struct ConvOrder { int t[36], u[36]; };
constexpr ConvOrder conv_order() {
  ConvOrder o = {};
  int done[8] = {}, last = -1;
  for (int k = 0; k < 36; ++k) {
    int best = -1;
    for (int t = 0; t < 8; ++t)
      if (t != last && done[t] <= t && (best < 0 || t - done[t] > best - done[best])) best = t;
    o.t[k] = best; o.u[k] = done[best]++; last = best;
  }
  return o;
}
DI unsigned tagbad(unsigned bad, v4u g, unsigned tagA, unsigned tagB) {
  return __builtin_amdgcn_bitop3_b32(__builtin_amdgcn_bitop3_b32(bad, g.y, tagA, 0xF6), g.w, tagB, 0xF6);
}
template <int R0, int R1, int R2, int R3> DI void put4(unsigned base, f32x2 a, f32x2 b, f32x2 c, f32x2 d) {
  asm volatile("s_mov_b32 m0, %4\n\ts_nop 0\n\tds_write_addtid_b32 %0 offset:%5\n\tds_write_addtid_b32 %1 offset:%6\n\t"
               "ds_write_addtid_b32 %2 offset:%7\n\tds_write_addtid_b32 %3 offset:%8"
               :: "v"(pack2(a.x, a.y)), "v"(pack2(b.x, b.y)), "v"(pack2(c.x, c.y)), "v"(pack2(d.x, d.y)), "s"(base),
                  "n"(R0 * UVS_ROW), "n"(R1 * UVS_ROW), "n"(R2 * UVS_ROW), "n"(R3 * UVS_ROW) : "memory");
}
DI unsigned add3(unsigned a, unsigned b, unsigned c) { unsigned r; asm("v_add3_u32 %0, %1, %2, %3" : "=v"(r) : "v"(a), "v"(b), "v"(c)); return r; }
DI unsigned sum4(unsigned m, v4u v) { return add3(add3(m, v.x, v.y), v.z, v.w); }
template <int I> DI unsigned red_step(unsigned v) {
  constexpr int ctrl[6] = {0xB1, 0x4E, 0x141, 0x140, 0x142, 0x143}, rows[6] = {0xF, 0xF, 0xF, 0xF, 0xA, 0xC};
  return v + (unsigned)__builtin_amdgcn_update_dpp(0, (int)v, ctrl[I], rows[I], 0xF, true);
}
DI unsigned red_all(unsigned v) { return red_step<5>(red_step<4>(red_step<3>(red_step<2>(red_step<1>(red_step<0>(v)))))); }
DI void swap_add32(float& a, float b) { const auto r = __builtin_amdgcn_permlane32_swap(f2u(a), f2u(b), false, false); a = u2f(r[0]) + u2f(r[1]); }
DI void swap_add16(float& a, float b) { const auto r = __builtin_amdgcn_permlane16_swap(f2u(a), f2u(b), false, false); a = u2f(r[0]) + u2f(r[1]); }

__global__ void __launch_bounds__(256, 1)
s4_block_kernel(const unsigned char* __restrict__ xin, const u16* __restrict__ pAre, const u16* __restrict__ pAim, const u16* __restrict__ pBre,
                const u16* __restrict__ pBim, const u16* __restrict__ pCre, const u16* __restrict__ pCim, const u16* __restrict__ pldt,
                const u16* __restrict__ pD, const unsigned char* __restrict__ w1, const unsigned char* __restrict__ w2, const u16* __restrict__ pc,
                const unsigned char* __restrict__ wmod, unsigned char* __restrict__ out, unsigned char* ws) {
  extern __shared__ __attribute__((aligned(16))) unsigned char smem[];
  LAS unsigned char* L = (LAS unsigned char*)smem;
  const int tid = threadIdx.x, lane = tid & 63, w = __builtin_amdgcn_readfirstlane(tid >> 6);
  const int b = blockIdx.x, xcd = b & 7, slot = b >> 3, id = xcd * 32 + slot;
  const int ks = (id >> 6) * 4 + w, cc = id & 63;
  const int rp = 4 * (xcd >> 1) + (slot >> 3), ct = 8 * (xcd & 1) + (slot & 7), rg = xcd & 1, dt = slot & 7;
  STAMP(0);

  const unsigned epoch_v = __hip_atomic_load((GAS unsigned*)ws, __ATOMIC_RELAXED, __HIP_MEMORY_SCOPE_AGENT);
  const int g16 = lane >> 4, q4 = (lane & 15) >> 2, p4 = lane & 3, hf = lane >> 5, blk = (lane >> 4) & 1, l16 = lane * 16;
  const unsigned lrw = L_R + w * R_WAVE;
#pragma unroll
  for (int i = 0; i < 3; ++i) {
    const unsigned idx = lane + 64 * i, row = idx / 6;
    dma_w1(wmod + ks * 32 * 6144 + cc * 96, row * (6144 - 96) + idx * 16, lrw + i * 1024);
  }
  const unsigned cval = ((const GAS u16*)pc)[ks * 32 + (lane & 31)];
  const GAS unsigned char* xrow = (const GAS unsigned char*)xin + id * 8192;
  const unsigned xo = (tid & 3) * 2048 + (tid >> 2) * 16;
  const v4u xa = *(const GAS v4u*)(xrow + xo), xb = *(const GAS v4u*)(xrow + 1024 + xo);
  const v4u ra = *(const GAS v4u*)(xrow + w * 2048 + (unsigned)l16), rb = *(const GAS v4u*)(xrow + w * 2048 + 1024 + (unsigned)l16);
  const unsigned t2 = tid * 2;
#define PARAM(p) (unsigned)*(const GAS u16*)((const GAS unsigned char*)(p) + id * 512 + t2)
  const unsigned uAre = PARAM(pAre), uAim = PARAM(pAim), uBre = PARAM(pBre), uBim = PARAM(pBim), uCre = PARAM(pCre), uCim = PARAM(pCim);
#undef PARAM
  const unsigned uldt = ((const unsigned*)pldt)[id], uD = ((const unsigned*)pD)[id];
  const unsigned char* pcode;
  asm volatile("s_getpc_b64 %0" : "=s"(pcode));
  const unsigned warm = *(const GAS unsigned*)((const GAS unsigned char*)pcode + (unsigned)min(((slot & 3) * 256 + tid) * 64, CODE_BYTES));
  const unsigned wt = *(const GAS unsigned*)((const GAS unsigned char*)(w & 1 ? w2 : w1) + id * 7168 + (w >> 1) * 3584 + (unsigned)min(lane, 27) * 128);
  SB();
  asm volatile("" : "+s"(out), "+s"(w1), "+s"(w2));
  LAS unsigned char* koff[11];
#pragma unroll
  for (int i = 0; i < 11; ++i) { koff[i] = L + ktab_off(8 * tid - 3 + i); PIN(koff[i]); }
  *(LAS unsigned*)(L + L_KT + 48 * 64) = 0u;
  const unsigned char* w2lo = w2 + (448 * w + 224 * rg) * 1024 + dt * 128;
  const unsigned char* w2hi = w2 + (448 * w - 224 * rg) * 1024 + dt * 128;
  unsigned w2v0 = (lane >> 3) * 1024 + (((lane & 7) ^ (blk << 2)) * 16), w2v1 = w2v0 + 7168;
  const unsigned char* w1w = w1 + ct * 224 + w * 64;
  unsigned w1v0 = (lane >> 2) * 3584 + (w == 3 ? lane & 1 : lane & 3) * 16, w1v1 = w1v0 + 57344 - 1024;
  const int zs = OFF_Z + (2 * id + (w >> 1)) * 64;
  int za = ((4 * (w & 1) + (g16 & 1)) * 8 + q4 * 2 + (g16 >> 1)) * 32768 + p4 * 16, zb = za + 16 * 32768;
  int tra = (hf * 8 + q4) * 64 + blk * 32 + p4 * 8;
  LAS unsigned char* w1a = L + lrw + tra;
  LAS unsigned char* ka = L + L_KC + ((w >> 1) * 4 + q4) * KC_STRIDE + 2 * (32 - 8 * g16 + 4 * p4) - 8 + 256 * (w & 1);
  LAS unsigned char* hb = L + L_H + (w >> 1) * H_STRIDE + (lane & 15) * 32 + g16 * 16 + 256 - 256 * (w & 1);
  LAS unsigned char* zp = L + (w == 0 ? L_KC + ((lane >> 1) & 7) * KC_STRIDE + (lane & 1) * 16 : L_H + hf * H_STRIDE + min(lane & 31, 29) * 16);
  LAS unsigned char* ub = L + L_UVS + (lane & 15) * UVS_ROW + g16 * 16;
  int gmo = OFF_MG + (g16 * 256 + id) * 256 + (lane & 15) * 16, gmo2 = OFF_MG + ((4 + blk) * 256 + id) * 256 + (lane & 15) * 16;
  int mgo = (lane & 15) < 6 ? (((lane & 15) >> 1) * 8 + g16 * 2 + (lane & 1)) * 256 : OOB;
  LAS unsigned char* ktw = L + L_KT + w * 1024 + (lane & 15) * 64 + g16 * 16;
  PIN(ktw);
  PIN(w2v0); PIN(w2v1); PIN(w1v0); PIN(w1v1); PIN(za); PIN(zb); PIN(tra); PIN(w1a); PIN(ka); PIN(hb); PIN(zp); PIN(ub); PIN(gmo); PIN(gmo2); PIN(mgo);
#define W1DMA(step, lds) dma_w1(w1w + (step) * 57344, w1v0, (lds))
#define W1DMA2(step, lds) dma_w2(w1w + (step) * 57344, w1v0, w1v1, (lds))
#define W2DMA(step, lds) dma_w2(((step) < 14 ? w2lo : w2hi) + (step) * 16384, w2v0, w2v1, (lds))
  SB();
  const unsigned epoch = __builtin_amdgcn_readfirstlane(epoch_v);
  const unsigned char* setp = ws + SET_BASE + (size_t)(epoch & 3u) * SET_STRIDE;
  const __amdgpu_buffer_rsrc_t rs = __builtin_amdgcn_make_buffer_rsrc((void*)setp, 0, SET_BYTES, 0x00020000);
  const unsigned tagA = epoch * 0x9E3779B1u + 0x7F4A7C15u, tagB = ~tagA;
  {
    const float c = bf_lo(cval);
    const float s = c * __builtin_amdgcn_rcpf(1.0f + __builtin_amdgcn_exp2f(-LOG2E * c));
    *(LAS u16*)(L + lrw + 3072 + (lane & 31) * 2) = (u16)pack2(s, s);
    SB();
#define XPUB(i) do { stg(rs, (tid >> 2) * 65536 + (tid & 3) * 16, OFF_XG + (i) * 16384 + id * 64, v4u{xa[i], tagA, xb[i], tagB}); SB(); } while (0)
    XPUB(0);
    XPUB(1);
    STAMP(1);
    LAS unsigned char* a = L + lrw + (8 * g16 + q4) * 96 + p4 * 8;
    const v4u sc = lds128(L + lrw + 3072 + g16 * 16);
    const v4u m0 = trd2(a, a + 384), m1 = trd2(a + 32, a + 416), m2 = trd2(a + 64, a + 448);
    SB();
    float fa[8], fb[8];
#pragma unroll
    for (int i = 0; i < 4; ++i) { fa[2 * i] = bf_lo(ra[i]); fa[2 * i + 1] = bf_hi(ra[i]); fb[2 * i] = bf_lo(rb[i]); fb[2 * i + 1] = bf_hi(rb[i]); }
    float sa_ = ((fa[0] + fa[1]) + (fa[2] + fa[3])) + ((fa[4] + fa[5]) + (fa[6] + fa[7]));
    float sb_ = ((fb[0] + fb[1]) + (fb[2] + fb[3])) + ((fb[4] + fb[5]) + (fb[6] + fb[7]));
    PIN(sa_); PIN(sb_);
    SB();
    const f32x4 c0 = mfma16(m0, sc, f32x4{}), c1 = mfma16(m1, sc, f32x4{}), c2 = mfma16(m2, sc, f32x4{});
    SB();
    float ma = wave_sum(sa_) * (1.0f / 512.0f), mb = wave_sum(sb_) * (1.0f / 512.0f);
    PIN(ma); PIN(mb);
    SB();
    XPUB(2);
    {
      const bool hi = lane & 1, t1 = lane & 2, t2_ = lane & 4;
      float x0 = hi ? c0[2] : c0[0], x1 = hi ? c0[3] : c0[1], y0 = hi ? c1[2] : c1[0], y1 = hi ? c1[3] : c1[1], z0 = hi ? c2[2] : c2[0], z1 = hi ? c2[3] : c2[1];
      PIN(x0); PIN(x1); PIN(y0); PIN(y1); PIN(z0); PIN(z1);
      stg(rs, mgo, OFF_MG + cc * 24 * 256 + ks * 16, v4u{f2u(t2_ ? z0 : t1 ? y0 : x0), tagA, f2u(t2_ ? z1 : t1 ? y1 : x1), tagB});
      SB();
    }
    float qa = 0.f, qb = 0.f;
#pragma unroll
    for (int i = 0; i < 8; ++i) { const float da = fa[i] - ma, db = fb[i] - mb; qa = __builtin_fmaf(da, da, qa); qb = __builtin_fmaf(db, db, qb); }
    const float va = rbf(rbf(wave_sum(qa) * (1.0f / 512.0f)) + 1e-5f), vb = rbf(rbf(wave_sum(qb) * (1.0f / 512.0f)) + 1e-5f);
    stg(rs, lane ? OOB : 0, OFF_SG + (id * 4 + w) * 16, v4u{pack2(ma, __builtin_amdgcn_sqrtf(va)), tagA, pack2(mb, __builtin_amdgcn_sqrtf(vb)), tagB});
    SB();
    XPUB(3);
#undef XPUB
  }
  STAMP(2);
  unsigned dsk = tid == 136 ? uD : 0u;
  PIN(dsk);

  {
    const float dtv = rbf(__builtin_amdgcn_exp2f(LOG2E * (w >> 1 ? bf_hi(uldt) : bf_lo(uldt))));
    const float are = dtv * bf_lo(uAre) * LOG2E, aim = dtv * bf_lo(uAim) * INV2PI;
    const float br = dtv * bf_lo(uBre), bi = dtv * bf_lo(uBim), cr = bf_lo(uCre), ci = bf_lo(uCim);
    const f32x2 wv = {cr * br - ci * bi, cr * bi + ci * br};
    const unsigned row = L_UVS + (w >> 1) * UVS_CH + (w & 1) * 256;
    const f32x2 e1 = cexp(are, aim), e8 = cexp(8.f * are, -8.f * aim), e128 = cexp(128.f * are, 128.f * aim);
    asm volatile("" :: "v"(wt), "v"(warm));
    const f32x2 e1p = {-e1.y, e1.x}, e8p = {-e8.y, e8.x}, e128p = {-e128.y, e128.x};
    const v4u pois = {POISON, POISON, POISON, POISON};
    f32x2 z[8], v, v2, y, y2;
#define M1(a) cmul(a, e1, e1p)
#define M8(a) cmul(a, e8, e8p)
#define MK(a) cmul(a, e128, e128p)
    z[0] = M1(wv);
    put4<40, 41, 42, 0>(row, wv, f32x2{1.f, 0.f}, e8, z[0]);
    stg(rs, za, zs, pois);
    v = M8(e8); z[1] = M1(z[0]); v2 = M8(v); z[2] = M1(z[1]);
    put4<43, 1, 44, 2>(row, v, z[1], v2, z[2]);
    v = M8(v2); z[3] = M1(z[2]); v2 = M8(v); z[4] = M1(z[3]);
    put4<45, 3, 46, 4>(row, v, z[3], v2, z[4]);
    stg(rs, zb, zs, pois);
    v = M8(v2); z[5] = M1(z[4]); v2 = M8(v); z[6] = M1(z[5]);
    put4<47, 5, 48, 6>(row, v, z[5], v2, z[6]);
    z[7] = M1(z[6]); z[0] = MK(z[0]); z[1] = MK(z[1]); z[2] = MK(z[2]);
    put4<7, 8, 9, 10>(row, z[7], z[0], z[1], z[2]);
    W1DMA2(0, lrw + 0);
    z[3] = MK(z[3]); z[4] = MK(z[4]); z[5] = MK(z[5]); z[6] = MK(z[6]);
    put4<11, 12, 13, 14>(row, z[3], z[4], z[5], z[6]);
    z[7] = MK(z[7]); z[1] = MK(z[1]); z[3] = MK(z[3]); z[5] = MK(z[5]);
    put4<15, 16, 17, 18>(row, z[7], z[1], z[3], z[5]);
    W1DMA2(2, lrw + 2048);
    z[7] = MK(z[7]); z[1] = MK(z[1]); z[3] = MK(z[3]); z[5] = MK(z[5]);
    put4<19, 20, 21, 22>(row, z[7], z[1], z[3], z[5]);
    y = MK(z[7]); z[3] = MK(z[3]); y2 = MK(y); z[4] = MK(z[3]);
    put4<23, 24, 25, 26>(row, y, z[3], y2, z[4]);
    W1DMA2(4, lrw + 4096);
    y = MK(y2); z[3] = MK(z[4]); y2 = MK(y); z[4] = MK(z[3]);
    put4<27, 28, 29, 30>(row, y, z[3], y2, z[4]);
    y = MK(y2); v = M8(v2); y2 = MK(y); v2 = M8(v);
    put4<31, 49, 32, 50>(row, y, v, y2, v2);
    W1DMA2(6, lrw + 6144);
    y = MK(y2); v = M8(v2); y2 = MK(y); v2 = M8(v);
    put4<33, 51, 34, 52>(row, y, v, y2, v2);
    y = MK(y2); v = M8(v2); y2 = MK(y); v2 = M8(v);
    put4<35, 53, 36, 54>(row, y, v, y2, v2);
    W1DMA2(8, lrw + 8192);
    y = MK(y2); v = M8(v2); y2 = MK(y); v2 = M8(v);
    put4<37, 55, 38, 56>(row, y, v, y2, v2);
    W1DMA2(10, lrw + 10240);
    y = MK(y2);
    *(LAS unsigned*)(L + row + 39 * UVS_ROW + lane * 4) = pack2(y.x, y.y);
#undef M1
#undef M8
#undef MK
  }
  BAR();
  STAMP(3);
  v4u gx[4], gs[4], gm, gn;
  const int xgo = OFF_XG + id * 16384;
#define S0LOADS() do { \
    _Pragma("unroll") for (int i = 0; i < 4; ++i) gx[i] = ldg(rs, tid * 16, xgo + i * 4096); \
    _Pragma("unroll") for (int i = 0; i < 4; ++i) gs[i] = ldg(rs, tid * 16, OFF_SG + i * 4096); \
    gm = ldg(rs, gmo, 0); gn = ldg(rs, gmo2, 0); } while (0)
  if (w < 3) {
    LAS unsigned char* ut = ub + w * 16 * UVS_ROW;
    v4u fv[2][8], fu[2][8];
#pragma unroll
    for (int s = 0; s < 8; ++s)
#pragma unroll
      for (int c = 0; c < 2; ++c) { fv[c][s] = lds128(ub + c * UVS_CH + 41 * UVS_ROW + s * 64); fu[c][s] = lds128(ut + c * UVS_CH + s * 64); SB(); }
    f32x4 a0 = {}, a1 = {};
#pragma unroll
    for (int s = 0; s < 8; ++s) { a0 = mfma16(fv[0][s], fu[0][s], a0); SB(); a1 = mfma16(fv[1][s], fu[1][s], a1); SB(); }
    *(LAS v4u*)ktw = v4u{pack2(a0[0] + bf_lo(dsk), a1[0] + bf_hi(dsk)), pack2(a0[1], a1[1]), pack2(a0[2], a1[2]), pack2(a0[3], a1[3])};
  }
  SB();
  S0LOADS();
  SB();
  BAR();
  {
    unsigned k[11];
#pragma unroll
    for (int i = 0; i < 11; ++i) k[i] = *(LAS unsigned*)koff[i];
    SB();
    if (w < 2) *(LAS v4u*)zp = v4u{0u, 0u, 0u, 0u};
#pragma unroll
    for (int c = 0; c < 2; ++c) {
      unsigned e[5], o[5];
#pragma unroll
      for (int i = 0; i < 5; ++i) {
        o[i] = __builtin_amdgcn_perm(k[2 * i + 1], k[2 * i], c ? 0x07060302u : 0x05040100u);
        e[i] = __builtin_amdgcn_perm(k[2 * i + 2], k[2 * i + 1], c ? 0x07060302u : 0x05040100u);
      }
      LAS unsigned char* d = L + L_KC + c * 4 * KC_STRIDE + 32 + tid * 16;
      *(LAS v4u*)(d) = v4u{e[1], e[2], e[3], e[4]};
      *(LAS v4u*)(d + KC_STRIDE) = v4u{o[1], o[2], o[3], o[4]};
      *(LAS v4u*)(d + 2 * KC_STRIDE) = v4u{e[0], e[1], e[2], e[3]};
      *(LAS v4u*)(d + 3 * KC_STRIDE) = v4u{o[0], o[1], o[2], o[3]};
    }
  }
  SB();
  STAMP(4);
  {
    unsigned bad = tagbad(tagbad(0u, gm, tagA, tagB), gn, tagA, tagB);
#pragma unroll
    for (int i = 0; i < 4; ++i) bad = tagbad(tagbad(bad, gx[i], tagA, tagB), gs[i], tagA, tagB);
    if (__builtin_expect(__any(bad != 0u), 0)) {
      for (unsigned spin = 0; spin < SPIN_CAP; ++spin) {
        FRESH();
        COUNT(20);
        bool again = false;
#pragma unroll
        for (int i = 0; i < 4; ++i) {
          if (__any(tagbad(0u, gx[i], tagA, tagB) != 0u)) { gx[i] = ldg(rs, tid * 16, xgo + i * 4096); again = true; }
          if (__any(tagbad(0u, gs[i], tagA, tagB) != 0u)) { gs[i] = ldg(rs, tid * 16, OFF_SG + i * 4096); again = true; }
        }
        if (__any(tagbad(tagbad(0u, gm, tagA, tagB), gn, tagA, tagB) != 0u)) { gm = ldg(rs, gmo, 0); gn = ldg(rs, gmo2, 0); again = true; }
        if (!again) break;
      }
    }
  }
  WAITV(0);
  STAMP(5);
  W1DMA2(12, lrw + 12288);
  float zsc;
  v4u mtp;
  {
    const int i0 = (int)pack2(row_sum(u2f(gm.x)), row_sum(u2f(gm.z))), i1 = (int)pack2(row_sum(u2f(gn.x)), row_sum(u2f(gn.z)));
    unsigned md[6];
#pragma unroll
    for (int k = 0; k < 6; ++k) md[k] = (unsigned)__builtin_amdgcn_readlane(k < 4 ? i0 : i1, 16 * (k & 3));
#define MOD(k, c) ((c) ? bf_hi(md[k]) : bf_lo(md[k]))
    float s0 = rbf(1.0f + MOD(4, 0)), s1 = rbf(1.0f + MOD(4, 1));
    s0 = s0 == 0.f ? 0x1p-30f : s0;
    s1 = s1 == 0.f ? 0x1p-30f : s1;
    const float G0 = MOD(2, 0) * s0, G1 = MOD(2, 1) * s1;
    const float A0 = rbf(1.0f + MOD(1, 0)) * G0, A1 = rbf(1.0f + MOD(1, 1)) * G1, B0 = MOD(0, 0) * G0, B1 = MOD(0, 1) * G1;
    zsc = w >> 1 ? s1 : s0;
    mtp = v4u{lane ? md[5] : pack2(s0, s1), tagA, md[3], tagB};
#undef MOD
#pragma unroll
    for (int i = 0; i < 4; ++i) {
      const float mA = bf_lo(gs[i].x), rA = __builtin_amdgcn_rcpf(bf_hi(gs[i].x)), mB = bf_lo(gs[i].z), rB = __builtin_amdgcn_rcpf(bf_hi(gs[i].z));
      const float n00 = (bf_lo(gx[i].x) - mA) * rA, n01 = (bf_hi(gx[i].x) - mA) * rA, n10 = (bf_lo(gx[i].z) - mB) * rB, n11 = (bf_hi(gx[i].z) - mB) * rB;
      const int o = (tid + 256 * i) * 4;
      W1DMA2(14 + 2 * i, lrw + (14 + 2 * i) * 1024);
      *(LAS unsigned*)(L + L_H + 480 + o) = pack2(__builtin_fmaf(n00, A0, B0), __builtin_fmaf(n10, A0, B0));
      *(LAS unsigned*)(L + L_H + H_STRIDE + 480 + o) = pack2(__builtin_fmaf(n01, A1, B1), __builtin_fmaf(n11, A1, B1));
      *(LAS unsigned*)(L + L_XB + o) = __builtin_amdgcn_perm(gx[i].z, gx[i].x, 0x05040100u);
      *(LAS unsigned*)(L + L_XB + 4096 + o) = __builtin_amdgcn_perm(gx[i].z, gx[i].x, 0x07060302u);
    }
  }
  BAR();
  STAMP(6);

  v4u w1f[32], w2f[28][2], mt0, mt1;
  {
    const int rh = w & 1;
    constexpr ConvOrder O = conv_order();
    v2u A[4][8][2], xk[4];
    v4u B[4][8];
    f32x4 acc[8];
#pragma unroll
    for (int u = 0; u < 8; ++u) {
      A[0][u][0] = trd(ka + 512 * u + 8);
      A[0][u][1] = trd(ka + 512 * u);
      B[0][7 - u] = lds128(hb + 64 * (8 * (7 - u) + 3));
      acc[u] = f32x4{};
    }
    SB();
    stg(rs, tid < 2 ? tid * 4096 : OOB, OFF_MT + id * 16, mtp);
    if (b == 0 && tid == 0) __hip_atomic_store((GAS unsigned*)ws, epoch + 1u, __ATOMIC_RELAXED, __HIP_MEMORY_SCOPE_AGENT);
    SB();
    static_for<0, 144>([&](auto Kc) __attribute__((always_inline)) {
      constexpr int K = Kc.v, r = K / 36, k = K % 36, t = O.t[k], u = O.u[k];
      if constexpr (k == 0 && r > 0) asm volatile("" :: "v"(B[r][7]));
      acc[t] = mfma16(v4u{A[r][u][0].x, A[r][u][0].y, A[r][u][1].x, A[r][u][1].y}, B[r][t - u], acc[t]);
      SB();
      if constexpr (K < 12) w1f[K] = trd2(w1a + K * 1024, w1a + K * 1024 + 256);
      if constexpr (K == 36) WAITV(2);
      if constexpr (K >= 36 && K < 46) w1f[K - 24] = trd2(w1a + (K - 24) * 1024, w1a + (K - 24) * 1024 + 256);
      constexpr int n = k - (r < 2 ? 9 : 4);
      if constexpr (r < 3 && n >= 0 && n < 24) {
        if constexpr (n % 3 == 2) B[r + 1][n / 3] = lds128(hb + 64 * (8 * (n / 3) - r + 2));
        else A[r + 1][n / 3][n % 3] = trd(ka + 512 * (n / 3) + 64 * (r + 1) + 8 - 8 * (n % 3));
      }
      if constexpr (r == 3 && k < 4) xk[k] = *(LAS v2u*)(L + L_XB + w * 2048 + k * 512 + (lane & 15) * 32 + g16 * 8);
      if constexpr (K == 14) W1DMA(22, L_RX + w * 3072);
      if constexpr (K == 22) W1DMA(23, L_TOP + w * 1024);
      if constexpr (K >= 37 && K < 69 && K % 8 == 5) W1DMA2(24 + (K - 37) / 4, lrw + (K - 37) / 4 * 1024);
      if constexpr (K >= 69 && K < 125 && K % 8 == 5) W2DMA((K - 69) / 8, lrw + 8192 + (K - 69) / 8 * 2048);
      if constexpr (K == 96) { mt0 = ldg(rs, tid * 16, OFF_MT); mt1 = ldg(rs, tid * 16, OFF_MT + 4096); }
      SB();
    });
    STAMP(7);
#define EX(wv, i) (L + l16 + ((i) < 2 ? L_XB + (wv) * 2048 + (i) * 1024 : L_RX + (wv) * 3072 + ((i) - 1) * 1024))
    f32x4 keep[4];
    asm volatile("" :: "v"(xk[3]));
    if (rh) {
#pragma unroll
      for (int i = 0; i < 4; ++i) { *(LAS f32x4*)EX(w, i) = acc[i]; keep[i] = acc[4 + i]; }
    } else {
#pragma unroll
      for (int i = 0; i < 4; ++i) { *(LAS f32x4*)EX(w, i) = acc[4 + i]; keep[i] = acc[i]; }
    }
#pragma unroll
    for (int i = 0; i < 4; ++i) {
      keep[i] = __builtin_elementwise_fma(f32x4{bf_lo(xk[i].x), bf_hi(xk[i].x), bf_lo(xk[i].y), bf_hi(xk[i].y)}, f32x4{zsc, zsc, zsc, zsc}, keep[i]);
      PIN(keep[i]);
    }
    BAR();
#pragma unroll
    for (int i = 0; i < 4; ++i) keep[i] += *(LAS f32x4*)EX(w ^ 1, i);
#undef EX
#pragma unroll
    for (int i = 0; i < 2; ++i) {
      const auto s0 = __builtin_amdgcn_permlane16_swap(pack2(keep[2 * i][0], keep[2 * i][1]), pack2(keep[2 * i + 1][0], keep[2 * i + 1][1]), false, false);
      const auto s1 = __builtin_amdgcn_permlane16_swap(pack2(keep[2 * i][2], keep[2 * i][3]), pack2(keep[2 * i + 1][2], keep[2 * i + 1][3]), false, false);
      stg(rs, i ? zb : za, zs, v4u{guard(s0[0]), guard(s1[0]), guard(s0[1]), guard(s1[1])});
    }
    SB();
  }
  STAMP(8);

  WAITV(2);
  if (__builtin_expect(__any(tagbad(tagbad(0u, mt0, tagA, tagB), mt1, tagA, tagB) != 0u), 0)) {
    for (unsigned spin = 0; spin < SPIN_CAP; ++spin) {
      FRESH();
      COUNT(21);
      mt0 = ldg(rs, tid * 16, OFF_MT);
      mt1 = ldg(rs, tid * 16, OFF_MT + 4096);
      if (!__any(tagbad(tagbad(0u, mt0, tagA, tagB), mt1, tagA, tagB) != 0u)) break;
    }
  }
  *(LAS unsigned*)(L + L_TS + tid * 4) = mt0.x;
  *(LAS unsigned*)(L + L_TB + tid * 4) = mt0.z;
  *(LAS unsigned*)(L + L_TG + tid * 4) = mt1.x;
  w1f[22] = trd2(L + L_RX + w * 3072 + tra, L + L_RX + w * 3072 + tra + 256);
  w1f[23] = trd2(L + L_TOP + w * 1024 + tra, L + L_TOP + w * 1024 + tra + 256);
  BAR();
  STAMP(9);
  const int w2t = (4 * hf + q4) * 128 + blk * 32 + (((p4 & 1) << 1) | (p4 >> 1)) * 8;
  LAS unsigned char* w2a = L + w2t + (q4 >> 1) * 64;
  LAS unsigned char* w2b = L + w2t + ((q4 >> 1) ^ 1) * 64;
  const int par = (ct >> 1) & 1;
  const unsigned char* xg = setp + OFF_Z + rp * 131072;
  const unsigned xl = L_XR + w * 8192;
#define XSRC(p) (xg + w * 8192 + ((p) ^ par) * 32768)
  const int slo = OFF_Z + (rp * 4 + w) * 32768 + ct * 2048;
  f32x4 uu[4], vv[4];
  v4u sl0, sl1;
  {
    LAS unsigned char* qr = L + ((lane & 31) == 0 ? L_TS : L_TB) + hf * 16;
    LAS unsigned char *pa = w2a + lrw + 8192, *pb = w2b + lrw + 8192;
    PIN(pa); PIN(pb);
    f32x16 uv;
    v4u qa[34];
    qa[0] = lds128(qr);
    qa[1] = lds128(qr + 32);
    static_for<0, 32>([&](auto Sc) __attribute__((always_inline)) {
      constexpr int s = Sc.v;
      mfma_va<s == 0>(uv, qa[s], w1f[s]);
      if constexpr (s < 30) qa[s + 2] = lds128(qr + (s + 2) * 32);
      if constexpr (s < 8) w1f[24 + s] = trd2(w1a + s * 1024, w1a + s * 1024 + 256);
      if constexpr (s >= 8 && s < 22) w2f[(s - 8) >> 1][s & 1] = trd2(((s & 1) ? pb : pa) + ((s - 8) >> 1) * 2048, ((s & 1) ? pb : pa) + ((s - 8) >> 1) * 2048 + 1024);
      if constexpr (s == XS0) {
        asm volatile("buffer_load_dwordx4 %0, %2, %3, %4 offen sc1\n\tbuffer_load_dwordx4 %1, %2, %3, %4 offen offset:1024 sc1"
                     : "=&v"(sl0), "=&v"(sl1) : "v"(l16), "s"(rs), "s"(slo) : "memory");
      }
      if constexpr (s > XS0 && s <= XS0 + 4) dma_x2(XSRC(0) + (s - XS0 - 1) * 2048, l16, xl + (s - XS0 - 1) * 2048);
      SB();
    });
    dma_x4(XSRC(1), l16, xl + 32768);
    dma_x4(XSRC(1) + 4096, l16, xl + 32768 + 4096);
    mfma_done(uv);
    LAS unsigned char* uw = L + L_UVT + (32 * w + (lane & 31)) * 4;
    if (lane < 32) { *(LAS float*)(uw) = uv[0]; *(LAS float*)(uw + 512) = -LOG2E * uv[1]; }
    LAS unsigned char* uvp = L + L_UVT + (32 * w + 4 * hf) * 4;
#pragma unroll
    for (int i = 0; i < 4; ++i) { uu[i] = *(LAS f32x4*)(uvp + i * 32); vv[i] = *(LAS f32x4*)(uvp + 512 + i * 32); }
  }
  {
    const float i0 = __builtin_amdgcn_rcpf(bf_lo(*(LAS u16*)(L + L_TS + (ct * 32 + (lane >> 2)) * 2)));
    const float i1 = __builtin_amdgcn_rcpf(bf_lo(*(LAS u16*)(L + L_TS + (ct * 32 + 16 + (lane >> 2)) * 2)));
    SB();
    asm volatile("s_waitcnt vmcnt(16)" : "+v"(sl0), "+v"(sl1) :: "memory");
    if (__builtin_expect(__any(max4(max4(0u, sl0), sl1) == POISON), 0)) {
      for (unsigned spin = 0; spin < SPIN_CAP; ++spin) {
        FRESH();
        COUNT(22);
        sl0 = ldg(rs, l16, slo);
        sl1 = ldg(rs, l16 + 1024, slo);
        if (!__any(max4(max4(0u, sl0), sl1) == POISON)) break;
      }
    }
    float sm[8], sq[8];
#pragma unroll
    for (int i = 0; i < 4; ++i) {
      const f32x2 a = f32x2{bf_lo(sl0[i]), bf_hi(sl0[i])} * f32x2{i0, i0}, c = f32x2{bf_lo(sl1[i]), bf_hi(sl1[i])} * f32x2{i1, i1};
      const f32x2 s2 = a + c, q2 = __builtin_elementwise_fma(a, a, c * c);
      sm[2 * i] = s2.x; sm[2 * i + 1] = s2.y; sq[2 * i] = q2.x; sq[2 * i + 1] = q2.y;
    }
#pragma unroll
    for (int i = 0; i < 4; ++i) { swap_add32(sm[i], sm[i + 4]); swap_add32(sq[i], sq[i + 4]); }
#pragma unroll
    for (int i = 0; i < 2; ++i) { swap_add16(sm[i], sm[i + 2]); swap_add16(sq[i], sq[i + 2]); }
#pragma unroll
    for (int i = 0; i < 2; ++i) { sm[i] = dpp_add<0x124>(dpp_add<0x128>(sm[i])); sq[i] = dpp_add<0x124>(dpp_add<0x128>(sq[i])); }
    const bool od = lane & 4;
    stg(rs, (lane & 8) ? OOB : (p4 * 8 + hf * 4 + blk * 2 + (od ? 1 : 0)) * 16, OFF_SX + ((rp * 4 + w) * 16 + ct) * 512,
        v4u{f2u(od ? sm[1] : sm[0]), tagA, f2u(od ? sq[1] : sq[0]), tagB});
    SB();
  }
  WAITV(9);
  STAMP(10);
  {
    v4u t[8];
    unsigned m = 0;
#pragma unroll
    for (int i = 0; i < 8; ++i) t[i] = lds128(L + xl + l16 + i * 1024);
#pragma unroll
    for (int i = 0; i < 8; ++i) m = max4(m, t[i]);
    if (__builtin_expect(__any(m == POISON), 0)) {
      for (unsigned spin = 0; spin < SPIN_CAP; ++spin) {
        COUNT(22);
        bool again = false;
        for (int i = 0; i < 8; ++i)
          if (__any(max4(0u, lds128(L + xl + l16 + i * 1024)) == POISON)) {
            again = true;
            dma_x1(XSRC(0) + i * 1024, l16, xl + i * 1024);
            dma_x1(XSRC(1) + i * 1024, l16, xl + 32768 + i * 1024);
          }
        if (!again) break;
        WAITV(0);
      }
    }
  }
  BAR();
  STAMP(11);

  constexpr int HW = 40;
  v4u hq[HW], res[2], sx[8];
  const int holo = OFF_HID + ((rp * 4 + rg) * 112 + 28 * w + 14 * rg) * 1024, hohi = holo - 28 * rg * 1024;
  const int cklo = OFF_CK + ((rp * 4 + rg) * 16 + 4 * w + 2 * rg) * 128, ckhi = cklo - 512 * rg, ckv = lane ? OOB : 0;
#define HLOAD(n) hq[(n) % HW] = (n) % 8 == 7 ? ldg(rs, ckv, ((n) % 32 < 16 ? cklo : ckhi) + ((n) % 32) / 8 * 128 + ((n) / 32) * 4096) \
    : ldg(rs, l16 + ((n) % 8 & 3) * 1024, ((n) % 32 < 16 ? holo : hohi) + ((n) % 32) / 8 * 7168 + ((n) / 32) * (2 * 114688) + ((n) % 8 >> 2) * 4096)
  const int hoff = OFF_HID + (rp * 4 * 112 + ct * 7 + 2 * w) * 1024, hv1 = w == 3 ? OOB : l16 + 1024;
  const int cko = OFF_CK + (rp * 64 + ct) * 128 + w * 4, ckw = lane == 63 ? 0 : OOB;
  const unsigned cmask = w == 3 ? 0u : ~0u;
  const int sxo = OFF_SX + (rp * 4 + w) * 8192;
  LAS unsigned char* xr[2];
#pragma unroll
  for (int i = 0; i < 2; ++i) { xr[i] = L + L_XR + tra + i * 65536; PIN(xr[i]); }
  LAS unsigned char* rr = L + L_XR + tra + (rg ^ par) * 32768 + (4 * dt + w) * 1024;
  LAS unsigned char* str = L + L_STAT + (lane & 31) * 8;
  PIN(str);
#define XA(t) (xr[(t) >> 6] + ((t) & 63) * 1024)
#define XF(t) trd2(XA(t), XA(t) + 256)
#define XEN
#define REDO(p) \
  if (XEN __builtin_expect(__any(!(acc[(p) % 3][0] == acc[(p) % 3][0])), 0)) { \
    for (unsigned spin = 0; spin < SPIN_CAP / 128; ++spin) { \
      COUNT(23); \
      for (int i = 0; i < 8; ++i) dma_x4(xg + ((p) ^ par) * 32768 + i * 4096, l16, L_XR + (p) * 32768 + i * 4096); \
      WAITV(0); \
      _Pragma("unroll") for (int s = 0; s < 32; ++s) { \
        const v4u t = XF(32 * (p) + s); \
        if (s == 0) mfma_av<true>(acc[(p) % 3], w1f[s], t); else mfma_av<false>(acc[(p) % 3], w1f[s], t); \
        SB(); \
      } \
      mfma_done(acc[(p) % 3]); \
      if (!__any(!(acc[(p) % 3][0] == acc[(p) % 3][0]))) break; \
    } \
  }
  f32x16 acc[3];
  f32x2 ec[4];
  float p1, p2, ee, er, ey;
  unsigned cs = 0, cr = 0;
  v4u x[128], eo;
#pragma unroll
  for (int t = 0; t < 4; ++t) x[t] = XF(t);
  SB();
  constexpr int VT = 40;
  static_for<0, EO + VT + 2>([&](auto Jc) __attribute__((always_inline)) {
    constexpr int J = Jc.v, p = J >> 4, j = J & 15;
    if constexpr (J < 64) {
      asm volatile("" :: "v"(x[2 * J + 1]));
      mfma_av<j == 0>(acc[p % 3], w1f[2 * j], x[2 * J]);
      if constexpr (J < 62) { x[2 * J + 4] = XF(2 * J + 4); x[2 * J + 5] = XF(2 * J + 5); }
      SB();
      mfma_av<false>(acc[p % 3], w1f[2 * j + 1], x[2 * J + 1]);
      SB();
    }
    if constexpr (J == 10) { WAITV(16); __builtin_amdgcn_s_barrier(); FRESH(); }
    if constexpr (J == EO - 2) {
      unsigned bad = 0;
#pragma unroll
      for (int i = 0; i < 8; ++i) bad = tagbad(bad, sx[i], tagA, tagB);
      if (__builtin_expect(__any(bad != 0u), 0)) {
        for (unsigned spin = 0; spin < SPIN_CAP; ++spin) {
          FRESH();
          COUNT(23);
          bad = 0;
#pragma unroll
          for (int i = 0; i < 8; ++i) sx[i] = ldg(rs, l16 + (i & 3) * 1024, sxo + (i >> 2) * 4096);
#pragma unroll
          for (int i = 0; i < 8; ++i) bad = tagbad(bad, sx[i], tagA, tagB);
          if (!__any(bad != 0u)) break;
        }
      }
      float sm = u2f(sx[0].x), sq = u2f(sx[0].z);
#pragma unroll
      for (int i = 1; i < 8; ++i) { sm += u2f(sx[i].x); PIN(sm); sq += u2f(sx[i].z); PIN(sq); }
      const float mean = swap32_add(sm) * (1.0f / 512.0f);
      const float ms = __builtin_fmaf(swap32_add(sq), 1.0f / 512.0f, 1e-5f);
      const float a = -LOG2E * __builtin_amdgcn_rsqf(fmaxf(__builtin_fmaf(-mean, mean, ms), 1e-5f));
      *(LAS f32x2*)(str + (w ^ par) * 256) = f32x2{a, -a * mean};
      SB();
    }
    if constexpr (J == EO - 1) {
      asm volatile("s_waitcnt lgkmcnt(4)" ::: "memory");
      __builtin_amdgcn_s_barrier();
      FRESH();
#pragma unroll
      for (int i = 0; i < 4; ++i) ec[i] = *(LAS f32x2*)(str + i * 256);
      SB();
    }
    if constexpr (J >= EO + 2) {
      constexpr int V = J - EO - 2, k = V & 15;
      float y = p2 * er;
      if constexpr (k & 1) { unsigned o = pack2(ey, y); PIN(o); eo[(k >> 1) & 3] = o; } else { PIN(y); ey = y; }
      if constexpr ((k & 7) == 7) {
        stg(rs, k >> 3 ? hv1 : l16, hoff + ((V >> 4) ^ par) * 114688, eo);
        if constexpr (k == 7) cs = sum4(0u, eo); else { cr = cs + (sum4(0u, eo) & cmask); }
        PIN(cs); PIN(cr);
      }
      if constexpr (V >= 16 && k < 2) { cr = red_step<3 * k + 2>(red_step<3 * k + 1>(red_step<3 * k>(cr))); PIN(cr); }
      if constexpr (V >= 16 && k == 1) __builtin_amdgcn_raw_buffer_store_b32(tagA - cr, rs, ckw, cko + (((V >> 4) - 1) ^ par) * 2048, SC1);
    }
    if constexpr (J >= EO + 1 && J < EO + VT + 1) { er = __builtin_amdgcn_rcpf(1.0f + ee); PIN(er); p2 = p1; }
    if constexpr (J >= EO && J < EO + VT) {
      constexpr int V = J - EO, k = V & 15;
      p1 = __builtin_fmaf(ec[V >> 4].x, acc[(V >> 4) % 3][k], __builtin_fmaf(ec[V >> 4].y, uu[k >> 2][k & 3], vv[k >> 2][k & 3]));
      ee = __builtin_amdgcn_exp2f(p1);
      PIN(ee);
    }
    SB();
    if constexpr (j == 1 && p >= 1) {
      if constexpr (p == 4) mfma_done(acc[0]);
      if constexpr (p == 2) REDO(1)
      if constexpr (p == 3) REDO(2)
      if constexpr (p == 4) REDO(3)
      if (((p - 1) & 1) == (rg ^ par)) res[(p - 1) >> 1] = trd2(rr + ((p - 1) >> 1) * 65536, rr + ((p - 1) >> 1) * 65536 + 256);
    }
    if constexpr (j == 2 && p >= 2) {
      if constexpr (p < 4) asm volatile("s_waitcnt lgkmcnt(4)" ::: "memory"); else asm volatile("s_waitcnt lgkmcnt(0)" ::: "memory");
      __builtin_amdgcn_s_barrier();
      FRESH();
    }
    if constexpr (J < 8) dma_x2(XSRC(2 + (J >> 2)) + (J & 3) * 2048, l16, xl + (2 + (J >> 2)) * 32768 + (J & 3) * 2048);
    if constexpr (J >= SXL && J < SXL + 4) {
      sx[2 * (J - SXL)] = ldg(rs, l16 + ((2 * (J - SXL)) & 3) * 1024, sxo + ((J - SXL) >> 1) * 4096);
      sx[2 * (J - SXL) + 1] = ldg(rs, l16 + ((2 * (J - SXL) + 1) & 3) * 1024, sxo + ((J - SXL) >> 1) * 4096);
    }
    if constexpr (J >= EO && J < EO + 22 && ((J - EO) & 1) == 0) {
      constexpr int n = (J - EO) >> 1;
      if constexpr (n < 3) W2DMA(7 + n, L_SP + w * 6144 + n * 2048);
      else W2DMA(7 + n, L_XR + ((n - 3) >> 2) * 32768 + w * 8192 + ((n - 3) & 3) * 2048);
    }
    if constexpr (J >= 51 && J < 58 && (J & 1)) W2DMA(18 + ((J - 51) >> 1), L_XR + 65536 + w * 8192 + ((J - 51) >> 1) * 2048);
    if constexpr (J >= 58 && J < 66) { HLOAD(2 * (J - 58)); HLOAD(2 * (J - 58) + 1); }
    if constexpr (J == 66) {
#pragma unroll
      for (int n = 0; n < 4; ++n) W2DMA(22 + n, L_XR + 98304 + w * 8192 + n * 2048);
    }
    SB();
    if constexpr (J == 15) STAMP(16);
    if constexpr (J == 31) STAMP(17);
    if constexpr (J == 47) STAMP(18);
    if constexpr (J == 63) STAMP(12);
  });
  static_assert(EO + 6 >= 26 && EO + 14 > 34 && EO + VT + 2 > 66 && VT % 8 == 0 && EO + 20 < 51, "transfer schedule");
#pragma unroll
  for (int h = VT / 8; h < 8; ++h) {
    v4u o;
#pragma unroll
    for (int i = 0; i < 4; ++i) {
      const int k = 8 * (h & 1) + 2 * i, u = h >> 1;
      const f32x2 t = __builtin_elementwise_fma(f32x2{ec[u].y, ec[u].y}, f32x2{uu[k >> 2][k & 3], uu[k >> 2][(k & 3) + 1]}, f32x2{vv[k >> 2][k & 3], vv[k >> 2][(k & 3) + 1]});
      const f32x2 pp = __builtin_elementwise_fma(f32x2{ec[u].x, ec[u].x}, f32x2{acc[u % 3][k], acc[u % 3][k + 1]}, t);
      const f32x2 d = f32x2{__builtin_amdgcn_exp2f(pp.x), __builtin_amdgcn_exp2f(pp.y)} + f32x2{1.0f, 1.0f};
      const f32x2 y = pp * f32x2{__builtin_amdgcn_rcpf(d.x), __builtin_amdgcn_rcpf(d.y)};
      o[i] = pack2(y.x, y.y);
    }
    stg(rs, (h & 1) ? hv1 : l16, hoff + ((h >> 1) ^ par) * 114688, o);
    if (h & 1) __builtin_amdgcn_raw_buffer_store_b32(tagA - red_all(cs + (sum4(0u, o) & cmask)), rs, ckw, cko + ((h >> 1) ^ par) * 2048, SC1);
    else cs = sum4(0u, o);
    SB();
  }
  static_for<16, 24>([&](auto Ic) __attribute__((always_inline)) { HLOAD(Ic.v); });
  SB();
  STAMP(13);

  f32x16 d[2][2];
  LAS unsigned char* redw = L + L_XR + w * 8192 + l16;
  LAS unsigned char* redr = L + L_XR + w * 2048 + l16;
#define REDW(un, q) *(LAS f32x4*)(redw + (un) * 32768 + (q) * 1024) = \
    f32x4{d[un][(q) >> 2][4 * ((q) & 3)], d[un][(q) >> 2][4 * ((q) & 3) + 1], d[un][(q) >> 2][4 * ((q) & 3) + 2], d[un][(q) >> 2][4 * ((q) & 3) + 3]}
  LAS unsigned char *w2sa = w2a + L_SP + w * 6144, *w2sb = w2b + L_SP + w * 6144;
  w2a += L_XR + w * 8192; w2b += L_XR + w * 8192;
  LAS unsigned char *w2ua = w2a + 65536, *w2ub = w2b + 65536;
  PIN(w2a); PIN(w2b); PIN(w2sa); PIN(w2sb); PIN(w2ua); PIN(w2ub);
#define PULL(step, pa, pb, off) do { w2f[step][0] = trd2((pa) + (off), (pa) + (off) + 1024); w2f[step][1] = trd2((pb) + (off), (pb) + (off) + 1024); } while (0)
  unsigned m = 0;
#pragma unroll
  for (int i = 0; i < 8; ++i) m = sum4(m, hq[i]);
  m = red_all(m);
  static_for<0, 8>([&](auto Gc) __attribute__((always_inline)) {
    constexpr int G = Gc.v, un = G >> 2, gq = G & 3;
    if (__builtin_expect((unsigned)__builtin_amdgcn_readlane((int)m, 63) != 4u * tagA, 0)) {
      for (unsigned spin = 0; spin < SPIN_CAP; ++spin) {
        FRESH();
        COUNT(24 + un);
        static_for<0, 8>([&](auto Ic) __attribute__((always_inline)) { HLOAD(8 * G + Ic.v); });
        m = 0;
#pragma unroll
        for (int i = 0; i < 8; ++i) m = sum4(m, hq[(8 * G + i) % HW]);
        if ((unsigned)__builtin_amdgcn_readlane((int)red_all(m), 63) == 4u * tagA) break;
      }
    }
    m = 0;
    static_for<0, 7>([&](auto Ic) __attribute__((always_inline)) {
      constexpr int i = Ic.v, I = 7 * G + i, n = 8 * G + i;
      mfma_av<gq == 0 && i == 0>(d[un][0], w2f[gq * 7 + i][0], hq[n % HW]);
      SB();
      if constexpr (I < 3) PULL(7 + I, w2sa, w2sb, I * 2048);
      if constexpr (I >= 3 && I < 11) PULL(7 + I, w2a, w2b, ((I - 3) >> 2) * 32768 + ((I - 3) & 3) * 2048);
      if constexpr (I >= 11 && I < 15) PULL(7 + I, w2ua, w2ub, (I - 11) * 2048);
      if constexpr (I >= 17 && I < 21) PULL(5 + I, w2ua, w2ub, 32768 + (I - 17) * 2048);
      if constexpr (I == 23 || I == 24) PULL(3 + I, w2sa, w2sb, (I - 23) * 2048);
      if constexpr (G == 4 && i >= 1) REDW(0, i - 1);
      if constexpr (G == 5 && i < 2) REDW(0, 6 + i);
      if constexpr (G < 7 && i >= 4) { m = red_step<2 * (i - 4)>(m); PIN(m); }
      SB();
      mfma_av<gq == 0 && i == 0>(d[un][1], w2f[gq * 7 + i][1], hq[n % HW]);
      SB();
      if constexpr (I == 8) { W2DMA(26, L_SP + w * 6144); W2DMA(27, L_SP + w * 6144 + 2048); }
      if constexpr (I < 8) HLOAD(24 + I);
      else if constexpr (G >= 1 && G < 5) { HLOAD(8 * G + 23 + i); if constexpr (i == 6) HLOAD(8 * G + 30); if constexpr (G == 4 && i == 6) HLOAD(63); }
      if constexpr (G < 7 && i >= 4) { m = red_step<2 * (i - 4) + 1>(m); PIN(m); }
      if constexpr (G < 7 && i < 4) { m = sum4(sum4(m, hq[(8 * G + 8 + 2 * i) % HW]), hq[(8 * G + 9 + 2 * i) % HW]); PIN(m); }
      SB();
    });
    if constexpr (G == 3) STAMP(14);
  });
  STAMP(19);
  const v4u gbv = lds128(L + L_TG + (64 * dt + 16 * w + 8 * hf) * 2), scv = lds128(L + L_TS + (64 * dt + 16 * w + 8 * hf) * 2);
  mfma_done(d[1][0]);
  PIN(d[1][1]);
#pragma unroll
  for (int q = 0; q < 8; ++q) REDW(1, q);
  f32x2 gb[4], rx[2][4];
#pragma unroll
  for (int i = 0; i < 4; ++i) {
    gb[i] = f32x2{bf_lo(gbv[i]), bf_hi(gbv[i])} * f32x2{-LN2, -LN2};
    const f32x2 inv = {__builtin_amdgcn_rcpf(bf_lo(scv[i])), __builtin_amdgcn_rcpf(bf_hi(scv[i]))};
    rx[0][i] = f32x2{bf_lo(res[0][i]), bf_hi(res[0][i])} * inv;
    rx[1][i] = f32x2{bf_lo(res[1][i]), bf_hi(res[1][i])} * inv;
    PIN(gb[i]); PIN(rx[0][i]); PIN(rx[1][i]);
  }
  BAR();
  const int orow = (rp * 128 + ((lane & 31) >> 3) * 16 + rg * 8 + (lane & 7)) * 1024 + dt * 128 + w * 32 + hf * 16;
#pragma unroll
  for (int un = 0; un < 2; ++un) {
    f32x4 s0 = {}, s1 = {};
#pragma unroll
    for (int k = 0; k < 4; ++k) { s0 += *(LAS f32x4*)(redr + k * 8192 + un * 32768); s1 += *(LAS f32x4*)(redr + k * 8192 + un * 32768 + 1024); }
    const f32x2 y0 = __builtin_elementwise_fma(gb[0], f32x2{s0[0], s0[1]}, rx[un][0]), y1 = __builtin_elementwise_fma(gb[1], f32x2{s0[2], s0[3]}, rx[un][1]);
    const f32x2 y2 = __builtin_elementwise_fma(gb[2], f32x2{s1[0], s1[1]}, rx[un][2]), y3 = __builtin_elementwise_fma(gb[3], f32x2{s1[2], s1[3]}, rx[un][3]);
    *(GAS v4u*)((GAS unsigned char*)out + orow + un * 65536) = v4u{pack2(y0.x, y0.y), pack2(y1.x, y1.y), pack2(y2.x, y2.y), pack2(y3.x, y3.y)};
  }
  STAMP(15);
}

extern "C" void kernel_launch(void* const* d_in, const int* in_sizes, int n_in, void* d_out, int out_size, void* d_ws, size_t ws_size,
                              hipStream_t stream) {
  (void)in_sizes; (void)n_in; (void)out_size; (void)ws_size;
  (void)hipFuncSetAttribute((const void*)s4_block_kernel, hipFuncAttributeMaxDynamicSharedMemorySize, LDS_BYTES);
  s4_block_kernel<<<256, 256, LDS_BYTES, stream>>>(
      (const unsigned char*)d_in[0], (const u16*)d_in[1], (const u16*)d_in[2], (const u16*)d_in[3], (const u16*)d_in[4], (const u16*)d_in[5],
      (const u16*)d_in[6], (const u16*)d_in[7], (const u16*)d_in[8], (const unsigned char*)d_in[10], (const unsigned char*)d_in[11],
      (const u16*)d_in[12], (const unsigned char*)d_in[13], (unsigned char*)d_out, (unsigned char*)d_ws);
}
```

```cpp
#include <hip/hip_runtime.h>

#define GAS __attribute__((address_space(1)))
#define LAS __attribute__((address_space(3)))
#define DI __device__ __forceinline__
typedef unsigned v4u __attribute__((ext_vector_type(4)));
typedef unsigned v2u __attribute__((ext_vector_type(2)));
typedef float f32x2 __attribute__((ext_vector_type(2)));
typedef float f32x4 __attribute__((ext_vector_type(4)));
typedef float f32x16 __attribute__((ext_vector_type(16)));
typedef __bf16 bf16x2 __attribute__((ext_vector_type(2)));
typedef __bf16 bf16x8 __attribute__((ext_vector_type(8)));
typedef short s16x4 __attribute__((ext_vector_type(4)));
typedef unsigned short u16;
typedef unsigned long long u64;

constexpr int SET_BASE = 4096, SET_STRIDE = 16 << 20;
constexpr int OFF_MT = 0;
constexpr int OFF_SG = 32768;
constexpr int OFF_MG = 65536;
constexpr int OFF_SX = 524288;
constexpr int OFF_XG = 1 << 20;
constexpr int OFF_Z = OFF_XG + (4 << 20);
constexpr int OFF_HID = OFF_Z + 2097152;
constexpr int OFF_CK = OFF_HID + 7340032;
constexpr int SET_BYTES = OFF_CK + 262144;
static_assert(SET_BYTES <= SET_STRIDE && OFF_MG + 393216 <= OFF_SX, "set size");

constexpr int L_TS = 0, L_TB = 1024, L_TG = 2048;
constexpr int L_UVT = 3072;
constexpr int L_M = 4096;
constexpr int L_UVS = L_M;
constexpr int UVS_ROW = 544, UVS_CH = 57 * UVS_ROW;
constexpr int L_KC = L_M;
constexpr int KC_STRIDE = 4160;
constexpr int L_H = L_KC + 8 * KC_STRIDE;
constexpr int H_STRIDE = 4576;
constexpr int L_XB = L_H + 2 * H_STRIDE;
constexpr int L_RX = L_XB + 8192;
constexpr int L_R = L_M + 65536;
constexpr int R_WAVE = 22528;
constexpr int L_TOP = L_R + 4 * R_WAVE;
constexpr int L_KT = L_TOP + 16;
constexpr int L_STAT = L_TOP;
constexpr int L_ETA = L_RX + 12288;
constexpr int L_XR = L_M;
constexpr int L_SP = L_M + 131072;
constexpr int LDS_BYTES = 163840;
static_assert(L_TOP + 4096 <= LDS_BYTES && L_ETA + 16 <= L_R && L_UVS + 2 * UVS_CH <= L_R && L_SP + 24576 <= L_TOP, "LDS map");
static_assert(L_H % 16 == 0 && L_XB % 16 == 0 && L_RX % 16 == 0 && L_KT % 16 == 0, "alignment");

#define ZLAG 30
#define MTK 124
constexpr int SXL = 11, EO = 26;
constexpr int CODE_BYTES = 44032;
constexpr int SC1 = 16;
constexpr int OOB = 0x7FFFFF00;
constexpr unsigned SPIN_CAP = 1u << 15;
constexpr unsigned POISON = 0xFFFFFFFFu;
constexpr float LOG2E = 1.4426950408889634f, LN2 = 0.6931471805599453f, INV2PI = 0.15915494309189535f;

#define SB() __builtin_amdgcn_sched_barrier(0)
#define WAITV(n) asm volatile("s_waitcnt vmcnt(" #n ")" ::: "memory")
#define PIN(x) asm volatile("" : "+v"(x))
#define FRESH() asm volatile("" ::: "memory")
#define PAUSE() asm volatile("s_sleep 4" ::: "memory")
#define BAR() do { asm volatile("s_waitcnt lgkmcnt(0)" ::: "memory"); __builtin_amdgcn_s_barrier(); asm volatile("" ::: "memory"); } while (0)
#define STAMP(i)
#define SARG
#define SPASS
#define COUNT(i)

#define STAMPA(i)
#define STAMPB(i) STAMP(i)
DI float u2f(unsigned u) { return __builtin_bit_cast(float, u); }
DI unsigned f2u(float f) { return __builtin_bit_cast(unsigned, f); }
DI float dot2(unsigned u, float c) { const bf16x2 v = __builtin_bit_cast(bf16x2, u); return __builtin_amdgcn_fdot2_f32_bf16(v, v, c, false); }
DI float bf_lo(unsigned u) { return __builtin_bit_cast(float, u << 16); }
DI float bf_hi(unsigned u) { return __builtin_bit_cast(float, u & 0xFFFF0000u); }
DI unsigned pack2(float a, float b) { f32x2 v = {a, b}; return __builtin_bit_cast(unsigned, __builtin_convertvector(v, bf16x2)); }
DI float rbf(float a) { return bf_lo(pack2(a, a)); }
DI unsigned sel(bool c, unsigned a, unsigned b) { asm("" : "+v"(a), "+v"(b)); return c ? a : b; }
DI unsigned guard(unsigned u) { return u < 0xFFFFFFFEu ? u : 0xFFFFFFFEu; }
DI unsigned max4(unsigned m, v4u v) {
  asm("v_max3_u32 %0, %0, %1, %2\n\tv_max3_u32 %0, %0, %3, %4" : "+v"(m) : "v"(v.x), "v"(v.y), "v"(v.z), "v"(v.w));
  return m;
}
DI f32x4 mfma16(v4u a, v4u b, f32x4 c) { return __builtin_amdgcn_mfma_f32_16x16x32_bf16(__builtin_bit_cast(bf16x8, a), __builtin_bit_cast(bf16x8, b), c, 0, 0, 0); }
DI f32x16 mfma32(v4u a, v4u b, f32x16 c) { return __builtin_amdgcn_mfma_f32_32x32x16_bf16(__builtin_bit_cast(bf16x8, a), __builtin_bit_cast(bf16x8, b), c, 0, 0, 0); }
template <bool FIRST> DI void mfma_av(f32x16& acc, const v4u& a, const v4u& b) {
  if (FIRST) asm volatile("v_mfma_f32_32x32x16_bf16 %0, %1, %2, 0" : "=&v"(acc) : "a"(a), "v"(b));
  else asm volatile("v_mfma_f32_32x32x16_bf16 %0, %1, %2, %0" : "+v"(acc) : "a"(a), "v"(b));
}
template <bool FIRST> DI void mfma_va(f32x16& acc, const v4u& a, const v4u& b) {
  if (FIRST) asm volatile("v_mfma_f32_32x32x16_bf16 %0, %1, %2, 0" : "=&v"(acc) : "v"(a), "a"(b));
  else asm volatile("v_mfma_f32_32x32x16_bf16 %0, %1, %2, %0" : "+v"(acc) : "v"(a), "a"(b));
}
template <bool FIRST> DI void mfma16_v(f32x4& acc, const v4u& a, const v4u& b) {
  if (FIRST) asm volatile("v_mfma_f32_16x16x32_bf16 %0, %1, %2, 0" : "=&v"(acc) : "v"(a), "v"(b));
  else asm volatile("v_mfma_f32_16x16x32_bf16 %0, %1, %2, %0" : "+v"(acc) : "v"(a), "v"(b));
}
DI void mfma_done(f32x16& acc) { asm volatile("s_nop 15" : "+v"(acc)); }
DI v2u trd(LAS unsigned char* p) { return __builtin_bit_cast(v2u, __builtin_amdgcn_ds_read_tr16_b64_v4i16((LAS s16x4*)p)); }
DI v4u trd2(LAS unsigned char* p0, LAS unsigned char* p1) { const v2u a = trd(p0), b = trd(p1); return v4u{a.x, a.y, b.x, b.y}; }
DI v4u lds128(LAS unsigned char* p) { return *(LAS v4u*)p; }
DI v4u ldg(__amdgpu_buffer_rsrc_t r, int voff, int soff) { return __builtin_amdgcn_raw_buffer_load_b128(r, voff, soff, SC1); }
DI void stg(__amdgpu_buffer_rsrc_t r, int voff, int soff, v4u v) {
  __builtin_amdgcn_raw_buffer_store_b128(v, r, voff, soff, SC1);
  asm volatile("s_nop 1" :: "v"(v) : "memory");
}
template <int CTRL> DI float dpp_add(float v) {
  return v + __builtin_bit_cast(float, __builtin_amdgcn_update_dpp(0, __builtin_bit_cast(int, v), CTRL, 0xF, 0xF, true));
}
DI float swap16_add(float v) { const unsigned u = __builtin_bit_cast(unsigned, v); const auto r = __builtin_amdgcn_permlane16_swap(u, u, false, false);
  return u2f(r[0]) + u2f(r[1]); }
DI float swap32_add(float v) { const unsigned u = __builtin_bit_cast(unsigned, v); const auto r = __builtin_amdgcn_permlane32_swap(u, u, false, false);
  return u2f(r[0]) + u2f(r[1]); }
DI float quad_sum(float v) { v = dpp_add<0xB1>(v); return dpp_add<0x4E>(v); }
DI float row_sum(float v) { return dpp_add<0x140>(dpp_add<0x141>(quad_sum(v))); }
DI float wave_sum(float v) { return swap32_add(swap16_add(row_sum(v))); }
DI f32x2 cmul(f32x2 a, f32x2 e, f32x2 ep) { const f32x2 t = f32x2{a.x, a.x} * e; return __builtin_elementwise_fma(f32x2{a.y, a.y}, ep, t); }
DI f32x2 cexp(float re2, float rev) {
  const float m = __builtin_amdgcn_exp2f(re2), f = __builtin_amdgcn_fractf(rev);
  return f32x2{m * __builtin_amdgcn_cosf(f), m * __builtin_amdgcn_sinf(f)};
}
DI void dma_w2(const unsigned char* sbase, unsigned v0, unsigned v1, unsigned lds_dst) {
  asm volatile("s_mov_b32 m0, %3\n\ts_nop 0\n\tglobal_load_lds_dwordx4 %0, %2\n\tglobal_load_lds_dwordx4 %1, %2 offset:1024"
               :: "v"(v0), "v"(v1), "s"(sbase), "s"(lds_dst) : "memory");
}
DI void dma_w1(const unsigned char* sbase, unsigned v, unsigned lds_dst) {
  asm volatile("s_mov_b32 m0, %2\n\ts_nop 0\n\tglobal_load_lds_dwordx4 %0, %1" :: "v"(v), "s"(sbase), "s"(lds_dst) : "memory");
}
DI void dma_x1(const unsigned char* sbase, unsigned v, unsigned lds_dst) {
  asm volatile("s_mov_b32 m0, %2\n\ts_nop 0\n\tglobal_load_lds_dwordx4 %0, %1 sc1" :: "v"(v), "s"(sbase), "s"(lds_dst) : "memory");
}
DI void set_m0(unsigned lds_dst) { asm volatile("s_mov_b32 m0, %0\n\ts_nop 0" :: "s"(lds_dst) : "memory"); }
template <int OFF> DI void dma_xn(const unsigned char* sbase, unsigned v) {
  asm volatile("global_load_lds_dwordx4 %0, %1 offset:%2 sc1" :: "v"(v), "s"(sbase), "n"(OFF) : "memory");
}
DI void dma_x2(const unsigned char* sbase, unsigned v, unsigned lds_dst) {
  asm volatile("s_mov_b32 m0, %2\n\ts_nop 0\n\tglobal_load_lds_dwordx4 %0, %1 sc1\n\tglobal_load_lds_dwordx4 %0, %1 offset:1024 sc1"
               :: "v"(v), "s"(sbase), "s"(lds_dst) : "memory");
}
DI void dma_x4(const unsigned char* sbase, unsigned v, unsigned lds_dst) {
  asm volatile("s_mov_b32 m0, %2\n\ts_nop 0\n\tglobal_load_lds_dwordx4 %0, %1 sc1\n\tglobal_load_lds_dwordx4 %0, %1 offset:1024 sc1\n\t"
               "global_load_lds_dwordx4 %0, %1 offset:2048 sc1\n\tglobal_load_lds_dwordx4 %0, %1 offset:3072 sc1"
               :: "v"(v), "s"(sbase), "s"(lds_dst) : "memory");
}
template <int I> struct IC { static constexpr int v = I; };
template <int I, int N, class F> DI void static_for(F&& f) { if constexpr (I < N) { f(IC<I>{}); static_for<I + 1, N>(f); } }
DI unsigned now() { u64 t; asm volatile("s_memrealtime %0\n\ts_waitcnt lgkmcnt(0)" : "=s"(t) :: "memory"); return (unsigned)t; }
template <int N> DI void waitv() { asm volatile("s_waitcnt vmcnt(%0)" :: "n"(N) : "memory"); }
struct ConvOrder { int t[36], u[36]; };
constexpr ConvOrder conv_order() {
  ConvOrder o = {};
  int done[8] = {}, last = -1;
  for (int k = 0; k < 36; ++k) {
    int best = -1;
    for (int t = 0; t < 8; ++t)
      if (t != last && done[t] <= t && (best < 0 || t - done[t] > best - done[best])) best = t;
    o.t[k] = best; o.u[k] = done[best]++; last = best;
  }
  return o;
}
constexpr ConvOrder conv_order_last() {
  ConvOrder o = {};
  int k = 0;
  for (int p = 0; p < 4; ++p)
    for (int u = 0; u <= 2 * p + 1; ++u) {
      o.t[k] = 2 * p + 1; o.u[k] = u; ++k;
      if (u <= 2 * p) { o.t[k] = 2 * p; o.u[k] = u; ++k; }
    }
  return o;
}
constexpr int epi_slot(int v) { return v < 20 ? 2 * EO + v : v < 32 ? 2 * EO + 20 + 2 * (v - 20) : 2 * 50 + (v - 32); }
constexpr int epi_dist(int v) { return v >= 20 && v < 32 ? 2 : 1; }
constexpr int hl_upto(int i) { return i < 4 ? 25 + (5 * i + 1) / 2 : i < 8 ? 35 : i < 36 ? 35 + 5 * (i - 8) / 4 : 70; }
DI unsigned tagbad(unsigned bad, v4u g, unsigned tagA, unsigned tagB) {
  return __builtin_amdgcn_bitop3_b32(__builtin_amdgcn_bitop3_b32(bad, g.y, tagA, 0xF6), g.w, tagB, 0xF6);
}
template <int R0, int R1, int R2, int R3> DI void put4(unsigned base, f32x2 a, f32x2 b, f32x2 c, f32x2 d) {
  asm volatile("s_mov_b32 m0, %4\n\ts_nop 0\n\tds_write_addtid_b32 %0 offset:%5\n\tds_write_addtid_b32 %1 offset:%6\n\t"
               "ds_write_addtid_b32 %2 offset:%7\n\tds_write_addtid_b32 %3 offset:%8"
               :: "v"(pack2(a.x, a.y)), "v"(pack2(b.x, b.y)), "v"(pack2(c.x, c.y)), "v"(pack2(d.x, d.y)), "s"(base),
                  "n"(R0 * UVS_ROW), "n"(R1 * UVS_ROW), "n"(R2 * UVS_ROW), "n"(R3 * UVS_ROW) : "memory");
}
DI unsigned sum4(unsigned m, v4u v) {
  asm("v_add3_u32 %0, %0, %1, %2\n\tv_add3_u32 %0, %0, %3, %4" : "+v"(m) : "v"(v.x), "v"(v.y), "v"(v.z), "v"(v.w));
  return m;
}
DI unsigned sum8(unsigned m, v4u a, v4u b) {
  asm("v_add3_u32 %0, %0, %1, %2\n\tv_add3_u32 %0, %0, %3, %4\n\tv_add3_u32 %0, %0, %5, %6\n\tv_add3_u32 %0, %0, %7, %8"
      : "+v"(m) : "v"(a.x), "v"(a.y), "v"(a.z), "v"(a.w), "v"(b.x), "v"(b.y), "v"(b.z), "v"(b.w));
  return m;
}
template <int I> DI unsigned red_step(unsigned v) {
  constexpr int ctrl[6] = {0xB1, 0x4E, 0x141, 0x140, 0x142, 0x143}, rows[6] = {0xF, 0xF, 0xF, 0xF, 0xA, 0xC};
  return v + (unsigned)__builtin_amdgcn_update_dpp(0, (int)v, ctrl[I], rows[I], 0xF, true);
}
DI unsigned red_all(unsigned v) { return red_step<5>(red_step<4>(red_step<3>(red_step<2>(red_step<1>(red_step<0>(v)))))); }
DI void swap_add32(float& a, float b) { const auto r = __builtin_amdgcn_permlane32_swap(f2u(a), f2u(b), false, false); a = u2f(r[0]) + u2f(r[1]); }
DI void swap_add16(float& a, float b) { const auto r = __builtin_amdgcn_permlane16_swap(f2u(a), f2u(b), false, false); a = u2f(r[0]) + u2f(r[1]); }

__global__ void __launch_bounds__(256, 1)
s4_block_kernel(const unsigned char* __restrict__ xin, const u16* __restrict__ pAre, const u16* __restrict__ pAim, const u16* __restrict__ pBre,
                const u16* __restrict__ pBim, const u16* __restrict__ pCre, const u16* __restrict__ pCim, const u16* __restrict__ pldt,
                const u16* __restrict__ pD, const unsigned char* __restrict__ w1, const unsigned char* __restrict__ w2, const u16* __restrict__ pc,
                const unsigned char* __restrict__ wmod, unsigned char* __restrict__ out, unsigned char* ws) {
  LAS unsigned char* const L = (LAS unsigned char*)16u - 16;
  const int tid = threadIdx.x, lane = tid & 63, w = __builtin_amdgcn_readfirstlane(tid >> 6);
  const int b = blockIdx.x, xcd = b & 7, slot = b >> 3, id = xcd * 32 + slot;
  const int ks = (id >> 6) * 4 + w, cc = id & 63;
  const int rp = 4 * (xcd >> 1) + (slot >> 3), rg = xcd & 1, dt = slot & 7, si = 8 * rg + dt;
  const int ct = 7 * rg + min(dt, 6);
  const int rot = (w & 1) ? 3 - 3 * rg : 4 * rg;
  STAMP(0);

  const unsigned char* pcode;
  asm volatile("s_getpc_b64 %0" : "=s"(pcode));
  const unsigned warm = *(const GAS unsigned*)((const GAS unsigned char*)pcode + (unsigned)min((((slot & 7) * 4 + w) * 11 + min(lane, 10)) * 128, CODE_BYTES));
  const int g16 = lane >> 4, q4 = (lane & 15) >> 2, p4 = lane & 3, hf = lane >> 5, blk = (lane >> 4) & 1, l16 = lane * 16;
  const unsigned lrw = L_R + w * R_WAVE;
  unsigned wmo[3], co = (ks * 32 + (lane & 31)) * 2, xo = id * 8192 + (tid & 3) * 2048 + (tid >> 2) * 16, ro = id * 8192 + w * 2048 + hf * 1024 + (lane & 31) * 16, po = id * 512 + tid * 2;
  unsigned wto = id * 7168 + (w >> 1) * 3584 + min(lane, 27) * 128;
#pragma unroll
  for (int i = 0; i < 3; ++i) { const unsigned idx = lane + 64 * i; wmo[i] = (ks * 32 * 6144 + cc * 96 - i * 1024) + idx / 6 * (6144 - 96) + idx * 16; PIN(wmo[i]); }
  PIN(co); PIN(xo); PIN(ro); PIN(po); PIN(wto);
  set_m0(lrw);
  SB();
  const unsigned epoch_v = __hip_atomic_load((GAS unsigned*)ws, __ATOMIC_RELAXED, __HIP_MEMORY_SCOPE_AGENT);
  asm volatile("global_load_lds_dwordx4 %0, %3\n\tglobal_load_lds_dwordx4 %1, %3 offset:1024\n\tglobal_load_lds_dwordx4 %2, %3 offset:2048"
               :: "v"(wmo[0]), "v"(wmo[1]), "v"(wmo[2]), "s"(wmod) : "memory");
  const unsigned cval = *(const GAS u16*)((const GAS unsigned char*)pc + co);
  SB();
  const v4u xa = *(const GAS v4u*)((const GAS unsigned char*)xin + xo), xb = *(const GAS v4u*)((const GAS unsigned char*)xin + 1024 + xo);
  const v4u ra = *(const GAS v4u*)((const GAS unsigned char*)xin + ro), rb = *(const GAS v4u*)((const GAS unsigned char*)xin + 512 + ro);
  SB();
#define PARAM(p) (unsigned)*(const GAS u16*)((const GAS unsigned char*)(p) + po)
  const unsigned uAre = PARAM(pAre), uAim = PARAM(pAim), uBre = PARAM(pBre), uBim = PARAM(pBim), uCre = PARAM(pCre), uCim = PARAM(pCim);
#undef PARAM
  SB();
  const unsigned wt = *(const GAS unsigned*)((const GAS unsigned char*)(w & 1 ? w2 : w1) + wto);
  unsigned uldt, uD;
  asm volatile("s_load_dword %0, %2, %4\n\ts_load_dword %1, %3, %4" : "=&s"(uldt), "=&s"(uD) : "s"(pldt), "s"(pD), "s"(id * 4));
  SB();
  STAMPA(26);
  asm volatile("" : "+s"(out), "+s"(w1), "+s"(w2));
  LAS unsigned char* koff[12];
  {
    float fj = (float)(8 * tid - 3);
    PIN(fj);
    const unsigned lin = (unsigned)(8 * tid - 3 + 0x4300 - 128) * 0x10001u + 0x10000u;
#pragma unroll
    for (int i = 0; i < 12; i += 2) {
      const unsigned r = sel(tid < 32, lin + i * 0x10001u, pack2(fj + (float)i, fj + (float)(i + 1)));
      koff[i] = L + ((r & 0xFFFFu) * 4 + (L_KT + (128 - 0x4300) * 4));
      koff[i + 1] = L + ((r >> 16) * 4 + (L_KT + (128 - 0x4300) * 4));
      PIN(koff[i]);
      if (i < 10) PIN(koff[i + 1]);
    }
  }
  *(LAS v4u*)(L + L_KT - 16) = v4u{0u, 0u, 0u, 0u};
  LAS unsigned char* ktw[4];
  {
    const int row = w * 16 + (lane & 15), sg = max((row >> 3) - 1, 0), rr_ = row - 8 * sg;
    const int idx0 = 128 * sg + ((rr_ >> (3 - sg)) << (7 - sg)) + (rr_ & ((8 >> sg) - 1)) + 1, st = 8 >> sg;
#pragma unroll
    for (int r = 0; r < 4; ++r) {
      const int idx = sel(row < 40, idx0 + st * (4 * g16 + r), tid == 136 && r == 0 ? 0 : 641 + lane);
      ktw[r] = L + L_KT + idx * 4;
      PIN(ktw[r]);
    }
  }
  const unsigned char* w2lo = w2 + (448 * w + 64 * rot) * 1024 + dt * 128;
  const unsigned char* w2c3 = rot == 4 ? w2lo - 448 * 1024 : w2lo;
  const unsigned char* w2hi = rot ? w2lo - 448 * 1024 : w2lo;
  unsigned w2v0 = (lane >> 3) * 1024 + (((lane & 7) ^ (blk << 2)) * 16), w2v1 = w2v0 + 7168;
  const unsigned char* w1w = w1 + ct * 256 + w * 64;
  unsigned w1v0 = (lane >> 2) * 3584 + p4 * 16, w1v1 = w1v0 + 57344 - 1024;
  const int zs = OFF_Z + (2 * id + (w >> 1)) * 64;
  int za = (((w & 1) + 2 * (g16 & 1)) * 8 + q4 * 2 + (g16 >> 1)) * 32768 + p4 * 16, zb = za + 32 * 32768;
  int tra = (hf * 8 + q4) * 64 + blk * 32 + p4 * 8;
  LAS unsigned char* w1a = L + lrw + tra;
  LAS unsigned char* ka = L + L_KC + ((w >> 1) * 4 + q4) * KC_STRIDE + 2 * (32 - 8 * g16 + 4 * p4) - 8 + 256 * (w & 1);
  LAS unsigned char* hb = L + L_H + (w >> 1) * H_STRIDE + (lane & 15) * 32 + g16 * 16 + 256 - 256 * (w & 1);
  LAS unsigned char* zp = L + sel(tid < 64, L_KC + ((lane >> 1) & 7) * KC_STRIDE + (lane & 1) * 16, L_H + hf * H_STRIDE + min(lane & 31, 29) * 16);
  LAS unsigned char* ub = L + L_UVS + (lane & 15) * UVS_ROW + g16 * 16;
  int gmo = OFF_MG + (g16 * 256 + id) * 256 + (lane & 15) * 16, gmo2 = OFF_MG + ((4 + blk) * 256 + id) * 256 + (lane & 15) * 16;
  int mgo = sel(lane < 48, (lane >> 1) * 256 + (lane & 1) * 8, OOB), sgo = sel(lane & 31, OOB, hf * 8);
  LAS unsigned char* xbw = L + L_XB + (tid >> 7) * 2048 + (tid & 127) * 4;
  LAS unsigned char* xbr = L + L_XB + w * 2048 + (lane & 15) * 32 + g16 * 8;
  LAS unsigned char *exa = L + L_XB + w * 2048 + l16, *exb = L + L_RX + w * 3072 + l16, *eya = L + L_XB + (w ^ 1) * 2048 + l16, *eyb = L + L_RX + (w ^ 1) * 3072 + l16;
  PIN(xbw); PIN(xbr); PIN(exa); PIN(exb); PIN(eya); PIN(eyb);
  int mto = (tid >> 1) * 128 + (tid & 1) * 16;
  LAS unsigned char* mtw = L + (tid & 1) * L_TG + (tid >> 1) * 4;
  PIN(mto); PIN(mtw);
  PIN(w2v0); PIN(w2v1); PIN(w1v0); PIN(w1v1); PIN(za); PIN(zb); PIN(tra); PIN(w1a); PIN(ka); PIN(hb); PIN(zp); PIN(ub); PIN(gmo); PIN(gmo2); PIN(mgo); PIN(sgo);
#define W1DMA(step, lds) dma_w1(w1w + (step) * 57344, w1v0, (lds))
#define W1DMA2(step, lds) dma_w2(w1w + (step) * 57344, w1v0, w1v1, (lds))
#define W2DMA(step, lds) dma_w2(((step) < 12 ? w2lo : (step) < 16 ? w2c3 : w2hi) + (step) * 16384, w2v0, w2v1, (lds))
  SB();
  STAMPA(27);
  asm volatile("s_waitcnt lgkmcnt(0)" : "+s"(uldt), "+s"(uD));
  const unsigned epoch = __builtin_amdgcn_readfirstlane(epoch_v);
  asm volatile("" :: "s"(epoch));
  STAMPA(28);
  const unsigned char* setp = ws + SET_BASE + (size_t)(epoch & 3u) * SET_STRIDE;
  const __amdgpu_buffer_rsrc_t rs = __builtin_amdgcn_make_buffer_rsrc((void*)setp, 0, SET_BYTES, 0x00020000);
  const unsigned tagA = epoch * 0x9E3779B1u + 0x7F4A7C15u, tagB = ~tagA;
  {
    const float c = bf_lo(cval);
    const float s = c * __builtin_amdgcn_rcpf(1.0f + __builtin_amdgcn_exp2f(-LOG2E * c));
    *(LAS u16*)(L + lrw + 3072 + (lane & 31) * 2) = (u16)pack2(s, s);
    SB();
#define XPUB(i) do { stg(rs, (tid >> 2) * 65536 + (tid & 3) * 16, OFF_XG + (i) * 16384 + id * 64, v4u{xa[i], tagA, xb[i], tagB}); SB(); } while (0)
    asm volatile("" :: "v"(xb));
    STAMPA(29);
    XPUB(0);
    XPUB(1);
    STAMP(1);
    LAS unsigned char* a = L + lrw + (8 * g16 + q4) * 96 + p4 * 8;
    const v4u sc = lds128(L + lrw + 3072 + g16 * 16);
    const v4u m0 = trd2(a, a + 384), m1 = trd2(a + 32, a + 416), m2 = trd2(a + 64, a + 448);
    SB();
    f32x2 fa[8];
#pragma unroll
    for (int i = 0; i < 4; ++i) { fa[i] = f32x2{bf_lo(ra[i]), bf_hi(ra[i])}; fa[4 + i] = f32x2{bf_lo(rb[i]), bf_hi(rb[i])}; }
    const f32x2 s2 = ((fa[0] + fa[1]) + (fa[2] + fa[3])) + ((fa[4] + fa[5]) + (fa[6] + fa[7]));
    float mean = s2.x + s2.y;
    PIN(mean);
    SB();
    const f32x4 c0 = mfma16(sc, m0, f32x4{}), c1 = mfma16(sc, m1, f32x4{}), c2 = mfma16(sc, m2, f32x4{});
    SB();
    mean = swap16_add(row_sum(mean)) * (1.0f / 512.0f);
    PIN(mean);
    SB();
    XPUB(2);
    __builtin_amdgcn_raw_buffer_store_b64(v2u{sel(lane < 16, f2u(c0[0]), sel(lane < 32, f2u(c1[0]), f2u(c2[0]))), (lane & 1) ? tagB : tagA}, rs, mgo,
                                          OFF_MG + cc * 24 * 256 + ks * 16, SC1);
    SB();
    f32x2 q2 = {0.f, 0.f}, q3 = {0.f, 0.f};
#pragma unroll
    for (int i = 0; i < 8; i += 2) {
      const f32x2 d0 = fa[i] - f32x2{mean, mean}, d1 = fa[i + 1] - f32x2{mean, mean};
      q2 = __builtin_elementwise_fma(d0, d0, q2);
      q3 = __builtin_elementwise_fma(d1, d1, q3);
    }
    q2 += q3;
    const float var = rbf(rbf(swap16_add(row_sum(q2.x + q2.y)) * (1.0f / 512.0f)) + 1e-5f);
    __builtin_amdgcn_raw_buffer_store_b64(v2u{pack2(mean, __builtin_amdgcn_sqrtf(var)), hf ? tagB : tagA}, rs, sgo, OFF_SG + (id * 4 + w) * 16, SC1);
    SB();
    XPUB(3);
#undef XPUB
  }
  STAMP(2);
  unsigned dsk = tid == 136 ? uD : 0u;
  PIN(dsk);

  {
    const float dtv = rbf(__builtin_amdgcn_exp2f(LOG2E * (w >> 1 ? bf_hi(uldt) : bf_lo(uldt))));
    const float are = dtv * bf_lo(uAre) * LOG2E, aim = dtv * bf_lo(uAim) * INV2PI;
    const float br = dtv * bf_lo(uBre), bi = dtv * bf_lo(uBim), cr = bf_lo(uCre), ci = bf_lo(uCim);
    const f32x2 wv = {cr * br - ci * bi, cr * bi + ci * br};
    const unsigned row = L_UVS + (w >> 1) * UVS_CH + (w & 1) * 256;
    const f32x2 e1 = cexp(are, aim), e8 = cexp(8.f * are, -8.f * aim), e128 = cexp(128.f * are, 128.f * aim);
    const f32x2 e1p = {-e1.y, e1.x}, e8p = {-e8.y, e8.x}, e128p = {-e128.y, e128.x};
    const v4u pois = {POISON, POISON, POISON, POISON};
    f32x2 z[8], v, v2, y, y2;
#define M1(a) cmul(a, e1, e1p)
#define M8(a) cmul(a, e8, e8p)
#define MK(a) cmul(a, e128, e128p)
    z[0] = M1(wv);
    put4<40, 41, 42, 0>(row, wv, f32x2{1.f, 0.f}, e8, z[0]);
    W1DMA2(0, lrw + 0);
    v = M8(e8); z[1] = M1(z[0]); v2 = M8(v); z[2] = M1(z[1]);
    put4<43, 1, 44, 2>(row, v, z[1], v2, z[2]);
    v = M8(v2); z[3] = M1(z[2]); v2 = M8(v); z[4] = M1(z[3]);
    put4<45, 3, 46, 4>(row, v, z[3], v2, z[4]);
    W1DMA2(2, lrw + 2048);
    v = M8(v2); z[5] = M1(z[4]); v2 = M8(v); z[6] = M1(z[5]);
    put4<47, 5, 48, 6>(row, v, z[5], v2, z[6]);
    z[7] = M1(z[6]); z[0] = MK(z[0]); z[1] = MK(z[1]); z[2] = MK(z[2]);
    put4<7, 8, 9, 10>(row, z[7], z[0], z[1], z[2]);
    W1DMA2(4, lrw + 4096);
    z[3] = MK(z[3]); z[4] = MK(z[4]); z[5] = MK(z[5]); z[6] = MK(z[6]);
    put4<11, 12, 13, 14>(row, z[3], z[4], z[5], z[6]);
    z[7] = MK(z[7]); z[1] = MK(z[1]); z[3] = MK(z[3]); z[5] = MK(z[5]);
    put4<15, 16, 17, 18>(row, z[7], z[1], z[3], z[5]);
    W1DMA2(6, lrw + 6144);
    z[7] = MK(z[7]); z[1] = MK(z[1]); z[3] = MK(z[3]); z[5] = MK(z[5]);
    put4<19, 20, 21, 22>(row, z[7], z[1], z[3], z[5]);
    y = MK(z[7]); z[3] = MK(z[3]); y2 = MK(y); z[4] = MK(z[3]);
    put4<23, 24, 25, 26>(row, y, z[3], y2, z[4]);
    W1DMA2(8, lrw + 8192);
    y = MK(y2); z[3] = MK(z[4]); y2 = MK(y); z[4] = MK(z[3]);
    put4<27, 28, 29, 30>(row, y, z[3], y2, z[4]);
    y = MK(y2); v = M8(v2); y2 = MK(y); v2 = M8(v);
    put4<31, 49, 32, 50>(row, y, v, y2, v2);
    W1DMA2(10, lrw + 10240);
    y = MK(y2); v = M8(v2); y2 = MK(y); v2 = M8(v);
    put4<33, 51, 34, 52>(row, y, v, y2, v2);
    y = MK(y2); v = M8(v2); y2 = MK(y); v2 = M8(v);
    put4<35, 53, 36, 54>(row, y, v, y2, v2);
    stg(rs, za, zs, pois);
    y = MK(y2); v = M8(v2); y2 = MK(y); v2 = M8(v);
    put4<37, 55, 38, 56>(row, y, v, y2, v2);
    y = MK(y2);
    *(LAS unsigned*)(L + row + 39 * UVS_ROW + lane * 4) = pack2(y.x, y.y);
    stg(rs, zb, zs, pois);
#undef M1
#undef M8
#undef MK
  }
  BAR();
  STAMP(3);
  v4u gx[4], gs[4], gm, gn;
  const int xgo = OFF_XG + id * 16384;
#define S0LOADS() do { \
    _Pragma("unroll") for (int i = 0; i < 4; ++i) gx[i] = ldg(rs, tid * 16, xgo + i * 4096); \
    _Pragma("unroll") for (int i = 0; i < 4; ++i) gs[i] = ldg(rs, tid * 16, OFF_SG + i * 4096); \
    gm = ldg(rs, gmo, 0); gn = ldg(rs, gmo2, 0); } while (0)
  if (w < 3) {
    LAS unsigned char* ut = ub + w * 16 * UVS_ROW;
    v4u fv[2][8], fu[2][8];
#pragma unroll
    for (int s = 0; s < 8; ++s)
#pragma unroll
      for (int c = 0; c < 2; ++c) { fv[c][s] = lds128(ub + c * UVS_CH + 41 * UVS_ROW + s * 64); fu[c][s] = lds128(ut + c * UVS_CH + s * 64); SB(); }
    f32x4 a0 = {}, a1 = {};
#pragma unroll
    for (int s = 0; s < 8; ++s) { a0 = mfma16(fv[0][s], fu[0][s], a0); SB(); a1 = mfma16(fv[1][s], fu[1][s], a1); SB(); }
    *(LAS unsigned*)ktw[0] = pack2(a0[0] + bf_lo(dsk), a1[0] + bf_hi(dsk));
#pragma unroll
    for (int r = 1; r < 4; ++r) *(LAS unsigned*)ktw[r] = pack2(a0[r], a1[r]);
  }
  SB();
  S0LOADS();
  SB();
  BAR();
  {
    unsigned k[11];
#pragma unroll
    for (int i = 0; i < 11; ++i) k[i] = *(LAS unsigned*)koff[i];
    SB();
    if (w < 2) *(LAS v4u*)zp = v4u{0u, 0u, 0u, 0u};
#pragma unroll
    for (int c = 0; c < 2; ++c) {
      unsigned e[5], o[5];
#pragma unroll
      for (int i = 0; i < 5; ++i) {
        o[i] = __builtin_amdgcn_perm(k[2 * i + 1], k[2 * i], c ? 0x07060302u : 0x05040100u);
        e[i] = __builtin_amdgcn_perm(k[2 * i + 2], k[2 * i + 1], c ? 0x07060302u : 0x05040100u);
      }
      LAS unsigned char* d = L + L_KC + c * 4 * KC_STRIDE + 32 + tid * 16;
      *(LAS v4u*)(d) = v4u{e[1], e[2], e[3], e[4]};
      *(LAS v4u*)(d + KC_STRIDE) = v4u{o[1], o[2], o[3], o[4]};
      *(LAS v4u*)(d + 2 * KC_STRIDE) = v4u{e[0], e[1], e[2], e[3]};
      *(LAS v4u*)(d + 3 * KC_STRIDE) = v4u{o[0], o[1], o[2], o[3]};
    }
  }
  SB();
  STAMP(4);
  {
    unsigned bad = tagbad(tagbad(0u, gm, tagA, tagB), gn, tagA, tagB);
#pragma unroll
    for (int i = 0; i < 4; ++i) bad = tagbad(tagbad(bad, gx[i], tagA, tagB), gs[i], tagA, tagB);
    if (__builtin_expect(__any(bad != 0u), 0)) {
      for (unsigned spin = 0; spin < SPIN_CAP; ++spin) {
        FRESH();
        if (spin > 1) PAUSE();
        COUNT(20);
        bool again = false;
#pragma unroll
        for (int i = 0; i < 4; ++i) {
          if (__any(tagbad(0u, gx[i], tagA, tagB) != 0u)) { gx[i] = ldg(rs, tid * 16, xgo + i * 4096); again = true; }
          if (__any(tagbad(0u, gs[i], tagA, tagB) != 0u)) { gs[i] = ldg(rs, tid * 16, OFF_SG + i * 4096); again = true; }
        }
        if (__any(tagbad(tagbad(0u, gm, tagA, tagB), gn, tagA, tagB) != 0u)) { gm = ldg(rs, gmo, 0); gn = ldg(rs, gmo2, 0); again = true; }
        if (!again) break;
      }
    }
  }
  asm volatile("" :: "v"(wt), "v"(warm));
  WAITV(0);
  STAMP(5);
  W1DMA2(12, lrw + 12288);
  float zsc;
  v4u mtp;
  {
    const int i0 = (int)pack2(row_sum(u2f(gm.x)), row_sum(u2f(gm.z))), i1 = (int)pack2(row_sum(u2f(gn.x)), row_sum(u2f(gn.z)));
    unsigned md[6];
#pragma unroll
    for (int k = 0; k < 6; ++k) md[k] = (unsigned)__builtin_amdgcn_readlane(k < 4 ? i0 : i1, 16 * (k & 3));
#define MOD(k, c) ((c) ? bf_hi(md[k]) : bf_lo(md[k]))
    float s0 = rbf(1.0f + MOD(4, 0)), s1 = rbf(1.0f + MOD(4, 1));
    s0 = s0 == 0.f ? 0x1p-30f : s0;
    s1 = s1 == 0.f ? 0x1p-30f : s1;
    const float G0 = MOD(2, 0) * s0, G1 = MOD(2, 1) * s1;
    const float A0 = rbf(1.0f + MOD(1, 0)) * G0, A1 = rbf(1.0f + MOD(1, 1)) * G1, B0 = MOD(0, 0) * G0, B1 = MOD(0, 1) * G1;
    zsc = w >> 1 ? s1 : s0;
    mtp = v4u{lane ? md[5] : pack2(s0, s1), tagA, md[3], tagB};
#undef MOD
#pragma unroll
    for (int i = 0; i < 4; ++i) {
      const float mA = bf_lo(gs[i].x), rA = __builtin_amdgcn_rcpf(bf_hi(gs[i].x)), mB = bf_lo(gs[i].z), rB = __builtin_amdgcn_rcpf(bf_hi(gs[i].z));
      const float n00 = (bf_lo(gx[i].x) - mA) * rA, n01 = (bf_hi(gx[i].x) - mA) * rA, n10 = (bf_lo(gx[i].z) - mB) * rB, n11 = (bf_hi(gx[i].z) - mB) * rB;
      const int o = (tid + 256 * i) * 4;
      W1DMA2(14 + 2 * i, lrw + (14 + 2 * i) * 1024);
      *(LAS unsigned*)(L + L_H + 480 + o) = pack2(__builtin_fmaf(n00, A0, B0), __builtin_fmaf(n10, A0, B0));
      *(LAS unsigned*)(L + L_H + H_STRIDE + 480 + o) = pack2(__builtin_fmaf(n01, A1, B1), __builtin_fmaf(n11, A1, B1));
      *(LAS unsigned*)(xbw + i * 512) = __builtin_amdgcn_perm(gx[i].z, gx[i].x, 0x05040100u);
      *(LAS unsigned*)(xbw + 4096 + i * 512) = __builtin_amdgcn_perm(gx[i].z, gx[i].x, 0x07060302u);
    }
  }
  BAR();
  const unsigned tb = now();
  STAMP(6);

  v4u w1f[32], w2f[28][2], mt0, mt1;
  {
    const bool rh = w & 1;
    constexpr ConvOrder O = conv_order(), OL = conv_order_last();
    v2u A[4][8][2], xk[4];
    v4u B[4][8];
    f32x4 acc[8], keep[4];
#pragma unroll
    for (int u = 0; u < 8; ++u) {
      A[0][u][0] = trd(ka + 512 * u + 8);
      A[0][u][1] = trd(ka + 512 * u);
      B[0][7 - u] = lds128(hb + 64 * (8 * (7 - u) + 3));
    }
    SB();
    if (lane) mtp.z = tb;
    stg(rs, tid < 2 ? tid * 16 : OOB, OFF_MT + id * 128, mtp);
    SB();
    auto hand_over = [&](auto Pc) __attribute__((always_inline)) {
      constexpr int i = Pc.v;
      PIN(acc[2 * i]); PIN(acc[2 * i + 1]);
      const f32x4 ev = acc[2 * i], od = acc[2 * i + 1];
      const f32x4 snd = rh ? ev : od, own = rh ? od : ev;
      *(LAS f32x4*)(i < 2 ? exa + i * 1024 : exb + (i - 1) * 1024) = snd;
#pragma unroll
      for (int c = 0; c < 4; ++c) { float t = keep[i][c] + own[c]; PIN(t); keep[i][c] = t; }
    };
    static_for<0, 144>([&](auto Kc) __attribute__((always_inline)) {
      constexpr int K = Kc.v, r = K / 36, k = K % 36, t = r < 3 ? O.t[k] : OL.t[k], u = r < 3 ? O.u[k] : OL.u[k];
      if constexpr (k == 0 && r > 0) asm volatile("" :: "v"(B[r][7]));
      mfma16_v<r == 0 && u == 0>(acc[t], v4u{A[r][u][0].x, A[r][u][0].y, A[r][u][1].x, A[r][u][1].y}, B[r][t - u]);
      SB();
      if constexpr (K < 12) w1f[K] = trd2(w1a + K * 1024, w1a + K * 1024 + 256);
      if constexpr (K == 60) WAITV(9);
      if constexpr (K >= 60 && K < 70) w1f[K - 48] = trd2(w1a + (K - 48) * 1024, w1a + (K - 48) * 1024 + 256);
      if constexpr (K == 76) asm volatile("" :: "a"(w1f[21]));
      if constexpr (r == 2 && k < 4) xk[k] = *(LAS v2u*)(xbr + k * 512);
      constexpr int n = k - (r == 0 ? 9 : r == 1 ? 0 : 4);
      if constexpr (r < 3 && n >= 0 && n < 24) {
        if constexpr (n % 3 == 2) B[r + 1][n / 3] = lds128(hb + 64 * (8 * (n / 3) - r + 2));
        else A[r + 1][n / 3][n % 3] = trd(ka + 512 * (n / 3) + 64 * (r + 1) + 8 - 8 * (n % 3));
      }
      if constexpr (K >= 100 && K < 116) {
        constexpr int e = K - 100;
        const unsigned xw = (e & 2) ? xk[e >> 2].y : xk[e >> 2].x;
        float t2_ = zsc * ((e & 1) ? bf_hi(xw) : bf_lo(xw));
        PIN(t2_);
        keep[e >> 2][e & 3] = t2_;
      }
      if constexpr (K == 108 + 8) hand_over(IC<0>{});
      if constexpr (K == 108 + 17) hand_over(IC<1>{});
      if constexpr (K == 108 + 30) hand_over(IC<2>{});
      if constexpr (K == 14) W1DMA(22, L_RX + w * 3072);
      if constexpr (K == 22) W1DMA(23, L_TOP + w * 1024);
      if constexpr (K >= 37 && K < 69 && K % 8 == 5) W1DMA2(24 + (K - 37) / 4, lrw + (K - 37) / 4 * 1024);
      if constexpr (K >= 69 && K < 125 && K % 8 == 5) W2DMA((K - 69) / 8, lrw + 8192 + (K - 69) / 8 * 2048);
      if constexpr (K == MTK)
        asm volatile("buffer_load_dwordx4 %0, %2, %3, 0 offen sc1\n\tbuffer_load_dwordx4 %1, %2, %3, %4 offen sc1"
                     : "=&v"(mt0), "=&v"(mt1) : "v"(mto), "s"(rs), "s"(16384) : "memory");
      SB();
    });
    STAMP(7);
    asm volatile("s_nop 7" : "+v"(acc[6]), "+v"(acc[7]));
    hand_over(IC<3>{});
    BAR();
#pragma unroll
    for (int i = 0; i < 4; ++i) keep[i] += *(LAS f32x4*)(i < 2 ? eya + i * 1024 : eyb + (i - 1) * 1024);
#pragma unroll
    for (int i = 0; i < 2; ++i) {
      const auto s0 = __builtin_amdgcn_permlane16_swap(pack2(keep[2 * i][0], keep[2 * i][1]), pack2(keep[2 * i + 1][0], keep[2 * i + 1][1]), false, false);
      const auto s1 = __builtin_amdgcn_permlane16_swap(pack2(keep[2 * i][2], keep[2 * i][3]), pack2(keep[2 * i + 1][2], keep[2 * i + 1][3]), false, false);
      stg(rs, i ? zb : za, zs, v4u{guard(s0[0]), guard(s1[0]), guard(s0[1]), guard(s1[1])});
    }
    SB();
  }
  STAMP(8);

  static_assert(OFF_MT == 0, "table offset");
  const unsigned te = now();
  asm volatile("s_waitcnt vmcnt(2)" : "+v"(mt0), "+v"(mt1) :: "memory");
  if (__builtin_expect(__any(tagbad(tagbad(0u, mt0, tagA, tagB), mt1, tagA, tagB) != 0u), 0)) {
    for (unsigned spin = 0; spin < SPIN_CAP; ++spin) {
      FRESH();
      COUNT(21);
      if (tagbad(0u, mt0, tagA, tagB)) mt0 = ldg(rs, mto, OFF_MT);
      if (tagbad(0u, mt1, tagA, tagB)) mt1 = ldg(rs, mto, OFF_MT + 16384);
      if (!__any(tagbad(tagbad(0u, mt0, tagA, tagB), mt1, tagA, tagB) != 0u)) break;
      PAUSE();
    }
  }
  *(LAS unsigned*)(mtw) = mt0.x;
  *(LAS unsigned*)(mtw + 512) = mt1.x;
  *(LAS unsigned*)(mtw + 1024) = mt0.z;
  *(LAS unsigned*)(mtw + 1536) = mt1.z;
  {
    int late = (lane & 1) ? max((int)(mt0.z - tb), (int)(mt1.z - tb)) : 0;
    late = max(late, __builtin_amdgcn_update_dpp(late, late, 0xB1, 0xF, 0xF, false));
    late = max(late, __builtin_amdgcn_update_dpp(late, late, 0x4E, 0xF, 0xF, false));
    late = max(late, __builtin_amdgcn_update_dpp(late, late, 0x141, 0xF, 0xF, false));
    late = max(late, __builtin_amdgcn_update_dpp(late, late, 0x140, 0xF, 0xF, false));
    late = max(late, __builtin_amdgcn_update_dpp(late, late, 0x142, 0xA, 0xF, false));
    late = max(late, __builtin_amdgcn_update_dpp(late, late, 0x143, 0xC, 0xF, false));
    if (lane == 63) *(LAS int*)(L + L_ETA + w * 4) = late;
  }
  w1f[22] = trd2(L + L_RX + w * 3072 + tra, L + L_RX + w * 3072 + tra + 256);
  w1f[23] = trd2(L + L_TOP + w * 1024 + tra, L + L_TOP + w * 1024 + tra + 256);
  BAR();
  STAMP(9);
  const int w2t = (4 * hf + q4) * 128 + blk * 32 + (((p4 & 1) << 1) | (p4 >> 1)) * 8;
  LAS unsigned char* w2a = L + w2t + (q4 >> 1) * 64;
  LAS unsigned char* w2b = L + w2t + ((q4 >> 1) ^ 1) * 64;
  const int par = (0x1C >> (ct % 7)) & 1;
  const unsigned char* xg = setp + OFF_Z + rp * 131072;
  const unsigned xl = L_XR + w * 8192;
#define XSRC(p) (xg + w * 8192 + ((p) ^ par) * 32768)
  const int slo = OFF_Z + (rp * 4 + w) * 32768 + si * 2048;
  f32x4 uu[4], vv[4];
  v4u sl0, sl1;
  {
    LAS unsigned char* qr = L + ((lane & 31) == 0 ? L_TS : L_TB) + hf * 16;
    LAS unsigned char *pa = w2a + lrw + 8192, *pb = w2b + lrw + 8192;
    PIN(pa); PIN(pb);
    f32x16 uv, uv2;
    v4u qa[32];
    int wait = 0;
    const v4u lt = lds128(L + L_ETA);
    bool sent = false;
    asm volatile("" : "=v"(sl0), "=v"(sl1));
#pragma unroll
    for (int i = 0; i < 4; ++i) qa[i] = lds128(qr + i * 32);
    static_for<0, 32>([&](auto Sc) __attribute__((always_inline)) {
      constexpr int s = Sc.v;
      mfma_va<(s < 2)>((s & 1) ? uv2 : uv, qa[s], w1f[s]);
      SB();
      if constexpr (s < 28) qa[s + 4] = lds128(qr + (s + 4) * 32);
      if constexpr (s > 0) asm volatile("" :: "v"(qa[s > 0 ? s - 1 : 0]));
      if constexpr (s < 8) w1f[24 + s] = trd2(w1a + s * 1024, w1a + s * 1024 + 256);
      if constexpr (s == 4) { wait = min(__builtin_amdgcn_readfirstlane(max(max((int)lt.x, (int)lt.y), max((int)lt.z, (int)lt.w))), 300) + ZLAG; }
      if constexpr (s >= 8 && s < 22) w2f[(s - 8) >> 1][s & 1] = trd2(((s & 1) ? pb : pa) + ((s - 8) >> 1) * 2048, ((s & 1) ? pb : pa) + ((s - 8) >> 1) * 2048 + 1024);
      if constexpr (s == 4 || s == 13 || s == 22 || s == 31) {
        if (!sent) {
          if constexpr (s == 31) { while ((int)(now() - te) < wait) { } }
          if (s == 31 || (int)(now() - te) >= wait) {
            sent = true;
            STAMPB(26);
            asm volatile("buffer_load_dwordx4 %0, %2, %3, %4 offen sc1\n\tbuffer_load_dwordx4 %1, %2, %3, %4 offen offset:1024 sc1"
                         :: "v"(sl0), "v"(sl1), "v"(l16), "s"(rs), "s"(slo) : "memory");
            dma_x4(XSRC(0), l16, xl);
            dma_x4(XSRC(0) + 4096, l16, xl + 4096);
          }
        }
      }
      SB();
    });
    dma_x4(XSRC(1), l16, xl + 32768);
    dma_x4(XSRC(1) + 4096, l16, xl + 32768 + 4096);
    STAMPB(27);
    BAR();
    mfma_done(uv2);
    PIN(uv);
    LAS unsigned char* uw = L + L_UVT + (32 * w + (lane & 31)) * 4;
    if (lane < 32) { *(LAS float*)(uw) = uv[0] + uv2[0]; *(LAS float*)(uw + 512) = -LOG2E * (uv[1] + uv2[1]); }
    LAS unsigned char* uvp = L + L_UVT + (32 * w + 4 * hf) * 4;
#pragma unroll
    for (int i = 0; i < 4; ++i) { uu[i] = *(LAS f32x4*)(uvp + i * 32); vv[i] = *(LAS f32x4*)(uvp + 512 + i * 32); }
  }
  {
    const float i0 = __builtin_amdgcn_rcpf(bf_lo(*(LAS u16*)(L + L_TS + (si * 32 + (lane >> 2)) * 2)));
    const float i1 = __builtin_amdgcn_rcpf(bf_lo(*(LAS u16*)(L + L_TS + (si * 32 + 16 + (lane >> 2)) * 2)));
    SB();
    asm volatile("s_waitcnt vmcnt(16)" : "+v"(sl0), "+v"(sl1) :: "memory");
    STAMPB(28);
    if (__builtin_expect(__any(max4(max4(0u, sl0), sl1) == POISON), 0)) {
      for (unsigned spin = 0; spin < SPIN_CAP; ++spin) {
        FRESH();
        if (spin > 1) PAUSE();
        COUNT(22);
        sl0 = ldg(rs, l16, slo);
        sl1 = ldg(rs, l16 + 1024, slo);
        if (!__any(max4(max4(0u, sl0), sl1) == POISON)) break;
      }
    }
    float sm[8], sq[8];
#pragma unroll
    for (int i = 0; i < 4; ++i) {
      const f32x2 a = f32x2{bf_lo(sl0[i]), bf_hi(sl0[i])} * f32x2{i0, i0}, c = f32x2{bf_lo(sl1[i]), bf_hi(sl1[i])} * f32x2{i1, i1};
      const f32x2 s2 = a + c, q2 = __builtin_elementwise_fma(a, a, c * c);
      sm[2 * i] = s2.x; sm[2 * i + 1] = s2.y; sq[2 * i] = q2.x; sq[2 * i + 1] = q2.y;
    }
#pragma unroll
    for (int i = 0; i < 4; ++i) { swap_add32(sm[i], sm[i + 4]); swap_add32(sq[i], sq[i + 4]); }
#pragma unroll
    for (int i = 0; i < 2; ++i) { swap_add16(sm[i], sm[i + 2]); swap_add16(sq[i], sq[i + 2]); }
#pragma unroll
    for (int i = 0; i < 2; ++i) { sm[i] = dpp_add<0x124>(dpp_add<0x128>(sm[i])); sq[i] = dpp_add<0x124>(dpp_add<0x128>(sq[i])); }
    const bool od = lane & 4;
    stg(rs, (lane & 8) ? OOB : (p4 * 8 + hf * 4 + blk * 2 + (od ? 1 : 0)) * 16, OFF_SX + ((rp * 4 + w) * 16 + si) * 512,
        v4u{f2u(od ? sm[1] : sm[0]), tagA, f2u(od ? sq[1] : sq[0]), tagB});
    SB();
  }
  STAMPB(29);
  dma_x4(XSRC(2), l16, xl + 65536);
  dma_x4(XSRC(2) + 4096, l16, xl + 65536 + 4096);
  WAITV(17);
  STAMP(10);
  {
    v4u t[8];
    unsigned m = 0, m2 = 0;
#pragma unroll
    for (int i = 0; i < 8; ++i) t[i] = lds128(L + xl + l16 + i * 1024);
#pragma unroll
    for (int i = 0; i < 8; i += 2) { m = max4(m, t[i]); m2 = max4(m2, t[i + 1]); }
    if (__builtin_expect(__any(max(m, m2) == POISON), 0)) {
      for (unsigned spin = 0; spin < SPIN_CAP; ++spin) {
        COUNT(22);
        bool again = false;
#pragma nounroll
        for (int i = 0; i < 8; ++i)
          if (__any(max4(0u, lds128(L + xl + l16 + i * 1024)) == POISON)) {
            again = true;
            dma_x1(XSRC(0) + i * 1024, l16, xl + i * 1024);
            dma_x1(XSRC(1) + i * 1024, l16, xl + 32768 + i * 1024);
            dma_x1(XSRC(2) + i * 1024, l16, xl + 65536 + i * 1024);
          }
        if (!again) break;
        WAITV(0);
        if (spin) PAUSE();
      }
    }
  }
  BAR();
  STAMP(11);

  constexpr int HW = 40;
  v4u hq[HW], res[2], sx[8];
  const int c0 = 7 * w + rot, c3 = rot == 4 ? c0 - 7 : c0, c4 = rot ? c0 - 7 : c0, ckv = lane < 8 ? l16 : OOB;
  const int hb0 = OFF_HID + (rp * 4 + rg) * 114688, kb0 = OFF_CK + (rp * 4 + rg) * 4096;
#define CHUNK(n) (((n) % 35 / 5 < 3 ? c0 : (n) % 35 / 5 == 3 ? c3 : c4) + (n) % 35 / 5)
#define HLOAD(n) hq[(n) % HW] = (n) % 5 == 4 ? ldg(rs, ckv, kb0 + CHUNK(n) * 128 + ((n) / 35) * 8192) \
    : ldg(rs, l16 + ((n) % 5) * 1024, hb0 + CHUNK(n) * 4096 + ((n) / 35) * (2 * 114688))
  const int hoff = OFF_HID + (rp * 4 * 112 + ct * 8 + 2 * w) * 1024, hv0 = dt == 7 ? OOB : l16, hv1 = dt == 7 ? OOB : l16 + 1024;
  const int cko = OFF_CK + rp * 16384 + (ct * 4 + w) * 64, ckw = (p4 || dt == 7) ? OOB : lane;
  const unsigned ckey = lane ? 0u : tagA;
#define CKPUT(sum, u) __builtin_amdgcn_raw_buffer_store_b32(ckey - red_step<1>(red_step<0>(sum)), rs, ckw, cko + ((u) ^ par) * 4096, SC1)
  const int sxo = OFF_SX + (rp * 4 + w) * 8192;
  LAS unsigned char* xr[2];
#pragma unroll
  for (int i = 0; i < 2; ++i) { xr[i] = L + L_XR + tra + i * 65536; PIN(xr[i]); }
  LAS unsigned char* rr = L + L_XR + tra + (rg ^ par) * 32768 + (4 * dt + w) * 1024;
  LAS unsigned char* str = L + L_STAT + (lane & 31) * 8;
  PIN(str);
#define XA(t) (xr[(t) >> 6] + ((t) & 63) * 1024)
#define XF(t) trd2(XA(t), XA(t) + 256)
#define XEN
#define REDO(p) \
  if (XEN __builtin_expect(__any(!(acc[(p) % 3][0] == acc[(p) % 3][0])), 0)) { \
    for (unsigned spin = 0; spin < SPIN_CAP / 128; ++spin) { \
      COUNT(23); \
      _Pragma("nounroll") for (int i = 0; i < 8; ++i) dma_x4(xg + ((p) ^ par) * 32768 + i * 4096, l16, L_XR + (p) * 32768 + i * 4096); \
      WAITV(0); \
      _Pragma("unroll") for (int s = 0; s < 32; ++s) { \
        const v4u t = XF(32 * (p) + s); \
        if (s == 0) mfma_av<true>(acc[(p) % 3], w1f[s], t); else mfma_av<false>(acc[(p) % 3], w1f[s], t); \
        SB(); \
      } \
      mfma_done(acc[(p) % 3]); \
      if (!__any(!(acc[(p) % 3][0] == acc[(p) % 3][0]))) break; \
    } \
  }
  f32x16 acc[3];
  f32x2 ec[4];
  float ep[48], ee[48], ey[48];
  unsigned cs = 0;
  v4u x[128], eo;
#pragma unroll
  for (int t = 0; t < 4; ++t) x[t] = XF(t);
  SB();
  static_for<0, 64>([&](auto Jc) __attribute__((always_inline)) {
    constexpr int J = Jc.v, p = J >> 4, j = J & 15;
    asm volatile("" :: "v"(x[2 * J + 1]));
    mfma_av<j == 0>(acc[p % 3], w1f[2 * j], x[2 * J]);
    if constexpr (J < 62) { x[2 * J + 4] = XF(2 * J + 4); x[2 * J + 5] = XF(2 * J + 5); }
    SB();
    mfma_av<false>(acc[p % 3], w1f[2 * j + 1], x[2 * J + 1]);
    SB();
    if constexpr (J == 10) { WAITV(16); __builtin_amdgcn_s_barrier(); FRESH(); }
    if constexpr (J == EO - 2) {
      unsigned bad = 0;
#pragma unroll
      for (int i = 0; i < 8; ++i) bad = tagbad(bad, sx[i], tagA, tagB);
      if (__builtin_expect(__any(bad != 0u), 0)) {
        for (unsigned spin = 0; spin < SPIN_CAP; ++spin) {
          FRESH();
          if (spin > 1) PAUSE();
          COUNT(23);
          bad = 0;
#pragma unroll
          for (int i = 0; i < 8; ++i) sx[i] = ldg(rs, l16 + (i & 3) * 1024, sxo + (i >> 2) * 4096);
#pragma unroll
          for (int i = 0; i < 8; ++i) bad = tagbad(bad, sx[i], tagA, tagB);
          if (!__any(bad != 0u)) break;
        }
      }
      float sm = u2f(sx[0].x), sq = u2f(sx[0].z);
#pragma unroll
      for (int i = 1; i < 8; ++i) { sm += u2f(sx[i].x); PIN(sm); sq += u2f(sx[i].z); PIN(sq); }
      const float mean = swap32_add(sm) * (1.0f / 512.0f);
      const float ms = __builtin_fmaf(swap32_add(sq), 1.0f / 512.0f, 1e-5f);
      const float a = -LOG2E * __builtin_amdgcn_rsqf(fmaxf(__builtin_fmaf(-mean, mean, ms), 1e-5f));
      *(LAS f32x2*)(str + (w ^ par) * 256) = f32x2{a, -a * mean};
      SB();
    }
    if constexpr (J == EO - 1) {
      asm volatile("s_waitcnt lgkmcnt(4)" ::: "memory");
      __builtin_amdgcn_s_barrier();
      FRESH();
#pragma unroll
      for (int i = 0; i < 4; ++i) ec[i] = *(LAS f32x2*)(str + i * 256);
      SB();
    }
    static_for<2 * J, 2 * J + 2>([&](auto Sc) __attribute__((always_inline)) {
      static_for<0, 48>([&](auto Vc) __attribute__((always_inline)) {
        constexpr int S = Sc.v, V = Vc.v, k = V & 15;
        if constexpr (S == epi_slot(V) + 2 * epi_dist(V)) {
          ey[V] = ep[V] * ee[V];
          if constexpr (k & 1) { unsigned o = pack2(ey[V - 1], ey[V]); PIN(o); eo[(k >> 1) & 3] = o; } else PIN(ey[V]);
          if constexpr (k == 7) { stg(rs, hv0, hoff + ((V >> 4) ^ par) * 114688, eo); cs = sum4(0u, eo); PIN(cs); }
          if constexpr (k == 15) { stg(rs, hv1, hoff + ((V >> 4) ^ par) * 114688, eo); CKPUT(sum4(cs, eo), V >> 4); }
        }
        if constexpr (S == epi_slot(V) + epi_dist(V)) { ee[V] = __builtin_amdgcn_rcpf(1.0f + ee[V]); PIN(ee[V]); }
        if constexpr (S == epi_slot(V)) {
          ep[V] = __builtin_fmaf(ec[V >> 4].x, acc[(V >> 4) % 3][k], __builtin_fmaf(ec[V >> 4].y, uu[k >> 2][k & 3], vv[k >> 2][k & 3]));
          ee[V] = __builtin_amdgcn_exp2f(ep[V]);
          PIN(ee[V]);
        }
      });
      SB();
    });
    if constexpr (j == 1 && p >= 1) {
      if constexpr (p == 2) REDO(1)
      if constexpr (p == 3) REDO(2)
      if (((p - 1) & 1) == (rg ^ par)) res[(p - 1) >> 1] = trd2(rr + ((p - 1) >> 1) * 65536, rr + ((p - 1) >> 1) * 65536 + 256);
    }
    if constexpr (j == 2 && p >= 2) {
      asm volatile("s_waitcnt lgkmcnt(4)" ::: "memory");
      __builtin_amdgcn_s_barrier();
      FRESH();
    }
    if constexpr (J < 8) {
      if constexpr ((J & 3) == 0) set_m0(xl + 98304 + J * 1024);
      dma_xn<(J & 3) * 1024>(XSRC(3) + (J >> 2) * 4096, l16);
    }
    if constexpr (J >= SXL && J < SXL + 8) sx[J - SXL] = ldg(rs, l16 + ((J - SXL) & 3) * 1024, sxo + ((J - SXL) >> 2) * 4096);
    if constexpr (J >= EO && J < EO + 22 && ((J - EO) & 1) == 0) {
      constexpr int n = (J - EO) >> 1;
      if constexpr (n < 3) W2DMA(7 + n, L_SP + w * 6144 + n * 2048);
      else W2DMA(7 + n, L_XR + ((n - 3) >> 2) * 32768 + w * 8192 + ((n - 3) & 3) * 2048);
    }
    if constexpr (J >= 48 && J < 56) { HLOAD(2 * (J - 48)); if constexpr (J < 55) HLOAD(2 * (J - 48) + 1); }
    if constexpr (J >= 56 && J < 64 && (J & 1) == 0) W2DMA(18 + ((J - 56) >> 1), L_XR + 65536 + w * 8192 + ((J - 56) >> 1) * 2048);
    SB();
    if constexpr (J == 15) STAMP(16);
    if constexpr (J == 31) STAMP(17);
    if constexpr (J == 47) STAMP(18);
  });
  STAMP(12);
  static_assert(EO + 6 >= 26 && EO + 14 > 34 && EO + 20 < 48 && epi_slot(16) >= 2 * 34 && epi_slot(31) < epi_slot(32) && epi_slot(47) + 2 < 128, "schedule");
  static_for<15, 25>([&](auto Ic) __attribute__((always_inline)) { HLOAD(Ic.v); });
  SB();
  mfma_done(acc[0]);
  REDO(3)
  if (rg ^ par) res[1] = trd2(rr + 65536, rr + 65536 + 256);
#pragma unroll
  for (int h = 0; h < 2; ++h) {
    v4u o;
#pragma unroll
    for (int i = 0; i < 4; ++i) {
      const int k = 8 * h + 2 * i;
      const f32x2 t = __builtin_elementwise_fma(f32x2{ec[3].y, ec[3].y}, f32x2{uu[k >> 2][k & 3], uu[k >> 2][(k & 3) + 1]}, f32x2{vv[k >> 2][k & 3], vv[k >> 2][(k & 3) + 1]});
      const f32x2 pp = __builtin_elementwise_fma(f32x2{ec[3].x, ec[3].x}, f32x2{acc[0][k], acc[0][k + 1]}, t);
      const f32x2 d = f32x2{__builtin_amdgcn_exp2f(pp.x), __builtin_amdgcn_exp2f(pp.y)} + f32x2{1.0f, 1.0f};
      const f32x2 y = pp * f32x2{__builtin_amdgcn_rcpf(d.x), __builtin_amdgcn_rcpf(d.y)};
      o[i] = pack2(y.x, y.y);
    }
    stg(rs, h ? hv1 : hv0, hoff + (3 ^ par) * 114688, o);
    if (h) CKPUT(sum4(cs, o), 3); else cs = sum4(0u, o);
    SB();
  }
  BAR();
  STAMP(13);

  f32x16 d[2][2];
  LAS unsigned char* redw = L + L_XR + w * 8192 + l16;
  LAS unsigned char* redr = L + L_XR + w * 2048 + l16;
#define REDW(un, q) *(LAS f32x4*)(redw + (un) * 32768 + (q) * 1024) = \
    f32x4{d[un][(q) >> 2][4 * ((q) & 3)], d[un][(q) >> 2][4 * ((q) & 3) + 1], d[un][(q) >> 2][4 * ((q) & 3) + 2], d[un][(q) >> 2][4 * ((q) & 3) + 3]}
  LAS unsigned char *w2sa = w2a + L_SP + w * 6144, *w2sb = w2b + L_SP + w * 6144;
  w2a += L_XR + w * 8192; w2b += L_XR + w * 8192;
  LAS unsigned char *w2ua = w2a + 65536, *w2ub = w2b + 65536;
  PIN(w2a); PIN(w2b); PIN(w2sa); PIN(w2sb); PIN(w2ua); PIN(w2ub);
#define PULL(step, pa, pb, off) do { w2f[step][0] = trd2((pa) + (off), (pa) + (off) + 1024); w2f[step][1] = trd2((pb) + (off), (pb) + (off) + 1024); } while (0)
  unsigned m = red_all(sum4(sum8(sum8(0u, hq[0], hq[1]), hq[2], hq[3]), hq[4]));
  v4u gbv, scv;
  f32x4 pr[8];
  float gb[8], rx[2][8];
  unsigned orow = (rp * 128 + ((lane & 31) >> 3) * 16 + rg * 8 + (lane & 7)) * 1024 + dt * 128 + w * 32 + hf * 16;
  static_for<0, 14>([&](auto Gc) __attribute__((always_inline)) {
    constexpr int G = Gc.v, un = G / 7, gq = G % 7;
    if (__builtin_expect((unsigned)__builtin_amdgcn_readlane((int)m, 63) != 2u * tagA, 0)) {
      for (unsigned spin = 0; spin < SPIN_CAP; ++spin) {
        FRESH();
        COUNT(24 + un);
        static_for<0, 5>([&](auto Ic) __attribute__((always_inline)) { HLOAD(5 * G + Ic.v); });
        m = sum4(sum8(sum8(0u, hq[(5 * G) % HW], hq[(5 * G + 1) % HW]), hq[(5 * G + 2) % HW], hq[(5 * G + 3) % HW]), hq[(5 * G + 4) % HW]);
        if ((unsigned)__builtin_amdgcn_readlane((int)red_all(m), 63) == 2u * tagA) break;
        PAUSE();
      }
    }
    static_for<0, 8>([&](auto Ic) __attribute__((always_inline)) {
      constexpr int c = Ic.v, i = G < 13 ? c >> 1 : c & 3, e = G < 13 ? c & 1 : c >> 2, I = 4 * G + i, n = 5 * G + i, X = 8 * G + c;
      mfma_av<gq == 0 && i == 0>(d[un][e], w2f[gq * 4 + i][e], hq[n % HW]);
      SB();
      if constexpr (G < 13 && e == 0) {
        if constexpr (I < 3) PULL(7 + I, w2sa, w2sb, I * 2048);
        if constexpr (I >= 3 && I < 11) PULL(7 + I, w2a, w2b, ((I - 3) >> 2) * 32768 + ((I - 3) & 3) * 2048);
        if constexpr (I >= 11 && I < 15) PULL(7 + I, w2ua, w2ub, (I - 11) * 2048);
        if constexpr (I == 18) waitv<4 + hl_upto(18) - 35>();
        if constexpr (I >= 18 && I < 22) PULL(4 + I, w2ua, w2ub, 32768 + (I - 18) * 2048);
        if constexpr (I == 23) waitv<hl_upto(23) - 35>();
        if constexpr (I == 23 || I == 24) PULL(3 + I, w2sa, w2sb, (I - 23) * 2048);
        if constexpr (I >= 29 && I < 37) REDW(0, I - 29);
      }
      if constexpr (G < 13 && e == 1) {
        if constexpr (I >= 4 && I < 8) W2DMA(18 + I, L_XR + 98304 + w * 8192 + (I - 4) * 2048);
        if constexpr (I == 8) { W2DMA(26, L_SP + w * 6144); W2DMA(27, L_SP + w * 6144 + 2048); }
        static_for<hl_upto(I), hl_upto(I + 1)>([&](auto Nc) __attribute__((always_inline)) { HLOAD(Nc.v); });
      }
      if constexpr (G < 13) {
        constexpr int q = 5 * G + 5;
        if constexpr (c == 1) m = sum8(0u, hq[q % HW], hq[(q + 1) % HW]);
        if constexpr (c == 3) m = sum4(sum8(m, hq[(q + 2) % HW], hq[(q + 3) % HW]), hq[(q + 4) % HW]);
        if constexpr (c == 4) m = red_step<0>(m);
        if constexpr (c == 5) m = red_step<2>(red_step<1>(m));
        if constexpr (c == 6) m = red_step<3>(m);
        if constexpr (c == 7) m = red_step<5>(red_step<4>(m));
        PIN(m);
      }
      if constexpr (X == 74) { gbv = lds128(L + L_TG + (64 * dt + 16 * w + 8 * hf) * 2); scv = lds128(L + L_TS + (64 * dt + 16 * w + 8 * hf) * 2); PIN(orow); }
      if constexpr (X >= 76 && X < 92 && (X & 1) == 0) {
        constexpr int q = (X - 76) >> 1;
        const float inv = __builtin_amdgcn_rcpf((q & 1) ? bf_hi(scv[q >> 1]) : bf_lo(scv[q >> 1]));
        gb[q] = -LN2 * ((q & 1) ? bf_hi(gbv[q >> 1]) : bf_lo(gbv[q >> 1]));
        rx[0][q] = inv * ((q & 1) ? bf_hi(res[0][q >> 1]) : bf_lo(res[0][q >> 1]));
        rx[1][q] = inv * ((q & 1) ? bf_hi(res[1][q >> 1]) : bf_lo(res[1][q >> 1]));
        PIN(gb[q]); PIN(rx[0][q]); PIN(rx[1][q]);
      }
      if constexpr (X == 92) BAR();
      if constexpr (X >= 93 && X < 97) { pr[2 * (X - 93)] = *(LAS f32x4*)(redr + (X - 93) * 8192); pr[2 * (X - 93) + 1] = *(LAS f32x4*)(redr + (X - 93) * 8192 + 1024); }
      if constexpr (X >= 98 && X < 106) {
        constexpr int q = X - 98;
        float t = (pr[q >> 2][q & 3] + pr[2 + (q >> 2)][q & 3]);
        PIN(t);
        float t2_ = (pr[4 + (q >> 2)][q & 3] + pr[6 + (q >> 2)][q & 3]);
        PIN(t2_);
        rx[0][q] = __builtin_fmaf(gb[q], t + t2_, rx[0][q]);
        PIN(rx[0][q]);
      }
      if constexpr (X == 106)
        *(GAS v4u*)((GAS unsigned char*)out + orow) = v4u{pack2(rx[0][0], rx[0][1]), pack2(rx[0][2], rx[0][3]), pack2(rx[0][4], rx[0][5]), pack2(rx[0][6], rx[0][7])};
      if constexpr (X == 109) asm volatile("s_nop 9" : "+v"(d[1][0]));
      if constexpr (X >= 109) REDW(1, X - 109);
      SB();
    });
    if constexpr (G == 6) STAMP(14);
  });
  STAMP(19);
  REDW(1, 3);
  mfma_done(d[1][1]);
#pragma unroll
  for (int q = 4; q < 8; ++q) REDW(1, q);
  BAR();
  {
    f32x4 s0 = {}, s1 = {};
#pragma unroll
    for (int k = 0; k < 4; ++k) { s0 += *(LAS f32x4*)(redr + k * 8192 + 32768); s1 += *(LAS f32x4*)(redr + k * 8192 + 32768 + 1024); }
    float y[8];
#pragma unroll
    for (int q = 0; q < 8; ++q) y[q] = __builtin_fmaf(gb[q], q < 4 ? s0[q & 3] : s1[q & 3], rx[1][q]);
    *(GAS v4u*)((GAS unsigned char*)out + orow + 65536) = v4u{pack2(y[0], y[1]), pack2(y[2], y[3]), pack2(y[4], y[5]), pack2(y[6], y[7])};
  }
  if (b == 0 && tid == 0) __hip_atomic_store((GAS unsigned*)ws, epoch + 1u, __ATOMIC_RELAXED, __HIP_MEMORY_SCOPE_AGENT);
  STAMP(15);
}

extern "C" void kernel_launch(void* const* d_in, const int* in_sizes, int n_in, void* d_out, int out_size, void* d_ws, size_t ws_size,
                              hipStream_t stream) {
  (void)in_sizes; (void)n_in; (void)out_size; (void)ws_size;
  (void)hipFuncSetAttribute((const void*)s4_block_kernel, hipFuncAttributeMaxDynamicSharedMemorySize, LDS_BYTES);
  s4_block_kernel<<<256, 256, LDS_BYTES, stream>>>(
      (const unsigned char*)d_in[0], (const u16*)d_in[1], (const u16*)d_in[2], (const u16*)d_in[3], (const u16*)d_in[4], (const u16*)d_in[5],
      (const u16*)d_in[6], (const u16*)d_in[7], (const u16*)d_in[8], (const unsigned char*)d_in[10], (const unsigned char*)d_in[11],
      (const u16*)d_in[12], (const unsigned char*)d_in[13], (unsigned char*)d_out, (unsigned char*)d_ws);
}
```

```cpp
#include <hip/hip_runtime.h>

#define GAS __attribute__((address_space(1)))
#define LAS __attribute__((address_space(3)))
#define DI __device__ __forceinline__
typedef unsigned v4u __attribute__((ext_vector_type(4)));
typedef unsigned v2u __attribute__((ext_vector_type(2)));
typedef float f32x2 __attribute__((ext_vector_type(2)));
typedef float f32x4 __attribute__((ext_vector_type(4)));
typedef float f32x16 __attribute__((ext_vector_type(16)));
typedef __bf16 bf16x2 __attribute__((ext_vector_type(2)));
typedef __bf16 bf16x8 __attribute__((ext_vector_type(8)));
typedef short s16x4 __attribute__((ext_vector_type(4)));
typedef unsigned short u16;
typedef unsigned long long u64;

constexpr int SET_BASE = 4096, SET_STRIDE = 16 << 20;
constexpr int OFF_MT = 0;
constexpr int OFF_SG = 32768;
constexpr int OFF_MG = 65536;
constexpr int OFF_SX = 524288;
constexpr int OFF_XG = 1 << 20;
constexpr int OFF_Z = OFF_XG + (4 << 20);
constexpr int OFF_HID = OFF_Z + 2097152;
constexpr int OFF_CK = OFF_HID + 7340032;
constexpr int SET_BYTES = OFF_CK + 262144;
static_assert(SET_BYTES <= SET_STRIDE && OFF_MG + 393216 <= OFF_SX, "set size");

constexpr int L_TS = 0, L_TB = 1024, L_TG = 2048;
constexpr int L_UVT = 3072;
constexpr int L_M = 4096;
constexpr int L_UVS = L_M;
constexpr int UVS_ROW = 544, UVS_CH = 57 * UVS_ROW;
constexpr int L_KC = L_M;
constexpr int KC_STRIDE = 4160;
constexpr int L_H = L_KC + 8 * KC_STRIDE;
constexpr int H_STRIDE = 4576;
constexpr int L_XB = L_H + 2 * H_STRIDE;
constexpr int L_RX = L_XB + 8192;
constexpr int L_R = L_M + 65536;
constexpr int R_WAVE = 22528;
constexpr int L_TOP = L_R + 4 * R_WAVE;
constexpr int L_KT = L_TOP + 16;
constexpr int L_STAT = L_TOP;
constexpr int L_ETA = L_RX + 12288;
constexpr int L_XR = L_M;
constexpr int L_SP = L_M + 131072;
constexpr int LDS_BYTES = 163840;
static_assert(L_TOP + 4096 <= LDS_BYTES && L_ETA + 16 <= L_R && L_UVS + 2 * UVS_CH <= L_R && L_SP + 24576 <= L_TOP, "LDS map");
static_assert(L_H % 16 == 0 && L_XB % 16 == 0 && L_RX % 16 == 0 && L_KT % 16 == 0, "alignment");

#define ZLAG 30
#define MTK 124
constexpr int SXL = 11, EO = 26;
constexpr int CODE_BYTES = 44032;
constexpr int SC1 = 16;
constexpr int OOB = 0x7FFFFF00;
constexpr unsigned SPIN_CAP = 1u << 15;
constexpr unsigned POISON = 0xFFFFFFFFu;
constexpr float LOG2E = 1.4426950408889634f, LN2 = 0.6931471805599453f, INV2PI = 0.15915494309189535f;

#define SB() __builtin_amdgcn_sched_barrier(0)
#define WAITV(n) asm volatile("s_waitcnt vmcnt(" #n ")" ::: "memory")
#define PIN(x) asm volatile("" : "+v"(x))
#define FRESH() asm volatile("" ::: "memory")
#define PAUSE() asm volatile("s_sleep 4" ::: "memory")
#define BAR() do { asm volatile("s_waitcnt lgkmcnt(0)" ::: "memory"); __builtin_amdgcn_s_barrier(); asm volatile("" ::: "memory"); } while (0)
#define STAMP(i)
#define SARG
#define SPASS
#define COUNT(i)

#define STAMPA(i)
#define STAMPB(i) STAMP(i)
DI float u2f(unsigned u) { return __builtin_bit_cast(float, u); }
DI unsigned f2u(float f) { return __builtin_bit_cast(unsigned, f); }
DI float dot2(unsigned u, float c) { const bf16x2 v = __builtin_bit_cast(bf16x2, u); return __builtin_amdgcn_fdot2_f32_bf16(v, v, c, false); }
DI float bf_lo(unsigned u) { return __builtin_bit_cast(float, u << 16); }
DI float bf_hi(unsigned u) { return __builtin_bit_cast(float, u & 0xFFFF0000u); }
DI unsigned pack2(float a, float b) { f32x2 v = {a, b}; return __builtin_bit_cast(unsigned, __builtin_convertvector(v, bf16x2)); }
DI float rbf(float a) { return bf_lo(pack2(a, a)); }
DI unsigned sel(bool c, unsigned a, unsigned b) { asm("" : "+v"(a), "+v"(b)); return c ? a : b; }
DI unsigned guard(unsigned u) { return u < 0xFFFFFFFEu ? u : 0xFFFFFFFEu; }
DI unsigned max4(unsigned m, v4u v) {
  asm("v_max3_u32 %0, %0, %1, %2\n\tv_max3_u32 %0, %0, %3, %4" : "+v"(m) : "v"(v.x), "v"(v.y), "v"(v.z), "v"(v.w));
  return m;
}
DI f32x4 mfma16(v4u a, v4u b, f32x4 c) { return __builtin_amdgcn_mfma_f32_16x16x32_bf16(__builtin_bit_cast(bf16x8, a), __builtin_bit_cast(bf16x8, b), c, 0, 0, 0); }
DI f32x16 mfma32(v4u a, v4u b, f32x16 c) { return __builtin_amdgcn_mfma_f32_32x32x16_bf16(__builtin_bit_cast(bf16x8, a), __builtin_bit_cast(bf16x8, b), c, 0, 0, 0); }
template <bool FIRST> DI void mfma_av(f32x16& acc, const v4u& a, const v4u& b) {
  if (FIRST) asm volatile("v_mfma_f32_32x32x16_bf16 %0, %1, %2, 0" : "=&v"(acc) : "a"(a), "v"(b));
  else asm volatile("v_mfma_f32_32x32x16_bf16 %0, %1, %2, %0" : "+v"(acc) : "a"(a), "v"(b));
}
template <bool FIRST> DI void mfma_va(f32x16& acc, const v4u& a, const v4u& b) {
  if (FIRST) asm volatile("v_mfma_f32_32x32x16_bf16 %0, %1, %2, 0" : "=&v"(acc) : "v"(a), "a"(b));
  else asm volatile("v_mfma_f32_32x32x16_bf16 %0, %1, %2, %0" : "+v"(acc) : "v"(a), "a"(b));
}
template <bool FIRST> DI void mfma16_v(f32x4& acc, const v4u& a, const v4u& b) {
  if (FIRST) asm volatile("v_mfma_f32_16x16x32_bf16 %0, %1, %2, 0" : "=&v"(acc) : "v"(a), "v"(b));
  else asm volatile("v_mfma_f32_16x16x32_bf16 %0, %1, %2, %0" : "+v"(acc) : "v"(a), "v"(b));
}
DI void mfma_done(f32x16& acc) { asm volatile("s_nop 15" : "+v"(acc)); }
DI v2u trd(LAS unsigned char* p) { return __builtin_bit_cast(v2u, __builtin_amdgcn_ds_read_tr16_b64_v4i16((LAS s16x4*)p)); }
DI v4u trd2(LAS unsigned char* p0, LAS unsigned char* p1) { const v2u a = trd(p0), b = trd(p1); return v4u{a.x, a.y, b.x, b.y}; }
DI v4u lds128(LAS unsigned char* p) { return *(LAS v4u*)p; }
DI v4u ldg(__amdgpu_buffer_rsrc_t r, int voff, int soff) { return __builtin_amdgcn_raw_buffer_load_b128(r, voff, soff, SC1); }
DI void stg(__amdgpu_buffer_rsrc_t r, int voff, int soff, v4u v) {
  __builtin_amdgcn_raw_buffer_store_b128(v, r, voff, soff, SC1);
  asm volatile("s_nop 1" :: "v"(v) : "memory");
}
template <int CTRL> DI float dpp_add(float v) {
  return v + __builtin_bit_cast(float, __builtin_amdgcn_update_dpp(0, __builtin_bit_cast(int, v), CTRL, 0xF, 0xF, true));
}
DI float swap16_add(float v) { const unsigned u = __builtin_bit_cast(unsigned, v); const auto r = __builtin_amdgcn_permlane16_swap(u, u, false, false);
  return u2f(r[0]) + u2f(r[1]); }
DI float swap32_add(float v) { const unsigned u = __builtin_bit_cast(unsigned, v); const auto r = __builtin_amdgcn_permlane32_swap(u, u, false, false);
  return u2f(r[0]) + u2f(r[1]); }
DI float quad_sum(float v) { v = dpp_add<0xB1>(v); return dpp_add<0x4E>(v); }
DI float row_sum(float v) { return dpp_add<0x140>(dpp_add<0x141>(quad_sum(v))); }
DI float wave_sum(float v) { return swap32_add(swap16_add(row_sum(v))); }
DI f32x2 cmul(f32x2 a, f32x2 e, f32x2 ep) { const f32x2 t = f32x2{a.x, a.x} * e; return __builtin_elementwise_fma(f32x2{a.y, a.y}, ep, t); }
DI f32x2 cexp(float re2, float rev) {
  const float m = __builtin_amdgcn_exp2f(re2), f = __builtin_amdgcn_fractf(rev);
  return f32x2{m * __builtin_amdgcn_cosf(f), m * __builtin_amdgcn_sinf(f)};
}
DI void dma_w2(const unsigned char* sbase, unsigned v0, unsigned v1, unsigned lds_dst) {
  asm volatile("s_mov_b32 m0, %3\n\ts_nop 0\n\tglobal_load_lds_dwordx4 %0, %2\n\tglobal_load_lds_dwordx4 %1, %2 offset:1024"
               :: "v"(v0), "v"(v1), "s"(sbase), "s"(lds_dst) : "memory");
}
DI void dma_w1(const unsigned char* sbase, unsigned v, unsigned lds_dst) {
  asm volatile("s_mov_b32 m0, %2\n\ts_nop 0\n\tglobal_load_lds_dwordx4 %0, %1" :: "v"(v), "s"(sbase), "s"(lds_dst) : "memory");
}
DI void dma_x1(const unsigned char* sbase, unsigned v, unsigned lds_dst) {
  asm volatile("s_mov_b32 m0, %2\n\ts_nop 0\n\tglobal_load_lds_dwordx4 %0, %1 sc1" :: "v"(v), "s"(sbase), "s"(lds_dst) : "memory");
}
DI void set_m0(unsigned lds_dst) { asm volatile("s_mov_b32 m0, %0\n\ts_nop 0" :: "s"(lds_dst) : "memory"); }
template <int OFF> DI void dma_xn(const unsigned char* sbase, unsigned v) {
  asm volatile("global_load_lds_dwordx4 %0, %1 offset:%2 sc1" :: "v"(v), "s"(sbase), "n"(OFF) : "memory");
}
DI void dma_x2(const unsigned char* sbase, unsigned v, unsigned lds_dst) {
  asm volatile("s_mov_b32 m0, %2\n\ts_nop 0\n\tglobal_load_lds_dwordx4 %0, %1 sc1\n\tglobal_load_lds_dwordx4 %0, %1 offset:1024 sc1"
               :: "v"(v), "s"(sbase), "s"(lds_dst) : "memory");
}
DI void dma_x4(const unsigned char* sbase, unsigned v, unsigned lds_dst) {
  asm volatile("s_mov_b32 m0, %2\n\ts_nop 0\n\tglobal_load_lds_dwordx4 %0, %1 sc1\n\tglobal_load_lds_dwordx4 %0, %1 offset:1024 sc1\n\t"
               "global_load_lds_dwordx4 %0, %1 offset:2048 sc1\n\tglobal_load_lds_dwordx4 %0, %1 offset:3072 sc1"
               :: "v"(v), "s"(sbase), "s"(lds_dst) : "memory");
}
template <int I> struct IC { static constexpr int v = I; };
template <int I, int N, class F> DI void static_for(F&& f) { if constexpr (I < N) { f(IC<I>{}); static_for<I + 1, N>(f); } }
DI unsigned now() { u64 t; asm volatile("s_memrealtime %0\n\ts_waitcnt lgkmcnt(0)" : "=s"(t) :: "memory"); return (unsigned)t; }
template <int N> DI void waitv() { asm volatile("s_waitcnt vmcnt(%0)" :: "n"(N) : "memory"); }
struct ConvOrder { int t[36], u[36]; };
constexpr ConvOrder conv_order() {
  ConvOrder o = {};
  int done[8] = {}, last = -1;
  for (int k = 0; k < 36; ++k) {
    int best = -1;
    for (int t = 0; t < 8; ++t)
      if (t != last && done[t] <= t && (best < 0 || t - done[t] > best - done[best])) best = t;
    o.t[k] = best; o.u[k] = done[best]++; last = best;
  }
  return o;
}
constexpr ConvOrder conv_order_last() {
  ConvOrder o = {};
  int k = 0;
  for (int p = 0; p < 4; ++p)
    for (int u = 0; u <= 2 * p + 1; ++u) {
      o.t[k] = 2 * p + 1; o.u[k] = u; ++k;
      if (u <= 2 * p) { o.t[k] = 2 * p; o.u[k] = u; ++k; }
    }
  return o;
}
constexpr int epi_slot(int v) { return v < 20 ? 2 * EO + v : v < 32 ? 2 * EO + 20 + 2 * (v - 20) : 2 * 50 + (v - 32); }
constexpr int epi_dist(int v) { return v >= 20 && v < 32 ? 2 : 1; }
constexpr int hl_upto(int i) { return i < 4 ? 25 + (5 * i + 1) / 2 : i < 8 ? 35 : i < 36 ? 35 + 5 * (i - 8) / 4 : 70; }
DI unsigned tagbad(unsigned bad, v4u g, unsigned tagA, unsigned tagB) {
  return __builtin_amdgcn_bitop3_b32(__builtin_amdgcn_bitop3_b32(bad, g.y, tagA, 0xF6), g.w, tagB, 0xF6);
}
template <int R0, int R1, int R2, int R3> DI void put4(unsigned base, f32x2 a, f32x2 b, f32x2 c, f32x2 d) {
  asm volatile("s_mov_b32 m0, %4\n\ts_nop 0\n\tds_write_addtid_b32 %0 offset:%5\n\tds_write_addtid_b32 %1 offset:%6\n\t"
               "ds_write_addtid_b32 %2 offset:%7\n\tds_write_addtid_b32 %3 offset:%8"
               :: "v"(pack2(a.x, a.y)), "v"(pack2(b.x, b.y)), "v"(pack2(c.x, c.y)), "v"(pack2(d.x, d.y)), "s"(base),
                  "n"(R0 * UVS_ROW), "n"(R1 * UVS_ROW), "n"(R2 * UVS_ROW), "n"(R3 * UVS_ROW) : "memory");
}
DI unsigned sum4(unsigned m, v4u v) {
  asm("v_add3_u32 %0, %0, %1, %2\n\tv_add3_u32 %0, %0, %3, %4" : "+v"(m) : "v"(v.x), "v"(v.y), "v"(v.z), "v"(v.w));
  return m;
}
DI unsigned sum8(unsigned m, v4u a, v4u b) {
  asm("v_add3_u32 %0, %0, %1, %2\n\tv_add3_u32 %0, %0, %3, %4\n\tv_add3_u32 %0, %0, %5, %6\n\tv_add3_u32 %0, %0, %7, %8"
      : "+v"(m) : "v"(a.x), "v"(a.y), "v"(a.z), "v"(a.w), "v"(b.x), "v"(b.y), "v"(b.z), "v"(b.w));
  return m;
}
template <int I> DI unsigned red_step(unsigned v) {
  constexpr int ctrl[6] = {0xB1, 0x4E, 0x141, 0x140, 0x142, 0x143}, rows[6] = {0xF, 0xF, 0xF, 0xF, 0xA, 0xC};
  return v + (unsigned)__builtin_amdgcn_update_dpp(0, (int)v, ctrl[I], rows[I], 0xF, true);
}
DI unsigned red_all(unsigned v) { return red_step<5>(red_step<4>(red_step<3>(red_step<2>(red_step<1>(red_step<0>(v)))))); }
DI void swap_add32(float& a, float b) { const auto r = __builtin_amdgcn_permlane32_swap(f2u(a), f2u(b), false, false); a = u2f(r[0]) + u2f(r[1]); }
DI void swap_add16(float& a, float b) { const auto r = __builtin_amdgcn_permlane16_swap(f2u(a), f2u(b), false, false); a = u2f(r[0]) + u2f(r[1]); }

typedef const unsigned char* cptr;
typedef unsigned v16u __attribute__((ext_vector_type(16)));
DI cptr mkptr(unsigned lo, unsigned hi) { return (cptr)(((u64)hi << 32) | lo); }

__global__ void __launch_bounds__(256, 1)
s4_block_kernel(cptr, cptr, cptr, cptr, cptr, cptr, cptr, cptr, cptr, cptr, cptr, cptr, cptr, unsigned char*, unsigned char*, cptr) {
  STAMP(0);
  v16u ka0, ka1;
  asm volatile("s_load_dwordx16 %0, %2, 0x0\n\ts_load_dwordx16 %1, %2, 0x40" : "=&s"(ka0), "=&s"(ka1) : "s"(__builtin_amdgcn_kernarg_segment_ptr()));
  SB();
  LAS unsigned char* const L = (LAS unsigned char*)16u - 16;
  const int tid = threadIdx.x, lane = tid & 63, w = __builtin_amdgcn_readfirstlane(tid >> 6);
  const int b = blockIdx.x, xcd = b & 7, slot = b >> 3, id = xcd * 32 + slot;
  const int ks = (id >> 6) * 4 + w, cc = id & 63;
  const int rp = 4 * (xcd >> 1) + (slot >> 3), rg = xcd & 1, dt = slot & 7, si = 8 * rg + dt;
  const int ct = 7 * rg + min(dt, 6);
  const int rot = (0x0340 >> (4 * (2 * (w & 1) + rg))) & 7;

  const unsigned char* pcode;
  asm volatile("s_getpc_b64 %0" : "=s"(pcode));
  const unsigned warm = *(const GAS unsigned*)((const GAS unsigned char*)pcode + (unsigned)min((((slot & 7) * 4 + w) * 11 + min(lane, 10)) * 128, CODE_BYTES));
  const int g16 = lane >> 4, q4 = (lane & 15) >> 2, p4 = lane & 3, hf = lane >> 5, blk = (lane >> 4) & 1, l16 = lane * 16;
  const unsigned lrw = L_R + w * R_WAVE;
  unsigned wmo[3], co = (ks * 32 + (lane & 31)) * 2, xo = id * 8192 + (tid & 3) * 2048 + (tid >> 2) * 16, ro = id * 8192 + w * 2048 + hf * 1024 + (lane & 31) * 16, po = id * 512 + tid * 2;
  unsigned wto = id * 7168 + (w >> 1) * 3584 + min(lane, 27) * 128;
#pragma unroll
  for (int i = 0; i < 3; ++i) { const unsigned idx = lane + 64 * i; wmo[i] = (ks * 32 * 6144 + cc * 96 - i * 1024) + idx / 6 * (6144 - 96) + idx * 16; PIN(wmo[i]); }
  PIN(co); PIN(xo); PIN(ro); PIN(po); PIN(wto);
  set_m0(lrw);
  SB();
  unsigned w2v0 = (lane >> 3) * 1024 + (((lane & 7) ^ (blk << 2)) * 16), w2v1 = w2v0 + 7168;
  unsigned w1v0 = (lane >> 2) * 3584 + p4 * 16, w1v1 = w1v0 + 57344 - 1024;
  const int zs = OFF_Z + (2 * id + (w >> 1)) * 64;
  int za = (((w & 1) + 2 * (g16 & 1)) * 8 + q4 * 2 + (g16 >> 1)) * 32768 + p4 * 16, zb = za + 32 * 32768;
  int tra = (hf * 8 + q4) * 64 + blk * 32 + p4 * 8;
  LAS unsigned char* w1a = L + lrw + tra;
  LAS unsigned char* ka = L + L_KC + ((w >> 1) * 4 + q4) * KC_STRIDE + 2 * (32 - 8 * g16 + 4 * p4) - 8 + 256 * (w & 1);
  LAS unsigned char* hb = L + L_H + (w >> 1) * H_STRIDE + (lane & 15) * 32 + g16 * 16 + 256 - 256 * (w & 1);
  LAS unsigned char* zp = L + sel(tid < 64, L_KC + ((lane >> 1) & 7) * KC_STRIDE + (lane & 1) * 16, L_H + hf * H_STRIDE + min(lane & 31, 29) * 16);
  LAS unsigned char* ub = L + L_UVS + (lane & 15) * UVS_ROW + g16 * 16;
  int gmo = OFF_MG + (g16 * 256 + id) * 256 + (lane & 15) * 16, gmo2 = OFF_MG + ((4 + blk) * 256 + id) * 256 + (lane & 15) * 16;
  int mgo = sel(lane < 48, (lane >> 1) * 256 + (lane & 1) * 8, OOB), sgo = sel(lane & 31, OOB, hf * 8);
  LAS unsigned char* xbw = L + L_XB + (tid >> 7) * 2048 + (tid & 127) * 4;
  LAS unsigned char* xbr = L + L_XB + w * 2048 + (lane & 15) * 32 + g16 * 8;
  LAS unsigned char *exa = L + L_XB + w * 2048 + l16, *exb = L + L_RX + w * 3072 + l16, *eya = L + L_XB + (w ^ 1) * 2048 + l16, *eyb = L + L_RX + (w ^ 1) * 3072 + l16;
  PIN(xbw); PIN(xbr); PIN(exa); PIN(exb); PIN(eya); PIN(eyb);
  int mto = (tid >> 1) * 128 + (tid & 1) * 16;
  LAS unsigned char* mtw = L + (tid & 1) * L_TG + (tid >> 1) * 4;
  PIN(mto); PIN(mtw);
  PIN(w2v0); PIN(w2v1); PIN(w1v0); PIN(w1v1); PIN(za); PIN(zb); PIN(tra); PIN(w1a); PIN(ka); PIN(hb); PIN(zp); PIN(ub); PIN(gmo); PIN(gmo2); PIN(mgo); PIN(sgo);
#define W1DMA(step, lds) dma_w1(w1w + (step) * 57344, w1v0, (lds))
#define W1DMA2(step, lds) dma_w2(w1w + (step) * 57344, w1v0, w1v1, (lds))
#define W2DMA(step, lds) dma_w2(((step) < 12 ? w2lo : (step) < 16 ? w2c3 : w2hi) + (step) * 16384, w2v0, w2v1, (lds))
  SB();
  STAMPA(24);
  asm volatile("s_waitcnt lgkmcnt(0)" : "+s"(ka0), "+s"(ka1));
  STAMPA(25);
  const cptr xin = mkptr(ka0[0], ka0[1]), pAre = mkptr(ka0[2], ka0[3]), pAim = mkptr(ka0[4], ka0[5]), pBre = mkptr(ka0[6], ka0[7]);
  const cptr pBim = mkptr(ka0[8], ka0[9]), pCre = mkptr(ka0[10], ka0[11]), pCim = mkptr(ka0[12], ka0[13]), pldt = mkptr(ka0[14], ka0[15]);
  const cptr pD = mkptr(ka1[0], ka1[1]), w1 = mkptr(ka1[2], ka1[3]), w2 = mkptr(ka1[4], ka1[5]), pc = mkptr(ka1[6], ka1[7]);
  const cptr wmod = mkptr(ka1[8], ka1[9]), out = mkptr(ka1[10], ka1[11]), ws = mkptr(ka1[12], ka1[13]);
  const unsigned epoch_v = __hip_atomic_load((GAS unsigned*)ws, __ATOMIC_RELAXED, __HIP_MEMORY_SCOPE_AGENT);
  asm volatile("global_load_lds_dwordx4 %0, %3\n\tglobal_load_lds_dwordx4 %1, %3 offset:1024\n\tglobal_load_lds_dwordx4 %2, %3 offset:2048"
               :: "v"(wmo[0]), "v"(wmo[1]), "v"(wmo[2]), "s"(wmod) : "memory");
  const unsigned cval = *(const GAS u16*)((const GAS unsigned char*)pc + co);
  SB();
  const v4u xa = *(const GAS v4u*)((const GAS unsigned char*)xin + xo), xb = *(const GAS v4u*)((const GAS unsigned char*)xin + 1024 + xo);
  const v4u ra = *(const GAS v4u*)((const GAS unsigned char*)xin + ro), rb = *(const GAS v4u*)((const GAS unsigned char*)xin + 512 + ro);
  SB();
#define PARAM(p) (unsigned)*(const GAS u16*)((const GAS unsigned char*)(p) + po)
  const unsigned uAre = PARAM(pAre), uAim = PARAM(pAim), uBre = PARAM(pBre), uBim = PARAM(pBim), uCre = PARAM(pCre), uCim = PARAM(pCim);
#undef PARAM
  SB();
  const unsigned wt = *(const GAS unsigned*)((const GAS unsigned char*)(w1 + (w & 1) * (w2 - w1)) + wto);
  unsigned uldt, uD;
  asm volatile("s_load_dword %0, %2, %4\n\ts_load_dword %1, %3, %4" : "=&s"(uldt), "=&s"(uD) : "s"(pldt), "s"(pD), "s"(id * 4));
  SB();
  STAMPA(26);
  LAS unsigned char* koff[12];
  {
    float fj = (float)(8 * tid - 3);
    PIN(fj);
    const unsigned lin = (unsigned)(8 * tid - 3 + 0x4300 - 128) * 0x10001u + 0x10000u;
#pragma unroll
    for (int i = 0; i < 12; i += 2) {
      const unsigned r = sel(tid < 32, lin + i * 0x10001u, pack2(fj + (float)i, fj + (float)(i + 1)));
      koff[i] = L + ((r & 0xFFFFu) * 4 + (L_KT + (128 - 0x4300) * 4));
      koff[i + 1] = L + ((r >> 16) * 4 + (L_KT + (128 - 0x4300) * 4));
      PIN(koff[i]);
      if (i < 10) PIN(koff[i + 1]);
    }
  }
  *(LAS v4u*)(L + L_KT - 16) = v4u{0u, 0u, 0u, 0u};
  LAS unsigned char* ktw[4];
  {
    const int row = w * 16 + (lane & 15), sg = max((row >> 3) - 1, 0), rr_ = row - 8 * sg;
    const int idx0 = 128 * sg + ((rr_ >> (3 - sg)) << (7 - sg)) + (rr_ & ((8 >> sg) - 1)) + 1, st = 8 >> sg;
#pragma unroll
    for (int r = 0; r < 4; ++r) {
      const int idx = sel(row < 40, idx0 + st * (4 * g16 + r), tid == 136 && r == 0 ? 0 : 641 + lane);
      ktw[r] = L + L_KT + idx * 4;
      PIN(ktw[r]);
    }
  }
  const unsigned char* w2lo = w2 + (448 * w + 64 * rot) * 1024 + dt * 128;
  const unsigned char* w2c3 = w2lo - (rot >> 2) * (448 * 1024);
  const unsigned char* w2hi = w2lo - ((rot + 1) >> 2) * (448 * 1024);
  const unsigned char* w1w = w1 + ct * 256 + w * 64;
  SB();
  STAMPA(27);
  asm volatile("s_waitcnt lgkmcnt(0)" : "+s"(uldt), "+s"(uD));
  const unsigned epoch = __builtin_amdgcn_readfirstlane(epoch_v);
  asm volatile("" :: "s"(epoch));
  STAMPA(28);
  const unsigned char* setp = ws + SET_BASE + (size_t)(epoch & 3u) * SET_STRIDE;
  const __amdgpu_buffer_rsrc_t rs = __builtin_amdgcn_make_buffer_rsrc((void*)setp, 0, SET_BYTES, 0x00020000);
  const unsigned tagA = epoch * 0x9E3779B1u + 0x7F4A7C15u, tagB = ~tagA;
  {
    const float c = bf_lo(cval);
    const float s = c * __builtin_amdgcn_rcpf(1.0f + __builtin_amdgcn_exp2f(-LOG2E * c));
    *(LAS u16*)(L + lrw + 3072 + (lane & 31) * 2) = (u16)pack2(s, s);
    SB();
#define XPUB(i) do { stg(rs, (tid >> 2) * 65536 + (tid & 3) * 16, OFF_XG + (i) * 16384 + id * 64, v4u{xa[i], tagA, xb[i], tagB}); SB(); } while (0)
    asm volatile("" :: "v"(xb));
    STAMPA(29);
    XPUB(0);
    XPUB(1);
    STAMP(1);
    LAS unsigned char* a = L + lrw + (8 * g16 + q4) * 96 + p4 * 8;
    const v4u sc = lds128(L + lrw + 3072 + g16 * 16);
    const v4u m0 = trd2(a, a + 384), m1 = trd2(a + 32, a + 416), m2 = trd2(a + 64, a + 448);
    SB();
    f32x2 fa[8];
#pragma unroll
    for (int i = 0; i < 4; ++i) { fa[i] = f32x2{bf_lo(ra[i]), bf_hi(ra[i])}; fa[4 + i] = f32x2{bf_lo(rb[i]), bf_hi(rb[i])}; }
    const f32x2 s2 = ((fa[0] + fa[1]) + (fa[2] + fa[3])) + ((fa[4] + fa[5]) + (fa[6] + fa[7]));
    float mean = s2.x + s2.y;
    PIN(mean);
    SB();
    const f32x4 c0 = mfma16(sc, m0, f32x4{}), c1 = mfma16(sc, m1, f32x4{}), c2 = mfma16(sc, m2, f32x4{});
    SB();
    mean = swap16_add(row_sum(mean)) * (1.0f / 512.0f);
    PIN(mean);
    SB();
    XPUB(2);
    __builtin_amdgcn_raw_buffer_store_b64(v2u{sel(lane < 16, f2u(c0[0]), sel(lane < 32, f2u(c1[0]), f2u(c2[0]))), (lane & 1) ? tagB : tagA}, rs, mgo,
                                          OFF_MG + cc * 24 * 256 + ks * 16, SC1);
    SB();
    f32x2 q2 = {0.f, 0.f}, q3 = {0.f, 0.f};
#pragma unroll
    for (int i = 0; i < 8; i += 2) {
      const f32x2 d0 = fa[i] - f32x2{mean, mean}, d1 = fa[i + 1] - f32x2{mean, mean};
      q2 = __builtin_elementwise_fma(d0, d0, q2);
      q3 = __builtin_elementwise_fma(d1, d1, q3);
    }
    q2 += q3;
    const float var = rbf(rbf(swap16_add(row_sum(q2.x + q2.y)) * (1.0f / 512.0f)) + 1e-5f);
    __builtin_amdgcn_raw_buffer_store_b64(v2u{pack2(mean, __builtin_amdgcn_sqrtf(var)), hf ? tagB : tagA}, rs, sgo, OFF_SG + (id * 4 + w) * 16, SC1);
    SB();
    XPUB(3);
#undef XPUB
  }
  STAMP(2);
  unsigned dsk = tid == 136 ? uD : 0u;
  PIN(dsk);

  {
    const float dtv = rbf(__builtin_amdgcn_exp2f(LOG2E * (w >> 1 ? bf_hi(uldt) : bf_lo(uldt))));
    const float are = dtv * bf_lo(uAre) * LOG2E, aim = dtv * bf_lo(uAim) * INV2PI;
    const float br = dtv * bf_lo(uBre), bi = dtv * bf_lo(uBim), cr = bf_lo(uCre), ci = bf_lo(uCim);
    const f32x2 wv = {cr * br - ci * bi, cr * bi + ci * br};
    const unsigned row = L_UVS + (w >> 1) * UVS_CH + (w & 1) * 256;
    const f32x2 e1 = cexp(are, aim), e8 = cexp(8.f * are, -8.f * aim), e128 = cexp(128.f * are, 128.f * aim);
    const f32x2 e1p = {-e1.y, e1.x}, e8p = {-e8.y, e8.x}, e128p = {-e128.y, e128.x};
    const v4u pois = {POISON, POISON, POISON, POISON};
    f32x2 z[8], v, v2, y, y2;
#define M1(a) cmul(a, e1, e1p)
#define M8(a) cmul(a, e8, e8p)
#define MK(a) cmul(a, e128, e128p)
    z[0] = M1(wv);
    put4<40, 41, 42, 0>(row, wv, f32x2{1.f, 0.f}, e8, z[0]);
    W1DMA2(0, lrw + 0);
    v = M8(e8); z[1] = M1(z[0]); v2 = M8(v); z[2] = M1(z[1]);
    put4<43, 1, 44, 2>(row, v, z[1], v2, z[2]);
    v = M8(v2); z[3] = M1(z[2]); v2 = M8(v); z[4] = M1(z[3]);
    put4<45, 3, 46, 4>(row, v, z[3], v2, z[4]);
    W1DMA2(2, lrw + 2048);
    v = M8(v2); z[5] = M1(z[4]); v2 = M8(v); z[6] = M1(z[5]);
    put4<47, 5, 48, 6>(row, v, z[5], v2, z[6]);
    z[7] = M1(z[6]); z[0] = MK(z[0]); z[1] = MK(z[1]); z[2] = MK(z[2]);
    put4<7, 8, 9, 10>(row, z[7], z[0], z[1], z[2]);
    W1DMA2(4, lrw + 4096);
    z[3] = MK(z[3]); z[4] = MK(z[4]); z[5] = MK(z[5]); z[6] = MK(z[6]);
    put4<11, 12, 13, 14>(row, z[3], z[4], z[5], z[6]);
    z[7] = MK(z[7]); z[1] = MK(z[1]); z[3] = MK(z[3]); z[5] = MK(z[5]);
    put4<15, 16, 17, 18>(row, z[7], z[1], z[3], z[5]);
    W1DMA2(6, lrw + 6144);
    z[7] = MK(z[7]); z[1] = MK(z[1]); z[3] = MK(z[3]); z[5] = MK(z[5]);
    put4<19, 20, 21, 22>(row, z[7], z[1], z[3], z[5]);
    y = MK(z[7]); z[3] = MK(z[3]); y2 = MK(y); z[4] = MK(z[3]);
    put4<23, 24, 25, 26>(row, y, z[3], y2, z[4]);
    W1DMA2(8, lrw + 8192);
    y = MK(y2); z[3] = MK(z[4]); y2 = MK(y); z[4] = MK(z[3]);
    put4<27, 28, 29, 30>(row, y, z[3], y2, z[4]);
    y = MK(y2); v = M8(v2); y2 = MK(y); v2 = M8(v);
    put4<31, 49, 32, 50>(row, y, v, y2, v2);
    W1DMA2(10, lrw + 10240);
    y = MK(y2); v = M8(v2); y2 = MK(y); v2 = M8(v);
    put4<33, 51, 34, 52>(row, y, v, y2, v2);
    y = MK(y2); v = M8(v2); y2 = MK(y); v2 = M8(v);
    put4<35, 53, 36, 54>(row, y, v, y2, v2);
    stg(rs, za, zs, pois);
    y = MK(y2); v = M8(v2); y2 = MK(y); v2 = M8(v);
    put4<37, 55, 38, 56>(row, y, v, y2, v2);
    y = MK(y2);
    *(LAS unsigned*)(L + row + 39 * UVS_ROW + lane * 4) = pack2(y.x, y.y);
    stg(rs, zb, zs, pois);
#undef M1
#undef M8
#undef MK
  }
  BAR();
  STAMP(3);
  v4u gx[4], gs[4], gm, gn;
  const int xgo = OFF_XG + id * 16384;
#define S0LOADS() do { \
    _Pragma("unroll") for (int i = 0; i < 4; ++i) gx[i] = ldg(rs, tid * 16, xgo + i * 4096); \
    _Pragma("unroll") for (int i = 0; i < 4; ++i) gs[i] = ldg(rs, tid * 16, OFF_SG + i * 4096); \
    gm = ldg(rs, gmo, 0); gn = ldg(rs, gmo2, 0); } while (0)
  if (w < 3) {
    LAS unsigned char* ut = ub + w * 16 * UVS_ROW;
    v4u fv[2][8], fu[2][8];
#pragma unroll
    for (int s = 0; s < 8; ++s)
#pragma unroll
      for (int c = 0; c < 2; ++c) { fv[c][s] = lds128(ub + c * UVS_CH + 41 * UVS_ROW + s * 64); fu[c][s] = lds128(ut + c * UVS_CH + s * 64); SB(); }
    f32x4 a0 = {}, a1 = {};
#pragma unroll
    for (int s = 0; s < 8; ++s) { a0 = mfma16(fv[0][s], fu[0][s], a0); SB(); a1 = mfma16(fv[1][s], fu[1][s], a1); SB(); }
    *(LAS unsigned*)ktw[0] = pack2(a0[0] + bf_lo(dsk), a1[0] + bf_hi(dsk));
#pragma unroll
    for (int r = 1; r < 4; ++r) *(LAS unsigned*)ktw[r] = pack2(a0[r], a1[r]);
  }
  SB();
  S0LOADS();
  SB();
  BAR();
  {
    unsigned k[11];
#pragma unroll
    for (int i = 0; i < 11; ++i) k[i] = *(LAS unsigned*)koff[i];
    SB();
    if (w < 2) *(LAS v4u*)zp = v4u{0u, 0u, 0u, 0u};
#pragma unroll
    for (int c = 0; c < 2; ++c) {
      unsigned e[5], o[5];
#pragma unroll
      for (int i = 0; i < 5; ++i) {
        o[i] = __builtin_amdgcn_perm(k[2 * i + 1], k[2 * i], c ? 0x07060302u : 0x05040100u);
        e[i] = __builtin_amdgcn_perm(k[2 * i + 2], k[2 * i + 1], c ? 0x07060302u : 0x05040100u);
      }
      LAS unsigned char* d = L + L_KC + c * 4 * KC_STRIDE + 32 + tid * 16;
      *(LAS v4u*)(d) = v4u{e[1], e[2], e[3], e[4]};
      *(LAS v4u*)(d + KC_STRIDE) = v4u{o[1], o[2], o[3], o[4]};
      *(LAS v4u*)(d + 2 * KC_STRIDE) = v4u{e[0], e[1], e[2], e[3]};
      *(LAS v4u*)(d + 3 * KC_STRIDE) = v4u{o[0], o[1], o[2], o[3]};
    }
  }
  SB();
  STAMP(4);
  {
    unsigned bad = tagbad(tagbad(0u, gm, tagA, tagB), gn, tagA, tagB);
#pragma unroll
    for (int i = 0; i < 4; ++i) bad = tagbad(tagbad(bad, gx[i], tagA, tagB), gs[i], tagA, tagB);
    if (__builtin_expect(__any(bad != 0u), 0)) {
      for (unsigned spin = 0; spin < SPIN_CAP; ++spin) {
        FRESH();
        if (spin > 1) PAUSE();
        COUNT(20);
        bool again = false;
#pragma unroll
        for (int i = 0; i < 4; ++i) {
          if (__any(tagbad(0u, gx[i], tagA, tagB) != 0u)) { gx[i] = ldg(rs, tid * 16, xgo + i * 4096); again = true; }
          if (__any(tagbad(0u, gs[i], tagA, tagB) != 0u)) { gs[i] = ldg(rs, tid * 16, OFF_SG + i * 4096); again = true; }
        }
        if (__any(tagbad(tagbad(0u, gm, tagA, tagB), gn, tagA, tagB) != 0u)) { gm = ldg(rs, gmo, 0); gn = ldg(rs, gmo2, 0); again = true; }
        if (!again) break;
      }
    }
  }
  asm volatile("" :: "v"(wt), "v"(warm));
  WAITV(0);
  STAMP(5);
  W1DMA2(12, lrw + 12288);
  float zsc;
  v4u mtp;
  {
    const int i0 = (int)pack2(row_sum(u2f(gm.x)), row_sum(u2f(gm.z))), i1 = (int)pack2(row_sum(u2f(gn.x)), row_sum(u2f(gn.z)));
    unsigned md[6];
#pragma unroll
    for (int k = 0; k < 6; ++k) md[k] = (unsigned)__builtin_amdgcn_readlane(k < 4 ? i0 : i1, 16 * (k & 3));
#define MOD(k, c) ((c) ? bf_hi(md[k]) : bf_lo(md[k]))
    float s0 = rbf(1.0f + MOD(4, 0)), s1 = rbf(1.0f + MOD(4, 1));
    s0 = s0 == 0.f ? 0x1p-30f : s0;
    s1 = s1 == 0.f ? 0x1p-30f : s1;
    const float G0 = MOD(2, 0) * s0, G1 = MOD(2, 1) * s1;
    const float A0 = rbf(1.0f + MOD(1, 0)) * G0, A1 = rbf(1.0f + MOD(1, 1)) * G1, B0 = MOD(0, 0) * G0, B1 = MOD(0, 1) * G1;
    zsc = w >> 1 ? s1 : s0;
    mtp = v4u{lane ? md[5] : pack2(s0, s1), tagA, md[3], tagB};
#undef MOD
#pragma unroll
    for (int i = 0; i < 4; ++i) {
      const float mA = bf_lo(gs[i].x), rA = __builtin_amdgcn_rcpf(bf_hi(gs[i].x)), mB = bf_lo(gs[i].z), rB = __builtin_amdgcn_rcpf(bf_hi(gs[i].z));
      const float n00 = (bf_lo(gx[i].x) - mA) * rA, n01 = (bf_hi(gx[i].x) - mA) * rA, n10 = (bf_lo(gx[i].z) - mB) * rB, n11 = (bf_hi(gx[i].z) - mB) * rB;
      const int o = (tid + 256 * i) * 4;
      W1DMA2(14 + 2 * i, lrw + (14 + 2 * i) * 1024);
      *(LAS unsigned*)(L + L_H + 480 + o) = pack2(__builtin_fmaf(n00, A0, B0), __builtin_fmaf(n10, A0, B0));
      *(LAS unsigned*)(L + L_H + H_STRIDE + 480 + o) = pack2(__builtin_fmaf(n01, A1, B1), __builtin_fmaf(n11, A1, B1));
      *(LAS unsigned*)(xbw + i * 512) = __builtin_amdgcn_perm(gx[i].z, gx[i].x, 0x05040100u);
      *(LAS unsigned*)(xbw + 4096 + i * 512) = __builtin_amdgcn_perm(gx[i].z, gx[i].x, 0x07060302u);
    }
  }
  BAR();
  const unsigned tb = now();
  STAMP(6);

  v4u w1f[32], w2f[28][2], mt0, mt1;
  {
    const bool rh = w & 1;
    constexpr ConvOrder O = conv_order(), OL = conv_order_last();
    v2u A[4][8][2], xk[4];
    v4u B[4][8];
    f32x4 acc[8], keep[4];
#pragma unroll
    for (int u = 0; u < 8; ++u) {
      A[0][u][0] = trd(ka + 512 * u + 8);
      A[0][u][1] = trd(ka + 512 * u);
      B[0][7 - u] = lds128(hb + 64 * (8 * (7 - u) + 3));
    }
    SB();
    if (lane) mtp.z = tb;
    stg(rs, tid < 2 ? tid * 16 : OOB, OFF_MT + id * 128, mtp);
    SB();
    auto hand_over = [&](auto Pc) __attribute__((always_inline)) {
      constexpr int i = Pc.v;
      PIN(acc[2 * i]); PIN(acc[2 * i + 1]);
      const f32x4 ev = acc[2 * i], od = acc[2 * i + 1];
      const f32x4 snd = rh ? ev : od, own = rh ? od : ev;
      *(LAS f32x4*)(i < 2 ? exa + i * 1024 : exb + (i - 1) * 1024) = snd;
#pragma unroll
      for (int c = 0; c < 4; ++c) { float t = keep[i][c] + own[c]; PIN(t); keep[i][c] = t; }
    };
    static_for<0, 144>([&](auto Kc) __attribute__((always_inline)) {
      constexpr int K = Kc.v, r = K / 36, k = K % 36, t = r < 3 ? O.t[k] : OL.t[k], u = r < 3 ? O.u[k] : OL.u[k];
      if constexpr (k == 0 && r > 0) asm volatile("" :: "v"(B[r][7]));
      mfma16_v<r == 0 && u == 0>(acc[t], v4u{A[r][u][0].x, A[r][u][0].y, A[r][u][1].x, A[r][u][1].y}, B[r][t - u]);
      SB();
      if constexpr (K < 12) w1f[K] = trd2(w1a + K * 1024, w1a + K * 1024 + 256);
      if constexpr (K == 60) WAITV(9);
      if constexpr (K >= 60 && K < 70) w1f[K - 48] = trd2(w1a + (K - 48) * 1024, w1a + (K - 48) * 1024 + 256);
      if constexpr (K == 76) asm volatile("" :: "a"(w1f[21]));
      if constexpr (r == 2 && k < 4) xk[k] = *(LAS v2u*)(xbr + k * 512);
      constexpr int n = k - (r == 0 ? 9 : r == 1 ? 0 : 4);
      if constexpr (r < 3 && n >= 0 && n < 24) {
        if constexpr (n % 3 == 2) B[r + 1][n / 3] = lds128(hb + 64 * (8 * (n / 3) - r + 2));
        else A[r + 1][n / 3][n % 3] = trd(ka + 512 * (n / 3) + 64 * (r + 1) + 8 - 8 * (n % 3));
      }
      if constexpr (K >= 100 && K < 116) {
        constexpr int e = K - 100;
        const unsigned xw = (e & 2) ? xk[e >> 2].y : xk[e >> 2].x;
        float t2_ = zsc * ((e & 1) ? bf_hi(xw) : bf_lo(xw));
        PIN(t2_);
        keep[e >> 2][e & 3] = t2_;
      }
      if constexpr (K == 108 + 8) hand_over(IC<0>{});
      if constexpr (K == 108 + 17) hand_over(IC<1>{});
      if constexpr (K == 108 + 30) hand_over(IC<2>{});
      if constexpr (K == 14) W1DMA(22, L_RX + w * 3072);
      if constexpr (K == 22) W1DMA(23, L_TOP + w * 1024);
      if constexpr (K >= 37 && K < 69 && K % 8 == 5) W1DMA2(24 + (K - 37) / 4, lrw + (K - 37) / 4 * 1024);
      if constexpr (K >= 69 && K < 125 && K % 8 == 5) W2DMA((K - 69) / 8, lrw + 8192 + (K - 69) / 8 * 2048);
      if constexpr (K == MTK)
        asm volatile("buffer_load_dwordx4 %0, %2, %3, 0 offen sc1\n\tbuffer_load_dwordx4 %1, %2, %3, %4 offen sc1"
                     : "=&v"(mt0), "=&v"(mt1) : "v"(mto), "s"(rs), "s"(16384) : "memory");
      SB();
    });
    STAMP(7);
    asm volatile("s_nop 7" : "+v"(acc[6]), "+v"(acc[7]));
    hand_over(IC<3>{});
    BAR();
#pragma unroll
    for (int i = 0; i < 4; ++i) keep[i] += *(LAS f32x4*)(i < 2 ? eya + i * 1024 : eyb + (i - 1) * 1024);
#pragma unroll
    for (int i = 0; i < 2; ++i) {
      const auto s0 = __builtin_amdgcn_permlane16_swap(pack2(keep[2 * i][0], keep[2 * i][1]), pack2(keep[2 * i + 1][0], keep[2 * i + 1][1]), false, false);
      const auto s1 = __builtin_amdgcn_permlane16_swap(pack2(keep[2 * i][2], keep[2 * i][3]), pack2(keep[2 * i + 1][2], keep[2 * i + 1][3]), false, false);
      stg(rs, i ? zb : za, zs, v4u{guard(s0[0]), guard(s1[0]), guard(s0[1]), guard(s1[1])});
    }
    SB();
  }
  STAMP(8);

  static_assert(OFF_MT == 0, "table offset");
  const unsigned te = now();
  asm volatile("s_waitcnt vmcnt(2)" : "+v"(mt0), "+v"(mt1) :: "memory");
  if (__builtin_expect(__any(tagbad(tagbad(0u, mt0, tagA, tagB), mt1, tagA, tagB) != 0u), 0)) {
    for (unsigned spin = 0; spin < SPIN_CAP; ++spin) {
      FRESH();
      COUNT(21);
      if (tagbad(0u, mt0, tagA, tagB)) mt0 = ldg(rs, mto, OFF_MT);
      if (tagbad(0u, mt1, tagA, tagB)) mt1 = ldg(rs, mto, OFF_MT + 16384);
      if (!__any(tagbad(tagbad(0u, mt0, tagA, tagB), mt1, tagA, tagB) != 0u)) break;
      PAUSE();
    }
  }
  *(LAS unsigned*)(mtw) = mt0.x;
  *(LAS unsigned*)(mtw + 512) = mt1.x;
  *(LAS unsigned*)(mtw + 1024) = mt0.z;
  *(LAS unsigned*)(mtw + 1536) = mt1.z;
  {
    int late = (lane & 1) ? max((int)(mt0.z - tb), (int)(mt1.z - tb)) : 0;
    late = max(late, __builtin_amdgcn_update_dpp(late, late, 0xB1, 0xF, 0xF, false));
    late = max(late, __builtin_amdgcn_update_dpp(late, late, 0x4E, 0xF, 0xF, false));
    late = max(late, __builtin_amdgcn_update_dpp(late, late, 0x141, 0xF, 0xF, false));
    late = max(late, __builtin_amdgcn_update_dpp(late, late, 0x140, 0xF, 0xF, false));
    late = max(late, __builtin_amdgcn_update_dpp(late, late, 0x142, 0xA, 0xF, false));
    late = max(late, __builtin_amdgcn_update_dpp(late, late, 0x143, 0xC, 0xF, false));
    if (lane == 63) *(LAS int*)(L + L_ETA + w * 4) = late;
  }
  w1f[22] = trd2(L + L_RX + w * 3072 + tra, L + L_RX + w * 3072 + tra + 256);
  w1f[23] = trd2(L + L_TOP + w * 1024 + tra, L + L_TOP + w * 1024 + tra + 256);
  BAR();
  STAMP(9);
  const int w2t = (4 * hf + q4) * 128 + blk * 32 + (((p4 & 1) << 1) | (p4 >> 1)) * 8;
  LAS unsigned char* w2a = L + w2t + (q4 >> 1) * 64;
  LAS unsigned char* w2b = L + w2t + ((q4 >> 1) ^ 1) * 64;
  const int par = (0x1C >> (ct % 7)) & 1;
  const unsigned char* xg = setp + OFF_Z + rp * 131072;
  const unsigned xl = L_XR + w * 8192;
#define XSRC(p) (xg + w * 8192 + ((p) ^ par) * 32768)
  const int slo = OFF_Z + (rp * 4 + w) * 32768 + si * 2048;
  f32x4 uu[4], vv[4];
  v4u sl0, sl1;
  {
    LAS unsigned char* qr = L + ((lane & 31) == 0 ? L_TS : L_TB) + hf * 16;
    LAS unsigned char *pa = w2a + lrw + 8192, *pb = w2b + lrw + 8192;
    PIN(pa); PIN(pb);
    f32x16 uv, uv2;
    v4u qa[32];
    int wait = 0;
    const v4u lt = lds128(L + L_ETA);
    bool sent = false;
    asm volatile("" : "=v"(sl0), "=v"(sl1));
#pragma unroll
    for (int i = 0; i < 4; ++i) qa[i] = lds128(qr + i * 32);
    static_for<0, 32>([&](auto Sc) __attribute__((always_inline)) {
      constexpr int s = Sc.v;
      mfma_va<(s < 2)>((s & 1) ? uv2 : uv, qa[s], w1f[s]);
      SB();
      if constexpr (s < 28) qa[s + 4] = lds128(qr + (s + 4) * 32);
      if constexpr (s > 0) asm volatile("" :: "v"(qa[s > 0 ? s - 1 : 0]));
      if constexpr (s < 8) w1f[24 + s] = trd2(w1a + s * 1024, w1a + s * 1024 + 256);
      if constexpr (s == 4) { wait = min(__builtin_amdgcn_readfirstlane(max(max((int)lt.x, (int)lt.y), max((int)lt.z, (int)lt.w))), 300) + ZLAG; }
      if constexpr (s >= 8 && s < 22) w2f[(s - 8) >> 1][s & 1] = trd2(((s & 1) ? pb : pa) + ((s - 8) >> 1) * 2048, ((s & 1) ? pb : pa) + ((s - 8) >> 1) * 2048 + 1024);
      if constexpr (s == 4 || s == 13 || s == 22 || s == 31) {
        if (!sent) {
          if constexpr (s == 31) { while ((int)(now() - te) < wait) { } }
          if (s == 31 || (int)(now() - te) >= wait) {
            sent = true;
            STAMPB(26);
            asm volatile("buffer_load_dwordx4 %0, %2, %3, %4 offen sc1\n\tbuffer_load_dwordx4 %1, %2, %3, %4 offen offset:1024 sc1"
                         :: "v"(sl0), "v"(sl1), "v"(l16), "s"(rs), "s"(slo) : "memory");
            dma_x4(XSRC(0), l16, xl);
            dma_x4(XSRC(0) + 4096, l16, xl + 4096);
          }
        }
      }
      SB();
    });
    dma_x4(XSRC(1), l16, xl + 32768);
    dma_x4(XSRC(1) + 4096, l16, xl + 32768 + 4096);
    STAMPB(27);
    BAR();
    mfma_done(uv2);
    PIN(uv);
    LAS unsigned char* uw = L + L_UVT + (32 * w + (lane & 31)) * 4;
    if (lane < 32) { *(LAS float*)(uw) = uv[0] + uv2[0]; *(LAS float*)(uw + 512) = -LOG2E * (uv[1] + uv2[1]); }
    LAS unsigned char* uvp = L + L_UVT + (32 * w + 4 * hf) * 4;
#pragma unroll
    for (int i = 0; i < 4; ++i) { uu[i] = *(LAS f32x4*)(uvp + i * 32); vv[i] = *(LAS f32x4*)(uvp + 512 + i * 32); }
  }
  {
    const float i0 = __builtin_amdgcn_rcpf(bf_lo(*(LAS u16*)(L + L_TS + (si * 32 + (lane >> 2)) * 2)));
    const float i1 = __builtin_amdgcn_rcpf(bf_lo(*(LAS u16*)(L + L_TS + (si * 32 + 16 + (lane >> 2)) * 2)));
    SB();
    asm volatile("s_waitcnt vmcnt(16)" : "+v"(sl0), "+v"(sl1) :: "memory");
    STAMPB(28);
    if (__builtin_expect(__any(max4(max4(0u, sl0), sl1) == POISON), 0)) {
      for (unsigned spin = 0; spin < SPIN_CAP; ++spin) {
        FRESH();
        if (spin > 1) PAUSE();
        COUNT(22);
        sl0 = ldg(rs, l16, slo);
        sl1 = ldg(rs, l16 + 1024, slo);
        if (!__any(max4(max4(0u, sl0), sl1) == POISON)) break;
      }
    }
    float sm[8], sq[8];
#pragma unroll
    for (int i = 0; i < 4; ++i) {
      const f32x2 a = f32x2{bf_lo(sl0[i]), bf_hi(sl0[i])} * f32x2{i0, i0}, c = f32x2{bf_lo(sl1[i]), bf_hi(sl1[i])} * f32x2{i1, i1};
      const f32x2 s2 = a + c, q2 = __builtin_elementwise_fma(a, a, c * c);
      sm[2 * i] = s2.x; sm[2 * i + 1] = s2.y; sq[2 * i] = q2.x; sq[2 * i + 1] = q2.y;
    }
#pragma unroll
    for (int i = 0; i < 4; ++i) { swap_add32(sm[i], sm[i + 4]); swap_add32(sq[i], sq[i + 4]); }
#pragma unroll
    for (int i = 0; i < 2; ++i) { swap_add16(sm[i], sm[i + 2]); swap_add16(sq[i], sq[i + 2]); }
#pragma unroll
    for (int i = 0; i < 2; ++i) { sm[i] = dpp_add<0x124>(dpp_add<0x128>(sm[i])); sq[i] = dpp_add<0x124>(dpp_add<0x128>(sq[i])); }
    const bool od = lane & 4;
    stg(rs, (lane & 8) ? OOB : (p4 * 8 + hf * 4 + blk * 2 + (od ? 1 : 0)) * 16, OFF_SX + ((rp * 4 + w) * 16 + si) * 512,
        v4u{f2u(od ? sm[1] : sm[0]), tagA, f2u(od ? sq[1] : sq[0]), tagB});
    SB();
  }
  STAMPB(29);
  dma_x4(XSRC(2), l16, xl + 65536);
  dma_x4(XSRC(2) + 4096, l16, xl + 65536 + 4096);
  WAITV(17);
  STAMP(10);
  {
    v4u t[8];
    unsigned m = 0, m2 = 0;
#pragma unroll
    for (int i = 0; i < 8; ++i) t[i] = lds128(L + xl + l16 + i * 1024);
#pragma unroll
    for (int i = 0; i < 8; i += 2) { m = max4(m, t[i]); m2 = max4(m2, t[i + 1]); }
    if (__builtin_expect(__any(max(m, m2) == POISON), 0)) {
      for (unsigned spin = 0; spin < SPIN_CAP; ++spin) {
        COUNT(22);
        bool again = false;
#pragma nounroll
        for (int i = 0; i < 8; ++i)
          if (__any(max4(0u, lds128(L + xl + l16 + i * 1024)) == POISON)) {
            again = true;
            dma_x1(XSRC(0) + i * 1024, l16, xl + i * 1024);
            dma_x1(XSRC(1) + i * 1024, l16, xl + 32768 + i * 1024);
            dma_x1(XSRC(2) + i * 1024, l16, xl + 65536 + i * 1024);
          }
        if (!again) break;
        WAITV(0);
        if (spin) PAUSE();
      }
    }
  }
  BAR();
  STAMP(11);

  constexpr int HW = 40;
  v4u hq[HW], res[2], sx[8];
  const int c0 = 7 * w + rot, c3 = c0 - 7 * (rot >> 2), c4 = c0 - 7 * ((rot + 1) >> 2), ckv = lane < 8 ? l16 : OOB;
  const int hb0 = OFF_HID + (rp * 4 + rg) * 114688, kb0 = OFF_CK + (rp * 4 + rg) * 4096;
#define CHUNK(n) (((n) % 35 / 5 < 3 ? c0 : (n) % 35 / 5 == 3 ? c3 : c4) + (n) % 35 / 5)
#define HLOAD(n) hq[(n) % HW] = (n) % 5 == 4 ? ldg(rs, ckv, kb0 + CHUNK(n) * 128 + ((n) / 35) * 8192) \
    : ldg(rs, l16 + ((n) % 5) * 1024, hb0 + CHUNK(n) * 4096 + ((n) / 35) * (2 * 114688))
  const int hoff = OFF_HID + (rp * 4 * 112 + ct * 8 + 2 * w) * 1024, hv0 = dt == 7 ? OOB : l16, hv1 = dt == 7 ? OOB : l16 + 1024;
  const int cko = OFF_CK + rp * 16384 + (ct * 4 + w) * 64, ckw = (p4 || dt == 7) ? OOB : lane;
  const unsigned ckey = lane ? 0u : tagA;
#define CKPUT(sum, u) __builtin_amdgcn_raw_buffer_store_b32(ckey - red_step<1>(red_step<0>(sum)), rs, ckw, cko + ((u) ^ par) * 4096, SC1)
  const int sxo = OFF_SX + (rp * 4 + w) * 8192;
  LAS unsigned char* xr[2];
#pragma unroll
  for (int i = 0; i < 2; ++i) { xr[i] = L + L_XR + tra + i * 65536; PIN(xr[i]); }
  LAS unsigned char* rr = L + L_XR + tra + (rg ^ par) * 32768 + (4 * dt + w) * 1024;
  LAS unsigned char* str = L + L_STAT + (lane & 31) * 8;
  PIN(str);
#define XA(t) (xr[(t) >> 6] + ((t) & 63) * 1024)
#define XF(t) trd2(XA(t), XA(t) + 256)
#define XEN
#define REDO(p) \
  if (XEN __builtin_expect(__any(!(acc[(p) % 3][0] == acc[(p) % 3][0])), 0)) { \
    for (unsigned spin = 0; spin < SPIN_CAP / 128; ++spin) { \
      COUNT(23); \
      _Pragma("nounroll") for (int i = 0; i < 8; ++i) dma_x4(xg + ((p) ^ par) * 32768 + i * 4096, l16, L_XR + (p) * 32768 + i * 4096); \
      WAITV(0); \
      _Pragma("unroll") for (int s = 0; s < 32; ++s) { \
        const v4u t = XF(32 * (p) + s); \
        if (s == 0) mfma_av<true>(acc[(p) % 3], w1f[s], t); else mfma_av<false>(acc[(p) % 3], w1f[s], t); \
        SB(); \
      } \
      mfma_done(acc[(p) % 3]); \
      if (!__any(!(acc[(p) % 3][0] == acc[(p) % 3][0]))) break; \
    } \
  }
  f32x16 acc[3];
  f32x2 ec[4];
  float ep[48], ee[48], ey[48];
  unsigned cs = 0;
  v4u x[128], eo;
#pragma unroll
  for (int t = 0; t < 4; ++t) x[t] = XF(t);
  SB();
  static_for<0, 64>([&](auto Jc) __attribute__((always_inline)) {
    constexpr int J = Jc.v, p = J >> 4, j = J & 15;
    asm volatile("" :: "v"(x[2 * J + 1]));
    mfma_av<j == 0>(acc[p % 3], w1f[2 * j], x[2 * J]);
    if constexpr (J < 62) { x[2 * J + 4] = XF(2 * J + 4); x[2 * J + 5] = XF(2 * J + 5); }
    SB();
    mfma_av<false>(acc[p % 3], w1f[2 * j + 1], x[2 * J + 1]);
    SB();
    if constexpr (J == 10) { WAITV(16); __builtin_amdgcn_s_barrier(); FRESH(); }
    if constexpr (J == EO - 2) {
      unsigned bad = 0;
#pragma unroll
      for (int i = 0; i < 8; ++i) bad = tagbad(bad, sx[i], tagA, tagB);
      if (__builtin_expect(__any(bad != 0u), 0)) {
        for (unsigned spin = 0; spin < SPIN_CAP; ++spin) {
          FRESH();
          if (spin > 1) PAUSE();
          COUNT(23);
          bad = 0;
#pragma unroll
          for (int i = 0; i < 8; ++i) sx[i] = ldg(rs, l16 + (i & 3) * 1024, sxo + (i >> 2) * 4096);
#pragma unroll
          for (int i = 0; i < 8; ++i) bad = tagbad(bad, sx[i], tagA, tagB);
          if (!__any(bad != 0u)) break;
        }
      }
      float sm = u2f(sx[0].x), sq = u2f(sx[0].z);
#pragma unroll
      for (int i = 1; i < 8; ++i) { sm += u2f(sx[i].x); PIN(sm); sq += u2f(sx[i].z); PIN(sq); }
      const float mean = swap32_add(sm) * (1.0f / 512.0f);
      const float ms = __builtin_fmaf(swap32_add(sq), 1.0f / 512.0f, 1e-5f);
      const float a = -LOG2E * __builtin_amdgcn_rsqf(fmaxf(__builtin_fmaf(-mean, mean, ms), 1e-5f));
      *(LAS f32x2*)(str + (w ^ par) * 256) = f32x2{a, -a * mean};
      SB();
    }
    if constexpr (J == EO - 1) {
      asm volatile("s_waitcnt lgkmcnt(4)" ::: "memory");
      __builtin_amdgcn_s_barrier();
      FRESH();
#pragma unroll
      for (int i = 0; i < 4; ++i) ec[i] = *(LAS f32x2*)(str + i * 256);
      SB();
    }
    static_for<2 * J, 2 * J + 2>([&](auto Sc) __attribute__((always_inline)) {
      static_for<0, 48>([&](auto Vc) __attribute__((always_inline)) {
        constexpr int S = Sc.v, V = Vc.v, k = V & 15;
        if constexpr (S == epi_slot(V) + 2 * epi_dist(V)) {
          ey[V] = ep[V] * ee[V];
          if constexpr (k & 1) { unsigned o = pack2(ey[V - 1], ey[V]); PIN(o); eo[(k >> 1) & 3] = o; } else PIN(ey[V]);
          if constexpr (k == 7) { stg(rs, hv0, hoff + ((V >> 4) ^ par) * 114688, eo); cs = sum4(0u, eo); PIN(cs); }
          if constexpr (k == 15) { stg(rs, hv1, hoff + ((V >> 4) ^ par) * 114688, eo); CKPUT(sum4(cs, eo), V >> 4); }
        }
        if constexpr (S == epi_slot(V) + epi_dist(V)) { ee[V] = __builtin_amdgcn_rcpf(1.0f + ee[V]); PIN(ee[V]); }
        if constexpr (S == epi_slot(V)) {
          ep[V] = __builtin_fmaf(ec[V >> 4].x, acc[(V >> 4) % 3][k], __builtin_fmaf(ec[V >> 4].y, uu[k >> 2][k & 3], vv[k >> 2][k & 3]));
          ee[V] = __builtin_amdgcn_exp2f(ep[V]);
          PIN(ee[V]);
        }
      });
      SB();
    });
    if constexpr (j == 1 && p >= 1) {
      if constexpr (p == 2) REDO(1)
      if constexpr (p == 3) REDO(2)
      if (((p - 1) & 1) == (rg ^ par)) res[(p - 1) >> 1] = trd2(rr + ((p - 1) >> 1) * 65536, rr + ((p - 1) >> 1) * 65536 + 256);
    }
    if constexpr (j == 2 && p >= 2) {
      asm volatile("s_waitcnt lgkmcnt(4)" ::: "memory");
      __builtin_amdgcn_s_barrier();
      FRESH();
    }
    if constexpr (J < 8) {
      if constexpr ((J & 3) == 0) set_m0(xl + 98304 + J * 1024);
      dma_xn<(J & 3) * 1024>(XSRC(3) + (J >> 2) * 4096, l16);
    }
    if constexpr (J >= SXL && J < SXL + 8) sx[J - SXL] = ldg(rs, l16 + ((J - SXL) & 3) * 1024, sxo + ((J - SXL) >> 2) * 4096);
    if constexpr (J >= EO && J < EO + 22 && ((J - EO) & 1) == 0) {
      constexpr int n = (J - EO) >> 1;
      if constexpr (n < 3) W2DMA(7 + n, L_SP + w * 6144 + n * 2048);
      else W2DMA(7 + n, L_XR + ((n - 3) >> 2) * 32768 + w * 8192 + ((n - 3) & 3) * 2048);
    }
    if constexpr (J >= 48 && J < 56) { HLOAD(2 * (J - 48)); if constexpr (J < 55) HLOAD(2 * (J - 48) + 1); }
    if constexpr (J >= 56 && J < 64 && (J & 1) == 0) W2DMA(18 + ((J - 56) >> 1), L_XR + 65536 + w * 8192 + ((J - 56) >> 1) * 2048);
    SB();
    if constexpr (J == 15) STAMP(16);
    if constexpr (J == 31) STAMP(17);
    if constexpr (J == 47) STAMP(18);
  });
  STAMP(12);
  static_assert(EO + 6 >= 26 && EO + 14 > 34 && EO + 20 < 48 && epi_slot(16) >= 2 * 34 && epi_slot(31) < epi_slot(32) && epi_slot(47) + 2 < 128, "schedule");
  static_for<15, 25>([&](auto Ic) __attribute__((always_inline)) { HLOAD(Ic.v); });
  SB();
  mfma_done(acc[0]);
  REDO(3)
  if (rg ^ par) res[1] = trd2(rr + 65536, rr + 65536 + 256);
#pragma unroll
  for (int h = 0; h < 2; ++h) {
    v4u o;
#pragma unroll
    for (int i = 0; i < 4; ++i) {
      const int k = 8 * h + 2 * i;
      const f32x2 t = __builtin_elementwise_fma(f32x2{ec[3].y, ec[3].y}, f32x2{uu[k >> 2][k & 3], uu[k >> 2][(k & 3) + 1]}, f32x2{vv[k >> 2][k & 3], vv[k >> 2][(k & 3) + 1]});
      const f32x2 pp = __builtin_elementwise_fma(f32x2{ec[3].x, ec[3].x}, f32x2{acc[0][k], acc[0][k + 1]}, t);
      const f32x2 d = f32x2{__builtin_amdgcn_exp2f(pp.x), __builtin_amdgcn_exp2f(pp.y)} + f32x2{1.0f, 1.0f};
      const f32x2 y = pp * f32x2{__builtin_amdgcn_rcpf(d.x), __builtin_amdgcn_rcpf(d.y)};
      o[i] = pack2(y.x, y.y);
    }
    stg(rs, h ? hv1 : hv0, hoff + (3 ^ par) * 114688, o);
    if (h) CKPUT(sum4(cs, o), 3); else cs = sum4(0u, o);
    SB();
  }
  BAR();
  STAMP(13);

  f32x16 d[2][2];
  LAS unsigned char* redw = L + L_XR + w * 8192 + l16;
  LAS unsigned char* redr = L + L_XR + w * 2048 + l16;
#define REDW(un, q) *(LAS f32x4*)(redw + (un) * 32768 + (q) * 1024) = \
    f32x4{d[un][(q) >> 2][4 * ((q) & 3)], d[un][(q) >> 2][4 * ((q) & 3) + 1], d[un][(q) >> 2][4 * ((q) & 3) + 2], d[un][(q) >> 2][4 * ((q) & 3) + 3]}
  LAS unsigned char *w2sa = w2a + L_SP + w * 6144, *w2sb = w2b + L_SP + w * 6144;
  w2a += L_XR + w * 8192; w2b += L_XR + w * 8192;
  LAS unsigned char *w2ua = w2a + 65536, *w2ub = w2b + 65536;
  PIN(w2a); PIN(w2b); PIN(w2sa); PIN(w2sb); PIN(w2ua); PIN(w2ub);
#define PULL(step, pa, pb, off) do { w2f[step][0] = trd2((pa) + (off), (pa) + (off) + 1024); w2f[step][1] = trd2((pb) + (off), (pb) + (off) + 1024); } while (0)
  unsigned m = red_all(sum4(sum8(sum8(0u, hq[0], hq[1]), hq[2], hq[3]), hq[4]));
  v4u gbv, scv;
  f32x4 pr[8];
  float gb[8], rx[2][8];
  unsigned orow = (rp * 128 + ((lane & 31) >> 3) * 16 + rg * 8 + (lane & 7)) * 1024 + dt * 128 + w * 32 + hf * 16;
  static_for<0, 14>([&](auto Gc) __attribute__((always_inline)) {
    constexpr int G = Gc.v, un = G / 7, gq = G % 7;
    if (__builtin_expect((unsigned)__builtin_amdgcn_readlane((int)m, 63) != 2u * tagA, 0)) {
      for (unsigned spin = 0; spin < SPIN_CAP; ++spin) {
        FRESH();
        COUNT(24 + un);
        static_for<0, 5>([&](auto Ic) __attribute__((always_inline)) { HLOAD(5 * G + Ic.v); });
        m = sum4(sum8(sum8(0u, hq[(5 * G) % HW], hq[(5 * G + 1) % HW]), hq[(5 * G + 2) % HW], hq[(5 * G + 3) % HW]), hq[(5 * G + 4) % HW]);
        if ((unsigned)__builtin_amdgcn_readlane((int)red_all(m), 63) == 2u * tagA) break;
        PAUSE();
      }
    }
    static_for<0, 8>([&](auto Ic) __attribute__((always_inline)) {
      constexpr int c = Ic.v, i = G < 13 ? c >> 1 : c & 3, e = G < 13 ? c & 1 : c >> 2, I = 4 * G + i, n = 5 * G + i, X = 8 * G + c;
      mfma_av<gq == 0 && i == 0>(d[un][e], w2f[gq * 4 + i][e], hq[n % HW]);
      SB();
      if constexpr (G < 13 && e == 0) {
        if constexpr (I < 3) PULL(7 + I, w2sa, w2sb, I * 2048);
        if constexpr (I >= 3 && I < 11) PULL(7 + I, w2a, w2b, ((I - 3) >> 2) * 32768 + ((I - 3) & 3) * 2048);
        if constexpr (I >= 11 && I < 15) PULL(7 + I, w2ua, w2ub, (I - 11) * 2048);
        if constexpr (I == 18) waitv<4 + hl_upto(18) - 35>();
        if constexpr (I >= 18 && I < 22) PULL(4 + I, w2ua, w2ub, 32768 + (I - 18) * 2048);
        if constexpr (I == 23) waitv<hl_upto(23) - 35>();
        if constexpr (I == 23 || I == 24) PULL(3 + I, w2sa, w2sb, (I - 23) * 2048);
        if constexpr (I >= 29 && I < 37) REDW(0, I - 29);
      }
      if constexpr (G < 13 && e == 1) {
        if constexpr (I >= 4 && I < 8) W2DMA(18 + I, L_XR + 98304 + w * 8192 + (I - 4) * 2048);
        if constexpr (I == 8) { W2DMA(26, L_SP + w * 6144); W2DMA(27, L_SP + w * 6144 + 2048); }
        static_for<hl_upto(I), hl_upto(I + 1)>([&](auto Nc) __attribute__((always_inline)) { HLOAD(Nc.v); });
      }
      if constexpr (G < 13) {
        constexpr int q = 5 * G + 5;
        if constexpr (c == 1) m = sum8(0u, hq[q % HW], hq[(q + 1) % HW]);
        if constexpr (c == 3) m = sum4(sum8(m, hq[(q + 2) % HW], hq[(q + 3) % HW]), hq[(q + 4) % HW]);
        if constexpr (c == 4) m = red_step<0>(m);
        if constexpr (c == 5) m = red_step<2>(red_step<1>(m));
        if constexpr (c == 6) m = red_step<3>(m);
        if constexpr (c == 7) m = red_step<5>(red_step<4>(m));
        PIN(m);
      }
      if constexpr (X == 74) { gbv = lds128(L + L_TG + (64 * dt + 16 * w + 8 * hf) * 2); scv = lds128(L + L_TS + (64 * dt + 16 * w + 8 * hf) * 2); PIN(orow); }
      if constexpr (X >= 76 && X < 92 && (X & 1) == 0) {
        constexpr int q = (X - 76) >> 1;
        const float inv = __builtin_amdgcn_rcpf((q & 1) ? bf_hi(scv[q >> 1]) : bf_lo(scv[q >> 1]));
        gb[q] = -LN2 * ((q & 1) ? bf_hi(gbv[q >> 1]) : bf_lo(gbv[q >> 1]));
        rx[0][q] = inv * ((q & 1) ? bf_hi(res[0][q >> 1]) : bf_lo(res[0][q >> 1]));
        rx[1][q] = inv * ((q & 1) ? bf_hi(res[1][q >> 1]) : bf_lo(res[1][q >> 1]));
        PIN(gb[q]); PIN(rx[0][q]); PIN(rx[1][q]);
      }
      if constexpr (X == 92) BAR();
      if constexpr (X >= 93 && X < 97) { pr[2 * (X - 93)] = *(LAS f32x4*)(redr + (X - 93) * 8192); pr[2 * (X - 93) + 1] = *(LAS f32x4*)(redr + (X - 93) * 8192 + 1024); }
      if constexpr (X >= 98 && X < 106) {
        constexpr int q = X - 98;
        float t = (pr[q >> 2][q & 3] + pr[2 + (q >> 2)][q & 3]);
        PIN(t);
        float t2_ = (pr[4 + (q >> 2)][q & 3] + pr[6 + (q >> 2)][q & 3]);
        PIN(t2_);
        rx[0][q] = __builtin_fmaf(gb[q], t + t2_, rx[0][q]);
        PIN(rx[0][q]);
      }
      if constexpr (X == 106)
        *(GAS v4u*)((GAS unsigned char*)out + orow) = v4u{pack2(rx[0][0], rx[0][1]), pack2(rx[0][2], rx[0][3]), pack2(rx[0][4], rx[0][5]), pack2(rx[0][6], rx[0][7])};
      if constexpr (X == 109) asm volatile("s_nop 9" : "+v"(d[1][0]));
      if constexpr (X >= 109) REDW(1, X - 109);
      SB();
    });
    if constexpr (G == 6) STAMP(14);
  });
  STAMP(19);
  REDW(1, 3);
  mfma_done(d[1][1]);
#pragma unroll
  for (int q = 4; q < 8; ++q) REDW(1, q);
  BAR();
  {
    f32x4 s0 = {}, s1 = {};
#pragma unroll
    for (int k = 0; k < 4; ++k) { s0 += *(LAS f32x4*)(redr + k * 8192 + 32768); s1 += *(LAS f32x4*)(redr + k * 8192 + 32768 + 1024); }
    float y[8];
#pragma unroll
    for (int q = 0; q < 8; ++q) y[q] = __builtin_fmaf(gb[q], q < 4 ? s0[q & 3] : s1[q & 3], rx[1][q]);
    *(GAS v4u*)((GAS unsigned char*)out + orow + 65536) = v4u{pack2(y[0], y[1]), pack2(y[2], y[3]), pack2(y[4], y[5]), pack2(y[6], y[7])};
  }
  if (b == 0 && tid == 0) __hip_atomic_store((GAS unsigned*)ws, epoch + 1u, __ATOMIC_RELAXED, __HIP_MEMORY_SCOPE_AGENT);
  STAMP(15);
}

extern "C" void kernel_launch(void* const* d_in, const int* in_sizes, int n_in, void* d_out, int out_size, void* d_ws, size_t ws_size,
                              hipStream_t stream) {
  (void)in_sizes; (void)n_in; (void)out_size; (void)ws_size;
  (void)hipFuncSetAttribute((const void*)s4_block_kernel, hipFuncAttributeMaxDynamicSharedMemorySize, LDS_BYTES);
  s4_block_kernel<<<256, 256, LDS_BYTES, stream>>>((cptr)d_in[0], (cptr)d_in[1], (cptr)d_in[2], (cptr)d_in[3], (cptr)d_in[4], (cptr)d_in[5], (cptr)d_in[6],
                                                   (cptr)d_in[7], (cptr)d_in[8], (cptr)d_in[10], (cptr)d_in[11], (cptr)d_in[12], (cptr)d_in[13],
                                                   (unsigned char*)d_out, (unsigned char*)d_ws, (cptr)d_in[9]);
}
```

```cpp
#define PROLOG
#define ECHO 2
#include <hip/hip_runtime.h>

#define GAS __attribute__((address_space(1)))
#define LAS __attribute__((address_space(3)))
#define DI __device__ __forceinline__
typedef unsigned v4u __attribute__((ext_vector_type(4)));
typedef unsigned v2u __attribute__((ext_vector_type(2)));
typedef float f32x2 __attribute__((ext_vector_type(2)));
typedef float f32x4 __attribute__((ext_vector_type(4)));
typedef float f32x16 __attribute__((ext_vector_type(16)));
typedef __bf16 bf16x2 __attribute__((ext_vector_type(2)));
typedef __bf16 bf16x8 __attribute__((ext_vector_type(8)));
typedef short s16x4 __attribute__((ext_vector_type(4)));
typedef unsigned short u16;
typedef unsigned long long u64;

constexpr int SET_BASE = 4096, SET_STRIDE = 16 << 20;
constexpr int OFF_MT = 0;
constexpr int OFF_SG = 32768;
constexpr int OFF_MG = 65536;
constexpr int OFF_SX = 524288;
constexpr int OFF_XG = 1 << 20;
constexpr int OFF_Z = OFF_XG + (4 << 20);
constexpr int OFF_HID = OFF_Z + 2097152;
constexpr int OFF_CK = OFF_HID + 7340032;
constexpr int SET_BYTES = OFF_CK + 262144;
static_assert(SET_BYTES <= SET_STRIDE && OFF_MG + 393216 <= OFF_SX, "set size");

constexpr int L_TS = 0, L_TB = 1024, L_TG = 2048;
constexpr int L_UVT = 3072;
constexpr int L_M = 4096;
constexpr int L_UVS = L_M;
constexpr int UVS_ROW = 544, UVS_CH = 57 * UVS_ROW;
constexpr int L_KC = L_M;
constexpr int KC_STRIDE = 4160;
constexpr int L_H = L_KC + 8 * KC_STRIDE;
constexpr int H_STRIDE = 4576;
constexpr int L_XB = L_H + 2 * H_STRIDE;
constexpr int L_RX = L_XB + 8192;
constexpr int L_R = L_M + 65536;
constexpr int R_WAVE = 22528;
constexpr int L_TOP = L_R + 4 * R_WAVE;
constexpr int L_KT = L_TOP + 16;
constexpr int L_STAT = L_TOP;
constexpr int L_ETA = L_RX + 12288;
constexpr int L_XR = L_M;
constexpr int L_SP = L_M + 131072;
constexpr int LDS_BYTES = 163840;
static_assert(L_TOP + 4096 <= LDS_BYTES && L_ETA + 16 <= L_R && L_UVS + 2 * UVS_CH <= L_R && L_SP + 24576 <= L_TOP, "LDS map");
static_assert(L_H % 16 == 0 && L_XB % 16 == 0 && L_RX % 16 == 0 && L_KT % 16 == 0, "alignment");

#define ZLAG 30
#define MTK 124
constexpr int SXL = 11, EO = 26;
constexpr int CODE_BYTES = 44032;
constexpr int SC1 = 16;
constexpr int OOB = 0x7FFFFF00;
constexpr unsigned SPIN_CAP = 1u << 15;
constexpr unsigned POISON = 0xFFFFFFFFu;
constexpr float LOG2E = 1.4426950408889634f, LN2 = 0.6931471805599453f, INV2PI = 0.15915494309189535f;

#define SB() __builtin_amdgcn_sched_barrier(0)
#define WAITV(n) asm volatile("s_waitcnt vmcnt(" #n ")" ::: "memory")
#define PIN(x) asm volatile("" : "+v"(x))
#define FRESH() asm volatile("" ::: "memory")
#define PAUSE() asm volatile("s_sleep 4" ::: "memory")
#define BAR() do { asm volatile("s_waitcnt lgkmcnt(0)" ::: "memory"); __builtin_amdgcn_s_barrier(); asm volatile("" ::: "memory"); } while (0)
#define STAMP(i) do { if ((i) == 0 || (i) == ECHO || (i) == 15) { SB(); u64 t_; asm volatile("s_memrealtime %0\n\ts_waitcnt lgkmcnt(0)" : "=s"(t_) :: "memory"); if ((i) == 0) echo0 = (unsigned)t_; if ((i) == ECHO) echo1 = (unsigned)t_; if ((i) == 15) { while ((unsigned)__builtin_amdgcn_s_memrealtime() - (unsigned)t_ < echo1 - echo0) __builtin_amdgcn_s_sleep(1); } SB(); } } while (0)
#define SARG
#define SPASS
#define COUNT(i)

#define STAMPA(i) STAMP(i)
#define STAMPB(i)
DI float u2f(unsigned u) { return __builtin_bit_cast(float, u); }
DI unsigned f2u(float f) { return __builtin_bit_cast(unsigned, f); }
DI float dot2(unsigned u, float c) { const bf16x2 v = __builtin_bit_cast(bf16x2, u); return __builtin_amdgcn_fdot2_f32_bf16(v, v, c, false); }
DI float bf_lo(unsigned u) { return __builtin_bit_cast(float, u << 16); }
DI float bf_hi(unsigned u) { return __builtin_bit_cast(float, u & 0xFFFF0000u); }
DI unsigned pack2(float a, float b) { f32x2 v = {a, b}; return __builtin_bit_cast(unsigned, __builtin_convertvector(v, bf16x2)); }
DI float rbf(float a) { return bf_lo(pack2(a, a)); }
DI unsigned sel(bool c, unsigned a, unsigned b) { asm("" : "+v"(a), "+v"(b)); return c ? a : b; }
DI unsigned guard(unsigned u) { return u < 0xFFFFFFFEu ? u : 0xFFFFFFFEu; }
DI unsigned max4(unsigned m, v4u v) {
  asm("v_max3_u32 %0, %0, %1, %2\n\tv_max3_u32 %0, %0, %3, %4" : "+v"(m) : "v"(v.x), "v"(v.y), "v"(v.z), "v"(v.w));
  return m;
}
DI f32x4 mfma16(v4u a, v4u b, f32x4 c) { return __builtin_amdgcn_mfma_f32_16x16x32_bf16(__builtin_bit_cast(bf16x8, a), __builtin_bit_cast(bf16x8, b), c, 0, 0, 0); }
DI f32x16 mfma32(v4u a, v4u b, f32x16 c) { return __builtin_amdgcn_mfma_f32_32x32x16_bf16(__builtin_bit_cast(bf16x8, a), __builtin_bit_cast(bf16x8, b), c, 0, 0, 0); }
template <bool FIRST> DI void mfma_av(f32x16& acc, const v4u& a, const v4u& b) {
  if (FIRST) asm volatile("v_mfma_f32_32x32x16_bf16 %0, %1, %2, 0" : "=&v"(acc) : "a"(a), "v"(b));
  else asm volatile("v_mfma_f32_32x32x16_bf16 %0, %1, %2, %0" : "+v"(acc) : "a"(a), "v"(b));
}
template <bool FIRST> DI void mfma_va(f32x16& acc, const v4u& a, const v4u& b) {
  if (FIRST) asm volatile("v_mfma_f32_32x32x16_bf16 %0, %1, %2, 0" : "=&v"(acc) : "v"(a), "a"(b));
  else asm volatile("v_mfma_f32_32x32x16_bf16 %0, %1, %2, %0" : "+v"(acc) : "v"(a), "a"(b));
}
template <bool FIRST> DI void mfma16_v(f32x4& acc, const v4u& a, const v4u& b) {
  if (FIRST) asm volatile("v_mfma_f32_16x16x32_bf16 %0, %1, %2, 0" : "=&v"(acc) : "v"(a), "v"(b));
  else asm volatile("v_mfma_f32_16x16x32_bf16 %0, %1, %2, %0" : "+v"(acc) : "v"(a), "v"(b));
}
DI void mfma_done(f32x16& acc) { asm volatile("s_nop 15" : "+v"(acc)); }
DI v2u trd(LAS unsigned char* p) { return __builtin_bit_cast(v2u, __builtin_amdgcn_ds_read_tr16_b64_v4i16((LAS s16x4*)p)); }
DI v4u trd2(LAS unsigned char* p0, LAS unsigned char* p1) { const v2u a = trd(p0), b = trd(p1); return v4u{a.x, a.y, b.x, b.y}; }
DI v4u lds128(LAS unsigned char* p) { return *(LAS v4u*)p; }
DI v4u ldg(__amdgpu_buffer_rsrc_t r, int voff, int soff) { return __builtin_amdgcn_raw_buffer_load_b128(r, voff, soff, SC1); }
DI void stg(__amdgpu_buffer_rsrc_t r, int voff, int soff, v4u v) {
  __builtin_amdgcn_raw_buffer_store_b128(v, r, voff, soff, SC1);
  asm volatile("s_nop 1" :: "v"(v) : "memory");
}
template <int CTRL> DI float dpp_add(float v) {
  return v + __builtin_bit_cast(float, __builtin_amdgcn_update_dpp(0, __builtin_bit_cast(int, v), CTRL, 0xF, 0xF, true));
}
DI float swap16_add(float v) { const unsigned u = __builtin_bit_cast(unsigned, v); const auto r = __builtin_amdgcn_permlane16_swap(u, u, false, false);
  return u2f(r[0]) + u2f(r[1]); }
DI float swap32_add(float v) { const unsigned u = __builtin_bit_cast(unsigned, v); const auto r = __builtin_amdgcn_permlane32_swap(u, u, false, false);
  return u2f(r[0]) + u2f(r[1]); }
DI float quad_sum(float v) { v = dpp_add<0xB1>(v); return dpp_add<0x4E>(v); }
DI float row_sum(float v) { return dpp_add<0x140>(dpp_add<0x141>(quad_sum(v))); }
DI float wave_sum(float v) { return swap32_add(swap16_add(row_sum(v))); }
DI f32x2 cmul(f32x2 a, f32x2 e, f32x2 ep) { const f32x2 t = f32x2{a.x, a.x} * e; return __builtin_elementwise_fma(f32x2{a.y, a.y}, ep, t); }
DI f32x2 cexp(float re2, float rev) {
  const float m = __builtin_amdgcn_exp2f(re2), f = __builtin_amdgcn_fractf(rev);
  return f32x2{m * __builtin_amdgcn_cosf(f), m * __builtin_amdgcn_sinf(f)};
}
DI void dma_w2(const unsigned char* sbase, unsigned v0, unsigned v1, unsigned lds_dst) {
  asm volatile("s_mov_b32 m0, %3\n\ts_nop 0\n\tglobal_load_lds_dwordx4 %0, %2\n\tglobal_load_lds_dwordx4 %1, %2 offset:1024"
               :: "v"(v0), "v"(v1), "s"(sbase), "s"(lds_dst) : "memory");
}
DI void dma_w1(const unsigned char* sbase, unsigned v, unsigned lds_dst) {
  asm volatile("s_mov_b32 m0, %2\n\ts_nop 0\n\tglobal_load_lds_dwordx4 %0, %1" :: "v"(v), "s"(sbase), "s"(lds_dst) : "memory");
}
DI void dma_x1(const unsigned char* sbase, unsigned v, unsigned lds_dst) {
  asm volatile("s_mov_b32 m0, %2\n\ts_nop 0\n\tglobal_load_lds_dwordx4 %0, %1 sc1" :: "v"(v), "s"(sbase), "s"(lds_dst) : "memory");
}
DI void set_m0(unsigned lds_dst) { asm volatile("s_mov_b32 m0, %0\n\ts_nop 0" :: "s"(lds_dst) : "memory"); }
template <int OFF> DI void dma_xn(const unsigned char* sbase, unsigned v) {
  asm volatile("global_load_lds_dwordx4 %0, %1 offset:%2 sc1" :: "v"(v), "s"(sbase), "n"(OFF) : "memory");
}
DI void dma_x2(const unsigned char* sbase, unsigned v, unsigned lds_dst) {
  asm volatile("s_mov_b32 m0, %2\n\ts_nop 0\n\tglobal_load_lds_dwordx4 %0, %1 sc1\n\tglobal_load_lds_dwordx4 %0, %1 offset:1024 sc1"
               :: "v"(v), "s"(sbase), "s"(lds_dst) : "memory");
}
DI void dma_x4(const unsigned char* sbase, unsigned v, unsigned lds_dst) {
  asm volatile("s_mov_b32 m0, %2\n\ts_nop 0\n\tglobal_load_lds_dwordx4 %0, %1 sc1\n\tglobal_load_lds_dwordx4 %0, %1 offset:1024 sc1\n\t"
               "global_load_lds_dwordx4 %0, %1 offset:2048 sc1\n\tglobal_load_lds_dwordx4 %0, %1 offset:3072 sc1"
               :: "v"(v), "s"(sbase), "s"(lds_dst) : "memory");
}
template <int I> struct IC { static constexpr int v = I; };
template <int I, int N, class F> DI void static_for(F&& f) { if constexpr (I < N) { f(IC<I>{}); static_for<I + 1, N>(f); } }
DI unsigned now() { u64 t; asm volatile("s_memrealtime %0\n\ts_waitcnt lgkmcnt(0)" : "=s"(t) :: "memory"); return (unsigned)t; }
template <int N> DI void waitv() { asm volatile("s_waitcnt vmcnt(%0)" :: "n"(N) : "memory"); }
struct ConvOrder { int t[36], u[36]; };
constexpr ConvOrder conv_order() {
  ConvOrder o = {};
  int done[8] = {}, last = -1;
  for (int k = 0; k < 36; ++k) {
    int best = -1;
    for (int t = 0; t < 8; ++t)
      if (t != last && done[t] <= t && (best < 0 || t - done[t] > best - done[best])) best = t;
    o.t[k] = best; o.u[k] = done[best]++; last = best;
  }
  return o;
}
constexpr ConvOrder conv_order_last() {
  ConvOrder o = {};
  int k = 0;
  for (int p = 0; p < 4; ++p)
    for (int u = 0; u <= 2 * p + 1; ++u) {
      o.t[k] = 2 * p + 1; o.u[k] = u; ++k;
      if (u <= 2 * p) { o.t[k] = 2 * p; o.u[k] = u; ++k; }
    }
  return o;
}
constexpr int epi_slot(int v) { return v < 20 ? 2 * EO + v : v < 32 ? 2 * EO + 20 + 2 * (v - 20) : 2 * 50 + (v - 32); }
constexpr int epi_dist(int v) { return v >= 20 && v < 32 ? 2 : 1; }
constexpr int hl_upto(int i) { return i < 4 ? 25 + (5 * i + 1) / 2 : i < 8 ? 35 : i < 36 ? 35 + 5 * (i - 8) / 4 : 70; }
DI unsigned tagbad(unsigned bad, v4u g, unsigned tagA, unsigned tagB) {
  return __builtin_amdgcn_bitop3_b32(__builtin_amdgcn_bitop3_b32(bad, g.y, tagA, 0xF6), g.w, tagB, 0xF6);
}
template <int R0, int R1, int R2, int R3> DI void put4(unsigned base, f32x2 a, f32x2 b, f32x2 c, f32x2 d) {
  asm volatile("s_mov_b32 m0, %4\n\ts_nop 0\n\tds_write_addtid_b32 %0 offset:%5\n\tds_write_addtid_b32 %1 offset:%6\n\t"
               "ds_write_addtid_b32 %2 offset:%7\n\tds_write_addtid_b32 %3 offset:%8"
               :: "v"(pack2(a.x, a.y)), "v"(pack2(b.x, b.y)), "v"(pack2(c.x, c.y)), "v"(pack2(d.x, d.y)), "s"(base),
                  "n"(R0 * UVS_ROW), "n"(R1 * UVS_ROW), "n"(R2 * UVS_ROW), "n"(R3 * UVS_ROW) : "memory");
}
DI unsigned sum4(unsigned m, v4u v) {
  asm("v_add3_u32 %0, %0, %1, %2\n\tv_add3_u32 %0, %0, %3, %4" : "+v"(m) : "v"(v.x), "v"(v.y), "v"(v.z), "v"(v.w));
  return m;
}
DI unsigned sum8(unsigned m, v4u a, v4u b) {
  asm("v_add3_u32 %0, %0, %1, %2\n\tv_add3_u32 %0, %0, %3, %4\n\tv_add3_u32 %0, %0, %5, %6\n\tv_add3_u32 %0, %0, %7, %8"
      : "+v"(m) : "v"(a.x), "v"(a.y), "v"(a.z), "v"(a.w), "v"(b.x), "v"(b.y), "v"(b.z), "v"(b.w));
  return m;
}
template <int I> DI unsigned red_step(unsigned v) {
  constexpr int ctrl[6] = {0xB1, 0x4E, 0x141, 0x140, 0x142, 0x143}, rows[6] = {0xF, 0xF, 0xF, 0xF, 0xA, 0xC};
  return v + (unsigned)__builtin_amdgcn_update_dpp(0, (int)v, ctrl[I], rows[I], 0xF, true);
}
DI unsigned red_all(unsigned v) { return red_step<5>(red_step<4>(red_step<3>(red_step<2>(red_step<1>(red_step<0>(v)))))); }
DI void swap_add32(float& a, float b) { const auto r = __builtin_amdgcn_permlane32_swap(f2u(a), f2u(b), false, false); a = u2f(r[0]) + u2f(r[1]); }
DI void swap_add16(float& a, float b) { const auto r = __builtin_amdgcn_permlane16_swap(f2u(a), f2u(b), false, false); a = u2f(r[0]) + u2f(r[1]); }

__global__ void __launch_bounds__(256, 1)
s4_block_kernel(const unsigned char* __restrict__ xin, const u16* __restrict__ pAre, const u16* __restrict__ pAim, const u16* __restrict__ pBre,
                const u16* __restrict__ pBim, const u16* __restrict__ pCre, const u16* __restrict__ pCim, const u16* __restrict__ pldt,
                const u16* __restrict__ pD, const unsigned char* __restrict__ w1, const unsigned char* __restrict__ w2, const u16* __restrict__ pc,
                const unsigned char* __restrict__ wmod, unsigned char* __restrict__ out, unsigned char* ws) {
  LAS unsigned char* const L = (LAS unsigned char*)16u - 16;
  const int tid = threadIdx.x, lane = tid & 63, w = __builtin_amdgcn_readfirstlane(tid >> 6);
  const int b = blockIdx.x, xcd = b & 7, slot = b >> 3, id = xcd * 32 + slot;
  const int ks = (id >> 6) * 4 + w, cc = id & 63;
  const int rp = 4 * (xcd >> 1) + (slot >> 3), rg = xcd & 1, dt = slot & 7, si = 8 * rg + dt;
  const int ct = 7 * rg + min(dt, 6);
  const int rot = (w & 1) ? 3 - 3 * rg : 4 * rg;
  unsigned echo0 = 0, echo1 = 0;
  STAMP(0);

  const unsigned char* pcode;
  asm volatile("s_getpc_b64 %0" : "=s"(pcode));
  const unsigned warm = *(const GAS unsigned*)((const GAS unsigned char*)pcode + (unsigned)min((((slot & 7) * 4 + w) * 11 + min(lane, 10)) * 128, CODE_BYTES));
  const int g16 = lane >> 4, q4 = (lane & 15) >> 2, p4 = lane & 3, hf = lane >> 5, blk = (lane >> 4) & 1, l16 = lane * 16;
  const unsigned lrw = L_R + w * R_WAVE;
  unsigned wmo[3], co = (ks * 32 + (lane & 31)) * 2, xo = id * 8192 + (tid & 3) * 2048 + (tid >> 2) * 16, ro = id * 8192 + w * 2048 + hf * 1024 + (lane & 31) * 16, po = id * 512 + tid * 2;
  unsigned wto = id * 7168 + (w >> 1) * 3584 + min(lane, 27) * 128;
#pragma unroll
  for (int i = 0; i < 3; ++i) { const unsigned idx = lane + 64 * i; wmo[i] = (ks * 32 * 6144 + cc * 96 - i * 1024) + idx / 6 * (6144 - 96) + idx * 16; PIN(wmo[i]); }
  PIN(co); PIN(xo); PIN(ro); PIN(po); PIN(wto);
  set_m0(lrw);
  SB();
  STAMPA(24);
  asm volatile("" :: "s"(ws), "s"(xin), "s"(wmod), "s"(pCim));
  STAMPA(25);
  const unsigned epoch_v = __hip_atomic_load((GAS unsigned*)ws, __ATOMIC_RELAXED, __HIP_MEMORY_SCOPE_AGENT);
  asm volatile("global_load_lds_dwordx4 %0, %3\n\tglobal_load_lds_dwordx4 %1, %3 offset:1024\n\tglobal_load_lds_dwordx4 %2, %3 offset:2048"
               :: "v"(wmo[0]), "v"(wmo[1]), "v"(wmo[2]), "s"(wmod) : "memory");
  const unsigned cval = *(const GAS u16*)((const GAS unsigned char*)pc + co);
  SB();
  const v4u xa = *(const GAS v4u*)((const GAS unsigned char*)xin + xo), xb = *(const GAS v4u*)((const GAS unsigned char*)xin + 1024 + xo);
  const v4u ra = *(const GAS v4u*)((const GAS unsigned char*)xin + ro), rb = *(const GAS v4u*)((const GAS unsigned char*)xin + 512 + ro);
  SB();
#define PARAM(p) (unsigned)*(const GAS u16*)((const GAS unsigned char*)(p) + po)
  const unsigned uAre = PARAM(pAre), uAim = PARAM(pAim), uBre = PARAM(pBre), uBim = PARAM(pBim), uCre = PARAM(pCre), uCim = PARAM(pCim);
#undef PARAM
  SB();
  const unsigned wt = *(const GAS unsigned*)((const GAS unsigned char*)(w & 1 ? w2 : w1) + wto);
  unsigned uldt, uD;
  asm volatile("s_load_dword %0, %2, %4\n\ts_load_dword %1, %3, %4" : "=&s"(uldt), "=&s"(uD) : "s"(pldt), "s"(pD), "s"(id * 4));
  SB();
  STAMPA(26);
  asm volatile("" : "+s"(out), "+s"(w1), "+s"(w2));
  LAS unsigned char* koff[12];
  {
    float fj = (float)(8 * tid - 3);
    PIN(fj);
    const unsigned lin = (unsigned)(8 * tid - 3 + 0x4300 - 128) * 0x10001u + 0x10000u;
#pragma unroll
    for (int i = 0; i < 12; i += 2) {
      const unsigned r = sel(tid < 32, lin + i * 0x10001u, pack2(fj + (float)i, fj + (float)(i + 1)));
      koff[i] = L + ((r & 0xFFFFu) * 4 + (L_KT + (128 - 0x4300) * 4));
      koff[i + 1] = L + ((r >> 16) * 4 + (L_KT + (128 - 0x4300) * 4));
      PIN(koff[i]);
      if (i < 10) PIN(koff[i + 1]);
    }
  }
  *(LAS v4u*)(L + L_KT - 16) = v4u{0u, 0u, 0u, 0u};
  LAS unsigned char* ktw[4];
  {
    const int row = w * 16 + (lane & 15), sg = max((row >> 3) - 1, 0), rr_ = row - 8 * sg;
    const int idx0 = 128 * sg + ((rr_ >> (3 - sg)) << (7 - sg)) + (rr_ & ((8 >> sg) - 1)) + 1, st = 8 >> sg;
#pragma unroll
    for (int r = 0; r < 4; ++r) {
      const int idx = sel(row < 40, idx0 + st * (4 * g16 + r), tid == 136 && r == 0 ? 0 : 641 + lane);
      ktw[r] = L + L_KT + idx * 4;
      PIN(ktw[r]);
    }
  }
  const unsigned char* w2lo = w2 + (448 * w + 64 * rot) * 1024 + dt * 128;
  const unsigned char* w2c3 = rot == 4 ? w2lo - 448 * 1024 : w2lo;
  const unsigned char* w2hi = rot ? w2lo - 448 * 1024 : w2lo;
  unsigned w2v0 = (lane >> 3) * 1024 + (((lane & 7) ^ (blk << 2)) * 16), w2v1 = w2v0 + 7168;
  const unsigned char* w1w = w1 + ct * 256 + w * 64;
  unsigned w1v0 = (lane >> 2) * 3584 + p4 * 16, w1v1 = w1v0 + 57344 - 1024;
  const int zs = OFF_Z + (2 * id + (w >> 1)) * 64;
  int za = (((w & 1) + 2 * (g16 & 1)) * 8 + q4 * 2 + (g16 >> 1)) * 32768 + p4 * 16, zb = za + 32 * 32768;
  int tra = (hf * 8 + q4) * 64 + blk * 32 + p4 * 8;
  LAS unsigned char* w1a = L + lrw + tra;
  LAS unsigned char* ka = L + L_KC + ((w >> 1) * 4 + q4) * KC_STRIDE + 2 * (32 - 8 * g16 + 4 * p4) - 8 + 256 * (w & 1);
  LAS unsigned char* hb = L + L_H + (w >> 1) * H_STRIDE + (lane & 15) * 32 + g16 * 16 + 256 - 256 * (w & 1);
  LAS unsigned char* zp = L + sel(tid < 64, L_KC + ((lane >> 1) & 7) * KC_STRIDE + (lane & 1) * 16, L_H + hf * H_STRIDE + min(lane & 31, 29) * 16);
  LAS unsigned char* ub = L + L_UVS + (lane & 15) * UVS_ROW + g16 * 16;
  int gmo = OFF_MG + (g16 * 256 + id) * 256 + (lane & 15) * 16, gmo2 = OFF_MG + ((4 + blk) * 256 + id) * 256 + (lane & 15) * 16;
  int mgo = sel(lane < 48, (lane >> 1) * 256 + (lane & 1) * 8, OOB), sgo = sel(lane & 31, OOB, hf * 8);
  LAS unsigned char* xbw = L + L_XB + (tid >> 7) * 2048 + (tid & 127) * 4;
  LAS unsigned char* xbr = L + L_XB + w * 2048 + (lane & 15) * 32 + g16 * 8;
  LAS unsigned char *exa = L + L_XB + w * 2048 + l16, *exb = L + L_RX + w * 3072 + l16, *eya = L + L_XB + (w ^ 1) * 2048 + l16, *eyb = L + L_RX + (w ^ 1) * 3072 + l16;
  PIN(xbw); PIN(xbr); PIN(exa); PIN(exb); PIN(eya); PIN(eyb);
  int mto = (tid >> 1) * 128 + (tid & 1) * 16;
  LAS unsigned char* mtw = L + (tid & 1) * L_TG + (tid >> 1) * 4;
  PIN(mto); PIN(mtw);
  PIN(w2v0); PIN(w2v1); PIN(w1v0); PIN(w1v1); PIN(za); PIN(zb); PIN(tra); PIN(w1a); PIN(ka); PIN(hb); PIN(zp); PIN(ub); PIN(gmo); PIN(gmo2); PIN(mgo); PIN(sgo);
#define W1DMA(step, lds) dma_w1(w1w + (step) * 57344, w1v0, (lds))
#define W1DMA2(step, lds) dma_w2(w1w + (step) * 57344, w1v0, w1v1, (lds))
#define W2DMA(step, lds) dma_w2(((step) < 12 ? w2lo : (step) < 16 ? w2c3 : w2hi) + (step) * 16384, w2v0, w2v1, (lds))
  SB();
  STAMPA(27);
  asm volatile("s_waitcnt lgkmcnt(0)" : "+s"(uldt), "+s"(uD));
  const unsigned epoch = __builtin_amdgcn_readfirstlane(epoch_v);
  asm volatile("" :: "s"(epoch));
  STAMPA(28);
  const unsigned char* setp = ws + SET_BASE + (size_t)(epoch & 3u) * SET_STRIDE;
  const __amdgpu_buffer_rsrc_t rs = __builtin_amdgcn_make_buffer_rsrc((void*)setp, 0, SET_BYTES, 0x00020000);
  const unsigned tagA = epoch * 0x9E3779B1u + 0x7F4A7C15u, tagB = ~tagA;
  {
    const float c = bf_lo(cval);
    const float s = c * __builtin_amdgcn_rcpf(1.0f + __builtin_amdgcn_exp2f(-LOG2E * c));
    *(LAS u16*)(L + lrw + 3072 + (lane & 31) * 2) = (u16)pack2(s, s);
    SB();
#define XPUB(i) do { stg(rs, (tid >> 2) * 65536 + (tid & 3) * 16, OFF_XG + (i) * 16384 + id * 64, v4u{xa[i], tagA, xb[i], tagB}); SB(); } while (0)
    asm volatile("" :: "v"(xb));
    STAMPA(29);
    XPUB(0);
    XPUB(1);
    STAMP(1);
    LAS unsigned char* a = L + lrw + (8 * g16 + q4) * 96 + p4 * 8;
    const v4u sc = lds128(L + lrw + 3072 + g16 * 16);
    const v4u m0 = trd2(a, a + 384), m1 = trd2(a + 32, a + 416), m2 = trd2(a + 64, a + 448);
    SB();
    f32x2 fa[8];
#pragma unroll
    for (int i = 0; i < 4; ++i) { fa[i] = f32x2{bf_lo(ra[i]), bf_hi(ra[i])}; fa[4 + i] = f32x2{bf_lo(rb[i]), bf_hi(rb[i])}; }
    const f32x2 s2 = ((fa[0] + fa[1]) + (fa[2] + fa[3])) + ((fa[4] + fa[5]) + (fa[6] + fa[7]));
    float mean = s2.x + s2.y;
    PIN(mean);
    SB();
    const f32x4 c0 = mfma16(sc, m0, f32x4{}), c1 = mfma16(sc, m1, f32x4{}), c2 = mfma16(sc, m2, f32x4{});
    SB();
    mean = swap16_add(row_sum(mean)) * (1.0f / 512.0f);
    PIN(mean);
    SB();
    XPUB(2);
    __builtin_amdgcn_raw_buffer_store_b64(v2u{sel(lane < 16, f2u(c0[0]), sel(lane < 32, f2u(c1[0]), f2u(c2[0]))), (lane & 1) ? tagB : tagA}, rs, mgo,
                                          OFF_MG + cc * 24 * 256 + ks * 16, SC1);
    SB();
    f32x2 q2 = {0.f, 0.f}, q3 = {0.f, 0.f};
#pragma unroll
    for (int i = 0; i < 8; i += 2) {
      const f32x2 d0 = fa[i] - f32x2{mean, mean}, d1 = fa[i + 1] - f32x2{mean, mean};
      q2 = __builtin_elementwise_fma(d0, d0, q2);
      q3 = __builtin_elementwise_fma(d1, d1, q3);
    }
    q2 += q3;
    const float var = rbf(rbf(swap16_add(row_sum(q2.x + q2.y)) * (1.0f / 512.0f)) + 1e-5f);
    __builtin_amdgcn_raw_buffer_store_b64(v2u{pack2(mean, __builtin_amdgcn_sqrtf(var)), hf ? tagB : tagA}, rs, sgo, OFF_SG + (id * 4 + w) * 16, SC1);
    SB();
    XPUB(3);
#undef XPUB
  }
  STAMP(2);
  unsigned dsk = tid == 136 ? uD : 0u;
  PIN(dsk);

  {
    const float dtv = rbf(__builtin_amdgcn_exp2f(LOG2E * (w >> 1 ? bf_hi(uldt) : bf_lo(uldt))));
    const float are = dtv * bf_lo(uAre) * LOG2E, aim = dtv * bf_lo(uAim) * INV2PI;
    const float br = dtv * bf_lo(uBre), bi = dtv * bf_lo(uBim), cr = bf_lo(uCre), ci = bf_lo(uCim);
    const f32x2 wv = {cr * br - ci * bi, cr * bi + ci * br};
    const unsigned row = L_UVS + (w >> 1) * UVS_CH + (w & 1) * 256;
    const f32x2 e1 = cexp(are, aim), e8 = cexp(8.f * are, -8.f * aim), e128 = cexp(128.f * are, 128.f * aim);
    const f32x2 e1p = {-e1.y, e1.x}, e8p = {-e8.y, e8.x}, e128p = {-e128.y, e128.x};
    const v4u pois = {POISON, POISON, POISON, POISON};
    f32x2 z[8], v, v2, y, y2;
#define M1(a) cmul(a, e1, e1p)
#define M8(a) cmul(a, e8, e8p)
#define MK(a) cmul(a, e128, e128p)
    z[0] = M1(wv);
    put4<40, 41, 42, 0>(row, wv, f32x2{1.f, 0.f}, e8, z[0]);
    W1DMA2(0, lrw + 0);
    v = M8(e8); z[1] = M1(z[0]); v2 = M8(v); z[2] = M1(z[1]);
    put4<43, 1, 44, 2>(row, v, z[1], v2, z[2]);
    v = M8(v2); z[3] = M1(z[2]); v2 = M8(v); z[4] = M1(z[3]);
    put4<45, 3, 46, 4>(row, v, z[3], v2, z[4]);
    W1DMA2(2, lrw + 2048);
    v = M8(v2); z[5] = M1(z[4]); v2 = M8(v); z[6] = M1(z[5]);
    put4<47, 5, 48, 6>(row, v, z[5], v2, z[6]);
    z[7] = M1(z[6]); z[0] = MK(z[0]); z[1] = MK(z[1]); z[2] = MK(z[2]);
    put4<7, 8, 9, 10>(row, z[7], z[0], z[1], z[2]);
    W1DMA2(4, lrw + 4096);
    z[3] = MK(z[3]); z[4] = MK(z[4]); z[5] = MK(z[5]); z[6] = MK(z[6]);
    put4<11, 12, 13, 14>(row, z[3], z[4], z[5], z[6]);
    z[7] = MK(z[7]); z[1] = MK(z[1]); z[3] = MK(z[3]); z[5] = MK(z[5]);
    put4<15, 16, 17, 18>(row, z[7], z[1], z[3], z[5]);
    W1DMA2(6, lrw + 6144);
    z[7] = MK(z[7]); z[1] = MK(z[1]); z[3] = MK(z[3]); z[5] = MK(z[5]);
    put4<19, 20, 21, 22>(row, z[7], z[1], z[3], z[5]);
    y = MK(z[7]); z[3] = MK(z[3]); y2 = MK(y); z[4] = MK(z[3]);
    put4<23, 24, 25, 26>(row, y, z[3], y2, z[4]);
    W1DMA2(8, lrw + 8192);
    y = MK(y2); z[3] = MK(z[4]); y2 = MK(y); z[4] = MK(z[3]);
    put4<27, 28, 29, 30>(row, y, z[3], y2, z[4]);
    y = MK(y2); v = M8(v2); y2 = MK(y); v2 = M8(v);
    put4<31, 49, 32, 50>(row, y, v, y2, v2);
    W1DMA2(10, lrw + 10240);
    y = MK(y2); v = M8(v2); y2 = MK(y); v2 = M8(v);
    put4<33, 51, 34, 52>(row, y, v, y2, v2);
    y = MK(y2); v = M8(v2); y2 = MK(y); v2 = M8(v);
    put4<35, 53, 36, 54>(row, y, v, y2, v2);
    stg(rs, za, zs, pois);
    y = MK(y2); v = M8(v2); y2 = MK(y); v2 = M8(v);
    put4<37, 55, 38, 56>(row, y, v, y2, v2);
    y = MK(y2);
    *(LAS unsigned*)(L + row + 39 * UVS_ROW + lane * 4) = pack2(y.x, y.y);
    stg(rs, zb, zs, pois);
#undef M1
#undef M8
#undef MK
  }
  BAR();
  STAMP(3);
  v4u gx[4], gs[4], gm, gn;
  const int xgo = OFF_XG + id * 16384;
#define S0LOADS() do { \
    _Pragma("unroll") for (int i = 0; i < 4; ++i) gx[i] = ldg(rs, tid * 16, xgo + i * 4096); \
    _Pragma("unroll") for (int i = 0; i < 4; ++i) gs[i] = ldg(rs, tid * 16, OFF_SG + i * 4096); \
    gm = ldg(rs, gmo, 0); gn = ldg(rs, gmo2, 0); } while (0)
  if (w < 3) {
    LAS unsigned char* ut = ub + w * 16 * UVS_ROW;
    v4u fv[2][8], fu[2][8];
#pragma unroll
    for (int s = 0; s < 8; ++s)
#pragma unroll
      for (int c = 0; c < 2; ++c) { fv[c][s] = lds128(ub + c * UVS_CH + 41 * UVS_ROW + s * 64); fu[c][s] = lds128(ut + c * UVS_CH + s * 64); SB(); }
    f32x4 a0 = {}, a1 = {};
#pragma unroll
    for (int s = 0; s < 8; ++s) { a0 = mfma16(fv[0][s], fu[0][s], a0); SB(); a1 = mfma16(fv[1][s], fu[1][s], a1); SB(); }
    *(LAS unsigned*)ktw[0] = pack2(a0[0] + bf_lo(dsk), a1[0] + bf_hi(dsk));
#pragma unroll
    for (int r = 1; r < 4; ++r) *(LAS unsigned*)ktw[r] = pack2(a0[r], a1[r]);
  }
  SB();
  S0LOADS();
  SB();
  BAR();
  {
    unsigned k[11];
#pragma unroll
    for (int i = 0; i < 11; ++i) k[i] = *(LAS unsigned*)koff[i];
    SB();
    if (w < 2) *(LAS v4u*)zp = v4u{0u, 0u, 0u, 0u};
#pragma unroll
    for (int c = 0; c < 2; ++c) {
      unsigned e[5], o[5];
#pragma unroll
      for (int i = 0; i < 5; ++i) {
        o[i] = __builtin_amdgcn_perm(k[2 * i + 1], k[2 * i], c ? 0x07060302u : 0x05040100u);
        e[i] = __builtin_amdgcn_perm(k[2 * i + 2], k[2 * i + 1], c ? 0x07060302u : 0x05040100u);
      }
      LAS unsigned char* d = L + L_KC + c * 4 * KC_STRIDE + 32 + tid * 16;
      *(LAS v4u*)(d) = v4u{e[1], e[2], e[3], e[4]};
      *(LAS v4u*)(d + KC_STRIDE) = v4u{o[1], o[2], o[3], o[4]};
      *(LAS v4u*)(d + 2 * KC_STRIDE) = v4u{e[0], e[1], e[2], e[3]};
      *(LAS v4u*)(d + 3 * KC_STRIDE) = v4u{o[0], o[1], o[2], o[3]};
    }
  }
  SB();
  STAMP(4);
  {
    unsigned bad = tagbad(tagbad(0u, gm, tagA, tagB), gn, tagA, tagB);
#pragma unroll
    for (int i = 0; i < 4; ++i) bad = tagbad(tagbad(bad, gx[i], tagA, tagB), gs[i], tagA, tagB);
    if (__builtin_expect(__any(bad != 0u), 0)) {
      for (unsigned spin = 0; spin < SPIN_CAP; ++spin) {
        FRESH();
        if (spin > 1) PAUSE();
        COUNT(20);
        bool again = false;
#pragma unroll
        for (int i = 0; i < 4; ++i) {
          if (__any(tagbad(0u, gx[i], tagA, tagB) != 0u)) { gx[i] = ldg(rs, tid * 16, xgo + i * 4096); again = true; }
          if (__any(tagbad(0u, gs[i], tagA, tagB) != 0u)) { gs[i] = ldg(rs, tid * 16, OFF_SG + i * 4096); again = true; }
        }
        if (__any(tagbad(tagbad(0u, gm, tagA, tagB), gn, tagA, tagB) != 0u)) { gm = ldg(rs, gmo, 0); gn = ldg(rs, gmo2, 0); again = true; }
        if (!again) break;
      }
    }
  }
  asm volatile("" :: "v"(wt), "v"(warm));
  WAITV(0);
  STAMP(5);
  W1DMA2(12, lrw + 12288);
  float zsc;
  v4u mtp;
  {
    const int i0 = (int)pack2(row_sum(u2f(gm.x)), row_sum(u2f(gm.z))), i1 = (int)pack2(row_sum(u2f(gn.x)), row_sum(u2f(gn.z)));
    unsigned md[6];
#pragma unroll
    for (int k = 0; k < 6; ++k) md[k] = (unsigned)__builtin_amdgcn_readlane(k < 4 ? i0 : i1, 16 * (k & 3));
#define MOD(k, c) ((c) ? bf_hi(md[k]) : bf_lo(md[k]))
    float s0 = rbf(1.0f + MOD(4, 0)), s1 = rbf(1.0f + MOD(4, 1));
    s0 = s0 == 0.f ? 0x1p-30f : s0;
    s1 = s1 == 0.f ? 0x1p-30f : s1;
    const float G0 = MOD(2, 0) * s0, G1 = MOD(2, 1) * s1;
    const float A0 = rbf(1.0f + MOD(1, 0)) * G0, A1 = rbf(1.0f + MOD(1, 1)) * G1, B0 = MOD(0, 0) * G0, B1 = MOD(0, 1) * G1;
    zsc = w >> 1 ? s1 : s0;
    mtp = v4u{lane ? md[5] : pack2(s0, s1), tagA, md[3], tagB};
#undef MOD
#pragma unroll
    for (int i = 0; i < 4; ++i) {
      const float mA = bf_lo(gs[i].x), rA = __builtin_amdgcn_rcpf(bf_hi(gs[i].x)), mB = bf_lo(gs[i].z), rB = __builtin_amdgcn_rcpf(bf_hi(gs[i].z));
      const float n00 = (bf_lo(gx[i].x) - mA) * rA, n01 = (bf_hi(gx[i].x) - mA) * rA, n10 = (bf_lo(gx[i].z) - mB) * rB, n11 = (bf_hi(gx[i].z) - mB) * rB;
      const int o = (tid + 256 * i) * 4;
      W1DMA2(14 + 2 * i, lrw + (14 + 2 * i) * 1024);
      *(LAS unsigned*)(L + L_H + 480 + o) = pack2(__builtin_fmaf(n00, A0, B0), __builtin_fmaf(n10, A0, B0));
      *(LAS unsigned*)(L + L_H + H_STRIDE + 480 + o) = pack2(__builtin_fmaf(n01, A1, B1), __builtin_fmaf(n11, A1, B1));
      *(LAS unsigned*)(xbw + i * 512) = __builtin_amdgcn_perm(gx[i].z, gx[i].x, 0x05040100u);
      *(LAS unsigned*)(xbw + 4096 + i * 512) = __builtin_amdgcn_perm(gx[i].z, gx[i].x, 0x07060302u);
    }
  }
  BAR();
  const unsigned tb = now();
  STAMP(6);

  v4u w1f[32], w2f[28][2], mt0, mt1;
  {
    const bool rh = w & 1;
    constexpr ConvOrder O = conv_order(), OL = conv_order_last();
    v2u A[4][8][2], xk[4];
    v4u B[4][8];
    f32x4 acc[8], keep[4];
#pragma unroll
    for (int u = 0; u < 8; ++u) {
      A[0][u][0] = trd(ka + 512 * u + 8);
      A[0][u][1] = trd(ka + 512 * u);
      B[0][7 - u] = lds128(hb + 64 * (8 * (7 - u) + 3));
    }
    SB();
    if (lane) mtp.z = tb;
    stg(rs, tid < 2 ? tid * 16 : OOB, OFF_MT + id * 128, mtp);
    SB();
    auto hand_over = [&](auto Pc) __attribute__((always_inline)) {
      constexpr int i = Pc.v;
      PIN(acc[2 * i]); PIN(acc[2 * i + 1]);
      const f32x4 ev = acc[2 * i], od = acc[2 * i + 1];
      const f32x4 snd = rh ? ev : od, own = rh ? od : ev;
      *(LAS f32x4*)(i < 2 ? exa + i * 1024 : exb + (i - 1) * 1024) = snd;
#pragma unroll
      for (int c = 0; c < 4; ++c) { float t = keep[i][c] + own[c]; PIN(t); keep[i][c] = t; }
    };
    static_for<0, 144>([&](auto Kc) __attribute__((always_inline)) {
      constexpr int K = Kc.v, r = K / 36, k = K % 36, t = r < 3 ? O.t[k] : OL.t[k], u = r < 3 ? O.u[k] : OL.u[k];
      if constexpr (k == 0 && r > 0) asm volatile("" :: "v"(B[r][7]));
      mfma16_v<r == 0 && u == 0>(acc[t], v4u{A[r][u][0].x, A[r][u][0].y, A[r][u][1].x, A[r][u][1].y}, B[r][t - u]);
      SB();
      if constexpr (K < 12) w1f[K] = trd2(w1a + K * 1024, w1a + K * 1024 + 256);
      if constexpr (K == 60) WAITV(9);
      if constexpr (K >= 60 && K < 70) w1f[K - 48] = trd2(w1a + (K - 48) * 1024, w1a + (K - 48) * 1024 + 256);
      if constexpr (K == 76) asm volatile("" :: "a"(w1f[21]));
      if constexpr (r == 2 && k < 4) xk[k] = *(LAS v2u*)(xbr + k * 512);
      constexpr int n = k - (r == 0 ? 9 : r == 1 ? 0 : 4);
      if constexpr (r < 3 && n >= 0 && n < 24) {
        if constexpr (n % 3 == 2) B[r + 1][n / 3] = lds128(hb + 64 * (8 * (n / 3) - r + 2));
        else A[r + 1][n / 3][n % 3] = trd(ka + 512 * (n / 3) + 64 * (r + 1) + 8 - 8 * (n % 3));
      }
      if constexpr (K >= 100 && K < 116) {
        constexpr int e = K - 100;
        const unsigned xw = (e & 2) ? xk[e >> 2].y : xk[e >> 2].x;
        float t2_ = zsc * ((e & 1) ? bf_hi(xw) : bf_lo(xw));
        PIN(t2_);
        keep[e >> 2][e & 3] = t2_;
      }
      if constexpr (K == 108 + 8) hand_over(IC<0>{});
      if constexpr (K == 108 + 17) hand_over(IC<1>{});
      if constexpr (K == 108 + 30) hand_over(IC<2>{});
      if constexpr (K == 14) W1DMA(22, L_RX + w * 3072);
      if constexpr (K == 22) W1DMA(23, L_TOP + w * 1024);
      if constexpr (K >= 37 && K < 69 && K % 8 == 5) W1DMA2(24 + (K - 37) / 4, lrw + (K - 37) / 4 * 1024);
      if constexpr (K >= 69 && K < 125 && K % 8 == 5) W2DMA((K - 69) / 8, lrw + 8192 + (K - 69) / 8 * 2048);
      if constexpr (K == MTK)
        asm volatile("buffer_load_dwordx4 %0, %2, %3, 0 offen sc1\n\tbuffer_load_dwordx4 %1, %2, %3, %4 offen sc1"
                     : "=&v"(mt0), "=&v"(mt1) : "v"(mto), "s"(rs), "s"(16384) : "memory");
      SB();
    });
    STAMP(7);
    asm volatile("s_nop 7" : "+v"(acc[6]), "+v"(acc[7]));
    hand_over(IC<3>{});
    BAR();
#pragma unroll
    for (int i = 0; i < 4; ++i) keep[i] += *(LAS f32x4*)(i < 2 ? eya + i * 1024 : eyb + (i - 1) * 1024);
#pragma unroll
    for (int i = 0; i < 2; ++i) {
      const auto s0 = __builtin_amdgcn_permlane16_swap(pack2(keep[2 * i][0], keep[2 * i][1]), pack2(keep[2 * i + 1][0], keep[2 * i + 1][1]), false, false);
      const auto s1 = __builtin_amdgcn_permlane16_swap(pack2(keep[2 * i][2], keep[2 * i][3]), pack2(keep[2 * i + 1][2], keep[2 * i + 1][3]), false, false);
      stg(rs, i ? zb : za, zs, v4u{guard(s0[0]), guard(s1[0]), guard(s0[1]), guard(s1[1])});
    }
    SB();
  }
  STAMP(8);

  static_assert(OFF_MT == 0, "table offset");
  const unsigned te = now();
  asm volatile("s_waitcnt vmcnt(2)" : "+v"(mt0), "+v"(mt1) :: "memory");
  if (__builtin_expect(__any(tagbad(tagbad(0u, mt0, tagA, tagB), mt1, tagA, tagB) != 0u), 0)) {
    for (unsigned spin = 0; spin < SPIN_CAP; ++spin) {
      FRESH();
      COUNT(21);
      if (tagbad(0u, mt0, tagA, tagB)) mt0 = ldg(rs, mto, OFF_MT);
      if (tagbad(0u, mt1, tagA, tagB)) mt1 = ldg(rs, mto, OFF_MT + 16384);
      if (!__any(tagbad(tagbad(0u, mt0, tagA, tagB), mt1, tagA, tagB) != 0u)) break;
      PAUSE();
    }
  }
  *(LAS unsigned*)(mtw) = mt0.x;
  *(LAS unsigned*)(mtw + 512) = mt1.x;
  *(LAS unsigned*)(mtw + 1024) = mt0.z;
  *(LAS unsigned*)(mtw + 1536) = mt1.z;
  {
    int late = (lane & 1) ? max((int)(mt0.z - tb), (int)(mt1.z - tb)) : 0;
    late = max(late, __builtin_amdgcn_update_dpp(late, late, 0xB1, 0xF, 0xF, false));
    late = max(late, __builtin_amdgcn_update_dpp(late, late, 0x4E, 0xF, 0xF, false));
    late = max(late, __builtin_amdgcn_update_dpp(late, late, 0x141, 0xF, 0xF, false));
    late = max(late, __builtin_amdgcn_update_dpp(late, late, 0x140, 0xF, 0xF, false));
    late = max(late, __builtin_amdgcn_update_dpp(late, late, 0x142, 0xA, 0xF, false));
    late = max(late, __builtin_amdgcn_update_dpp(late, late, 0x143, 0xC, 0xF, false));
    if (lane == 63) *(LAS int*)(L + L_ETA + w * 4) = late;
  }
  w1f[22] = trd2(L + L_RX + w * 3072 + tra, L + L_RX + w * 3072 + tra + 256);
  w1f[23] = trd2(L + L_TOP + w * 1024 + tra, L + L_TOP + w * 1024 + tra + 256);
  BAR();
  STAMP(9);
  const int w2t = (4 * hf + q4) * 128 + blk * 32 + (((p4 & 1) << 1) | (p4 >> 1)) * 8;
  LAS unsigned char* w2a = L + w2t + (q4 >> 1) * 64;
  LAS unsigned char* w2b = L + w2t + ((q4 >> 1) ^ 1) * 64;
  const int par = (0x1C >> (ct % 7)) & 1;
  const unsigned char* xg = setp + OFF_Z + rp * 131072;
  const unsigned xl = L_XR + w * 8192;
#define XSRC(p) (xg + w * 8192 + ((p) ^ par) * 32768)
  const int slo = OFF_Z + (rp * 4 + w) * 32768 + si * 2048;
  f32x4 uu[4], vv[4];
  v4u sl0, sl1;
  {
    LAS unsigned char* qr = L + ((lane & 31) == 0 ? L_TS : L_TB) + hf * 16;
    LAS unsigned char *pa = w2a + lrw + 8192, *pb = w2b + lrw + 8192;
    PIN(pa); PIN(pb);
    f32x16 uv, uv2;
    v4u qa[32];
    int wait = 0;
    const v4u lt = lds128(L + L_ETA);
    bool sent = false;
    asm volatile("" : "=v"(sl0), "=v"(sl1));
#pragma unroll
    for (int i = 0; i < 4; ++i) qa[i] = lds128(qr + i * 32);
    static_for<0, 32>([&](auto Sc) __attribute__((always_inline)) {
      constexpr int s = Sc.v;
      mfma_va<(s < 2)>((s & 1) ? uv2 : uv, qa[s], w1f[s]);
      SB();
      if constexpr (s < 28) qa[s + 4] = lds128(qr + (s + 4) * 32);
      if constexpr (s > 0) asm volatile("" :: "v"(qa[s > 0 ? s - 1 : 0]));
      if constexpr (s < 8) w1f[24 + s] = trd2(w1a + s * 1024, w1a + s * 1024 + 256);
      if constexpr (s == 4) { wait = min(__builtin_amdgcn_readfirstlane(max(max((int)lt.x, (int)lt.y), max((int)lt.z, (int)lt.w))), 300) + ZLAG; }
      if constexpr (s >= 8 && s < 22) w2f[(s - 8) >> 1][s & 1] = trd2(((s & 1) ? pb : pa) + ((s - 8) >> 1) * 2048, ((s & 1) ? pb : pa) + ((s - 8) >> 1) * 2048 + 1024);
      if constexpr (s == 4 || s == 13 || s == 22 || s == 31) {
        if (!sent) {
          if constexpr (s == 31) { while ((int)(now() - te) < wait) { } }
          if (s == 31 || (int)(now() - te) >= wait) {
            sent = true;
            STAMPB(26);
            asm volatile("buffer_load_dwordx4 %0, %2, %3, %4 offen sc1\n\tbuffer_load_dwordx4 %1, %2, %3, %4 offen offset:1024 sc1"
                         :: "v"(sl0), "v"(sl1), "v"(l16), "s"(rs), "s"(slo) : "memory");
            dma_x4(XSRC(0), l16, xl);
            dma_x4(XSRC(0) + 4096, l16, xl + 4096);
          }
        }
      }
      SB();
    });
    dma_x4(XSRC(1), l16, xl + 32768);
    dma_x4(XSRC(1) + 4096, l16, xl + 32768 + 4096);
    STAMPB(27);
    BAR();
    mfma_done(uv2);
    PIN(uv);
    LAS unsigned char* uw = L + L_UVT + (32 * w + (lane & 31)) * 4;
    if (lane < 32) { *(LAS float*)(uw) = uv[0] + uv2[0]; *(LAS float*)(uw + 512) = -LOG2E * (uv[1] + uv2[1]); }
    LAS unsigned char* uvp = L + L_UVT + (32 * w + 4 * hf) * 4;
#pragma unroll
    for (int i = 0; i < 4; ++i) { uu[i] = *(LAS f32x4*)(uvp + i * 32); vv[i] = *(LAS f32x4*)(uvp + 512 + i * 32); }
  }
  {
    const float i0 = __builtin_amdgcn_rcpf(bf_lo(*(LAS u16*)(L + L_TS + (si * 32 + (lane >> 2)) * 2)));
    const float i1 = __builtin_amdgcn_rcpf(bf_lo(*(LAS u16*)(L + L_TS + (si * 32 + 16 + (lane >> 2)) * 2)));
    SB();
    asm volatile("s_waitcnt vmcnt(16)" : "+v"(sl0), "+v"(sl1) :: "memory");
    STAMPB(28);
    if (__builtin_expect(__any(max4(max4(0u, sl0), sl1) == POISON), 0)) {
      for (unsigned spin = 0; spin < SPIN_CAP; ++spin) {
        FRESH();
        if (spin > 1) PAUSE();
        COUNT(22);
        sl0 = ldg(rs, l16, slo);
        sl1 = ldg(rs, l16 + 1024, slo);
        if (!__any(max4(max4(0u, sl0), sl1) == POISON)) break;
      }
    }
    float sm[8], sq[8];
#pragma unroll
    for (int i = 0; i < 4; ++i) {
      const f32x2 a = f32x2{bf_lo(sl0[i]), bf_hi(sl0[i])} * f32x2{i0, i0}, c = f32x2{bf_lo(sl1[i]), bf_hi(sl1[i])} * f32x2{i1, i1};
      const f32x2 s2 = a + c, q2 = __builtin_elementwise_fma(a, a, c * c);
      sm[2 * i] = s2.x; sm[2 * i + 1] = s2.y; sq[2 * i] = q2.x; sq[2 * i + 1] = q2.y;
    }
#pragma unroll
    for (int i = 0; i < 4; ++i) { swap_add32(sm[i], sm[i + 4]); swap_add32(sq[i], sq[i + 4]); }
#pragma unroll
    for (int i = 0; i < 2; ++i) { swap_add16(sm[i], sm[i + 2]); swap_add16(sq[i], sq[i + 2]); }
#pragma unroll
    for (int i = 0; i < 2; ++i) { sm[i] = dpp_add<0x124>(dpp_add<0x128>(sm[i])); sq[i] = dpp_add<0x124>(dpp_add<0x128>(sq[i])); }
    const bool od = lane & 4;
    stg(rs, (lane & 8) ? OOB : (p4 * 8 + hf * 4 + blk * 2 + (od ? 1 : 0)) * 16, OFF_SX + ((rp * 4 + w) * 16 + si) * 512,
        v4u{f2u(od ? sm[1] : sm[0]), tagA, f2u(od ? sq[1] : sq[0]), tagB});
    SB();
  }
  STAMPB(29);
  dma_x4(XSRC(2), l16, xl + 65536);
  dma_x4(XSRC(2) + 4096, l16, xl + 65536 + 4096);
  WAITV(17);
  STAMP(10);
  {
    v4u t[8];
    unsigned m = 0, m2 = 0;
#pragma unroll
    for (int i = 0; i < 8; ++i) t[i] = lds128(L + xl + l16 + i * 1024);
#pragma unroll
    for (int i = 0; i < 8; i += 2) { m = max4(m, t[i]); m2 = max4(m2, t[i + 1]); }
    if (__builtin_expect(__any(max(m, m2) == POISON), 0)) {
      for (unsigned spin = 0; spin < SPIN_CAP; ++spin) {
        COUNT(22);
        bool again = false;
#pragma nounroll
        for (int i = 0; i < 8; ++i)
          if (__any(max4(0u, lds128(L + xl + l16 + i * 1024)) == POISON)) {
            again = true;
            dma_x1(XSRC(0) + i * 1024, l16, xl + i * 1024);
            dma_x1(XSRC(1) + i * 1024, l16, xl + 32768 + i * 1024);
            dma_x1(XSRC(2) + i * 1024, l16, xl + 65536 + i * 1024);
          }
        if (!again) break;
        WAITV(0);
        if (spin) PAUSE();
      }
    }
  }
  BAR();
  STAMP(11);

  constexpr int HW = 40;
  v4u hq[HW], res[2], sx[8];
  const int c0 = 7 * w + rot, c3 = rot == 4 ? c0 - 7 : c0, c4 = rot ? c0 - 7 : c0, ckv = lane < 8 ? l16 : OOB;
  const int hb0 = OFF_HID + (rp * 4 + rg) * 114688, kb0 = OFF_CK + (rp * 4 + rg) * 4096;
#define CHUNK(n) (((n) % 35 / 5 < 3 ? c0 : (n) % 35 / 5 == 3 ? c3 : c4) + (n) % 35 / 5)
#define HLOAD(n) hq[(n) % HW] = (n) % 5 == 4 ? ldg(rs, ckv, kb0 + CHUNK(n) * 128 + ((n) / 35) * 8192) \
    : ldg(rs, l16 + ((n) % 5) * 1024, hb0 + CHUNK(n) * 4096 + ((n) / 35) * (2 * 114688))
  const int hoff = OFF_HID + (rp * 4 * 112 + ct * 8 + 2 * w) * 1024, hv0 = dt == 7 ? OOB : l16, hv1 = dt == 7 ? OOB : l16 + 1024;
  const int cko = OFF_CK + rp * 16384 + (ct * 4 + w) * 64, ckw = (p4 || dt == 7) ? OOB : lane;
  const unsigned ckey = lane ? 0u : tagA;
#define CKPUT(sum, u) __builtin_amdgcn_raw_buffer_store_b32(ckey - red_step<1>(red_step<0>(sum)), rs, ckw, cko + ((u) ^ par) * 4096, SC1)
  const int sxo = OFF_SX + (rp * 4 + w) * 8192;
  LAS unsigned char* xr[2];
#pragma unroll
  for (int i = 0; i < 2; ++i) { xr[i] = L + L_XR + tra + i * 65536; PIN(xr[i]); }
  LAS unsigned char* rr = L + L_XR + tra + (rg ^ par) * 32768 + (4 * dt + w) * 1024;
  LAS unsigned char* str = L + L_STAT + (lane & 31) * 8;
  PIN(str);
#define XA(t) (xr[(t) >> 6] + ((t) & 63) * 1024)
#define XF(t) trd2(XA(t), XA(t) + 256)
#define XEN
#define REDO(p) \
  if (XEN __builtin_expect(__any(!(acc[(p) % 3][0] == acc[(p) % 3][0])), 0)) { \
    for (unsigned spin = 0; spin < SPIN_CAP / 128; ++spin) { \
      COUNT(23); \
      _Pragma("nounroll") for (int i = 0; i < 8; ++i) dma_x4(xg + ((p) ^ par) * 32768 + i * 4096, l16, L_XR + (p) * 32768 + i * 4096); \
      WAITV(0); \
      _Pragma("unroll") for (int s = 0; s < 32; ++s) { \
        const v4u t = XF(32 * (p) + s); \
        if (s == 0) mfma_av<true>(acc[(p) % 3], w1f[s], t); else mfma_av<false>(acc[(p) % 3], w1f[s], t); \
        SB(); \
      } \
      mfma_done(acc[(p) % 3]); \
      if (!__any(!(acc[(p) % 3][0] == acc[(p) % 3][0]))) break; \
    } \
  }
  f32x16 acc[3];
  f32x2 ec[4];
  float ep[48], ee[48], ey[48];
  unsigned cs = 0;
  v4u x[128], eo;
#pragma unroll
  for (int t = 0; t < 4; ++t) x[t] = XF(t);
  SB();
  static_for<0, 64>([&](auto Jc) __attribute__((always_inline)) {
    constexpr int J = Jc.v, p = J >> 4, j = J & 15;
    asm volatile("" :: "v"(x[2 * J + 1]));
    mfma_av<j == 0>(acc[p % 3], w1f[2 * j], x[2 * J]);
    if constexpr (J < 62) { x[2 * J + 4] = XF(2 * J + 4); x[2 * J + 5] = XF(2 * J + 5); }
    SB();
    mfma_av<false>(acc[p % 3], w1f[2 * j + 1], x[2 * J + 1]);
    SB();
    if constexpr (J == 10) { WAITV(16); __builtin_amdgcn_s_barrier(); FRESH(); }
    if constexpr (J == EO - 2) {
      unsigned bad = 0;
#pragma unroll
      for (int i = 0; i < 8; ++i) bad = tagbad(bad, sx[i], tagA, tagB);
      if (__builtin_expect(__any(bad != 0u), 0)) {
        for (unsigned spin = 0; spin < SPIN_CAP; ++spin) {
          FRESH();
          if (spin > 1) PAUSE();
          COUNT(23);
          bad = 0;
#pragma unroll
          for (int i = 0; i < 8; ++i) sx[i] = ldg(rs, l16 + (i & 3) * 1024, sxo + (i >> 2) * 4096);
#pragma unroll
          for (int i = 0; i < 8; ++i) bad = tagbad(bad, sx[i], tagA, tagB);
          if (!__any(bad != 0u)) break;
        }
      }
      float sm = u2f(sx[0].x), sq = u2f(sx[0].z);
#pragma unroll
      for (int i = 1; i < 8; ++i) { sm += u2f(sx[i].x); PIN(sm); sq += u2f(sx[i].z); PIN(sq); }
      const float mean = swap32_add(sm) * (1.0f / 512.0f);
      const float ms = __builtin_fmaf(swap32_add(sq), 1.0f / 512.0f, 1e-5f);
      const float a = -LOG2E * __builtin_amdgcn_rsqf(fmaxf(__builtin_fmaf(-mean, mean, ms), 1e-5f));
      *(LAS f32x2*)(str + (w ^ par) * 256) = f32x2{a, -a * mean};
      SB();
    }
    if constexpr (J == EO - 1) {
      asm volatile("s_waitcnt lgkmcnt(4)" ::: "memory");
      __builtin_amdgcn_s_barrier();
      FRESH();
#pragma unroll
      for (int i = 0; i < 4; ++i) ec[i] = *(LAS f32x2*)(str + i * 256);
      SB();
    }
    static_for<2 * J, 2 * J + 2>([&](auto Sc) __attribute__((always_inline)) {
      static_for<0, 48>([&](auto Vc) __attribute__((always_inline)) {
        constexpr int S = Sc.v, V = Vc.v, k = V & 15;
        if constexpr (S == epi_slot(V) + 2 * epi_dist(V)) {
          ey[V] = ep[V] * ee[V];
          if constexpr (k & 1) { unsigned o = pack2(ey[V - 1], ey[V]); PIN(o); eo[(k >> 1) & 3] = o; } else PIN(ey[V]);
          if constexpr (k == 7) { stg(rs, hv0, hoff + ((V >> 4) ^ par) * 114688, eo); cs = sum4(0u, eo); PIN(cs); }
          if constexpr (k == 15) { stg(rs, hv1, hoff + ((V >> 4) ^ par) * 114688, eo); CKPUT(sum4(cs, eo), V >> 4); }
        }
        if constexpr (S == epi_slot(V) + epi_dist(V)) { ee[V] = __builtin_amdgcn_rcpf(1.0f + ee[V]); PIN(ee[V]); }
        if constexpr (S == epi_slot(V)) {
          ep[V] = __builtin_fmaf(ec[V >> 4].x, acc[(V >> 4) % 3][k], __builtin_fmaf(ec[V >> 4].y, uu[k >> 2][k & 3], vv[k >> 2][k & 3]));
          ee[V] = __builtin_amdgcn_exp2f(ep[V]);
          PIN(ee[V]);
        }
      });
      SB();
    });
    if constexpr (j == 1 && p >= 1) {
      if constexpr (p == 2) REDO(1)
      if constexpr (p == 3) REDO(2)
      if (((p - 1) & 1) == (rg ^ par)) res[(p - 1) >> 1] = trd2(rr + ((p - 1) >> 1) * 65536, rr + ((p - 1) >> 1) * 65536 + 256);
    }
    if constexpr (j == 2 && p >= 2) {
      asm volatile("s_waitcnt lgkmcnt(4)" ::: "memory");
      __builtin_amdgcn_s_barrier();
      FRESH();
    }
    if constexpr (J < 8) {
      if constexpr ((J & 3) == 0) set_m0(xl + 98304 + J * 1024);
      dma_xn<(J & 3) * 1024>(XSRC(3) + (J >> 2) * 4096, l16);
    }
    if constexpr (J >= SXL && J < SXL + 8) sx[J - SXL] = ldg(rs, l16 + ((J - SXL) & 3) * 1024, sxo + ((J - SXL) >> 2) * 4096);
    if constexpr (J >= EO && J < EO + 22 && ((J - EO) & 1) == 0) {
      constexpr int n = (J - EO) >> 1;
      if constexpr (n < 3) W2DMA(7 + n, L_SP + w * 6144 + n * 2048);
      else W2DMA(7 + n, L_XR + ((n - 3) >> 2) * 32768 + w * 8192 + ((n - 3) & 3) * 2048);
    }
    if constexpr (J >= 48 && J < 56) { HLOAD(2 * (J - 48)); if constexpr (J < 55) HLOAD(2 * (J - 48) + 1); }
    if constexpr (J >= 56 && J < 64 && (J & 1) == 0) W2DMA(18 + ((J - 56) >> 1), L_XR + 65536 + w * 8192 + ((J - 56) >> 1) * 2048);
    SB();
    if constexpr (J == 15) STAMP(16);
    if constexpr (J == 31) STAMP(17);
    if constexpr (J == 47) STAMP(18);
  });
  STAMP(12);
  static_assert(EO + 6 >= 26 && EO + 14 > 34 && EO + 20 < 48 && epi_slot(16) >= 2 * 34 && epi_slot(31) < epi_slot(32) && epi_slot(47) + 2 < 128, "schedule");
  static_for<15, 25>([&](auto Ic) __attribute__((always_inline)) { HLOAD(Ic.v); });
  SB();
  mfma_done(acc[0]);
  REDO(3)
  if (rg ^ par) res[1] = trd2(rr + 65536, rr + 65536 + 256);
#pragma unroll
  for (int h = 0; h < 2; ++h) {
    v4u o;
#pragma unroll
    for (int i = 0; i < 4; ++i) {
      const int k = 8 * h + 2 * i;
      const f32x2 t = __builtin_elementwise_fma(f32x2{ec[3].y, ec[3].y}, f32x2{uu[k >> 2][k & 3], uu[k >> 2][(k & 3) + 1]}, f32x2{vv[k >> 2][k & 3], vv[k >> 2][(k & 3) + 1]});
      const f32x2 pp = __builtin_elementwise_fma(f32x2{ec[3].x, ec[3].x}, f32x2{acc[0][k], acc[0][k + 1]}, t);
      const f32x2 d = f32x2{__builtin_amdgcn_exp2f(pp.x), __builtin_amdgcn_exp2f(pp.y)} + f32x2{1.0f, 1.0f};
      const f32x2 y = pp * f32x2{__builtin_amdgcn_rcpf(d.x), __builtin_amdgcn_rcpf(d.y)};
      o[i] = pack2(y.x, y.y);
    }
    stg(rs, h ? hv1 : hv0, hoff + (3 ^ par) * 114688, o);
    if (h) CKPUT(sum4(cs, o), 3); else cs = sum4(0u, o);
    SB();
  }
  BAR();
  STAMP(13);

  f32x16 d[2][2];
  LAS unsigned char* redw = L + L_XR + w * 8192 + l16;
  LAS unsigned char* redr = L + L_XR + w * 2048 + l16;
#define REDW(un, q) *(LAS f32x4*)(redw + (un) * 32768 + (q) * 1024) = \
    f32x4{d[un][(q) >> 2][4 * ((q) & 3)], d[un][(q) >> 2][4 * ((q) & 3) + 1], d[un][(q) >> 2][4 * ((q) & 3) + 2], d[un][(q) >> 2][4 * ((q) & 3) + 3]}
  LAS unsigned char *w2sa = w2a + L_SP + w * 6144, *w2sb = w2b + L_SP + w * 6144;
  w2a += L_XR + w * 8192; w2b += L_XR + w * 8192;
  LAS unsigned char *w2ua = w2a + 65536, *w2ub = w2b + 65536;
  PIN(w2a); PIN(w2b); PIN(w2sa); PIN(w2sb); PIN(w2ua); PIN(w2ub);
#define PULL(step, pa, pb, off) do { w2f[step][0] = trd2((pa) + (off), (pa) + (off) + 1024); w2f[step][1] = trd2((pb) + (off), (pb) + (off) + 1024); } while (0)
  unsigned m = red_all(sum4(sum8(sum8(0u, hq[0], hq[1]), hq[2], hq[3]), hq[4]));
  v4u gbv, scv;
  f32x4 pr[8];
  float gb[8], rx[2][8];
  unsigned orow = (rp * 128 + ((lane & 31) >> 3) * 16 + rg * 8 + (lane & 7)) * 1024 + dt * 128 + w * 32 + hf * 16;
  static_for<0, 14>([&](auto Gc) __attribute__((always_inline)) {
    constexpr int G = Gc.v, un = G / 7, gq = G % 7;
    if (__builtin_expect((unsigned)__builtin_amdgcn_readlane((int)m, 63) != 2u * tagA, 0)) {
      for (unsigned spin = 0; spin < SPIN_CAP; ++spin) {
        FRESH();
        COUNT(24 + un);
        static_for<0, 5>([&](auto Ic) __attribute__((always_inline)) { HLOAD(5 * G + Ic.v); });
        m = sum4(sum8(sum8(0u, hq[(5 * G) % HW], hq[(5 * G + 1) % HW]), hq[(5 * G + 2) % HW], hq[(5 * G + 3) % HW]), hq[(5 * G + 4) % HW]);
        if ((unsigned)__builtin_amdgcn_readlane((int)red_all(m), 63) == 2u * tagA) break;
        PAUSE();
      }
    }
    static_for<0, 8>([&](auto Ic) __attribute__((always_inline)) {
      constexpr int c = Ic.v, i = G < 13 ? c >> 1 : c & 3, e = G < 13 ? c & 1 : c >> 2, I = 4 * G + i, n = 5 * G + i, X = 8 * G + c;
      mfma_av<gq == 0 && i == 0>(d[un][e], w2f[gq * 4 + i][e], hq[n % HW]);
      SB();
      if constexpr (G < 13 && e == 0) {
        if constexpr (I < 3) PULL(7 + I, w2sa, w2sb, I * 2048);
        if constexpr (I >= 3 && I < 11) PULL(7 + I, w2a, w2b, ((I - 3) >> 2) * 32768 + ((I - 3) & 3) * 2048);
        if constexpr (I >= 11 && I < 15) PULL(7 + I, w2ua, w2ub, (I - 11) * 2048);
        if constexpr (I == 18) waitv<4 + hl_upto(18) - 35>();
        if constexpr (I >= 18 && I < 22) PULL(4 + I, w2ua, w2ub, 32768 + (I - 18) * 2048);
        if constexpr (I == 23) waitv<hl_upto(23) - 35>();
        if constexpr (I == 23 || I == 24) PULL(3 + I, w2sa, w2sb, (I - 23) * 2048);
        if constexpr (I >= 29 && I < 37) REDW(0, I - 29);
      }
      if constexpr (G < 13 && e == 1) {
        if constexpr (I >= 4 && I < 8) W2DMA(18 + I, L_XR + 98304 + w * 8192 + (I - 4) * 2048);
        if constexpr (I == 8) { W2DMA(26, L_SP + w * 6144); W2DMA(27, L_SP + w * 6144 + 2048); }
        static_for<hl_upto(I), hl_upto(I + 1)>([&](auto Nc) __attribute__((always_inline)) { HLOAD(Nc.v); });
      }
      if constexpr (G < 13) {
        constexpr int q = 5 * G + 5;
        if constexpr (c == 1) m = sum8(0u, hq[q % HW], hq[(q + 1) % HW]);
        if constexpr (c == 3) m = sum4(sum8(m, hq[(q + 2) % HW], hq[(q + 3) % HW]), hq[(q + 4) % HW]);
        if constexpr (c == 4) m = red_step<0>(m);
        if constexpr (c == 5) m = red_step<2>(red_step<1>(m));
        if constexpr (c == 6) m = red_step<3>(m);
        if constexpr (c == 7) m = red_step<5>(red_step<4>(m));
        PIN(m);
      }
      if constexpr (X == 74) { gbv = lds128(L + L_TG + (64 * dt + 16 * w + 8 * hf) * 2); scv = lds128(L + L_TS + (64 * dt + 16 * w + 8 * hf) * 2); PIN(orow); }
      if constexpr (X >= 76 && X < 92 && (X & 1) == 0) {
        constexpr int q = (X - 76) >> 1;
        const float inv = __builtin_amdgcn_rcpf((q & 1) ? bf_hi(scv[q >> 1]) : bf_lo(scv[q >> 1]));
        gb[q] = -LN2 * ((q & 1) ? bf_hi(gbv[q >> 1]) : bf_lo(gbv[q >> 1]));
        rx[0][q] = inv * ((q & 1) ? bf_hi(res[0][q >> 1]) : bf_lo(res[0][q >> 1]));
        rx[1][q] = inv * ((q & 1) ? bf_hi(res[1][q >> 1]) : bf_lo(res[1][q >> 1]));
        PIN(gb[q]); PIN(rx[0][q]); PIN(rx[1][q]);
      }
      if constexpr (X == 92) BAR();
      if constexpr (X >= 93 && X < 97) { pr[2 * (X - 93)] = *(LAS f32x4*)(redr + (X - 93) * 8192); pr[2 * (X - 93) + 1] = *(LAS f32x4*)(redr + (X - 93) * 8192 + 1024); }
      if constexpr (X >= 98 && X < 106) {
        constexpr int q = X - 98;
        float t = (pr[q >> 2][q & 3] + pr[2 + (q >> 2)][q & 3]);
        PIN(t);
        float t2_ = (pr[4 + (q >> 2)][q & 3] + pr[6 + (q >> 2)][q & 3]);
        PIN(t2_);
        rx[0][q] = __builtin_fmaf(gb[q], t + t2_, rx[0][q]);
        PIN(rx[0][q]);
      }
      if constexpr (X == 106)
        *(GAS v4u*)((GAS unsigned char*)out + orow) = v4u{pack2(rx[0][0], rx[0][1]), pack2(rx[0][2], rx[0][3]), pack2(rx[0][4], rx[0][5]), pack2(rx[0][6], rx[0][7])};
      if constexpr (X == 109) asm volatile("s_nop 9" : "+v"(d[1][0]));
      if constexpr (X >= 109) REDW(1, X - 109);
      SB();
    });
    if constexpr (G == 6) STAMP(14);
  });
  STAMP(19);
  REDW(1, 3);
  mfma_done(d[1][1]);
#pragma unroll
  for (int q = 4; q < 8; ++q) REDW(1, q);
  BAR();
  {
    f32x4 s0 = {}, s1 = {};
#pragma unroll
    for (int k = 0; k < 4; ++k) { s0 += *(LAS f32x4*)(redr + k * 8192 + 32768); s1 += *(LAS f32x4*)(redr + k * 8192 + 32768 + 1024); }
    float y[8];
#pragma unroll
    for (int q = 0; q < 8; ++q) y[q] = __builtin_fmaf(gb[q], q < 4 ? s0[q & 3] : s1[q & 3], rx[1][q]);
    *(GAS v4u*)((GAS unsigned char*)out + orow + 65536) = v4u{pack2(y[0], y[1]), pack2(y[2], y[3]), pack2(y[4], y[5]), pack2(y[6], y[7])};
  }
  if (b == 0 && tid == 0) __hip_atomic_store((GAS unsigned*)ws, epoch + 1u, __ATOMIC_RELAXED, __HIP_MEMORY_SCOPE_AGENT);
  STAMP(15);
}

extern "C" void kernel_launch(void* const* d_in, const int* in_sizes, int n_in, void* d_out, int out_size, void* d_ws, size_t ws_size,
                              hipStream_t stream) {
  (void)in_sizes; (void)n_in; (void)out_size; (void)ws_size;
  (void)hipFuncSetAttribute((const void*)s4_block_kernel, hipFuncAttributeMaxDynamicSharedMemorySize, LDS_BYTES);
  s4_block_kernel<<<256, 256, LDS_BYTES, stream>>>(
      (const unsigned char*)d_in[0], (const u16*)d_in[1], (const u16*)d_in[2], (const u16*)d_in[3], (const u16*)d_in[4], (const u16*)d_in[5],
      (const u16*)d_in[6], (const u16*)d_in[7], (const u16*)d_in[8], (const unsigned char*)d_in[10], (const unsigned char*)d_in[11],
      (const u16*)d_in[12], (const unsigned char*)d_in[13], (unsigned char*)d_out, (unsigned char*)d_ws);
}
```

```cpp
#define PTEXPR ((slot & 1) * 192 + tid) * 65536
#define SETMASK 0u
#include <hip/hip_runtime.h>

#define GAS __attribute__((address_space(1)))
#define LAS __attribute__((address_space(3)))
#define DI __device__ __forceinline__
typedef unsigned v4u __attribute__((ext_vector_type(4)));
typedef unsigned v2u __attribute__((ext_vector_type(2)));
typedef float f32x2 __attribute__((ext_vector_type(2)));
typedef float f32x4 __attribute__((ext_vector_type(4)));
typedef float f32x16 __attribute__((ext_vector_type(16)));
typedef __bf16 bf16x2 __attribute__((ext_vector_type(2)));
typedef __bf16 bf16x8 __attribute__((ext_vector_type(8)));
typedef short s16x4 __attribute__((ext_vector_type(4)));
typedef unsigned short u16;
typedef unsigned long long u64;

constexpr int SET_BASE = 4096, SET_STRIDE = 16 << 20;
constexpr int OFF_MT = 0;
constexpr int OFF_SG = 32768;
constexpr int OFF_MG = 65536;
constexpr int OFF_SX = 524288;
constexpr int OFF_XG = 1 << 20;
constexpr int OFF_Z = OFF_XG + (4 << 20);
constexpr int OFF_HID = OFF_Z + 2097152;
constexpr int OFF_CK = OFF_HID + 7340032;
constexpr int SET_BYTES = OFF_CK + 262144;
static_assert(SET_BYTES <= SET_STRIDE && OFF_MG + 393216 <= OFF_SX, "set size");

constexpr int L_TS = 0, L_TB = 1024, L_TG = 2048;
constexpr int L_UVT = 3072;
constexpr int L_M = 4096;
constexpr int L_UVS = L_M;
constexpr int UVS_ROW = 544, UVS_CH = 57 * UVS_ROW;
constexpr int L_KC = L_M;
constexpr int KC_STRIDE = 4160;
constexpr int L_H = L_KC + 8 * KC_STRIDE;
constexpr int H_STRIDE = 4576;
constexpr int L_XB = L_H + 2 * H_STRIDE;
constexpr int L_RX = L_XB + 8192;
constexpr int L_R = L_M + 65536;
constexpr int R_WAVE = 22528;
constexpr int L_TOP = L_R + 4 * R_WAVE;
constexpr int L_KT = L_TOP + 16;
constexpr int L_STAT = L_TOP;
constexpr int L_ETA = L_RX + 12288;
constexpr int L_XR = L_M;
constexpr int L_SP = L_M + 131072;
constexpr int LDS_BYTES = 163840;
static_assert(L_TOP + 4096 <= LDS_BYTES && L_ETA + 16 <= L_R && L_UVS + 2 * UVS_CH <= L_R && L_SP + 24576 <= L_TOP, "LDS map");
static_assert(L_H % 16 == 0 && L_XB % 16 == 0 && L_RX % 16 == 0 && L_KT % 16 == 0, "alignment");

#define ZLAG 30
#define MTK 124
constexpr int SXL = 11, EO = 26;
constexpr int CODE_BYTES = 44032;
constexpr int SC1 = 16;
constexpr int OOB = 0x7FFFFF00;
constexpr unsigned SPIN_CAP = 1u << 15;
constexpr unsigned POISON = 0xFFFFFFFFu;
constexpr float LOG2E = 1.4426950408889634f, LN2 = 0.6931471805599453f, INV2PI = 0.15915494309189535f;

#define SB() __builtin_amdgcn_sched_barrier(0)
#define WAITV(n) asm volatile("s_waitcnt vmcnt(" #n ")" ::: "memory")
#define PIN(x) asm volatile("" : "+v"(x))
#define FRESH() asm volatile("" ::: "memory")
#define PAUSE() asm volatile("s_sleep 4" ::: "memory")
#define BAR() do { asm volatile("s_waitcnt lgkmcnt(0)" ::: "memory"); __builtin_amdgcn_s_barrier(); asm volatile("" ::: "memory"); } while (0)
#define STAMP(i)
#define SARG
#define SPASS
#define COUNT(i)

#define STAMPA(i)
#define STAMPB(i) STAMP(i)
DI float u2f(unsigned u) { return __builtin_bit_cast(float, u); }
DI unsigned f2u(float f) { return __builtin_bit_cast(unsigned, f); }
DI float dot2(unsigned u, float c) { const bf16x2 v = __builtin_bit_cast(bf16x2, u); return __builtin_amdgcn_fdot2_f32_bf16(v, v, c, false); }
DI float bf_lo(unsigned u) { return __builtin_bit_cast(float, u << 16); }
DI float bf_hi(unsigned u) { return __builtin_bit_cast(float, u & 0xFFFF0000u); }
DI unsigned pack2(float a, float b) { f32x2 v = {a, b}; return __builtin_bit_cast(unsigned, __builtin_convertvector(v, bf16x2)); }
DI float rbf(float a) { return bf_lo(pack2(a, a)); }
DI unsigned sel(bool c, unsigned a, unsigned b) { asm("" : "+v"(a), "+v"(b)); return c ? a : b; }
DI unsigned guard(unsigned u) { return u < 0xFFFFFFFEu ? u : 0xFFFFFFFEu; }
DI unsigned max4(unsigned m, v4u v) {
  asm("v_max3_u32 %0, %0, %1, %2\n\tv_max3_u32 %0, %0, %3, %4" : "+v"(m) : "v"(v.x), "v"(v.y), "v"(v.z), "v"(v.w));
  return m;
}
DI f32x4 mfma16(v4u a, v4u b, f32x4 c) { return __builtin_amdgcn_mfma_f32_16x16x32_bf16(__builtin_bit_cast(bf16x8, a), __builtin_bit_cast(bf16x8, b), c, 0, 0, 0); }
DI f32x16 mfma32(v4u a, v4u b, f32x16 c) { return __builtin_amdgcn_mfma_f32_32x32x16_bf16(__builtin_bit_cast(bf16x8, a), __builtin_bit_cast(bf16x8, b), c, 0, 0, 0); }
template <bool FIRST> DI void mfma_av(f32x16& acc, const v4u& a, const v4u& b) {
  if (FIRST) asm volatile("v_mfma_f32_32x32x16_bf16 %0, %1, %2, 0" : "=&v"(acc) : "a"(a), "v"(b));
  else asm volatile("v_mfma_f32_32x32x16_bf16 %0, %1, %2, %0" : "+v"(acc) : "a"(a), "v"(b));
}
template <bool FIRST> DI void mfma_va(f32x16& acc, const v4u& a, const v4u& b) {
  if (FIRST) asm volatile("v_mfma_f32_32x32x16_bf16 %0, %1, %2, 0" : "=&v"(acc) : "v"(a), "a"(b));
  else asm volatile("v_mfma_f32_32x32x16_bf16 %0, %1, %2, %0" : "+v"(acc) : "v"(a), "a"(b));
}
template <bool FIRST> DI void mfma16_v(f32x4& acc, const v4u& a, const v4u& b) {
  if (FIRST) asm volatile("v_mfma_f32_16x16x32_bf16 %0, %1, %2, 0" : "=&v"(acc) : "v"(a), "v"(b));
  else asm volatile("v_mfma_f32_16x16x32_bf16 %0, %1, %2, %0" : "+v"(acc) : "v"(a), "v"(b));
}
DI void mfma_done(f32x16& acc) { asm volatile("s_nop 15" : "+v"(acc)); }
DI v2u trd(LAS unsigned char* p) { return __builtin_bit_cast(v2u, __builtin_amdgcn_ds_read_tr16_b64_v4i16((LAS s16x4*)p)); }
DI v4u trd2(LAS unsigned char* p0, LAS unsigned char* p1) { const v2u a = trd(p0), b = trd(p1); return v4u{a.x, a.y, b.x, b.y}; }
DI v4u lds128(LAS unsigned char* p) { return *(LAS v4u*)p; }
DI v4u ldg(__amdgpu_buffer_rsrc_t r, int voff, int soff) { return __builtin_amdgcn_raw_buffer_load_b128(r, voff, soff, SC1); }
DI void stg(__amdgpu_buffer_rsrc_t r, int voff, int soff, v4u v) {
  __builtin_amdgcn_raw_buffer_store_b128(v, r, voff, soff, SC1);
  asm volatile("s_nop 1" :: "v"(v) : "memory");
}
template <int CTRL> DI float dpp_add(float v) {
  return v + __builtin_bit_cast(float, __builtin_amdgcn_update_dpp(0, __builtin_bit_cast(int, v), CTRL, 0xF, 0xF, true));
}
DI float swap16_add(float v) { const unsigned u = __builtin_bit_cast(unsigned, v); const auto r = __builtin_amdgcn_permlane16_swap(u, u, false, false);
  return u2f(r[0]) + u2f(r[1]); }
DI float swap32_add(float v) { const unsigned u = __builtin_bit_cast(unsigned, v); const auto r = __builtin_amdgcn_permlane32_swap(u, u, false, false);
  return u2f(r[0]) + u2f(r[1]); }
DI float quad_sum(float v) { v = dpp_add<0xB1>(v); return dpp_add<0x4E>(v); }
DI float row_sum(float v) { return dpp_add<0x140>(dpp_add<0x141>(quad_sum(v))); }
DI float wave_sum(float v) { return swap32_add(swap16_add(row_sum(v))); }
DI f32x2 cmul(f32x2 a, f32x2 e, f32x2 ep) { const f32x2 t = f32x2{a.x, a.x} * e; return __builtin_elementwise_fma(f32x2{a.y, a.y}, ep, t); }
DI f32x2 cexp(float re2, float rev) {
  const float m = __builtin_amdgcn_exp2f(re2), f = __builtin_amdgcn_fractf(rev);
  return f32x2{m * __builtin_amdgcn_cosf(f), m * __builtin_amdgcn_sinf(f)};
}
DI void dma_w2(const unsigned char* sbase, unsigned v0, unsigned v1, unsigned lds_dst) {
  asm volatile("s_mov_b32 m0, %3\n\ts_nop 0\n\tglobal_load_lds_dwordx4 %0, %2\n\tglobal_load_lds_dwordx4 %1, %2 offset:1024"
               :: "v"(v0), "v"(v1), "s"(sbase), "s"(lds_dst) : "memory");
}
DI void dma_w1(const unsigned char* sbase, unsigned v, unsigned lds_dst) {
  asm volatile("s_mov_b32 m0, %2\n\ts_nop 0\n\tglobal_load_lds_dwordx4 %0, %1" :: "v"(v), "s"(sbase), "s"(lds_dst) : "memory");
}
DI void dma_x1(const unsigned char* sbase, unsigned v, unsigned lds_dst) {
  asm volatile("s_mov_b32 m0, %2\n\ts_nop 0\n\tglobal_load_lds_dwordx4 %0, %1 sc1" :: "v"(v), "s"(sbase), "s"(lds_dst) : "memory");
}
DI void set_m0(unsigned lds_dst) { asm volatile("s_mov_b32 m0, %0\n\ts_nop 0" :: "s"(lds_dst) : "memory"); }
template <int OFF> DI void dma_xn(const unsigned char* sbase, unsigned v) {
  asm volatile("global_load_lds_dwordx4 %0, %1 offset:%2 sc1" :: "v"(v), "s"(sbase), "n"(OFF) : "memory");
}
DI void dma_x2(const unsigned char* sbase, unsigned v, unsigned lds_dst) {
  asm volatile("s_mov_b32 m0, %2\n\ts_nop 0\n\tglobal_load_lds_dwordx4 %0, %1 sc1\n\tglobal_load_lds_dwordx4 %0, %1 offset:1024 sc1"
               :: "v"(v), "s"(sbase), "s"(lds_dst) : "memory");
}
DI void dma_x4(const unsigned char* sbase, unsigned v, unsigned lds_dst) {
  asm volatile("s_mov_b32 m0, %2\n\ts_nop 0\n\tglobal_load_lds_dwordx4 %0, %1 sc1\n\tglobal_load_lds_dwordx4 %0, %1 offset:1024 sc1\n\t"
               "global_load_lds_dwordx4 %0, %1 offset:2048 sc1\n\tglobal_load_lds_dwordx4 %0, %1 offset:3072 sc1"
               :: "v"(v), "s"(sbase), "s"(lds_dst) : "memory");
}
template <int I> struct IC { static constexpr int v = I; };
template <int I, int N, class F> DI void static_for(F&& f) { if constexpr (I < N) { f(IC<I>{}); static_for<I + 1, N>(f); } }
DI unsigned now() { u64 t; asm volatile("s_memrealtime %0\n\ts_waitcnt lgkmcnt(0)" : "=s"(t) :: "memory"); return (unsigned)t; }
template <int N> DI void waitv() { asm volatile("s_waitcnt vmcnt(%0)" :: "n"(N) : "memory"); }
struct ConvOrder { int t[36], u[36]; };
constexpr ConvOrder conv_order() {
  ConvOrder o = {};
  int done[8] = {}, last = -1;
  for (int k = 0; k < 36; ++k) {
    int best = -1;
    for (int t = 0; t < 8; ++t)
      if (t != last && done[t] <= t && (best < 0 || t - done[t] > best - done[best])) best = t;
    o.t[k] = best; o.u[k] = done[best]++; last = best;
  }
  return o;
}
constexpr ConvOrder conv_order_last() {
  ConvOrder o = {};
  int k = 0;
  for (int p = 0; p < 4; ++p)
    for (int u = 0; u <= 2 * p + 1; ++u) {
      o.t[k] = 2 * p + 1; o.u[k] = u; ++k;
      if (u <= 2 * p) { o.t[k] = 2 * p; o.u[k] = u; ++k; }
    }
  return o;
}
constexpr int epi_slot(int v) { return v < 20 ? 2 * EO + v : v < 32 ? 2 * EO + 20 + 2 * (v - 20) : 2 * 50 + (v - 32); }
constexpr int epi_dist(int v) { return v >= 20 && v < 32 ? 2 : 1; }
constexpr int hl_upto(int i) { return i < 4 ? 25 + (5 * i + 1) / 2 : i < 8 ? 35 : i < 36 ? 35 + 5 * (i - 8) / 4 : 70; }
DI unsigned tagbad(unsigned bad, v4u g, unsigned tagA, unsigned tagB) {
  return __builtin_amdgcn_bitop3_b32(__builtin_amdgcn_bitop3_b32(bad, g.y, tagA, 0xF6), g.w, tagB, 0xF6);
}
template <int R0, int R1, int R2, int R3> DI void put4(unsigned base, f32x2 a, f32x2 b, f32x2 c, f32x2 d) {
  asm volatile("s_mov_b32 m0, %4\n\ts_nop 0\n\tds_write_addtid_b32 %0 offset:%5\n\tds_write_addtid_b32 %1 offset:%6\n\t"
               "ds_write_addtid_b32 %2 offset:%7\n\tds_write_addtid_b32 %3 offset:%8"
               :: "v"(pack2(a.x, a.y)), "v"(pack2(b.x, b.y)), "v"(pack2(c.x, c.y)), "v"(pack2(d.x, d.y)), "s"(base),
                  "n"(R0 * UVS_ROW), "n"(R1 * UVS_ROW), "n"(R2 * UVS_ROW), "n"(R3 * UVS_ROW) : "memory");
}
DI unsigned sum4(unsigned m, v4u v) {
  asm("v_add3_u32 %0, %0, %1, %2\n\tv_add3_u32 %0, %0, %3, %4" : "+v"(m) : "v"(v.x), "v"(v.y), "v"(v.z), "v"(v.w));
  return m;
}
DI unsigned sum8(unsigned m, v4u a, v4u b) {
  asm("v_add3_u32 %0, %0, %1, %2\n\tv_add3_u32 %0, %0, %3, %4\n\tv_add3_u32 %0, %0, %5, %6\n\tv_add3_u32 %0, %0, %7, %8"
      : "+v"(m) : "v"(a.x), "v"(a.y), "v"(a.z), "v"(a.w), "v"(b.x), "v"(b.y), "v"(b.z), "v"(b.w));
  return m;
}
template <int I> DI unsigned red_step(unsigned v) {
  constexpr int ctrl[6] = {0xB1, 0x4E, 0x141, 0x140, 0x142, 0x143}, rows[6] = {0xF, 0xF, 0xF, 0xF, 0xA, 0xC};
  return v + (unsigned)__builtin_amdgcn_update_dpp(0, (int)v, ctrl[I], rows[I], 0xF, true);
}
DI unsigned red_all(unsigned v) { return red_step<5>(red_step<4>(red_step<3>(red_step<2>(red_step<1>(red_step<0>(v)))))); }
DI void swap_add32(float& a, float b) { const auto r = __builtin_amdgcn_permlane32_swap(f2u(a), f2u(b), false, false); a = u2f(r[0]) + u2f(r[1]); }
DI void swap_add16(float& a, float b) { const auto r = __builtin_amdgcn_permlane16_swap(f2u(a), f2u(b), false, false); a = u2f(r[0]) + u2f(r[1]); }

typedef const unsigned char* cptr;
typedef unsigned v16u __attribute__((ext_vector_type(16)));
DI cptr mkptr(unsigned lo, unsigned hi) { return (cptr)(((u64)hi << 32) | lo); }

__global__ void __launch_bounds__(256, 1)
s4_block_kernel(cptr, cptr, cptr, cptr, cptr, cptr, cptr, cptr, cptr, cptr, cptr, cptr, cptr, unsigned char*, unsigned char*, cptr) {
  STAMP(0);
  v16u ka0, ka1;
  asm volatile("s_load_dwordx16 %0, %2, 0x0\n\ts_load_dwordx16 %1, %2, 0x40" : "=&s"(ka0), "=&s"(ka1) : "s"(__builtin_amdgcn_kernarg_segment_ptr()));
  SB();
  LAS unsigned char* const L = (LAS unsigned char*)16u - 16;
  const int tid = threadIdx.x, lane = tid & 63, w = __builtin_amdgcn_readfirstlane(tid >> 6);
  const int b = blockIdx.x, xcd = b & 7, slot = b >> 3, id = xcd * 32 + slot;
  const int ks = (id >> 6) * 4 + w, cc = id & 63;
  const int rp = 4 * (xcd >> 1) + (slot >> 3), rg = xcd & 1, dt = slot & 7, si = 8 * rg + dt;
  const int ct = 7 * rg + min(dt, 6);
  const int rot = (0x0340 >> (4 * (2 * (w & 1) + rg))) & 7;

  const unsigned char* pcode;
  asm volatile("s_getpc_b64 %0" : "=s"(pcode));
  const unsigned warm = *(const GAS unsigned*)((const GAS unsigned char*)pcode + (unsigned)min((((slot & 7) * 4 + w) * 11 + min(lane, 10)) * 128, CODE_BYTES));
  const int g16 = lane >> 4, q4 = (lane & 15) >> 2, p4 = lane & 3, hf = lane >> 5, blk = (lane >> 4) & 1, l16 = lane * 16;
  const unsigned lrw = L_R + w * R_WAVE;
  unsigned wmo[3], co = (ks * 32 + (lane & 31)) * 2, xo = id * 8192 + (tid & 3) * 2048 + (tid >> 2) * 16, ro = id * 8192 + w * 2048 + hf * 1024 + (lane & 31) * 16, po = id * 512 + tid * 2;
  unsigned wto = id * 7168 + (w >> 1) * 3584 + min(lane, 27) * 128;
#pragma unroll
  for (int i = 0; i < 3; ++i) { const unsigned idx = lane + 64 * i; wmo[i] = (ks * 32 * 6144 + cc * 96 - i * 1024) + idx / 6 * (6144 - 96) + idx * 16; PIN(wmo[i]); }
  unsigned pto = sel(w == 3, (lane & 31) * 65536, PTEXPR);
  PIN(pto);
  PIN(co); PIN(xo); PIN(ro); PIN(po); PIN(wto);
  set_m0(lrw);
  SB();
  unsigned w2v0 = (lane >> 3) * 1024 + (((lane & 7) ^ (blk << 2)) * 16), w2v1 = w2v0 + 7168;
  unsigned w1v0 = (lane >> 2) * 3584 + p4 * 16, w1v1 = w1v0 + 57344 - 1024;
  const int zs = OFF_Z + (2 * id + (w >> 1)) * 64;
  int za = (((w & 1) + 2 * (g16 & 1)) * 8 + q4 * 2 + (g16 >> 1)) * 32768 + p4 * 16, zb = za + 32 * 32768;
  int tra = (hf * 8 + q4) * 64 + blk * 32 + p4 * 8;
  LAS unsigned char* w1a = L + lrw + tra;
  LAS unsigned char* ka = L + L_KC + ((w >> 1) * 4 + q4) * KC_STRIDE + 2 * (32 - 8 * g16 + 4 * p4) - 8 + 256 * (w & 1);
  LAS unsigned char* hb = L + L_H + (w >> 1) * H_STRIDE + (lane & 15) * 32 + g16 * 16 + 256 - 256 * (w & 1);
  LAS unsigned char* zp = L + sel(tid < 64, L_KC + ((lane >> 1) & 7) * KC_STRIDE + (lane & 1) * 16, L_H + hf * H_STRIDE + min(lane & 31, 29) * 16);
  LAS unsigned char* ub = L + L_UVS + (lane & 15) * UVS_ROW + g16 * 16;
  int gmo = OFF_MG + (g16 * 256 + id) * 256 + (lane & 15) * 16, gmo2 = OFF_MG + ((4 + blk) * 256 + id) * 256 + (lane & 15) * 16;
  int mgo = sel(lane < 48, (lane >> 1) * 256 + (lane & 1) * 8, OOB), sgo = sel(lane & 31, OOB, hf * 8);
  LAS unsigned char* xbw = L + L_XB + (tid >> 7) * 2048 + (tid & 127) * 4;
  LAS unsigned char* xbr = L + L_XB + w * 2048 + (lane & 15) * 32 + g16 * 8;
  LAS unsigned char *exa = L + L_XB + w * 2048 + l16, *exb = L + L_RX + w * 3072 + l16, *eya = L + L_XB + (w ^ 1) * 2048 + l16, *eyb = L + L_RX + (w ^ 1) * 3072 + l16;
  PIN(xbw); PIN(xbr); PIN(exa); PIN(exb); PIN(eya); PIN(eyb);
  int mto = (tid >> 1) * 128 + (tid & 1) * 16;
  LAS unsigned char* mtw = L + (tid & 1) * L_TG + (tid >> 1) * 4;
  PIN(mto); PIN(mtw);
  PIN(w2v0); PIN(w2v1); PIN(w1v0); PIN(w1v1); PIN(za); PIN(zb); PIN(tra); PIN(w1a); PIN(ka); PIN(hb); PIN(zp); PIN(ub); PIN(gmo); PIN(gmo2); PIN(mgo); PIN(sgo);
#define W1DMA(step, lds) dma_w1(w1w + (step) * 57344, w1v0, (lds))
#define W1DMA2(step, lds) dma_w2(w1w + (step) * 57344, w1v0, w1v1, (lds))
#define W2DMA(step, lds) dma_w2(((step) < 12 ? w2lo : (step) < 16 ? w2c3 : w2hi) + (step) * 16384, w2v0, w2v1, (lds))
  SB();
  STAMPA(24);
  asm volatile("s_waitcnt lgkmcnt(0)" : "+s"(ka0), "+s"(ka1));
  STAMPA(25);
  const cptr xin = mkptr(ka0[0], ka0[1]), pAre = mkptr(ka0[2], ka0[3]), pAim = mkptr(ka0[4], ka0[5]), pBre = mkptr(ka0[6], ka0[7]);
  const cptr pBim = mkptr(ka0[8], ka0[9]), pCre = mkptr(ka0[10], ka0[11]), pCim = mkptr(ka0[12], ka0[13]), pldt = mkptr(ka0[14], ka0[15]);
  const cptr pD = mkptr(ka1[0], ka1[1]), w1 = mkptr(ka1[2], ka1[3]), w2 = mkptr(ka1[4], ka1[5]), pc = mkptr(ka1[6], ka1[7]);
  const cptr wmod = mkptr(ka1[8], ka1[9]), out = mkptr(ka1[10], ka1[11]), ws = mkptr(ka1[12], ka1[13]);
  const unsigned epoch_v = __hip_atomic_load((GAS unsigned*)ws, __ATOMIC_RELAXED, __HIP_MEMORY_SCOPE_AGENT);
  const unsigned pt = *(const GAS unsigned*)((const GAS unsigned char*)(ws + (w == 3) * (out - ws)) + pto);
  asm volatile("global_load_lds_dwordx4 %0, %3\n\tglobal_load_lds_dwordx4 %1, %3 offset:1024\n\tglobal_load_lds_dwordx4 %2, %3 offset:2048"
               :: "v"(wmo[0]), "v"(wmo[1]), "v"(wmo[2]), "s"(wmod) : "memory");
  const unsigned cval = *(const GAS u16*)((const GAS unsigned char*)pc + co);
  SB();
  const v4u xa = *(const GAS v4u*)((const GAS unsigned char*)xin + xo), xb = *(const GAS v4u*)((const GAS unsigned char*)xin + 1024 + xo);
  const v4u ra = *(const GAS v4u*)((const GAS unsigned char*)xin + ro), rb = *(const GAS v4u*)((const GAS unsigned char*)xin + 512 + ro);
  SB();
#define PARAM(p) (unsigned)*(const GAS u16*)((const GAS unsigned char*)(p) + po)
  const unsigned uAre = PARAM(pAre), uAim = PARAM(pAim), uBre = PARAM(pBre), uBim = PARAM(pBim), uCre = PARAM(pCre), uCim = PARAM(pCim);
#undef PARAM
  SB();
  const unsigned wt = *(const GAS unsigned*)((const GAS unsigned char*)(w1 + (w & 1) * (w2 - w1)) + wto);
  unsigned uldt, uD;
  asm volatile("s_load_dword %0, %2, %4\n\ts_load_dword %1, %3, %4" : "=&s"(uldt), "=&s"(uD) : "s"(pldt), "s"(pD), "s"(id * 4));
  SB();
  STAMPA(26);
  LAS unsigned char* koff[12];
  {
    float fj = (float)(8 * tid - 3);
    PIN(fj);
    const unsigned lin = (unsigned)(8 * tid - 3 + 0x4300 - 128) * 0x10001u + 0x10000u;
#pragma unroll
    for (int i = 0; i < 12; i += 2) {
      const unsigned r = sel(tid < 32, lin + i * 0x10001u, pack2(fj + (float)i, fj + (float)(i + 1)));
      koff[i] = L + ((r & 0xFFFFu) * 4 + (L_KT + (128 - 0x4300) * 4));
      koff[i + 1] = L + ((r >> 16) * 4 + (L_KT + (128 - 0x4300) * 4));
      PIN(koff[i]);
      if (i < 10) PIN(koff[i + 1]);
    }
  }
  *(LAS v4u*)(L + L_KT - 16) = v4u{0u, 0u, 0u, 0u};
  LAS unsigned char* ktw[4];
  {
    const int row = w * 16 + (lane & 15), sg = max((row >> 3) - 1, 0), rr_ = row - 8 * sg;
    const int idx0 = 128 * sg + ((rr_ >> (3 - sg)) << (7 - sg)) + (rr_ & ((8 >> sg) - 1)) + 1, st = 8 >> sg;
#pragma unroll
    for (int r = 0; r < 4; ++r) {
      const int idx = sel(row < 40, idx0 + st * (4 * g16 + r), tid == 136 && r == 0 ? 0 : 641 + lane);
      ktw[r] = L + L_KT + idx * 4;
      PIN(ktw[r]);
    }
  }
  const unsigned char* w2lo = w2 + (448 * w + 64 * rot) * 1024 + dt * 128;
  const unsigned char* w2c3 = w2lo - (rot >> 2) * (448 * 1024);
  const unsigned char* w2hi = w2lo - ((rot + 1) >> 2) * (448 * 1024);
  const unsigned char* w1w = w1 + ct * 256 + w * 64;
  SB();
  STAMPA(27);
  asm volatile("s_waitcnt lgkmcnt(0)" : "+s"(uldt), "+s"(uD));
  const unsigned epoch = __builtin_amdgcn_readfirstlane(epoch_v);
  asm volatile("" :: "s"(epoch));
  STAMPA(28);
  const unsigned char* setp = ws + SET_BASE + (size_t)(epoch & SETMASK) * SET_STRIDE;
  const __amdgpu_buffer_rsrc_t rs = __builtin_amdgcn_make_buffer_rsrc((void*)setp, 0, SET_BYTES, 0x00020000);
  const unsigned tagA = epoch * 0x9E3779B1u + 0x7F4A7C15u, tagB = ~tagA;
  {
    const float c = bf_lo(cval);
    const float s = c * __builtin_amdgcn_rcpf(1.0f + __builtin_amdgcn_exp2f(-LOG2E * c));
    *(LAS u16*)(L + lrw + 3072 + (lane & 31) * 2) = (u16)pack2(s, s);
    SB();
#define XPUB(i) do { stg(rs, (tid >> 2) * 65536 + (tid & 3) * 16, OFF_XG + (i) * 16384 + id * 64, v4u{xa[i], tagA, xb[i], tagB}); SB(); } while (0)
    asm volatile("" :: "v"(xb));
    STAMPA(29);
    XPUB(0);
    XPUB(1);
    STAMP(1);
    LAS unsigned char* a = L + lrw + (8 * g16 + q4) * 96 + p4 * 8;
    const v4u sc = lds128(L + lrw + 3072 + g16 * 16);
    const v4u m0 = trd2(a, a + 384), m1 = trd2(a + 32, a + 416), m2 = trd2(a + 64, a + 448);
    SB();
    f32x2 fa[8];
#pragma unroll
    for (int i = 0; i < 4; ++i) { fa[i] = f32x2{bf_lo(ra[i]), bf_hi(ra[i])}; fa[4 + i] = f32x2{bf_lo(rb[i]), bf_hi(rb[i])}; }
    const f32x2 s2 = ((fa[0] + fa[1]) + (fa[2] + fa[3])) + ((fa[4] + fa[5]) + (fa[6] + fa[7]));
    float mean = s2.x + s2.y;
    PIN(mean);
    SB();
    const f32x4 c0 = mfma16(sc, m0, f32x4{}), c1 = mfma16(sc, m1, f32x4{}), c2 = mfma16(sc, m2, f32x4{});
    SB();
    mean = swap16_add(row_sum(mean)) * (1.0f / 512.0f);
    PIN(mean);
    SB();
    XPUB(2);
    __builtin_amdgcn_raw_buffer_store_b64(v2u{sel(lane < 16, f2u(c0[0]), sel(lane < 32, f2u(c1[0]), f2u(c2[0]))), (lane & 1) ? tagB : tagA}, rs, mgo,
                                          OFF_MG + cc * 24 * 256 + ks * 16, SC1);
    SB();
    f32x2 q2 = {0.f, 0.f}, q3 = {0.f, 0.f};
#pragma unroll
    for (int i = 0; i < 8; i += 2) {
      const f32x2 d0 = fa[i] - f32x2{mean, mean}, d1 = fa[i + 1] - f32x2{mean, mean};
      q2 = __builtin_elementwise_fma(d0, d0, q2);
      q3 = __builtin_elementwise_fma(d1, d1, q3);
    }
    q2 += q3;
    const float var = rbf(rbf(swap16_add(row_sum(q2.x + q2.y)) * (1.0f / 512.0f)) + 1e-5f);
    __builtin_amdgcn_raw_buffer_store_b64(v2u{pack2(mean, __builtin_amdgcn_sqrtf(var)), hf ? tagB : tagA}, rs, sgo, OFF_SG + (id * 4 + w) * 16, SC1);
    SB();
    XPUB(3);
#undef XPUB
  }
  STAMP(2);
  unsigned dsk = tid == 136 ? uD : 0u;
  PIN(dsk);

  {
    const float dtv = rbf(__builtin_amdgcn_exp2f(LOG2E * (w >> 1 ? bf_hi(uldt) : bf_lo(uldt))));
    const float are = dtv * bf_lo(uAre) * LOG2E, aim = dtv * bf_lo(uAim) * INV2PI;
    const float br = dtv * bf_lo(uBre), bi = dtv * bf_lo(uBim), cr = bf_lo(uCre), ci = bf_lo(uCim);
    const f32x2 wv = {cr * br - ci * bi, cr * bi + ci * br};
    const unsigned row = L_UVS + (w >> 1) * UVS_CH + (w & 1) * 256;
    const f32x2 e1 = cexp(are, aim), e8 = cexp(8.f * are, -8.f * aim), e128 = cexp(128.f * are, 128.f * aim);
    const f32x2 e1p = {-e1.y, e1.x}, e8p = {-e8.y, e8.x}, e128p = {-e128.y, e128.x};
    const v4u pois = {POISON, POISON, POISON, POISON};
    f32x2 z[8], v, v2, y, y2;
#define M1(a) cmul(a, e1, e1p)
#define M8(a) cmul(a, e8, e8p)
#define MK(a) cmul(a, e128, e128p)
    z[0] = M1(wv);
    put4<40, 41, 42, 0>(row, wv, f32x2{1.f, 0.f}, e8, z[0]);
    W1DMA2(0, lrw + 0);
    v = M8(e8); z[1] = M1(z[0]); v2 = M8(v); z[2] = M1(z[1]);
    put4<43, 1, 44, 2>(row, v, z[1], v2, z[2]);
    v = M8(v2); z[3] = M1(z[2]); v2 = M8(v); z[4] = M1(z[3]);
    put4<45, 3, 46, 4>(row, v, z[3], v2, z[4]);
    W1DMA2(2, lrw + 2048);
    v = M8(v2); z[5] = M1(z[4]); v2 = M8(v); z[6] = M1(z[5]);
    put4<47, 5, 48, 6>(row, v, z[5], v2, z[6]);
    z[7] = M1(z[6]); z[0] = MK(z[0]); z[1] = MK(z[1]); z[2] = MK(z[2]);
    put4<7, 8, 9, 10>(row, z[7], z[0], z[1], z[2]);
    W1DMA2(4, lrw + 4096);
    z[3] = MK(z[3]); z[4] = MK(z[4]); z[5] = MK(z[5]); z[6] = MK(z[6]);
    put4<11, 12, 13, 14>(row, z[3], z[4], z[5], z[6]);
    z[7] = MK(z[7]); z[1] = MK(z[1]); z[3] = MK(z[3]); z[5] = MK(z[5]);
    put4<15, 16, 17, 18>(row, z[7], z[1], z[3], z[5]);
    W1DMA2(6, lrw + 6144);
    z[7] = MK(z[7]); z[1] = MK(z[1]); z[3] = MK(z[3]); z[5] = MK(z[5]);
    put4<19, 20, 21, 22>(row, z[7], z[1], z[3], z[5]);
    y = MK(z[7]); z[3] = MK(z[3]); y2 = MK(y); z[4] = MK(z[3]);
    put4<23, 24, 25, 26>(row, y, z[3], y2, z[4]);
    W1DMA2(8, lrw + 8192);
    y = MK(y2); z[3] = MK(z[4]); y2 = MK(y); z[4] = MK(z[3]);
    put4<27, 28, 29, 30>(row, y, z[3], y2, z[4]);
    y = MK(y2); v = M8(v2); y2 = MK(y); v2 = M8(v);
    put4<31, 49, 32, 50>(row, y, v, y2, v2);
    W1DMA2(10, lrw + 10240);
    y = MK(y2); v = M8(v2); y2 = MK(y); v2 = M8(v);
    put4<33, 51, 34, 52>(row, y, v, y2, v2);
    y = MK(y2); v = M8(v2); y2 = MK(y); v2 = M8(v);
    put4<35, 53, 36, 54>(row, y, v, y2, v2);
    stg(rs, za, zs, pois);
    y = MK(y2); v = M8(v2); y2 = MK(y); v2 = M8(v);
    put4<37, 55, 38, 56>(row, y, v, y2, v2);
    y = MK(y2);
    *(LAS unsigned*)(L + row + 39 * UVS_ROW + lane * 4) = pack2(y.x, y.y);
    stg(rs, zb, zs, pois);
#undef M1
#undef M8
#undef MK
  }
  BAR();
  STAMP(3);
  v4u gx[4], gs[4], gm, gn;
  const int xgo = OFF_XG + id * 16384;
#define S0LOADS() do { \
    _Pragma("unroll") for (int i = 0; i < 4; ++i) gx[i] = ldg(rs, tid * 16, xgo + i * 4096); \
    _Pragma("unroll") for (int i = 0; i < 4; ++i) gs[i] = ldg(rs, tid * 16, OFF_SG + i * 4096); \
    gm = ldg(rs, gmo, 0); gn = ldg(rs, gmo2, 0); } while (0)
  if (w < 3) {
    LAS unsigned char* ut = ub + w * 16 * UVS_ROW;
    v4u fv[2][8], fu[2][8];
#pragma unroll
    for (int s = 0; s < 8; ++s)
#pragma unroll
      for (int c = 0; c < 2; ++c) { fv[c][s] = lds128(ub + c * UVS_CH + 41 * UVS_ROW + s * 64); fu[c][s] = lds128(ut + c * UVS_CH + s * 64); SB(); }
    f32x4 a0 = {}, a1 = {};
#pragma unroll
    for (int s = 0; s < 8; ++s) { a0 = mfma16(fv[0][s], fu[0][s], a0); SB(); a1 = mfma16(fv[1][s], fu[1][s], a1); SB(); }
    *(LAS unsigned*)ktw[0] = pack2(a0[0] + bf_lo(dsk), a1[0] + bf_hi(dsk));
#pragma unroll
    for (int r = 1; r < 4; ++r) *(LAS unsigned*)ktw[r] = pack2(a0[r], a1[r]);
  }
  SB();
  S0LOADS();
  SB();
  BAR();
  {
    unsigned k[11];
#pragma unroll
    for (int i = 0; i < 11; ++i) k[i] = *(LAS unsigned*)koff[i];
    SB();
    if (w < 2) *(LAS v4u*)zp = v4u{0u, 0u, 0u, 0u};
#pragma unroll
    for (int c = 0; c < 2; ++c) {
      unsigned e[5], o[5];
#pragma unroll
      for (int i = 0; i < 5; ++i) {
        o[i] = __builtin_amdgcn_perm(k[2 * i + 1], k[2 * i], c ? 0x07060302u : 0x05040100u);
        e[i] = __builtin_amdgcn_perm(k[2 * i + 2], k[2 * i + 1], c ? 0x07060302u : 0x05040100u);
      }
      LAS unsigned char* d = L + L_KC + c * 4 * KC_STRIDE + 32 + tid * 16;
      *(LAS v4u*)(d) = v4u{e[1], e[2], e[3], e[4]};
      *(LAS v4u*)(d + KC_STRIDE) = v4u{o[1], o[2], o[3], o[4]};
      *(LAS v4u*)(d + 2 * KC_STRIDE) = v4u{e[0], e[1], e[2], e[3]};
      *(LAS v4u*)(d + 3 * KC_STRIDE) = v4u{o[0], o[1], o[2], o[3]};
    }
  }
  SB();
  STAMP(4);
  {
    unsigned bad = tagbad(tagbad(0u, gm, tagA, tagB), gn, tagA, tagB);
#pragma unroll
    for (int i = 0; i < 4; ++i) bad = tagbad(tagbad(bad, gx[i], tagA, tagB), gs[i], tagA, tagB);
    if (__builtin_expect(__any(bad != 0u), 0)) {
      for (unsigned spin = 0; spin < SPIN_CAP; ++spin) {
        FRESH();
        if (spin > 1) PAUSE();
        COUNT(20);
        bool again = false;
#pragma unroll
        for (int i = 0; i < 4; ++i) {
          if (__any(tagbad(0u, gx[i], tagA, tagB) != 0u)) { gx[i] = ldg(rs, tid * 16, xgo + i * 4096); again = true; }
          if (__any(tagbad(0u, gs[i], tagA, tagB) != 0u)) { gs[i] = ldg(rs, tid * 16, OFF_SG + i * 4096); again = true; }
        }
        if (__any(tagbad(tagbad(0u, gm, tagA, tagB), gn, tagA, tagB) != 0u)) { gm = ldg(rs, gmo, 0); gn = ldg(rs, gmo2, 0); again = true; }
        if (!again) break;
      }
    }
  }
  asm volatile("" :: "v"(wt), "v"(warm), "v"(pt));
  WAITV(0);
  STAMP(5);
  W1DMA2(12, lrw + 12288);
  float zsc;
  v4u mtp;
  {
    const int i0 = (int)pack2(row_sum(u2f(gm.x)), row_sum(u2f(gm.z))), i1 = (int)pack2(row_sum(u2f(gn.x)), row_sum(u2f(gn.z)));
    unsigned md[6];
#pragma unroll
    for (int k = 0; k < 6; ++k) md[k] = (unsigned)__builtin_amdgcn_readlane(k < 4 ? i0 : i1, 16 * (k & 3));
#define MOD(k, c) ((c) ? bf_hi(md[k]) : bf_lo(md[k]))
    float s0 = rbf(1.0f + MOD(4, 0)), s1 = rbf(1.0f + MOD(4, 1));
    s0 = s0 == 0.f ? 0x1p-30f : s0;
    s1 = s1 == 0.f ? 0x1p-30f : s1;
    const float G0 = MOD(2, 0) * s0, G1 = MOD(2, 1) * s1;
    const float A0 = rbf(1.0f + MOD(1, 0)) * G0, A1 = rbf(1.0f + MOD(1, 1)) * G1, B0 = MOD(0, 0) * G0, B1 = MOD(0, 1) * G1;
    zsc = w >> 1 ? s1 : s0;
    mtp = v4u{lane ? md[5] : pack2(s0, s1), tagA, md[3], tagB};
#undef MOD
#pragma unroll
    for (int i = 0; i < 4; ++i) {
      const float mA = bf_lo(gs[i].x), rA = __builtin_amdgcn_rcpf(bf_hi(gs[i].x)), mB = bf_lo(gs[i].z), rB = __builtin_amdgcn_rcpf(bf_hi(gs[i].z));
      const float n00 = (bf_lo(gx[i].x) - mA) * rA, n01 = (bf_hi(gx[i].x) - mA) * rA, n10 = (bf_lo(gx[i].z) - mB) * rB, n11 = (bf_hi(gx[i].z) - mB) * rB;
      const int o = (tid + 256 * i) * 4;
      W1DMA2(14 + 2 * i, lrw + (14 + 2 * i) * 1024);
      *(LAS unsigned*)(L + L_H + 480 + o) = pack2(__builtin_fmaf(n00, A0, B0), __builtin_fmaf(n10, A0, B0));
      *(LAS unsigned*)(L + L_H + H_STRIDE + 480 + o) = pack2(__builtin_fmaf(n01, A1, B1), __builtin_fmaf(n11, A1, B1));
      *(LAS unsigned*)(xbw + i * 512) = __builtin_amdgcn_perm(gx[i].z, gx[i].x, 0x05040100u);
      *(LAS unsigned*)(xbw + 4096 + i * 512) = __builtin_amdgcn_perm(gx[i].z, gx[i].x, 0x07060302u);
    }
  }
  BAR();
  const unsigned tb = now();
  STAMP(6);

  v4u w1f[32], w2f[28][2], mt0, mt1;
  {
    const bool rh = w & 1;
    constexpr ConvOrder O = conv_order(), OL = conv_order_last();
    v2u A[4][8][2], xk[4];
    v4u B[4][8];
    f32x4 acc[8], keep[4];
#pragma unroll
    for (int u = 0; u < 8; ++u) {
      A[0][u][0] = trd(ka + 512 * u + 8);
      A[0][u][1] = trd(ka + 512 * u);
      B[0][7 - u] = lds128(hb + 64 * (8 * (7 - u) + 3));
    }
    SB();
    if (lane) mtp.z = tb;
    stg(rs, tid < 2 ? tid * 16 : OOB, OFF_MT + id * 128, mtp);
    SB();
    auto hand_over = [&](auto Pc) __attribute__((always_inline)) {
      constexpr int i = Pc.v;
      PIN(acc[2 * i]); PIN(acc[2 * i + 1]);
      const f32x4 ev = acc[2 * i], od = acc[2 * i + 1];
      const f32x4 snd = rh ? ev : od, own = rh ? od : ev;
      *(LAS f32x4*)(i < 2 ? exa + i * 1024 : exb + (i - 1) * 1024) = snd;
#pragma unroll
      for (int c = 0; c < 4; ++c) { float t = keep[i][c] + own[c]; PIN(t); keep[i][c] = t; }
    };
    static_for<0, 144>([&](auto Kc) __attribute__((always_inline)) {
      constexpr int K = Kc.v, r = K / 36, k = K % 36, t = r < 3 ? O.t[k] : OL.t[k], u = r < 3 ? O.u[k] : OL.u[k];
      if constexpr (k == 0 && r > 0) asm volatile("" :: "v"(B[r][7]));
      mfma16_v<r == 0 && u == 0>(acc[t], v4u{A[r][u][0].x, A[r][u][0].y, A[r][u][1].x, A[r][u][1].y}, B[r][t - u]);
      SB();
      if constexpr (K < 12) w1f[K] = trd2(w1a + K * 1024, w1a + K * 1024 + 256);
      if constexpr (K == 60) WAITV(9);
      if constexpr (K >= 60 && K < 70) w1f[K - 48] = trd2(w1a + (K - 48) * 1024, w1a + (K - 48) * 1024 + 256);
      if constexpr (K == 76) asm volatile("" :: "a"(w1f[21]));
      if constexpr (r == 2 && k < 4) xk[k] = *(LAS v2u*)(xbr + k * 512);
      constexpr int n = k - (r == 0 ? 9 : r == 1 ? 0 : 4);
      if constexpr (r < 3 && n >= 0 && n < 24) {
        if constexpr (n % 3 == 2) B[r + 1][n / 3] = lds128(hb + 64 * (8 * (n / 3) - r + 2));
        else A[r + 1][n / 3][n % 3] = trd(ka + 512 * (n / 3) + 64 * (r + 1) + 8 - 8 * (n % 3));
      }
      if constexpr (K >= 100 && K < 116) {
        constexpr int e = K - 100;
        const unsigned xw = (e & 2) ? xk[e >> 2].y : xk[e >> 2].x;
        float t2_ = zsc * ((e & 1) ? bf_hi(xw) : bf_lo(xw));
        PIN(t2_);
        keep[e >> 2][e & 3] = t2_;
      }
      if constexpr (K == 108 + 8) hand_over(IC<0>{});
      if constexpr (K == 108 + 17) hand_over(IC<1>{});
      if constexpr (K == 108 + 30) hand_over(IC<2>{});
      if constexpr (K == 14) W1DMA(22, L_RX + w * 3072);
      if constexpr (K == 22) W1DMA(23, L_TOP + w * 1024);
      if constexpr (K >= 37 && K < 69 && K % 8 == 5) W1DMA2(24 + (K - 37) / 4, lrw + (K - 37) / 4 * 1024);
      if constexpr (K >= 69 && K < 125 && K % 8 == 5) W2DMA((K - 69) / 8, lrw + 8192 + (K - 69) / 8 * 2048);
      if constexpr (K == MTK)
        asm volatile("buffer_load_dwordx4 %0, %2, %3, 0 offen sc1\n\tbuffer_load_dwordx4 %1, %2, %3, %4 offen sc1"
                     : "=&v"(mt0), "=&v"(mt1) : "v"(mto), "s"(rs), "s"(16384) : "memory");
      SB();
    });
    STAMP(7);
    asm volatile("s_nop 7" : "+v"(acc[6]), "+v"(acc[7]));
    hand_over(IC<3>{});
    BAR();
#pragma unroll
    for (int i = 0; i < 4; ++i) keep[i] += *(LAS f32x4*)(i < 2 ? eya + i * 1024 : eyb + (i - 1) * 1024);
#pragma unroll
    for (int i = 0; i < 2; ++i) {
      const auto s0 = __builtin_amdgcn_permlane16_swap(pack2(keep[2 * i][0], keep[2 * i][1]), pack2(keep[2 * i + 1][0], keep[2 * i + 1][1]), false, false);
      const auto s1 = __builtin_amdgcn_permlane16_swap(pack2(keep[2 * i][2], keep[2 * i][3]), pack2(keep[2 * i + 1][2], keep[2 * i + 1][3]), false, false);
      stg(rs, i ? zb : za, zs, v4u{guard(s0[0]), guard(s1[0]), guard(s0[1]), guard(s1[1])});
    }
    SB();
  }
  STAMP(8);

  static_assert(OFF_MT == 0, "table offset");
  const unsigned te = now();
  asm volatile("s_waitcnt vmcnt(2)" : "+v"(mt0), "+v"(mt1) :: "memory");
  if (__builtin_expect(__any(tagbad(tagbad(0u, mt0, tagA, tagB), mt1, tagA, tagB) != 0u), 0)) {
    for (unsigned spin = 0; spin < SPIN_CAP; ++spin) {
      FRESH();
      COUNT(21);
      if (tagbad(0u, mt0, tagA, tagB)) mt0 = ldg(rs, mto, OFF_MT);
      if (tagbad(0u, mt1, tagA, tagB)) mt1 = ldg(rs, mto, OFF_MT + 16384);
      if (!__any(tagbad(tagbad(0u, mt0, tagA, tagB), mt1, tagA, tagB) != 0u)) break;
      PAUSE();
    }
  }
  *(LAS unsigned*)(mtw) = mt0.x;
  *(LAS unsigned*)(mtw + 512) = mt1.x;
  *(LAS unsigned*)(mtw + 1024) = mt0.z;
  *(LAS unsigned*)(mtw + 1536) = mt1.z;
  {
    int late = (lane & 1) ? max((int)(mt0.z - tb), (int)(mt1.z - tb)) : 0;
    late = max(late, __builtin_amdgcn_update_dpp(late, late, 0xB1, 0xF, 0xF, false));
    late = max(late, __builtin_amdgcn_update_dpp(late, late, 0x4E, 0xF, 0xF, false));
    late = max(late, __builtin_amdgcn_update_dpp(late, late, 0x141, 0xF, 0xF, false));
    late = max(late, __builtin_amdgcn_update_dpp(late, late, 0x140, 0xF, 0xF, false));
    late = max(late, __builtin_amdgcn_update_dpp(late, late, 0x142, 0xA, 0xF, false));
    late = max(late, __builtin_amdgcn_update_dpp(late, late, 0x143, 0xC, 0xF, false));
    if (lane == 63) *(LAS int*)(L + L_ETA + w * 4) = late;
  }
  w1f[22] = trd2(L + L_RX + w * 3072 + tra, L + L_RX + w * 3072 + tra + 256);
  w1f[23] = trd2(L + L_TOP + w * 1024 + tra, L + L_TOP + w * 1024 + tra + 256);
  BAR();
  STAMP(9);
  const int w2t = (4 * hf + q4) * 128 + blk * 32 + (((p4 & 1) << 1) | (p4 >> 1)) * 8;
  LAS unsigned char* w2a = L + w2t + (q4 >> 1) * 64;
  LAS unsigned char* w2b = L + w2t + ((q4 >> 1) ^ 1) * 64;
  const int par = (0x1C >> (ct % 7)) & 1;
  const unsigned char* xg = setp + OFF_Z + rp * 131072;
  const unsigned xl = L_XR + w * 8192;
#define XSRC(p) (xg + w * 8192 + ((p) ^ par) * 32768)
  const int slo = OFF_Z + (rp * 4 + w) * 32768 + si * 2048;
  f32x4 uu[4], vv[4];
  v4u sl0, sl1;
  {
    LAS unsigned char* qr = L + ((lane & 31) == 0 ? L_TS : L_TB) + hf * 16;
    LAS unsigned char *pa = w2a + lrw + 8192, *pb = w2b + lrw + 8192;
    PIN(pa); PIN(pb);
    f32x16 uv, uv2;
    v4u qa[32];
    int wait = 0;
    const v4u lt = lds128(L + L_ETA);
    bool sent = false;
    asm volatile("" : "=v"(sl0), "=v"(sl1));
#pragma unroll
    for (int i = 0; i < 4; ++i) qa[i] = lds128(qr + i * 32);
    static_for<0, 32>([&](auto Sc) __attribute__((always_inline)) {
      constexpr int s = Sc.v;
      mfma_va<(s < 2)>((s & 1) ? uv2 : uv, qa[s], w1f[s]);
      SB();
      if constexpr (s < 28) qa[s + 4] = lds128(qr + (s + 4) * 32);
      if constexpr (s > 0) asm volatile("" :: "v"(qa[s > 0 ? s - 1 : 0]));
      if constexpr (s < 8) w1f[24 + s] = trd2(w1a + s * 1024, w1a + s * 1024 + 256);
      if constexpr (s == 4) { wait = min(__builtin_amdgcn_readfirstlane(max(max((int)lt.x, (int)lt.y), max((int)lt.z, (int)lt.w))), 300) + ZLAG; }
      if constexpr (s >= 8 && s < 22) w2f[(s - 8) >> 1][s & 1] = trd2(((s & 1) ? pb : pa) + ((s - 8) >> 1) * 2048, ((s & 1) ? pb : pa) + ((s - 8) >> 1) * 2048 + 1024);
      if constexpr (s == 4 || s == 13 || s == 22 || s == 31) {
        if (!sent) {
          if constexpr (s == 31) { while ((int)(now() - te) < wait) { } }
          if (s == 31 || (int)(now() - te) >= wait) {
            sent = true;
            STAMPB(26);
            asm volatile("buffer_load_dwordx4 %0, %2, %3, %4 offen sc1\n\tbuffer_load_dwordx4 %1, %2, %3, %4 offen offset:1024 sc1"
                         :: "v"(sl0), "v"(sl1), "v"(l16), "s"(rs), "s"(slo) : "memory");
            dma_x4(XSRC(0), l16, xl);
            dma_x4(XSRC(0) + 4096, l16, xl + 4096);
          }
        }
      }
      SB();
    });
    dma_x4(XSRC(1), l16, xl + 32768);
    dma_x4(XSRC(1) + 4096, l16, xl + 32768 + 4096);
    STAMPB(27);
    BAR();
    mfma_done(uv2);
    PIN(uv);
    LAS unsigned char* uw = L + L_UVT + (32 * w + (lane & 31)) * 4;
    if (lane < 32) { *(LAS float*)(uw) = uv[0] + uv2[0]; *(LAS float*)(uw + 512) = -LOG2E * (uv[1] + uv2[1]); }
    LAS unsigned char* uvp = L + L_UVT + (32 * w + 4 * hf) * 4;
#pragma unroll
    for (int i = 0; i < 4; ++i) { uu[i] = *(LAS f32x4*)(uvp + i * 32); vv[i] = *(LAS f32x4*)(uvp + 512 + i * 32); }
  }
  {
    const float i0 = __builtin_amdgcn_rcpf(bf_lo(*(LAS u16*)(L + L_TS + (si * 32 + (lane >> 2)) * 2)));
    const float i1 = __builtin_amdgcn_rcpf(bf_lo(*(LAS u16*)(L + L_TS + (si * 32 + 16 + (lane >> 2)) * 2)));
    SB();
    asm volatile("s_waitcnt vmcnt(16)" : "+v"(sl0), "+v"(sl1) :: "memory");
    STAMPB(28);
    if (__builtin_expect(__any(max4(max4(0u, sl0), sl1) == POISON), 0)) {
      for (unsigned spin = 0; spin < SPIN_CAP; ++spin) {
        FRESH();
        if (spin > 1) PAUSE();
        COUNT(22);
        sl0 = ldg(rs, l16, slo);
        sl1 = ldg(rs, l16 + 1024, slo);
        if (!__any(max4(max4(0u, sl0), sl1) == POISON)) break;
      }
    }
    float sm[8], sq[8];
#pragma unroll
    for (int i = 0; i < 4; ++i) {
      const f32x2 a = f32x2{bf_lo(sl0[i]), bf_hi(sl0[i])} * f32x2{i0, i0}, c = f32x2{bf_lo(sl1[i]), bf_hi(sl1[i])} * f32x2{i1, i1};
      const f32x2 s2 = a + c, q2 = __builtin_elementwise_fma(a, a, c * c);
      sm[2 * i] = s2.x; sm[2 * i + 1] = s2.y; sq[2 * i] = q2.x; sq[2 * i + 1] = q2.y;
    }
#pragma unroll
    for (int i = 0; i < 4; ++i) { swap_add32(sm[i], sm[i + 4]); swap_add32(sq[i], sq[i + 4]); }
#pragma unroll
    for (int i = 0; i < 2; ++i) { swap_add16(sm[i], sm[i + 2]); swap_add16(sq[i], sq[i + 2]); }
#pragma unroll
    for (int i = 0; i < 2; ++i) { sm[i] = dpp_add<0x124>(dpp_add<0x128>(sm[i])); sq[i] = dpp_add<0x124>(dpp_add<0x128>(sq[i])); }
    const bool od = lane & 4;
    stg(rs, (lane & 8) ? OOB : (p4 * 8 + hf * 4 + blk * 2 + (od ? 1 : 0)) * 16, OFF_SX + ((rp * 4 + w) * 16 + si) * 512,
        v4u{f2u(od ? sm[1] : sm[0]), tagA, f2u(od ? sq[1] : sq[0]), tagB});
    SB();
  }
  STAMPB(29);
  dma_x4(XSRC(2), l16, xl + 65536);
  dma_x4(XSRC(2) + 4096, l16, xl + 65536 + 4096);
  WAITV(17);
  STAMP(10);
  {
    v4u t[8];
    unsigned m = 0, m2 = 0;
#pragma unroll
    for (int i = 0; i < 8; ++i) t[i] = lds128(L + xl + l16 + i * 1024);
#pragma unroll
    for (int i = 0; i < 8; i += 2) { m = max4(m, t[i]); m2 = max4(m2, t[i + 1]); }
    if (__builtin_expect(__any(max(m, m2) == POISON), 0)) {
      for (unsigned spin = 0; spin < SPIN_CAP; ++spin) {
        COUNT(22);
        bool again = false;
#pragma nounroll
        for (int i = 0; i < 8; ++i)
          if (__any(max4(0u, lds128(L + xl + l16 + i * 1024)) == POISON)) {
            again = true;
            dma_x1(XSRC(0) + i * 1024, l16, xl + i * 1024);
            dma_x1(XSRC(1) + i * 1024, l16, xl + 32768 + i * 1024);
            dma_x1(XSRC(2) + i * 1024, l16, xl + 65536 + i * 1024);
          }
        if (!again) break;
        WAITV(0);
        if (spin) PAUSE();
      }
    }
  }
  BAR();
  STAMP(11);

  constexpr int HW = 40;
  v4u hq[HW], res[2], sx[8];
  const int c0 = 7 * w + rot, c3 = c0 - 7 * (rot >> 2), c4 = c0 - 7 * ((rot + 1) >> 2), ckv = lane < 8 ? l16 : OOB;
  const int hb0 = OFF_HID + (rp * 4 + rg) * 114688, kb0 = OFF_CK + (rp * 4 + rg) * 4096;
#define CHUNK(n) (((n) % 35 / 5 < 3 ? c0 : (n) % 35 / 5 == 3 ? c3 : c4) + (n) % 35 / 5)
#define HLOAD(n) hq[(n) % HW] = (n) % 5 == 4 ? ldg(rs, ckv, kb0 + CHUNK(n) * 128 + ((n) / 35) * 8192) \
    : ldg(rs, l16 + ((n) % 5) * 1024, hb0 + CHUNK(n) * 4096 + ((n) / 35) * (2 * 114688))
  const int hoff = OFF_HID + (rp * 4 * 112 + ct * 8 + 2 * w) * 1024, hv0 = dt == 7 ? OOB : l16, hv1 = dt == 7 ? OOB : l16 + 1024;
  const int cko = OFF_CK + rp * 16384 + (ct * 4 + w) * 64, ckw = (p4 || dt == 7) ? OOB : lane;
  const unsigned ckey = lane ? 0u : tagA;
#define CKPUT(sum, u) __builtin_amdgcn_raw_buffer_store_b32(ckey - red_step<1>(red_step<0>(sum)), rs, ckw, cko + ((u) ^ par) * 4096, SC1)
  const int sxo = OFF_SX + (rp * 4 + w) * 8192;
  LAS unsigned char* xr[2];
#pragma unroll
  for (int i = 0; i < 2; ++i) { xr[i] = L + L_XR + tra + i * 65536; PIN(xr[i]); }
  LAS unsigned char* rr = L + L_XR + tra + (rg ^ par) * 32768 + (4 * dt + w) * 1024;
  LAS unsigned char* str = L + L_STAT + (lane & 31) * 8;
  PIN(str);
#define XA(t) (xr[(t) >> 6] + ((t) & 63) * 1024)
#define XF(t) trd2(XA(t), XA(t) + 256)
#define XEN
#define REDO(p) \
  if (XEN __builtin_expect(__any(!(acc[(p) % 3][0] == acc[(p) % 3][0])), 0)) { \
    for (unsigned spin = 0; spin < SPIN_CAP / 128; ++spin) { \
      COUNT(23); \
      _Pragma("nounroll") for (int i = 0; i < 8; ++i) dma_x4(xg + ((p) ^ par) * 32768 + i * 4096, l16, L_XR + (p) * 32768 + i * 4096); \
      WAITV(0); \
      _Pragma("unroll") for (int s = 0; s < 32; ++s) { \
        const v4u t = XF(32 * (p) + s); \
        if (s == 0) mfma_av<true>(acc[(p) % 3], w1f[s], t); else mfma_av<false>(acc[(p) % 3], w1f[s], t); \
        SB(); \
      } \
      mfma_done(acc[(p) % 3]); \
      if (!__any(!(acc[(p) % 3][0] == acc[(p) % 3][0]))) break; \
    } \
  }
  f32x16 acc[3];
  f32x2 ec[4];
  float ep[48], ee[48], ey[48];
  unsigned cs = 0;
  v4u x[128], eo;
#pragma unroll
  for (int t = 0; t < 4; ++t) x[t] = XF(t);
  SB();
  static_for<0, 64>([&](auto Jc) __attribute__((always_inline)) {
    constexpr int J = Jc.v, p = J >> 4, j = J & 15;
    asm volatile("" :: "v"(x[2 * J + 1]));
    mfma_av<j == 0>(acc[p % 3], w1f[2 * j], x[2 * J]);
    if constexpr (J < 62) { x[2 * J + 4] = XF(2 * J + 4); x[2 * J + 5] = XF(2 * J + 5); }
    SB();
    mfma_av<false>(acc[p % 3], w1f[2 * j + 1], x[2 * J + 1]);
    SB();
    if constexpr (J == 10) { WAITV(16); __builtin_amdgcn_s_barrier(); FRESH(); }
    if constexpr (J == EO - 2) {
      unsigned bad = 0;
#pragma unroll
      for (int i = 0; i < 8; ++i) bad = tagbad(bad, sx[i], tagA, tagB);
      if (__builtin_expect(__any(bad != 0u), 0)) {
        for (unsigned spin = 0; spin < SPIN_CAP; ++spin) {
          FRESH();
          if (spin > 1) PAUSE();
          COUNT(23);
          bad = 0;
#pragma unroll
          for (int i = 0; i < 8; ++i) sx[i] = ldg(rs, l16 + (i & 3) * 1024, sxo + (i >> 2) * 4096);
#pragma unroll
          for (int i = 0; i < 8; ++i) bad = tagbad(bad, sx[i], tagA, tagB);
          if (!__any(bad != 0u)) break;
        }
      }
      float sm = u2f(sx[0].x), sq = u2f(sx[0].z);
#pragma unroll
      for (int i = 1; i < 8; ++i) { sm += u2f(sx[i].x); PIN(sm); sq += u2f(sx[i].z); PIN(sq); }
      const float mean = swap32_add(sm) * (1.0f / 512.0f);
      const float ms = __builtin_fmaf(swap32_add(sq), 1.0f / 512.0f, 1e-5f);
      const float a = -LOG2E * __builtin_amdgcn_rsqf(fmaxf(__builtin_fmaf(-mean, mean, ms), 1e-5f));
      *(LAS f32x2*)(str + (w ^ par) * 256) = f32x2{a, -a * mean};
      SB();
    }
    if constexpr (J == EO - 1) {
      asm volatile("s_waitcnt lgkmcnt(4)" ::: "memory");
      __builtin_amdgcn_s_barrier();
      FRESH();
#pragma unroll
      for (int i = 0; i < 4; ++i) ec[i] = *(LAS f32x2*)(str + i * 256);
      SB();
    }
    static_for<2 * J, 2 * J + 2>([&](auto Sc) __attribute__((always_inline)) {
      static_for<0, 48>([&](auto Vc) __attribute__((always_inline)) {
        constexpr int S = Sc.v, V = Vc.v, k = V & 15;
        if constexpr (S == epi_slot(V) + 2 * epi_dist(V)) {
          ey[V] = ep[V] * ee[V];
          if constexpr (k & 1) { unsigned o = pack2(ey[V - 1], ey[V]); PIN(o); eo[(k >> 1) & 3] = o; } else PIN(ey[V]);
          if constexpr (k == 7) { stg(rs, hv0, hoff + ((V >> 4) ^ par) * 114688, eo); cs = sum4(0u, eo); PIN(cs); }
          if constexpr (k == 15) { stg(rs, hv1, hoff + ((V >> 4) ^ par) * 114688, eo); CKPUT(sum4(cs, eo), V >> 4); }
        }
        if constexpr (S == epi_slot(V) + epi_dist(V)) { ee[V] = __builtin_amdgcn_rcpf(1.0f + ee[V]); PIN(ee[V]); }
        if constexpr (S == epi_slot(V)) {
          ep[V] = __builtin_fmaf(ec[V >> 4].x, acc[(V >> 4) % 3][k], __builtin_fmaf(ec[V >> 4].y, uu[k >> 2][k & 3], vv[k >> 2][k & 3]));
          ee[V] = __builtin_amdgcn_exp2f(ep[V]);
          PIN(ee[V]);
        }
      });
      SB();
    });
    if constexpr (j == 1 && p >= 1) {
      if constexpr (p == 2) REDO(1)
      if constexpr (p == 3) REDO(2)
      if (((p - 1) & 1) == (rg ^ par)) res[(p - 1) >> 1] = trd2(rr + ((p - 1) >> 1) * 65536, rr + ((p - 1) >> 1) * 65536 + 256);
    }
    if constexpr (j == 2 && p >= 2) {
      asm volatile("s_waitcnt lgkmcnt(4)" ::: "memory");
      __builtin_amdgcn_s_barrier();
      FRESH();
    }
    if constexpr (J < 8) {
      if constexpr ((J & 3) == 0) set_m0(xl + 98304 + J * 1024);
      dma_xn<(J & 3) * 1024>(XSRC(3) + (J >> 2) * 4096, l16);
    }
    if constexpr (J >= SXL && J < SXL + 8) sx[J - SXL] = ldg(rs, l16 + ((J - SXL) & 3) * 1024, sxo + ((J - SXL) >> 2) * 4096);
    if constexpr (J >= EO && J < EO + 22 && ((J - EO) & 1) == 0) {
      constexpr int n = (J - EO) >> 1;
      if constexpr (n < 3) W2DMA(7 + n, L_SP + w * 6144 + n * 2048);
      else W2DMA(7 + n, L_XR + ((n - 3) >> 2) * 32768 + w * 8192 + ((n - 3) & 3) * 2048);
    }
    if constexpr (J >= 48 && J < 56) { HLOAD(2 * (J - 48)); if constexpr (J < 55) HLOAD(2 * (J - 48) + 1); }
    if constexpr (J >= 56 && J < 64 && (J & 1) == 0) W2DMA(18 + ((J - 56) >> 1), L_XR + 65536 + w * 8192 + ((J - 56) >> 1) * 2048);
    SB();
    if constexpr (J == 15) STAMP(16);
    if constexpr (J == 31) STAMP(17);
    if constexpr (J == 47) STAMP(18);
  });
  STAMP(12);
  static_assert(EO + 6 >= 26 && EO + 14 > 34 && EO + 20 < 48 && epi_slot(16) >= 2 * 34 && epi_slot(31) < epi_slot(32) && epi_slot(47) + 2 < 128, "schedule");
  static_for<15, 25>([&](auto Ic) __attribute__((always_inline)) { HLOAD(Ic.v); });
  SB();
  mfma_done(acc[0]);
  REDO(3)
  if (rg ^ par) res[1] = trd2(rr + 65536, rr + 65536 + 256);
#pragma unroll
  for (int h = 0; h < 2; ++h) {
    v4u o;
#pragma unroll
    for (int i = 0; i < 4; ++i) {
      const int k = 8 * h + 2 * i;
      const f32x2 t = __builtin_elementwise_fma(f32x2{ec[3].y, ec[3].y}, f32x2{uu[k >> 2][k & 3], uu[k >> 2][(k & 3) + 1]}, f32x2{vv[k >> 2][k & 3], vv[k >> 2][(k & 3) + 1]});
      const f32x2 pp = __builtin_elementwise_fma(f32x2{ec[3].x, ec[3].x}, f32x2{acc[0][k], acc[0][k + 1]}, t);
      const f32x2 d = f32x2{__builtin_amdgcn_exp2f(pp.x), __builtin_amdgcn_exp2f(pp.y)} + f32x2{1.0f, 1.0f};
      const f32x2 y = pp * f32x2{__builtin_amdgcn_rcpf(d.x), __builtin_amdgcn_rcpf(d.y)};
      o[i] = pack2(y.x, y.y);
    }
    stg(rs, h ? hv1 : hv0, hoff + (3 ^ par) * 114688, o);
    if (h) CKPUT(sum4(cs, o), 3); else cs = sum4(0u, o);
    SB();
  }
  BAR();
  STAMP(13);

  f32x16 d[2][2];
  LAS unsigned char* redw = L + L_XR + w * 8192 + l16;
  LAS unsigned char* redr = L + L_XR + w * 2048 + l16;
#define REDW(un, q) *(LAS f32x4*)(redw + (un) * 32768 + (q) * 1024) = \
    f32x4{d[un][(q) >> 2][4 * ((q) & 3)], d[un][(q) >> 2][4 * ((q) & 3) + 1], d[un][(q) >> 2][4 * ((q) & 3) + 2], d[un][(q) >> 2][4 * ((q) & 3) + 3]}
  LAS unsigned char *w2sa = w2a + L_SP + w * 6144, *w2sb = w2b + L_SP + w * 6144;
  w2a += L_XR + w * 8192; w2b += L_XR + w * 8192;
  LAS unsigned char *w2ua = w2a + 65536, *w2ub = w2b + 65536;
  PIN(w2a); PIN(w2b); PIN(w2sa); PIN(w2sb); PIN(w2ua); PIN(w2ub);
#define PULL(step, pa, pb, off) do { w2f[step][0] = trd2((pa) + (off), (pa) + (off) + 1024); w2f[step][1] = trd2((pb) + (off), (pb) + (off) + 1024); } while (0)
  unsigned m = red_all(sum4(sum8(sum8(0u, hq[0], hq[1]), hq[2], hq[3]), hq[4]));
  v4u gbv, scv;
  f32x4 pr[8];
  float gb[8], rx[2][8];
  unsigned orow = (rp * 128 + ((lane & 31) >> 3) * 16 + rg * 8 + (lane & 7)) * 1024 + dt * 128 + w * 32 + hf * 16;
  static_for<0, 14>([&](auto Gc) __attribute__((always_inline)) {
    constexpr int G = Gc.v, un = G / 7, gq = G % 7;
    if (__builtin_expect((unsigned)__builtin_amdgcn_readlane((int)m, 63) != 2u * tagA, 0)) {
      for (unsigned spin = 0; spin < SPIN_CAP; ++spin) {
        FRESH();
        COUNT(24 + un);
        static_for<0, 5>([&](auto Ic) __attribute__((always_inline)) { HLOAD(5 * G + Ic.v); });
        m = sum4(sum8(sum8(0u, hq[(5 * G) % HW], hq[(5 * G + 1) % HW]), hq[(5 * G + 2) % HW], hq[(5 * G + 3) % HW]), hq[(5 * G + 4) % HW]);
        if ((unsigned)__builtin_amdgcn_readlane((int)red_all(m), 63) == 2u * tagA) break;
        PAUSE();
      }
    }
    static_for<0, 8>([&](auto Ic) __attribute__((always_inline)) {
      constexpr int c = Ic.v, i = G < 13 ? c >> 1 : c & 3, e = G < 13 ? c & 1 : c >> 2, I = 4 * G + i, n = 5 * G + i, X = 8 * G + c;
      mfma_av<gq == 0 && i == 0>(d[un][e], w2f[gq * 4 + i][e], hq[n % HW]);
      SB();
      if constexpr (G < 13 && e == 0) {
        if constexpr (I < 3) PULL(7 + I, w2sa, w2sb, I * 2048);
        if constexpr (I >= 3 && I < 11) PULL(7 + I, w2a, w2b, ((I - 3) >> 2) * 32768 + ((I - 3) & 3) * 2048);
        if constexpr (I >= 11 && I < 15) PULL(7 + I, w2ua, w2ub, (I - 11) * 2048);
        if constexpr (I == 18) waitv<4 + hl_upto(18) - 35>();
        if constexpr (I >= 18 && I < 22) PULL(4 + I, w2ua, w2ub, 32768 + (I - 18) * 2048);
        if constexpr (I == 23) waitv<hl_upto(23) - 35>();
        if constexpr (I == 23 || I == 24) PULL(3 + I, w2sa, w2sb, (I - 23) * 2048);
        if constexpr (I >= 29 && I < 37) REDW(0, I - 29);
      }
      if constexpr (G < 13 && e == 1) {
        if constexpr (I >= 4 && I < 8) W2DMA(18 + I, L_XR + 98304 + w * 8192 + (I - 4) * 2048);
        if constexpr (I == 8) { W2DMA(26, L_SP + w * 6144); W2DMA(27, L_SP + w * 6144 + 2048); }
        static_for<hl_upto(I), hl_upto(I + 1)>([&](auto Nc) __attribute__((always_inline)) { HLOAD(Nc.v); });
      }
      if constexpr (G < 13) {
        constexpr int q = 5 * G + 5;
        if constexpr (c == 1) m = sum8(0u, hq[q % HW], hq[(q + 1) % HW]);
        if constexpr (c == 3) m = sum4(sum8(m, hq[(q + 2) % HW], hq[(q + 3) % HW]), hq[(q + 4) % HW]);
        if constexpr (c == 4) m = red_step<0>(m);
        if constexpr (c == 5) m = red_step<2>(red_step<1>(m));
        if constexpr (c == 6) m = red_step<3>(m);
        if constexpr (c == 7) m = red_step<5>(red_step<4>(m));
        PIN(m);
      }
      if constexpr (X == 74) { gbv = lds128(L + L_TG + (64 * dt + 16 * w + 8 * hf) * 2); scv = lds128(L + L_TS + (64 * dt + 16 * w + 8 * hf) * 2); PIN(orow); }
      if constexpr (X >= 76 && X < 92 && (X & 1) == 0) {
        constexpr int q = (X - 76) >> 1;
        const float inv = __builtin_amdgcn_rcpf((q & 1) ? bf_hi(scv[q >> 1]) : bf_lo(scv[q >> 1]));
        gb[q] = -LN2 * ((q & 1) ? bf_hi(gbv[q >> 1]) : bf_lo(gbv[q >> 1]));
        rx[0][q] = inv * ((q & 1) ? bf_hi(res[0][q >> 1]) : bf_lo(res[0][q >> 1]));
        rx[1][q] = inv * ((q & 1) ? bf_hi(res[1][q >> 1]) : bf_lo(res[1][q >> 1]));
        PIN(gb[q]); PIN(rx[0][q]); PIN(rx[1][q]);
      }
      if constexpr (X == 92) BAR();
      if constexpr (X >= 93 && X < 97) { pr[2 * (X - 93)] = *(LAS f32x4*)(redr + (X - 93) * 8192); pr[2 * (X - 93) + 1] = *(LAS f32x4*)(redr + (X - 93) * 8192 + 1024); }
      if constexpr (X >= 98 && X < 106) {
        constexpr int q = X - 98;
        float t = (pr[q >> 2][q & 3] + pr[2 + (q >> 2)][q & 3]);
        PIN(t);
        float t2_ = (pr[4 + (q >> 2)][q & 3] + pr[6 + (q >> 2)][q & 3]);
        PIN(t2_);
        rx[0][q] = __builtin_fmaf(gb[q], t + t2_, rx[0][q]);
        PIN(rx[0][q]);
      }
      if constexpr (X == 106)
        *(GAS v4u*)((GAS unsigned char*)out + orow) = v4u{pack2(rx[0][0], rx[0][1]), pack2(rx[0][2], rx[0][3]), pack2(rx[0][4], rx[0][5]), pack2(rx[0][6], rx[0][7])};
      if constexpr (X == 109) asm volatile("s_nop 9" : "+v"(d[1][0]));
      if constexpr (X >= 109) REDW(1, X - 109);
      SB();
    });
    if constexpr (G == 6) STAMP(14);
  });
  STAMP(19);
  REDW(1, 3);
  mfma_done(d[1][1]);
#pragma unroll
  for (int q = 4; q < 8; ++q) REDW(1, q);
  BAR();
  {
    f32x4 s0 = {}, s1 = {};
#pragma unroll
    for (int k = 0; k < 4; ++k) { s0 += *(LAS f32x4*)(redr + k * 8192 + 32768); s1 += *(LAS f32x4*)(redr + k * 8192 + 32768 + 1024); }
    float y[8];
#pragma unroll
    for (int q = 0; q < 8; ++q) y[q] = __builtin_fmaf(gb[q], q < 4 ? s0[q & 3] : s1[q & 3], rx[1][q]);
    *(GAS v4u*)((GAS unsigned char*)out + orow + 65536) = v4u{pack2(y[0], y[1]), pack2(y[2], y[3]), pack2(y[4], y[5]), pack2(y[6], y[7])};
  }
  if (b == 0 && tid == 0) __hip_atomic_store((GAS unsigned*)ws, epoch + 1u, __ATOMIC_RELAXED, __HIP_MEMORY_SCOPE_AGENT);
  STAMP(15);
}

extern "C" void kernel_launch(void* const* d_in, const int* in_sizes, int n_in, void* d_out, int out_size, void* d_ws, size_t ws_size,
                              hipStream_t stream) {
  (void)in_sizes; (void)n_in; (void)out_size; (void)ws_size;
  (void)hipFuncSetAttribute((const void*)s4_block_kernel, hipFuncAttributeMaxDynamicSharedMemorySize, LDS_BYTES);
  s4_block_kernel<<<256, 256, LDS_BYTES, stream>>>((cptr)d_in[0], (cptr)d_in[1], (cptr)d_in[2], (cptr)d_in[3], (cptr)d_in[4], (cptr)d_in[5], (cptr)d_in[6],
                                                   (cptr)d_in[7], (cptr)d_in[8], (cptr)d_in[10], (cptr)d_in[11], (cptr)d_in[12], (cptr)d_in[13],
                                                   (unsigned char*)d_out, (unsigned char*)d_ws, (cptr)d_in[9]);
}
```

```cpp
#include <hip/hip_runtime.h>

#define GAS __attribute__((address_space(1)))
#define LAS __attribute__((address_space(3)))
#define DI __device__ __forceinline__
typedef unsigned v4u __attribute__((ext_vector_type(4)));
typedef unsigned v2u __attribute__((ext_vector_type(2)));
typedef float f32x2 __attribute__((ext_vector_type(2)));
typedef float f32x4 __attribute__((ext_vector_type(4)));
typedef float f32x16 __attribute__((ext_vector_type(16)));
typedef __bf16 bf16x2 __attribute__((ext_vector_type(2)));
typedef __bf16 bf16x8 __attribute__((ext_vector_type(8)));
typedef short s16x4 __attribute__((ext_vector_type(4)));
typedef unsigned short u16;
typedef unsigned long long u64;

constexpr int SET_BASE = 4096;
constexpr int OFF_MT = 0;
constexpr int OFF_SG = 32768;
constexpr int OFF_MG = 65536;
constexpr int OFF_SX = 524288;
constexpr int OFF_XG = 1 << 20;
constexpr int OFF_Z = OFF_XG + (4 << 20);
constexpr int OFF_HID = OFF_Z + 2097152;
constexpr int OFF_CK = OFF_HID + 7340032;
constexpr int SET_BYTES = OFF_CK + 262144;
static_assert(OFF_MG + 393216 <= OFF_SX, "set size");

constexpr int L_TS = 0, L_TB = 1024, L_TG = 2048;
constexpr int L_UVT = 3072;
constexpr int L_M = 4096;
constexpr int L_UVS = L_M;
constexpr int UVS_ROW = 544, UVS_CH = 57 * UVS_ROW;
constexpr int L_KC = L_M;
constexpr int KC_STRIDE = 4160;
constexpr int L_H = L_KC + 8 * KC_STRIDE;
constexpr int H_STRIDE = 4576;
constexpr int L_XB = L_H + 2 * H_STRIDE;
constexpr int L_RX = L_XB + 8192;
constexpr int L_R = L_M + 65536;
constexpr int R_WAVE = 22528;
constexpr int L_TOP = L_R + 4 * R_WAVE;
constexpr int L_KT = L_TOP + 16;
constexpr int L_STAT = L_TOP;
constexpr int L_ETA = L_RX + 12288;
constexpr int L_XR = L_M;
constexpr int L_SP = L_M + 131072;
constexpr int LDS_BYTES = 163840;
static_assert(L_TOP + 4096 <= LDS_BYTES && L_ETA + 16 <= L_R && L_UVS + 2 * UVS_CH <= L_R && L_SP + 24576 <= L_TOP, "LDS map");
static_assert(L_H % 16 == 0 && L_XB % 16 == 0 && L_RX % 16 == 0 && L_KT % 16 == 0, "alignment");

#define ZLAG 30
#define MTK 124
constexpr int SXL = 11, EO = 26;
constexpr int CODE_BYTES = 44032;
constexpr int SC1 = 16;
constexpr int OOB = 0x7FFFFF00;
constexpr unsigned SPIN_CAP = 1u << 15;
constexpr unsigned POISON = 0xFFFFFFFFu;
constexpr float LOG2E = 1.4426950408889634f, LN2 = 0.6931471805599453f, INV2PI = 0.15915494309189535f;

#define SB() __builtin_amdgcn_sched_barrier(0)
#define WAITV(n) asm volatile("s_waitcnt vmcnt(" #n ")" ::: "memory")
#define PIN(x) asm volatile("" : "+v"(x))
#define FRESH() asm volatile("" ::: "memory")
#define PAUSE() asm volatile("s_sleep 4" ::: "memory")
#define BAR() do { asm volatile("s_waitcnt lgkmcnt(0)" ::: "memory"); __builtin_amdgcn_s_barrier(); asm volatile("" ::: "memory"); } while (0)
#define STAMP(i)
#define SARG
#define SPASS
#define COUNT(i)

#define STAMPA(i)
#define STAMPB(i) STAMP(i)
DI float u2f(unsigned u) { return __builtin_bit_cast(float, u); }
DI unsigned f2u(float f) { return __builtin_bit_cast(unsigned, f); }
DI float dot2(unsigned u, float c) { const bf16x2 v = __builtin_bit_cast(bf16x2, u); return __builtin_amdgcn_fdot2_f32_bf16(v, v, c, false); }
DI float bf_lo(unsigned u) { return __builtin_bit_cast(float, u << 16); }
DI float bf_hi(unsigned u) { return __builtin_bit_cast(float, u & 0xFFFF0000u); }
DI unsigned pack2(float a, float b) { f32x2 v = {a, b}; return __builtin_bit_cast(unsigned, __builtin_convertvector(v, bf16x2)); }
DI float rbf(float a) { return bf_lo(pack2(a, a)); }
DI unsigned sel(bool c, unsigned a, unsigned b) { asm("" : "+v"(a), "+v"(b)); return c ? a : b; }
DI unsigned guard(unsigned u) { return u < 0xFFFFFFFEu ? u : 0xFFFFFFFEu; }
DI unsigned max4(unsigned m, v4u v) {
  asm("v_max3_u32 %0, %0, %1, %2\n\tv_max3_u32 %0, %0, %3, %4" : "+v"(m) : "v"(v.x), "v"(v.y), "v"(v.z), "v"(v.w));
  return m;
}
DI f32x4 mfma16(v4u a, v4u b, f32x4 c) { return __builtin_amdgcn_mfma_f32_16x16x32_bf16(__builtin_bit_cast(bf16x8, a), __builtin_bit_cast(bf16x8, b), c, 0, 0, 0); }
DI f32x16 mfma32(v4u a, v4u b, f32x16 c) { return __builtin_amdgcn_mfma_f32_32x32x16_bf16(__builtin_bit_cast(bf16x8, a), __builtin_bit_cast(bf16x8, b), c, 0, 0, 0); }
template <bool FIRST> DI void mfma_av(f32x16& acc, const v4u& a, const v4u& b) {
  if (FIRST) asm volatile("v_mfma_f32_32x32x16_bf16 %0, %1, %2, 0" : "=&v"(acc) : "a"(a), "v"(b));
  else asm volatile("v_mfma_f32_32x32x16_bf16 %0, %1, %2, %0" : "+v"(acc) : "a"(a), "v"(b));
}
template <bool FIRST> DI void mfma_va(f32x16& acc, const v4u& a, const v4u& b) {
  if (FIRST) asm volatile("v_mfma_f32_32x32x16_bf16 %0, %1, %2, 0" : "=&v"(acc) : "v"(a), "a"(b));
  else asm volatile("v_mfma_f32_32x32x16_bf16 %0, %1, %2, %0" : "+v"(acc) : "v"(a), "a"(b));
}
template <bool FIRST> DI void mfma16_v(f32x4& acc, const v4u& a, const v4u& b) {
  if (FIRST) asm volatile("v_mfma_f32_16x16x32_bf16 %0, %1, %2, 0" : "=&v"(acc) : "v"(a), "v"(b));
  else asm volatile("v_mfma_f32_16x16x32_bf16 %0, %1, %2, %0" : "+v"(acc) : "v"(a), "v"(b));
}
DI void mfma_done(f32x16& acc) { asm volatile("s_nop 15" : "+v"(acc)); }
DI v2u trd(LAS unsigned char* p) { return __builtin_bit_cast(v2u, __builtin_amdgcn_ds_read_tr16_b64_v4i16((LAS s16x4*)p)); }
DI v4u trd2(LAS unsigned char* p0, LAS unsigned char* p1) { const v2u a = trd(p0), b = trd(p1); return v4u{a.x, a.y, b.x, b.y}; }
DI v4u lds128(LAS unsigned char* p) { return *(LAS v4u*)p; }
DI v4u ldg(__amdgpu_buffer_rsrc_t r, int voff, int soff) { return __builtin_amdgcn_raw_buffer_load_b128(r, voff, soff, SC1); }
DI void stg(__amdgpu_buffer_rsrc_t r, int voff, int soff, v4u v) {
  __builtin_amdgcn_raw_buffer_store_b128(v, r, voff, soff, SC1);
  asm volatile("s_nop 1" :: "v"(v) : "memory");
}
template <int CTRL> DI float dpp_add(float v) {
  return v + __builtin_bit_cast(float, __builtin_amdgcn_update_dpp(0, __builtin_bit_cast(int, v), CTRL, 0xF, 0xF, true));
}
DI float swap16_add(float v) { const unsigned u = __builtin_bit_cast(unsigned, v); const auto r = __builtin_amdgcn_permlane16_swap(u, u, false, false);
  return u2f(r[0]) + u2f(r[1]); }
DI float swap32_add(float v) { const unsigned u = __builtin_bit_cast(unsigned, v); const auto r = __builtin_amdgcn_permlane32_swap(u, u, false, false);
  return u2f(r[0]) + u2f(r[1]); }
DI float quad_sum(float v) { v = dpp_add<0xB1>(v); return dpp_add<0x4E>(v); }
DI float row_sum(float v) { return dpp_add<0x140>(dpp_add<0x141>(quad_sum(v))); }
DI float wave_sum(float v) { return swap32_add(swap16_add(row_sum(v))); }
DI f32x2 cmul(f32x2 a, f32x2 e, f32x2 ep) { const f32x2 t = f32x2{a.x, a.x} * e; return __builtin_elementwise_fma(f32x2{a.y, a.y}, ep, t); }
DI f32x2 cexp(float re2, float rev) {
  const float m = __builtin_amdgcn_exp2f(re2), f = __builtin_amdgcn_fractf(rev);
  return f32x2{m * __builtin_amdgcn_cosf(f), m * __builtin_amdgcn_sinf(f)};
}
DI void dma_w2(const unsigned char* sbase, unsigned v0, unsigned v1, unsigned lds_dst) {
  asm volatile("s_mov_b32 m0, %3\n\ts_nop 0\n\tglobal_load_lds_dwordx4 %0, %2\n\tglobal_load_lds_dwordx4 %1, %2 offset:1024"
               :: "v"(v0), "v"(v1), "s"(sbase), "s"(lds_dst) : "memory");
}
DI void dma_w1(const unsigned char* sbase, unsigned v, unsigned lds_dst) {
  asm volatile("s_mov_b32 m0, %2\n\ts_nop 0\n\tglobal_load_lds_dwordx4 %0, %1" :: "v"(v), "s"(sbase), "s"(lds_dst) : "memory");
}
DI void dma_x1(const unsigned char* sbase, unsigned v, unsigned lds_dst) {
  asm volatile("s_mov_b32 m0, %2\n\ts_nop 0\n\tglobal_load_lds_dwordx4 %0, %1 sc1" :: "v"(v), "s"(sbase), "s"(lds_dst) : "memory");
}
DI void set_m0(unsigned lds_dst) { asm volatile("s_mov_b32 m0, %0\n\ts_nop 0" :: "s"(lds_dst) : "memory"); }
template <int OFF> DI void dma_xn(const unsigned char* sbase, unsigned v) {
  asm volatile("global_load_lds_dwordx4 %0, %1 offset:%2 sc1" :: "v"(v), "s"(sbase), "n"(OFF) : "memory");
}
DI void dma_x2(const unsigned char* sbase, unsigned v, unsigned lds_dst) {
  asm volatile("s_mov_b32 m0, %2\n\ts_nop 0\n\tglobal_load_lds_dwordx4 %0, %1 sc1\n\tglobal_load_lds_dwordx4 %0, %1 offset:1024 sc1"
               :: "v"(v), "s"(sbase), "s"(lds_dst) : "memory");
}
DI void dma_x4(const unsigned char* sbase, unsigned v, unsigned lds_dst) {
  asm volatile("s_mov_b32 m0, %2\n\ts_nop 0\n\tglobal_load_lds_dwordx4 %0, %1 sc1\n\tglobal_load_lds_dwordx4 %0, %1 offset:1024 sc1\n\t"
               "global_load_lds_dwordx4 %0, %1 offset:2048 sc1\n\tglobal_load_lds_dwordx4 %0, %1 offset:3072 sc1"
               :: "v"(v), "s"(sbase), "s"(lds_dst) : "memory");
}
template <int I> struct IC { static constexpr int v = I; };
template <int I, int N, class F> DI void static_for(F&& f) { if constexpr (I < N) { f(IC<I>{}); static_for<I + 1, N>(f); } }
DI unsigned now() { u64 t; asm volatile("s_memrealtime %0\n\ts_waitcnt lgkmcnt(0)" : "=s"(t) :: "memory"); return (unsigned)t; }
template <int N> DI void waitv() { asm volatile("s_waitcnt vmcnt(%0)" :: "n"(N) : "memory"); }
struct ConvOrder { int t[36], u[36]; };
constexpr ConvOrder conv_order() {
  ConvOrder o = {};
  int done[8] = {}, last = -1;
  for (int k = 0; k < 36; ++k) {
    int best = -1;
    for (int t = 0; t < 8; ++t)
      if (t != last && done[t] <= t && (best < 0 || t - done[t] > best - done[best])) best = t;
    o.t[k] = best; o.u[k] = done[best]++; last = best;
  }
  return o;
}
constexpr ConvOrder conv_order_last() {
  ConvOrder o = {};
  int k = 0;
  for (int p = 0; p < 4; ++p)
    for (int u = 0; u <= 2 * p + 1; ++u) {
      o.t[k] = 2 * p + 1; o.u[k] = u; ++k;
      if (u <= 2 * p) { o.t[k] = 2 * p; o.u[k] = u; ++k; }
    }
  return o;
}
constexpr int epi_slot(int v) { return v < 20 ? 2 * EO + v : v < 32 ? 2 * EO + 20 + 2 * (v - 20) : 2 * 50 + (v - 32); }
constexpr int epi_dist(int v) { return v >= 20 && v < 32 ? 2 : 1; }
constexpr int hl_upto(int i) { return i < 4 ? 25 + (5 * i + 1) / 2 : i < 8 ? 35 : i < 36 ? 35 + 5 * (i - 8) / 4 : 70; }
DI unsigned tagbad(unsigned bad, v4u g, unsigned tagA, unsigned tagB) {
  return __builtin_amdgcn_bitop3_b32(__builtin_amdgcn_bitop3_b32(bad, g.y, tagA, 0xF6), g.w, tagB, 0xF6);
}
template <int R0, int R1, int R2, int R3> DI void put4(unsigned base, f32x2 a, f32x2 b, f32x2 c, f32x2 d) {
  asm volatile("s_mov_b32 m0, %4\n\ts_nop 0\n\tds_write_addtid_b32 %0 offset:%5\n\tds_write_addtid_b32 %1 offset:%6\n\t"
               "ds_write_addtid_b32 %2 offset:%7\n\tds_write_addtid_b32 %3 offset:%8"
               :: "v"(pack2(a.x, a.y)), "v"(pack2(b.x, b.y)), "v"(pack2(c.x, c.y)), "v"(pack2(d.x, d.y)), "s"(base),
                  "n"(R0 * UVS_ROW), "n"(R1 * UVS_ROW), "n"(R2 * UVS_ROW), "n"(R3 * UVS_ROW) : "memory");
}
DI unsigned sum4(unsigned m, v4u v) {
  asm("v_add3_u32 %0, %0, %1, %2\n\tv_add3_u32 %0, %0, %3, %4" : "+v"(m) : "v"(v.x), "v"(v.y), "v"(v.z), "v"(v.w));
  return m;
}
DI unsigned sum8(unsigned m, v4u a, v4u b) {
  asm("v_add3_u32 %0, %0, %1, %2\n\tv_add3_u32 %0, %0, %3, %4\n\tv_add3_u32 %0, %0, %5, %6\n\tv_add3_u32 %0, %0, %7, %8"
      : "+v"(m) : "v"(a.x), "v"(a.y), "v"(a.z), "v"(a.w), "v"(b.x), "v"(b.y), "v"(b.z), "v"(b.w));
  return m;
}
template <int I> DI unsigned red_step(unsigned v) {
  constexpr int ctrl[6] = {0xB1, 0x4E, 0x141, 0x140, 0x142, 0x143}, rows[6] = {0xF, 0xF, 0xF, 0xF, 0xA, 0xC};
  return v + (unsigned)__builtin_amdgcn_update_dpp(0, (int)v, ctrl[I], rows[I], 0xF, true);
}
DI unsigned red_all(unsigned v) { return red_step<5>(red_step<4>(red_step<3>(red_step<2>(red_step<1>(red_step<0>(v)))))); }
DI void swap_add32(float& a, float b) { const auto r = __builtin_amdgcn_permlane32_swap(f2u(a), f2u(b), false, false); a = u2f(r[0]) + u2f(r[1]); }
DI void swap_add16(float& a, float b) { const auto r = __builtin_amdgcn_permlane16_swap(f2u(a), f2u(b), false, false); a = u2f(r[0]) + u2f(r[1]); }

typedef const unsigned char* cptr;
typedef unsigned v16u __attribute__((ext_vector_type(16)));
DI cptr mkptr(unsigned lo, unsigned hi) { return (cptr)(((u64)hi << 32) | lo); }

__global__ void __launch_bounds__(256, 1)
s4_block_kernel(cptr, cptr, cptr, cptr, cptr, cptr, cptr, cptr, cptr, cptr, cptr, cptr, cptr, unsigned char*, unsigned char*, cptr) {
  STAMP(0);
  v16u ka0, ka1;
  asm volatile("s_load_dwordx16 %0, %2, 0x0\n\ts_load_dwordx16 %1, %2, 0x40" : "=&s"(ka0), "=&s"(ka1) : "s"(__builtin_amdgcn_kernarg_segment_ptr()));
  SB();
  LAS unsigned char* const L = (LAS unsigned char*)16u - 16;
  const int tid = threadIdx.x, lane = tid & 63, w = __builtin_amdgcn_readfirstlane(tid >> 6);
  const int b = blockIdx.x, xcd = b & 7, slot = b >> 3, id = xcd * 32 + slot;
  const int ks = (id >> 6) * 4 + w, cc = id & 63;
  const int rp = 4 * (xcd >> 1) + (slot >> 3), rg = xcd & 1, dt = slot & 7, si = 8 * rg + dt;
  const int ct = 7 * rg + min(dt, 6);
  const int rot = (0x0340 >> (4 * (2 * (w & 1) + rg))) & 7;

  const unsigned char* pcode;
  asm volatile("s_getpc_b64 %0" : "=s"(pcode));
  const unsigned warm = *(const GAS unsigned*)((const GAS unsigned char*)pcode + (unsigned)min((((slot & 7) * 4 + w) * 11 + min(lane, 10)) * 128, CODE_BYTES));
  const int g16 = lane >> 4, q4 = (lane & 15) >> 2, p4 = lane & 3, hf = lane >> 5, blk = (lane >> 4) & 1, l16 = lane * 16;
  const unsigned lrw = L_R + w * R_WAVE;
  unsigned wmo[3], co = (ks * 32 + (lane & 31)) * 2, xo = id * 8192 + (tid & 3) * 2048 + (tid >> 2) * 16, ro = id * 8192 + w * 2048 + hf * 1024 + (lane & 31) * 16, po = id * 512 + tid * 2;
  unsigned wto = id * 7168 + (w >> 1) * 3584 + min(lane, 27) * 128;
#pragma unroll
  for (int i = 0; i < 3; ++i) { const unsigned idx = lane + 64 * i; wmo[i] = (ks * 32 * 6144 + cc * 96 - i * 1024) + idx / 6 * (6144 - 96) + idx * 16; PIN(wmo[i]); }
  PIN(co); PIN(xo); PIN(ro); PIN(po); PIN(wto);
  set_m0(lrw);
  SB();
  unsigned w2v0 = (lane >> 3) * 1024 + (((lane & 7) ^ (blk << 2)) * 16), w2v1 = w2v0 + 7168;
  unsigned w1v0 = (lane >> 2) * 3584 + p4 * 16, w1v1 = w1v0 + 57344 - 1024;
  const int zs = OFF_Z + (2 * id + (w >> 1)) * 64;
  int za = (((w & 1) + 2 * (g16 & 1)) * 8 + q4 * 2 + (g16 >> 1)) * 32768 + p4 * 16, zb = za + 32 * 32768;
  int tra = (hf * 8 + q4) * 64 + blk * 32 + p4 * 8;
  LAS unsigned char* w1a = L + lrw + tra;
  LAS unsigned char* ka = L + L_KC + ((w >> 1) * 4 + q4) * KC_STRIDE + 2 * (32 - 8 * g16 + 4 * p4) - 8 + 256 * (w & 1);
  LAS unsigned char* hb = L + L_H + (w >> 1) * H_STRIDE + (lane & 15) * 32 + g16 * 16 + 256 - 256 * (w & 1);
  LAS unsigned char* zp = L + sel(tid < 64, L_KC + ((lane >> 1) & 7) * KC_STRIDE + (lane & 1) * 16, L_H + hf * H_STRIDE + min(lane & 31, 29) * 16);
  LAS unsigned char* ub = L + L_UVS + (lane & 15) * UVS_ROW + g16 * 16;
  int gmo = OFF_MG + (g16 * 256 + id) * 256 + (lane & 15) * 16, gmo2 = OFF_MG + ((4 + blk) * 256 + id) * 256 + (lane & 15) * 16;
  int mgo = sel(lane < 48, (lane >> 1) * 256 + (lane & 1) * 8, OOB), sgo = sel(lane & 31, OOB, hf * 8);
  LAS unsigned char* xbw = L + L_XB + (tid >> 7) * 2048 + (tid & 127) * 4;
  LAS unsigned char* xbr = L + L_XB + w * 2048 + (lane & 15) * 32 + g16 * 8;
  LAS unsigned char *exa = L + L_XB + w * 2048 + l16, *exb = L + L_RX + w * 3072 + l16, *eya = L + L_XB + (w ^ 1) * 2048 + l16, *eyb = L + L_RX + (w ^ 1) * 3072 + l16;
  PIN(xbw); PIN(xbr); PIN(exa); PIN(exb); PIN(eya); PIN(eyb);
  int mto = (tid >> 1) * 128 + (tid & 1) * 16;
  LAS unsigned char* mtw = L + (tid & 1) * L_TG + (tid >> 1) * 4;
  PIN(mto); PIN(mtw);
  PIN(w2v0); PIN(w2v1); PIN(w1v0); PIN(w1v1); PIN(za); PIN(zb); PIN(tra); PIN(w1a); PIN(ka); PIN(hb); PIN(zp); PIN(ub); PIN(gmo); PIN(gmo2); PIN(mgo); PIN(sgo);
#define W1DMA(step, lds) dma_w1(w1w + (step) * 57344, w1v0, (lds))
#define W1DMA2(step, lds) dma_w2(w1w + (step) * 57344, w1v0, w1v1, (lds))
#define W2DMA(step, lds) dma_w2(((step) < 12 ? w2lo : (step) < 16 ? w2c3 : w2hi) + (step) * 16384, w2v0, w2v1, (lds))
  SB();
  STAMPA(24);
  asm volatile("s_waitcnt lgkmcnt(0)" : "+s"(ka0), "+s"(ka1));
  STAMPA(25);
  const cptr xin = mkptr(ka0[0], ka0[1]), pAre = mkptr(ka0[2], ka0[3]), pAim = mkptr(ka0[4], ka0[5]), pBre = mkptr(ka0[6], ka0[7]);
  const cptr pBim = mkptr(ka0[8], ka0[9]), pCre = mkptr(ka0[10], ka0[11]), pCim = mkptr(ka0[12], ka0[13]), pldt = mkptr(ka0[14], ka0[15]);
  const cptr pD = mkptr(ka1[0], ka1[1]), w1 = mkptr(ka1[2], ka1[3]), w2 = mkptr(ka1[4], ka1[5]), pc = mkptr(ka1[6], ka1[7]);
  const cptr wmod = mkptr(ka1[8], ka1[9]), out = mkptr(ka1[10], ka1[11]), ws = mkptr(ka1[12], ka1[13]);
  const unsigned epoch_v = __hip_atomic_load((GAS unsigned*)ws, __ATOMIC_RELAXED, __HIP_MEMORY_SCOPE_AGENT);
  asm volatile("global_load_lds_dwordx4 %0, %3\n\tglobal_load_lds_dwordx4 %1, %3 offset:1024\n\tglobal_load_lds_dwordx4 %2, %3 offset:2048"
               :: "v"(wmo[0]), "v"(wmo[1]), "v"(wmo[2]), "s"(wmod) : "memory");
  const unsigned cval = *(const GAS u16*)((const GAS unsigned char*)pc + co);
  SB();
  const v4u xa = *(const GAS v4u*)((const GAS unsigned char*)xin + xo), xb = *(const GAS v4u*)((const GAS unsigned char*)xin + 1024 + xo);
  const v4u ra = *(const GAS v4u*)((const GAS unsigned char*)xin + ro), rb = *(const GAS v4u*)((const GAS unsigned char*)xin + 512 + ro);
  SB();
#define PARAM(p) (unsigned)*(const GAS u16*)((const GAS unsigned char*)(p) + po)
  const unsigned uAre = PARAM(pAre), uAim = PARAM(pAim), uBre = PARAM(pBre), uBim = PARAM(pBim), uCre = PARAM(pCre), uCim = PARAM(pCim);
#undef PARAM
  SB();
  const unsigned char* setp = ws + SET_BASE;
  const __amdgpu_buffer_rsrc_t rs = __builtin_amdgcn_make_buffer_rsrc((void*)setp, 0, SET_BYTES, 0x00020000);
  unsigned uldt, uD;
  asm volatile("s_load_dword %0, %2, %4\n\ts_load_dword %1, %3, %4" : "=&s"(uldt), "=&s"(uD) : "s"(pldt), "s"(pD), "s"(id * 4));
  SB();
  STAMPA(26);
  LAS unsigned char* koff[12];
  {
    float fj = (float)(8 * tid - 3);
    PIN(fj);
    const unsigned lin = (unsigned)(8 * tid - 3 + 0x4300 - 128) * 0x10001u + 0x10000u;
#pragma unroll
    for (int i = 0; i < 12; i += 2) {
      const unsigned r = sel(tid < 32, lin + i * 0x10001u, pack2(fj + (float)i, fj + (float)(i + 1)));
      koff[i] = L + ((r & 0xFFFFu) * 4 + (L_KT + (128 - 0x4300) * 4));
      koff[i + 1] = L + ((r >> 16) * 4 + (L_KT + (128 - 0x4300) * 4));
      PIN(koff[i]);
      if (i < 10) PIN(koff[i + 1]);
    }
  }
  *(LAS v4u*)(L + L_KT - 16) = v4u{0u, 0u, 0u, 0u};
  LAS unsigned char* ktw[4];
  {
    const int row = w * 16 + (lane & 15), sg = max((row >> 3) - 1, 0), rr_ = row - 8 * sg;
    const int idx0 = 128 * sg + ((rr_ >> (3 - sg)) << (7 - sg)) + (rr_ & ((8 >> sg) - 1)) + 1, st = 8 >> sg;
#pragma unroll
    for (int r = 0; r < 4; ++r) {
      const int idx = sel(row < 40, idx0 + st * (4 * g16 + r), tid == 136 && r == 0 ? 0 : 641 + lane);
      ktw[r] = L + L_KT + idx * 4;
      PIN(ktw[r]);
    }
  }
  const unsigned char* w2lo = w2 + (448 * w + 64 * rot) * 1024 + dt * 128;
  const unsigned char* w2c3 = w2lo - (rot >> 2) * (448 * 1024);
  const unsigned char* w2hi = w2lo - ((rot + 1) >> 2) * (448 * 1024);
  const unsigned char* w1w = w1 + ct * 256 + w * 64;
  SB();
  STAMPA(27);
  asm volatile("s_waitcnt lgkmcnt(0)" : "+s"(uldt), "+s"(uD));
  const unsigned epoch = __builtin_amdgcn_readfirstlane(epoch_v);
  asm volatile("" :: "s"(epoch));
  STAMPA(28);
  const unsigned tagA = epoch * 0x9E3779B1u + 0x7F4A7C15u, tagB = ~tagA;
  {
    const float c = bf_lo(cval);
    const float s = c * __builtin_amdgcn_rcpf(1.0f + __builtin_amdgcn_exp2f(-LOG2E * c));
    *(LAS u16*)(L + lrw + 3072 + (lane & 31) * 2) = (u16)pack2(s, s);
    SB();
#define XPUB(i) do { stg(rs, (tid >> 2) * 65536 + (tid & 3) * 16, OFF_XG + (i) * 16384 + id * 64, v4u{xa[i], tagA, xb[i], tagB}); SB(); } while (0)
    asm volatile("" :: "v"(xb));
    STAMPA(29);
    XPUB(0);
    XPUB(1);
    STAMP(1);
    LAS unsigned char* a = L + lrw + (8 * g16 + q4) * 96 + p4 * 8;
    const v4u sc = lds128(L + lrw + 3072 + g16 * 16);
    const v4u m0 = trd2(a, a + 384), m1 = trd2(a + 32, a + 416), m2 = trd2(a + 64, a + 448);
    SB();
    f32x2 fa[8];
#pragma unroll
    for (int i = 0; i < 4; ++i) { fa[i] = f32x2{bf_lo(ra[i]), bf_hi(ra[i])}; fa[4 + i] = f32x2{bf_lo(rb[i]), bf_hi(rb[i])}; }
    const f32x2 s2 = ((fa[0] + fa[1]) + (fa[2] + fa[3])) + ((fa[4] + fa[5]) + (fa[6] + fa[7]));
    float mean = s2.x + s2.y;
    PIN(mean);
    SB();
    const f32x4 c0 = mfma16(sc, m0, f32x4{}), c1 = mfma16(sc, m1, f32x4{}), c2 = mfma16(sc, m2, f32x4{});
    SB();
    mean = swap16_add(row_sum(mean)) * (1.0f / 512.0f);
    PIN(mean);
    SB();
    XPUB(2);
    __builtin_amdgcn_raw_buffer_store_b64(v2u{sel(lane < 16, f2u(c0[0]), sel(lane < 32, f2u(c1[0]), f2u(c2[0]))), (lane & 1) ? tagB : tagA}, rs, mgo,
                                          OFF_MG + cc * 24 * 256 + ks * 16, SC1);
    SB();
    f32x2 q2 = {0.f, 0.f}, q3 = {0.f, 0.f};
#pragma unroll
    for (int i = 0; i < 8; i += 2) {
      const f32x2 d0 = fa[i] - f32x2{mean, mean}, d1 = fa[i + 1] - f32x2{mean, mean};
      q2 = __builtin_elementwise_fma(d0, d0, q2);
      q3 = __builtin_elementwise_fma(d1, d1, q3);
    }
    q2 += q3;
    const float var = rbf(rbf(swap16_add(row_sum(q2.x + q2.y)) * (1.0f / 512.0f)) + 1e-5f);
    __builtin_amdgcn_raw_buffer_store_b64(v2u{pack2(mean, __builtin_amdgcn_sqrtf(var)), hf ? tagB : tagA}, rs, sgo, OFF_SG + (id * 4 + w) * 16, SC1);
    SB();
    XPUB(3);
#undef XPUB
  }
  const unsigned wt = *(const GAS unsigned*)((const GAS unsigned char*)(w1 + (w & 1) * (w2 - w1)) + wto);
  SB();
  STAMP(2);
  unsigned dsk = tid == 136 ? uD : 0u;
  PIN(dsk);

  {
    const float dtv = rbf(__builtin_amdgcn_exp2f(LOG2E * (w >> 1 ? bf_hi(uldt) : bf_lo(uldt))));
    const float are = dtv * bf_lo(uAre) * LOG2E, aim = dtv * bf_lo(uAim) * INV2PI;
    const float br = dtv * bf_lo(uBre), bi = dtv * bf_lo(uBim), cr = bf_lo(uCre), ci = bf_lo(uCim);
    const f32x2 wv = {cr * br - ci * bi, cr * bi + ci * br};
    const unsigned row = L_UVS + (w >> 1) * UVS_CH + (w & 1) * 256;
    const f32x2 e1 = cexp(are, aim), e8 = cexp(8.f * are, -8.f * aim), e128 = cexp(128.f * are, 128.f * aim);
    const f32x2 e1p = {-e1.y, e1.x}, e8p = {-e8.y, e8.x}, e128p = {-e128.y, e128.x};
    const v4u pois = {POISON, POISON, POISON, POISON};
    f32x2 z[8], v, v2, y, y2;
#define M1(a) cmul(a, e1, e1p)
#define M8(a) cmul(a, e8, e8p)
#define MK(a) cmul(a, e128, e128p)
    z[0] = M1(wv);
    put4<40, 41, 42, 0>(row, wv, f32x2{1.f, 0.f}, e8, z[0]);
    W1DMA2(0, lrw + 0);
    v = M8(e8); z[1] = M1(z[0]); v2 = M8(v); z[2] = M1(z[1]);
    put4<43, 1, 44, 2>(row, v, z[1], v2, z[2]);
    v = M8(v2); z[3] = M1(z[2]); v2 = M8(v); z[4] = M1(z[3]);
    put4<45, 3, 46, 4>(row, v, z[3], v2, z[4]);
    W1DMA2(2, lrw + 2048);
    v = M8(v2); z[5] = M1(z[4]); v2 = M8(v); z[6] = M1(z[5]);
    put4<47, 5, 48, 6>(row, v, z[5], v2, z[6]);
    z[7] = M1(z[6]); z[0] = MK(z[0]); z[1] = MK(z[1]); z[2] = MK(z[2]);
    put4<7, 8, 9, 10>(row, z[7], z[0], z[1], z[2]);
    W1DMA2(4, lrw + 4096);
    z[3] = MK(z[3]); z[4] = MK(z[4]); z[5] = MK(z[5]); z[6] = MK(z[6]);
    put4<11, 12, 13, 14>(row, z[3], z[4], z[5], z[6]);
    z[7] = MK(z[7]); z[1] = MK(z[1]); z[3] = MK(z[3]); z[5] = MK(z[5]);
    put4<15, 16, 17, 18>(row, z[7], z[1], z[3], z[5]);
    W1DMA2(6, lrw + 6144);
    z[7] = MK(z[7]); z[1] = MK(z[1]); z[3] = MK(z[3]); z[5] = MK(z[5]);
    put4<19, 20, 21, 22>(row, z[7], z[1], z[3], z[5]);
    y = MK(z[7]); z[3] = MK(z[3]); y2 = MK(y); z[4] = MK(z[3]);
    put4<23, 24, 25, 26>(row, y, z[3], y2, z[4]);
    W1DMA2(8, lrw + 8192);
    y = MK(y2); z[3] = MK(z[4]); y2 = MK(y); z[4] = MK(z[3]);
    put4<27, 28, 29, 30>(row, y, z[3], y2, z[4]);
    y = MK(y2); v = M8(v2); y2 = MK(y); v2 = M8(v);
    put4<31, 49, 32, 50>(row, y, v, y2, v2);
    W1DMA2(10, lrw + 10240);
    y = MK(y2); v = M8(v2); y2 = MK(y); v2 = M8(v);
    put4<33, 51, 34, 52>(row, y, v, y2, v2);
    y = MK(y2); v = M8(v2); y2 = MK(y); v2 = M8(v);
    put4<35, 53, 36, 54>(row, y, v, y2, v2);
    stg(rs, za, zs, pois);
    y = MK(y2); v = M8(v2); y2 = MK(y); v2 = M8(v);
    put4<37, 55, 38, 56>(row, y, v, y2, v2);
    y = MK(y2);
    *(LAS unsigned*)(L + row + 39 * UVS_ROW + lane * 4) = pack2(y.x, y.y);
    stg(rs, zb, zs, pois);
#undef M1
#undef M8
#undef MK
  }
  BAR();
  STAMP(3);
  v4u gx[4], gs[4], gm, gn;
  const int xgo = OFF_XG + id * 16384;
#define S0LOADS() do { \
    _Pragma("unroll") for (int i = 0; i < 4; ++i) gx[i] = ldg(rs, tid * 16, xgo + i * 4096); \
    _Pragma("unroll") for (int i = 0; i < 4; ++i) gs[i] = ldg(rs, tid * 16, OFF_SG + i * 4096); \
    gm = ldg(rs, gmo, 0); gn = ldg(rs, gmo2, 0); } while (0)
  if (w < 3) {
    LAS unsigned char* ut = ub + w * 16 * UVS_ROW;
    v4u fv[2][8], fu[2][8];
#pragma unroll
    for (int s = 0; s < 8; ++s)
#pragma unroll
      for (int c = 0; c < 2; ++c) { fv[c][s] = lds128(ub + c * UVS_CH + 41 * UVS_ROW + s * 64); fu[c][s] = lds128(ut + c * UVS_CH + s * 64); SB(); }
    f32x4 a0 = {}, a1 = {};
#pragma unroll
    for (int s = 0; s < 8; ++s) { a0 = mfma16(fv[0][s], fu[0][s], a0); SB(); a1 = mfma16(fv[1][s], fu[1][s], a1); SB(); }
    *(LAS unsigned*)ktw[0] = pack2(a0[0] + bf_lo(dsk), a1[0] + bf_hi(dsk));
#pragma unroll
    for (int r = 1; r < 4; ++r) *(LAS unsigned*)ktw[r] = pack2(a0[r], a1[r]);
  }
  SB();
  S0LOADS();
  SB();
  BAR();
  {
    unsigned k[11];
#pragma unroll
    for (int i = 0; i < 11; ++i) k[i] = *(LAS unsigned*)koff[i];
    SB();
    if (w < 2) *(LAS v4u*)zp = v4u{0u, 0u, 0u, 0u};
#pragma unroll
    for (int c = 0; c < 2; ++c) {
      unsigned e[5], o[5];
#pragma unroll
      for (int i = 0; i < 5; ++i) {
        o[i] = __builtin_amdgcn_perm(k[2 * i + 1], k[2 * i], c ? 0x07060302u : 0x05040100u);
        e[i] = __builtin_amdgcn_perm(k[2 * i + 2], k[2 * i + 1], c ? 0x07060302u : 0x05040100u);
      }
      LAS unsigned char* d = L + L_KC + c * 4 * KC_STRIDE + 32 + tid * 16;
      *(LAS v4u*)(d) = v4u{e[1], e[2], e[3], e[4]};
      *(LAS v4u*)(d + KC_STRIDE) = v4u{o[1], o[2], o[3], o[4]};
      *(LAS v4u*)(d + 2 * KC_STRIDE) = v4u{e[0], e[1], e[2], e[3]};
      *(LAS v4u*)(d + 3 * KC_STRIDE) = v4u{o[0], o[1], o[2], o[3]};
    }
  }
  SB();
  STAMP(4);
  {
    unsigned bad = tagbad(tagbad(0u, gm, tagA, tagB), gn, tagA, tagB);
#pragma unroll
    for (int i = 0; i < 4; ++i) bad = tagbad(tagbad(bad, gx[i], tagA, tagB), gs[i], tagA, tagB);
    if (__builtin_expect(__any(bad != 0u), 0)) {
      for (unsigned spin = 0; spin < SPIN_CAP; ++spin) {
        FRESH();
        if (spin > 1) PAUSE();
        COUNT(20);
        bool again = false;
#pragma unroll
        for (int i = 0; i < 4; ++i) {
          if (__any(tagbad(0u, gx[i], tagA, tagB) != 0u)) { gx[i] = ldg(rs, tid * 16, xgo + i * 4096); again = true; }
          if (__any(tagbad(0u, gs[i], tagA, tagB) != 0u)) { gs[i] = ldg(rs, tid * 16, OFF_SG + i * 4096); again = true; }
        }
        if (__any(tagbad(tagbad(0u, gm, tagA, tagB), gn, tagA, tagB) != 0u)) { gm = ldg(rs, gmo, 0); gn = ldg(rs, gmo2, 0); again = true; }
        if (!again) break;
      }
    }
  }
  asm volatile("" :: "v"(wt), "v"(warm));
  WAITV(0);
  STAMP(5);
  W1DMA2(12, lrw + 12288);
  float zsc;
  v4u mtp;
  {
    const int i0 = (int)pack2(row_sum(u2f(gm.x)), row_sum(u2f(gm.z))), i1 = (int)pack2(row_sum(u2f(gn.x)), row_sum(u2f(gn.z)));
    unsigned md[6];
#pragma unroll
    for (int k = 0; k < 6; ++k) md[k] = (unsigned)__builtin_amdgcn_readlane(k < 4 ? i0 : i1, 16 * (k & 3));
#define MOD(k, c) ((c) ? bf_hi(md[k]) : bf_lo(md[k]))
    float s0 = rbf(1.0f + MOD(4, 0)), s1 = rbf(1.0f + MOD(4, 1));
    s0 = s0 == 0.f ? 0x1p-30f : s0;
    s1 = s1 == 0.f ? 0x1p-30f : s1;
    const float G0 = MOD(2, 0) * s0, G1 = MOD(2, 1) * s1;
    const float A0 = rbf(1.0f + MOD(1, 0)) * G0, A1 = rbf(1.0f + MOD(1, 1)) * G1, B0 = MOD(0, 0) * G0, B1 = MOD(0, 1) * G1;
    zsc = w >> 1 ? s1 : s0;
    mtp = v4u{lane ? md[5] : pack2(s0, s1), tagA, md[3], tagB};
#undef MOD
#pragma unroll
    for (int i = 0; i < 4; ++i) {
      const float mA = bf_lo(gs[i].x), rA = __builtin_amdgcn_rcpf(bf_hi(gs[i].x)), mB = bf_lo(gs[i].z), rB = __builtin_amdgcn_rcpf(bf_hi(gs[i].z));
      const float n00 = (bf_lo(gx[i].x) - mA) * rA, n01 = (bf_hi(gx[i].x) - mA) * rA, n10 = (bf_lo(gx[i].z) - mB) * rB, n11 = (bf_hi(gx[i].z) - mB) * rB;
      const int o = (tid + 256 * i) * 4;
      W1DMA2(14 + 2 * i, lrw + (14 + 2 * i) * 1024);
      *(LAS unsigned*)(L + L_H + 480 + o) = pack2(__builtin_fmaf(n00, A0, B0), __builtin_fmaf(n10, A0, B0));
      *(LAS unsigned*)(L + L_H + H_STRIDE + 480 + o) = pack2(__builtin_fmaf(n01, A1, B1), __builtin_fmaf(n11, A1, B1));
      *(LAS unsigned*)(xbw + i * 512) = __builtin_amdgcn_perm(gx[i].z, gx[i].x, 0x05040100u);
      *(LAS unsigned*)(xbw + 4096 + i * 512) = __builtin_amdgcn_perm(gx[i].z, gx[i].x, 0x07060302u);
    }
  }
  BAR();
  const unsigned tb = now();
  STAMP(6);

  v4u w1f[32], w2f[28][2], mt0, mt1;
  {
    const bool rh = w & 1;
    constexpr ConvOrder O = conv_order(), OL = conv_order_last();
    v2u A[4][8][2], xk[4];
    v4u B[4][8];
    f32x4 acc[8], keep[4];
#pragma unroll
    for (int u = 0; u < 8; ++u) {
      A[0][u][0] = trd(ka + 512 * u + 8);
      A[0][u][1] = trd(ka + 512 * u);
      B[0][7 - u] = lds128(hb + 64 * (8 * (7 - u) + 3));
    }
    SB();
    if (lane) mtp.z = tb;
    stg(rs, tid < 2 ? tid * 16 : OOB, OFF_MT + id * 128, mtp);
    SB();
    auto hand_over = [&](auto Pc) __attribute__((always_inline)) {
      constexpr int i = Pc.v;
      PIN(acc[2 * i]); PIN(acc[2 * i + 1]);
      const f32x4 ev = acc[2 * i], od = acc[2 * i + 1];
      const f32x4 snd = rh ? ev : od, own = rh ? od : ev;
      *(LAS f32x4*)(i < 2 ? exa + i * 1024 : exb + (i - 1) * 1024) = snd;
#pragma unroll
      for (int c = 0; c < 4; ++c) { float t = keep[i][c] + own[c]; PIN(t); keep[i][c] = t; }
    };
    static_for<0, 144>([&](auto Kc) __attribute__((always_inline)) {
      constexpr int K = Kc.v, r = K / 36, k = K % 36, t = r < 3 ? O.t[k] : OL.t[k], u = r < 3 ? O.u[k] : OL.u[k];
      if constexpr (k == 0 && r > 0) asm volatile("" :: "v"(B[r][7]));
      mfma16_v<r == 0 && u == 0>(acc[t], v4u{A[r][u][0].x, A[r][u][0].y, A[r][u][1].x, A[r][u][1].y}, B[r][t - u]);
      SB();
      if constexpr (K < 12) w1f[K] = trd2(w1a + K * 1024, w1a + K * 1024 + 256);
      if constexpr (K == 60) WAITV(9);
      if constexpr (K >= 60 && K < 70) w1f[K - 48] = trd2(w1a + (K - 48) * 1024, w1a + (K - 48) * 1024 + 256);
      if constexpr (K == 76) asm volatile("" :: "a"(w1f[21]));
      if constexpr (r == 2 && k < 4) xk[k] = *(LAS v2u*)(xbr + k * 512);
      constexpr int n = k - (r == 0 ? 9 : r == 1 ? 0 : 4);
      if constexpr (r < 3 && n >= 0 && n < 24) {
        if constexpr (n % 3 == 2) B[r + 1][n / 3] = lds128(hb + 64 * (8 * (n / 3) - r + 2));
        else A[r + 1][n / 3][n % 3] = trd(ka + 512 * (n / 3) + 64 * (r + 1) + 8 - 8 * (n % 3));
      }
      if constexpr (K >= 100 && K < 116) {
        constexpr int e = K - 100;
        const unsigned xw = (e & 2) ? xk[e >> 2].y : xk[e >> 2].x;
        float t2_ = zsc * ((e & 1) ? bf_hi(xw) : bf_lo(xw));
        PIN(t2_);
        keep[e >> 2][e & 3] = t2_;
      }
      if constexpr (K == 108 + 8) hand_over(IC<0>{});
      if constexpr (K == 108 + 17) hand_over(IC<1>{});
      if constexpr (K == 108 + 30) hand_over(IC<2>{});
      if constexpr (K == 14) W1DMA(22, L_RX + w * 3072);
      if constexpr (K == 22) W1DMA(23, L_TOP + w * 1024);
      if constexpr (K >= 37 && K < 69 && K % 8 == 5) W1DMA2(24 + (K - 37) / 4, lrw + (K - 37) / 4 * 1024);
      if constexpr (K >= 69 && K < 125 && K % 8 == 5) W2DMA((K - 69) / 8, lrw + 8192 + (K - 69) / 8 * 2048);
      if constexpr (K == MTK)
        asm volatile("buffer_load_dwordx4 %0, %2, %3, 0 offen sc1\n\tbuffer_load_dwordx4 %1, %2, %3, %4 offen sc1"
                     : "=&v"(mt0), "=&v"(mt1) : "v"(mto), "s"(rs), "s"(16384) : "memory");
      SB();
    });
    STAMP(7);
    asm volatile("s_nop 7" : "+v"(acc[6]), "+v"(acc[7]));
    hand_over(IC<3>{});
    BAR();
#pragma unroll
    for (int i = 0; i < 4; ++i) keep[i] += *(LAS f32x4*)(i < 2 ? eya + i * 1024 : eyb + (i - 1) * 1024);
#pragma unroll
    for (int i = 0; i < 2; ++i) {
      const auto s0 = __builtin_amdgcn_permlane16_swap(pack2(keep[2 * i][0], keep[2 * i][1]), pack2(keep[2 * i + 1][0], keep[2 * i + 1][1]), false, false);
      const auto s1 = __builtin_amdgcn_permlane16_swap(pack2(keep[2 * i][2], keep[2 * i][3]), pack2(keep[2 * i + 1][2], keep[2 * i + 1][3]), false, false);
      stg(rs, i ? zb : za, zs, v4u{guard(s0[0]), guard(s1[0]), guard(s0[1]), guard(s1[1])});
    }
    SB();
  }
  STAMP(8);

  static_assert(OFF_MT == 0, "table offset");
  const unsigned te = now();
  asm volatile("s_waitcnt vmcnt(2)" : "+v"(mt0), "+v"(mt1) :: "memory");
  if (__builtin_expect(__any(tagbad(tagbad(0u, mt0, tagA, tagB), mt1, tagA, tagB) != 0u), 0)) {
    for (unsigned spin = 0; spin < SPIN_CAP; ++spin) {
      FRESH();
      COUNT(21);
      if (tagbad(0u, mt0, tagA, tagB)) mt0 = ldg(rs, mto, OFF_MT);
      if (tagbad(0u, mt1, tagA, tagB)) mt1 = ldg(rs, mto, OFF_MT + 16384);
      if (!__any(tagbad(tagbad(0u, mt0, tagA, tagB), mt1, tagA, tagB) != 0u)) break;
      PAUSE();
    }
  }
  *(LAS unsigned*)(mtw) = mt0.x;
  *(LAS unsigned*)(mtw + 512) = mt1.x;
  *(LAS unsigned*)(mtw + 1024) = mt0.z;
  *(LAS unsigned*)(mtw + 1536) = mt1.z;
  {
    int late = (lane & 1) ? max((int)(mt0.z - tb), (int)(mt1.z - tb)) : 0;
    late = max(late, __builtin_amdgcn_update_dpp(late, late, 0xB1, 0xF, 0xF, false));
    late = max(late, __builtin_amdgcn_update_dpp(late, late, 0x4E, 0xF, 0xF, false));
    late = max(late, __builtin_amdgcn_update_dpp(late, late, 0x141, 0xF, 0xF, false));
    late = max(late, __builtin_amdgcn_update_dpp(late, late, 0x140, 0xF, 0xF, false));
    late = max(late, __builtin_amdgcn_update_dpp(late, late, 0x142, 0xA, 0xF, false));
    late = max(late, __builtin_amdgcn_update_dpp(late, late, 0x143, 0xC, 0xF, false));
    if (lane == 63) *(LAS int*)(L + L_ETA + w * 4) = late;
  }
  w1f[22] = trd2(L + L_RX + w * 3072 + tra, L + L_RX + w * 3072 + tra + 256);
  w1f[23] = trd2(L + L_TOP + w * 1024 + tra, L + L_TOP + w * 1024 + tra + 256);
  BAR();
  STAMP(9);
  const int w2t = (4 * hf + q4) * 128 + blk * 32 + (((p4 & 1) << 1) | (p4 >> 1)) * 8;
  LAS unsigned char* w2a = L + w2t + (q4 >> 1) * 64;
  LAS unsigned char* w2b = L + w2t + ((q4 >> 1) ^ 1) * 64;
  const int par = (0x1C >> (ct % 7)) & 1;
  const unsigned char* xg = setp + OFF_Z + rp * 131072;
  const unsigned xl = L_XR + w * 8192;
#define XSRC(p) (xg + w * 8192 + ((p) ^ par) * 32768)
  const int slo = OFF_Z + (rp * 4 + w) * 32768 + si * 2048;
  f32x4 uu[4], vv[4];
  v4u sl0, sl1;
  {
    LAS unsigned char* qr = L + ((lane & 31) == 0 ? L_TS : L_TB) + hf * 16;
    LAS unsigned char *pa = w2a + lrw + 8192, *pb = w2b + lrw + 8192;
    PIN(pa); PIN(pb);
    f32x16 uv, uv2;
    v4u qa[32];
    int wait = 0;
    const v4u lt = lds128(L + L_ETA);
    bool sent = false;
    asm volatile("" : "=v"(sl0), "=v"(sl1));
#pragma unroll
    for (int i = 0; i < 4; ++i) qa[i] = lds128(qr + i * 32);
    static_for<0, 32>([&](auto Sc) __attribute__((always_inline)) {
      constexpr int s = Sc.v;
      mfma_va<(s < 2)>((s & 1) ? uv2 : uv, qa[s], w1f[s]);
      SB();
      if constexpr (s < 28) qa[s + 4] = lds128(qr + (s + 4) * 32);
      if constexpr (s > 0) asm volatile("" :: "v"(qa[s > 0 ? s - 1 : 0]));
      if constexpr (s < 8) w1f[24 + s] = trd2(w1a + s * 1024, w1a + s * 1024 + 256);
      if constexpr (s == 4) { wait = min(__builtin_amdgcn_readfirstlane(max(max((int)lt.x, (int)lt.y), max((int)lt.z, (int)lt.w))), 300) + ZLAG; }
      if constexpr (s >= 8 && s < 22) w2f[(s - 8) >> 1][s & 1] = trd2(((s & 1) ? pb : pa) + ((s - 8) >> 1) * 2048, ((s & 1) ? pb : pa) + ((s - 8) >> 1) * 2048 + 1024);
      if constexpr (s == 4 || s == 13 || s == 22 || s == 31) {
        if (!sent) {
          if constexpr (s == 31) { while ((int)(now() - te) < wait) { } }
          if (s == 31 || (int)(now() - te) >= wait) {
            sent = true;
            STAMPB(26);
            asm volatile("buffer_load_dwordx4 %0, %2, %3, %4 offen sc1\n\tbuffer_load_dwordx4 %1, %2, %3, %4 offen offset:1024 sc1"
                         :: "v"(sl0), "v"(sl1), "v"(l16), "s"(rs), "s"(slo) : "memory");
            dma_x4(XSRC(0), l16, xl);
            dma_x4(XSRC(0) + 4096, l16, xl + 4096);
          }
        }
      }
      SB();
    });
    dma_x4(XSRC(1), l16, xl + 32768);
    dma_x4(XSRC(1) + 4096, l16, xl + 32768 + 4096);
    STAMPB(27);
    BAR();
    mfma_done(uv2);
    PIN(uv);
    LAS unsigned char* uw = L + L_UVT + (32 * w + (lane & 31)) * 4;
    if (lane < 32) { *(LAS float*)(uw) = uv[0] + uv2[0]; *(LAS float*)(uw + 512) = -LOG2E * (uv[1] + uv2[1]); }
    LAS unsigned char* uvp = L + L_UVT + (32 * w + 4 * hf) * 4;
#pragma unroll
    for (int i = 0; i < 4; ++i) { uu[i] = *(LAS f32x4*)(uvp + i * 32); vv[i] = *(LAS f32x4*)(uvp + 512 + i * 32); }
  }
  {
    const float i0 = __builtin_amdgcn_rcpf(bf_lo(*(LAS u16*)(L + L_TS + (si * 32 + (lane >> 2)) * 2)));
    const float i1 = __builtin_amdgcn_rcpf(bf_lo(*(LAS u16*)(L + L_TS + (si * 32 + 16 + (lane >> 2)) * 2)));
    SB();
    asm volatile("s_waitcnt vmcnt(16)" : "+v"(sl0), "+v"(sl1) :: "memory");
    STAMPB(28);
    if (__builtin_expect(__any(max4(max4(0u, sl0), sl1) == POISON), 0)) {
      for (unsigned spin = 0; spin < SPIN_CAP; ++spin) {
        FRESH();
        if (spin > 1) PAUSE();
        COUNT(22);
        sl0 = ldg(rs, l16, slo);
        sl1 = ldg(rs, l16 + 1024, slo);
        if (!__any(max4(max4(0u, sl0), sl1) == POISON)) break;
      }
    }
    float sm[8], sq[8];
#pragma unroll
    for (int i = 0; i < 4; ++i) {
      const f32x2 a = f32x2{bf_lo(sl0[i]), bf_hi(sl0[i])} * f32x2{i0, i0}, c = f32x2{bf_lo(sl1[i]), bf_hi(sl1[i])} * f32x2{i1, i1};
      const f32x2 s2 = a + c, q2 = __builtin_elementwise_fma(a, a, c * c);
      sm[2 * i] = s2.x; sm[2 * i + 1] = s2.y; sq[2 * i] = q2.x; sq[2 * i + 1] = q2.y;
    }
#pragma unroll
    for (int i = 0; i < 4; ++i) { swap_add32(sm[i], sm[i + 4]); swap_add32(sq[i], sq[i + 4]); }
#pragma unroll
    for (int i = 0; i < 2; ++i) { swap_add16(sm[i], sm[i + 2]); swap_add16(sq[i], sq[i + 2]); }
#pragma unroll
    for (int i = 0; i < 2; ++i) { sm[i] = dpp_add<0x124>(dpp_add<0x128>(sm[i])); sq[i] = dpp_add<0x124>(dpp_add<0x128>(sq[i])); }
    const bool od = lane & 4;
    stg(rs, (lane & 8) ? OOB : (p4 * 8 + hf * 4 + blk * 2 + (od ? 1 : 0)) * 16, OFF_SX + ((rp * 4 + w) * 16 + si) * 512,
        v4u{f2u(od ? sm[1] : sm[0]), tagA, f2u(od ? sq[1] : sq[0]), tagB});
    SB();
  }
  STAMPB(29);
  dma_x4(XSRC(2), l16, xl + 65536);
  dma_x4(XSRC(2) + 4096, l16, xl + 65536 + 4096);
  WAITV(17);
  STAMP(10);
  {
    v4u t[8];
    unsigned m = 0, m2 = 0;
#pragma unroll
    for (int i = 0; i < 8; ++i) t[i] = lds128(L + xl + l16 + i * 1024);
#pragma unroll
    for (int i = 0; i < 8; i += 2) { m = max4(m, t[i]); m2 = max4(m2, t[i + 1]); }
    if (__builtin_expect(__any(max(m, m2) == POISON), 0)) {
      for (unsigned spin = 0; spin < SPIN_CAP; ++spin) {
        COUNT(22);
        bool again = false;
#pragma nounroll
        for (int i = 0; i < 8; ++i)
          if (__any(max4(0u, lds128(L + xl + l16 + i * 1024)) == POISON)) {
            again = true;
            dma_x1(XSRC(0) + i * 1024, l16, xl + i * 1024);
            dma_x1(XSRC(1) + i * 1024, l16, xl + 32768 + i * 1024);
            dma_x1(XSRC(2) + i * 1024, l16, xl + 65536 + i * 1024);
          }
        if (!again) break;
        WAITV(0);
        if (spin) PAUSE();
      }
    }
  }
  BAR();
  STAMP(11);

  constexpr int HW = 40;
  v4u hq[HW], res[2], sx[8];
  const int c0 = 7 * w + rot, c3 = c0 - 7 * (rot >> 2), c4 = c0 - 7 * ((rot + 1) >> 2), ckv = lane < 8 ? l16 : OOB;
  const int hb0 = OFF_HID + (rp * 4 + rg) * 114688, kb0 = OFF_CK + (rp * 4 + rg) * 4096;
#define CHUNK(n) (((n) % 35 / 5 < 3 ? c0 : (n) % 35 / 5 == 3 ? c3 : c4) + (n) % 35 / 5)
#define HLOAD(n) hq[(n) % HW] = (n) % 5 == 4 ? ldg(rs, ckv, kb0 + CHUNK(n) * 128 + ((n) / 35) * 8192) \
    : ldg(rs, l16 + ((n) % 5) * 1024, hb0 + CHUNK(n) * 4096 + ((n) / 35) * (2 * 114688))
  const int hoff = OFF_HID + (rp * 4 * 112 + ct * 8 + 2 * w) * 1024, hv0 = dt == 7 ? OOB : l16, hv1 = dt == 7 ? OOB : l16 + 1024;
  const int cko = OFF_CK + rp * 16384 + (ct * 4 + w) * 64, ckw = (p4 || dt == 7) ? OOB : lane;
  const unsigned ckey = lane ? 0u : tagA;
#define CKPUT(sum, u) __builtin_amdgcn_raw_buffer_store_b32(ckey - red_step<1>(red_step<0>(sum)), rs, ckw, cko + ((u) ^ par) * 4096, SC1)
  const int sxo = OFF_SX + (rp * 4 + w) * 8192;
  LAS unsigned char* xr[2];
#pragma unroll
  for (int i = 0; i < 2; ++i) { xr[i] = L + L_XR + tra + i * 65536; PIN(xr[i]); }
  LAS unsigned char* rr = L + L_XR + tra + (rg ^ par) * 32768 + (4 * dt + w) * 1024;
  LAS unsigned char* str = L + L_STAT + (lane & 31) * 8;
  PIN(str);
#define XA(t) (xr[(t) >> 6] + ((t) & 63) * 1024)
#define XF(t) trd2(XA(t), XA(t) + 256)
#define XEN
#define REDO(p) \
  if (XEN __builtin_expect(__any(!(acc[(p) % 3][0] == acc[(p) % 3][0])), 0)) { \
    for (unsigned spin = 0; spin < SPIN_CAP / 128; ++spin) { \
      COUNT(23); \
      _Pragma("nounroll") for (int i = 0; i < 8; ++i) dma_x4(xg + ((p) ^ par) * 32768 + i * 4096, l16, L_XR + (p) * 32768 + i * 4096); \
      WAITV(0); \
      _Pragma("unroll") for (int s = 0; s < 32; ++s) { \
        const v4u t = XF(32 * (p) + s); \
        if (s == 0) mfma_av<true>(acc[(p) % 3], w1f[s], t); else mfma_av<false>(acc[(p) % 3], w1f[s], t); \
        SB(); \
      } \
      mfma_done(acc[(p) % 3]); \
      if (!__any(!(acc[(p) % 3][0] == acc[(p) % 3][0]))) break; \
    } \
  }
  f32x16 acc[3];
  f32x2 ec[4];
  float ep[48], ee[48], ey[48];
  unsigned cs = 0;
  v4u x[128], eo;
#pragma unroll
  for (int t = 0; t < 4; ++t) x[t] = XF(t);
  SB();
  static_for<0, 64>([&](auto Jc) __attribute__((always_inline)) {
    constexpr int J = Jc.v, p = J >> 4, j = J & 15;
    asm volatile("" :: "v"(x[2 * J + 1]));
    mfma_av<j == 0>(acc[p % 3], w1f[2 * j], x[2 * J]);
    if constexpr (J < 62) { x[2 * J + 4] = XF(2 * J + 4); x[2 * J + 5] = XF(2 * J + 5); }
    SB();
    mfma_av<false>(acc[p % 3], w1f[2 * j + 1], x[2 * J + 1]);
    SB();
    if constexpr (J == 10) { WAITV(16); __builtin_amdgcn_s_barrier(); FRESH(); }
    if constexpr (J == EO - 2) {
      unsigned bad = 0;
#pragma unroll
      for (int i = 0; i < 8; ++i) bad = tagbad(bad, sx[i], tagA, tagB);
      if (__builtin_expect(__any(bad != 0u), 0)) {
        for (unsigned spin = 0; spin < SPIN_CAP; ++spin) {
          FRESH();
          if (spin > 1) PAUSE();
          COUNT(23);
          bad = 0;
#pragma unroll
          for (int i = 0; i < 8; ++i) sx[i] = ldg(rs, l16 + (i & 3) * 1024, sxo + (i >> 2) * 4096);
#pragma unroll
          for (int i = 0; i < 8; ++i) bad = tagbad(bad, sx[i], tagA, tagB);
          if (!__any(bad != 0u)) break;
        }
      }
      float sm = u2f(sx[0].x), sq = u2f(sx[0].z);
#pragma unroll
      for (int i = 1; i < 8; ++i) { sm += u2f(sx[i].x); PIN(sm); sq += u2f(sx[i].z); PIN(sq); }
      const float mean = swap32_add(sm) * (1.0f / 512.0f);
      const float ms = __builtin_fmaf(swap32_add(sq), 1.0f / 512.0f, 1e-5f);
      const float a = -LOG2E * __builtin_amdgcn_rsqf(fmaxf(__builtin_fmaf(-mean, mean, ms), 1e-5f));
      *(LAS f32x2*)(str + (w ^ par) * 256) = f32x2{a, -a * mean};
      SB();
    }
    if constexpr (J == EO - 1) {
      asm volatile("s_waitcnt lgkmcnt(4)" ::: "memory");
      __builtin_amdgcn_s_barrier();
      FRESH();
#pragma unroll
      for (int i = 0; i < 4; ++i) ec[i] = *(LAS f32x2*)(str + i * 256);
      SB();
    }
    static_for<2 * J, 2 * J + 2>([&](auto Sc) __attribute__((always_inline)) {
      static_for<0, 48>([&](auto Vc) __attribute__((always_inline)) {
        constexpr int S = Sc.v, V = Vc.v, k = V & 15;
        if constexpr (S == epi_slot(V) + 2 * epi_dist(V)) {
          ey[V] = ep[V] * ee[V];
          if constexpr (k & 1) { unsigned o = pack2(ey[V - 1], ey[V]); PIN(o); eo[(k >> 1) & 3] = o; } else PIN(ey[V]);
          if constexpr (k == 7) { stg(rs, hv0, hoff + ((V >> 4) ^ par) * 114688, eo); cs = sum4(0u, eo); PIN(cs); }
          if constexpr (k == 15) { stg(rs, hv1, hoff + ((V >> 4) ^ par) * 114688, eo); CKPUT(sum4(cs, eo), V >> 4); }
        }
        if constexpr (S == epi_slot(V) + epi_dist(V)) { ee[V] = __builtin_amdgcn_rcpf(1.0f + ee[V]); PIN(ee[V]); }
        if constexpr (S == epi_slot(V)) {
          ep[V] = __builtin_fmaf(ec[V >> 4].x, acc[(V >> 4) % 3][k], __builtin_fmaf(ec[V >> 4].y, uu[k >> 2][k & 3], vv[k >> 2][k & 3]));
          ee[V] = __builtin_amdgcn_exp2f(ep[V]);
          PIN(ee[V]);
        }
      });
      SB();
    });
    if constexpr (j == 1 && p >= 1) {
      if constexpr (p == 2) REDO(1)
      if constexpr (p == 3) REDO(2)
      if (((p - 1) & 1) == (rg ^ par)) res[(p - 1) >> 1] = trd2(rr + ((p - 1) >> 1) * 65536, rr + ((p - 1) >> 1) * 65536 + 256);
    }
    if constexpr (j == 2 && p >= 2) {
      asm volatile("s_waitcnt lgkmcnt(4)" ::: "memory");
      __builtin_amdgcn_s_barrier();
      FRESH();
    }
    if constexpr (J < 8) {
      if constexpr ((J & 3) == 0) set_m0(xl + 98304 + J * 1024);
      dma_xn<(J & 3) * 1024>(XSRC(3) + (J >> 2) * 4096, l16);
    }
    if constexpr (J >= SXL && J < SXL + 8) sx[J - SXL] = ldg(rs, l16 + ((J - SXL) & 3) * 1024, sxo + ((J - SXL) >> 2) * 4096);
    if constexpr (J >= EO && J < EO + 22 && ((J - EO) & 1) == 0) {
      constexpr int n = (J - EO) >> 1;
      if constexpr (n < 3) W2DMA(7 + n, L_SP + w * 6144 + n * 2048);
      else W2DMA(7 + n, L_XR + ((n - 3) >> 2) * 32768 + w * 8192 + ((n - 3) & 3) * 2048);
    }
    if constexpr (J >= 48 && J < 56) { HLOAD(2 * (J - 48)); if constexpr (J < 55) HLOAD(2 * (J - 48) + 1); }
    if constexpr (J >= 56 && J < 64 && (J & 1) == 0) W2DMA(18 + ((J - 56) >> 1), L_XR + 65536 + w * 8192 + ((J - 56) >> 1) * 2048);
    SB();
    if constexpr (J == 15) STAMP(16);
    if constexpr (J == 31) STAMP(17);
    if constexpr (J == 47) STAMP(18);
  });
  STAMP(12);
  static_assert(EO + 6 >= 26 && EO + 14 > 34 && EO + 20 < 48 && epi_slot(16) >= 2 * 34 && epi_slot(31) < epi_slot(32) && epi_slot(47) + 2 < 128, "schedule");
  static_for<15, 25>([&](auto Ic) __attribute__((always_inline)) { HLOAD(Ic.v); });
  SB();
  mfma_done(acc[0]);
  REDO(3)
  if (rg ^ par) res[1] = trd2(rr + 65536, rr + 65536 + 256);
#pragma unroll
  for (int h = 0; h < 2; ++h) {
    v4u o;
#pragma unroll
    for (int i = 0; i < 4; ++i) {
      const int k = 8 * h + 2 * i;
      const f32x2 t = __builtin_elementwise_fma(f32x2{ec[3].y, ec[3].y}, f32x2{uu[k >> 2][k & 3], uu[k >> 2][(k & 3) + 1]}, f32x2{vv[k >> 2][k & 3], vv[k >> 2][(k & 3) + 1]});
      const f32x2 pp = __builtin_elementwise_fma(f32x2{ec[3].x, ec[3].x}, f32x2{acc[0][k], acc[0][k + 1]}, t);
      const f32x2 d = f32x2{__builtin_amdgcn_exp2f(pp.x), __builtin_amdgcn_exp2f(pp.y)} + f32x2{1.0f, 1.0f};
      const f32x2 y = pp * f32x2{__builtin_amdgcn_rcpf(d.x), __builtin_amdgcn_rcpf(d.y)};
      o[i] = pack2(y.x, y.y);
    }
    stg(rs, h ? hv1 : hv0, hoff + (3 ^ par) * 114688, o);
    if (h) CKPUT(sum4(cs, o), 3); else cs = sum4(0u, o);
    SB();
  }
  BAR();
  STAMP(13);

  f32x16 d[2][2];
  LAS unsigned char* redw = L + L_XR + w * 8192 + l16;
  LAS unsigned char* redr = L + L_XR + w * 2048 + l16;
#define REDW(un, q) *(LAS f32x4*)(redw + (un) * 32768 + (q) * 1024) = \
    f32x4{d[un][(q) >> 2][4 * ((q) & 3)], d[un][(q) >> 2][4 * ((q) & 3) + 1], d[un][(q) >> 2][4 * ((q) & 3) + 2], d[un][(q) >> 2][4 * ((q) & 3) + 3]}
  LAS unsigned char *w2sa = w2a + L_SP + w * 6144, *w2sb = w2b + L_SP + w * 6144;
  w2a += L_XR + w * 8192; w2b += L_XR + w * 8192;
  LAS unsigned char *w2ua = w2a + 65536, *w2ub = w2b + 65536;
  PIN(w2a); PIN(w2b); PIN(w2sa); PIN(w2sb); PIN(w2ua); PIN(w2ub);
#define PULL(step, pa, pb, off) do { w2f[step][0] = trd2((pa) + (off), (pa) + (off) + 1024); w2f[step][1] = trd2((pb) + (off), (pb) + (off) + 1024); } while (0)
  unsigned m = red_all(sum4(sum8(sum8(0u, hq[0], hq[1]), hq[2], hq[3]), hq[4]));
  v4u gbv, scv;
  f32x4 pr[8];
  float gb[8], rx[2][8];
  unsigned orow = (rp * 128 + ((lane & 31) >> 3) * 16 + rg * 8 + (lane & 7)) * 1024 + dt * 128 + w * 32 + hf * 16;
  static_for<0, 14>([&](auto Gc) __attribute__((always_inline)) {
    constexpr int G = Gc.v, un = G / 7, gq = G % 7;
    if (__builtin_expect((unsigned)__builtin_amdgcn_readlane((int)m, 63) != 2u * tagA, 0)) {
      for (unsigned spin = 0; spin < SPIN_CAP; ++spin) {
        FRESH();
        COUNT(24 + un);
        static_for<0, 5>([&](auto Ic) __attribute__((always_inline)) { HLOAD(5 * G + Ic.v); });
        m = sum4(sum8(sum8(0u, hq[(5 * G) % HW], hq[(5 * G + 1) % HW]), hq[(5 * G + 2) % HW], hq[(5 * G + 3) % HW]), hq[(5 * G + 4) % HW]);
        if ((unsigned)__builtin_amdgcn_readlane((int)red_all(m), 63) == 2u * tagA) break;
        PAUSE();
      }
    }
    static_for<0, 8>([&](auto Ic) __attribute__((always_inline)) {
      constexpr int c = Ic.v, i = G < 13 ? c >> 1 : c & 3, e = G < 13 ? c & 1 : c >> 2, I = 4 * G + i, n = 5 * G + i, X = 8 * G + c;
      mfma_av<gq == 0 && i == 0>(d[un][e], w2f[gq * 4 + i][e], hq[n % HW]);
      SB();
      if constexpr (G < 13 && e == 0) {
        if constexpr (I < 3) PULL(7 + I, w2sa, w2sb, I * 2048);
        if constexpr (I >= 3 && I < 11) PULL(7 + I, w2a, w2b, ((I - 3) >> 2) * 32768 + ((I - 3) & 3) * 2048);
        if constexpr (I >= 11 && I < 15) PULL(7 + I, w2ua, w2ub, (I - 11) * 2048);
        if constexpr (I == 18) waitv<4 + hl_upto(18) - 35>();
        if constexpr (I >= 18 && I < 22) PULL(4 + I, w2ua, w2ub, 32768 + (I - 18) * 2048);
        if constexpr (I == 23) waitv<hl_upto(23) - 35>();
        if constexpr (I == 23 || I == 24) PULL(3 + I, w2sa, w2sb, (I - 23) * 2048);
        if constexpr (I >= 29 && I < 37) REDW(0, I - 29);
      }
      if constexpr (G < 13 && e == 1) {
        if constexpr (I >= 4 && I < 8) W2DMA(18 + I, L_XR + 98304 + w * 8192 + (I - 4) * 2048);
        if constexpr (I == 8) { W2DMA(26, L_SP + w * 6144); W2DMA(27, L_SP + w * 6144 + 2048); }
        static_for<hl_upto(I), hl_upto(I + 1)>([&](auto Nc) __attribute__((always_inline)) { HLOAD(Nc.v); });
      }
      if constexpr (G < 13) {
        constexpr int q = 5 * G + 5;
        if constexpr (c == 1) m = sum8(0u, hq[q % HW], hq[(q + 1) % HW]);
        if constexpr (c == 3) m = sum4(sum8(m, hq[(q + 2) % HW], hq[(q + 3) % HW]), hq[(q + 4) % HW]);
        if constexpr (c == 4) m = red_step<0>(m);
        if constexpr (c == 5) m = red_step<2>(red_step<1>(m));
        if constexpr (c == 6) m = red_step<3>(m);
        if constexpr (c == 7) m = red_step<5>(red_step<4>(m));
        PIN(m);
      }
      if constexpr (X == 74) { gbv = lds128(L + L_TG + (64 * dt + 16 * w + 8 * hf) * 2); scv = lds128(L + L_TS + (64 * dt + 16 * w + 8 * hf) * 2); PIN(orow); }
      if constexpr (X >= 76 && X < 92 && (X & 1) == 0) {
        constexpr int q = (X - 76) >> 1;
        const float inv = __builtin_amdgcn_rcpf((q & 1) ? bf_hi(scv[q >> 1]) : bf_lo(scv[q >> 1]));
        gb[q] = -LN2 * ((q & 1) ? bf_hi(gbv[q >> 1]) : bf_lo(gbv[q >> 1]));
        rx[0][q] = inv * ((q & 1) ? bf_hi(res[0][q >> 1]) : bf_lo(res[0][q >> 1]));
        rx[1][q] = inv * ((q & 1) ? bf_hi(res[1][q >> 1]) : bf_lo(res[1][q >> 1]));
        PIN(gb[q]); PIN(rx[0][q]); PIN(rx[1][q]);
      }
      if constexpr (X == 92) BAR();
      if constexpr (X >= 93 && X < 97) { pr[2 * (X - 93)] = *(LAS f32x4*)(redr + (X - 93) * 8192); pr[2 * (X - 93) + 1] = *(LAS f32x4*)(redr + (X - 93) * 8192 + 1024); }
      if constexpr (X >= 98 && X < 106) {
        constexpr int q = X - 98;
        float t = (pr[q >> 2][q & 3] + pr[2 + (q >> 2)][q & 3]);
        PIN(t);
        float t2_ = (pr[4 + (q >> 2)][q & 3] + pr[6 + (q >> 2)][q & 3]);
        PIN(t2_);
        rx[0][q] = __builtin_fmaf(gb[q], t + t2_, rx[0][q]);
        PIN(rx[0][q]);
      }
      if constexpr (X == 106)
        *(GAS v4u*)((GAS unsigned char*)out + orow) = v4u{pack2(rx[0][0], rx[0][1]), pack2(rx[0][2], rx[0][3]), pack2(rx[0][4], rx[0][5]), pack2(rx[0][6], rx[0][7])};
      if constexpr (X == 109) asm volatile("s_nop 9" : "+v"(d[1][0]));
      if constexpr (X >= 109) REDW(1, X - 109);
      SB();
    });
    if constexpr (G == 6) STAMP(14);
  });
  STAMP(19);
  REDW(1, 3);
  mfma_done(d[1][1]);
#pragma unroll
  for (int q = 4; q < 8; ++q) REDW(1, q);
  BAR();
  {
    f32x4 s0 = {}, s1 = {};
#pragma unroll
    for (int k = 0; k < 4; ++k) { s0 += *(LAS f32x4*)(redr + k * 8192 + 32768); s1 += *(LAS f32x4*)(redr + k * 8192 + 32768 + 1024); }
    float y[8];
#pragma unroll
    for (int q = 0; q < 8; ++q) y[q] = __builtin_fmaf(gb[q], q < 4 ? s0[q & 3] : s1[q & 3], rx[1][q]);
    *(GAS v4u*)((GAS unsigned char*)out + orow + 65536) = v4u{pack2(y[0], y[1]), pack2(y[2], y[3]), pack2(y[4], y[5]), pack2(y[6], y[7])};
  }
  if (b == 0 && tid == 0) __hip_atomic_store((GAS unsigned*)ws, epoch + 1u, __ATOMIC_RELAXED, __HIP_MEMORY_SCOPE_AGENT);
  STAMP(15);
}

extern "C" void kernel_launch(void* const* d_in, const int* in_sizes, int n_in, void* d_out, int out_size, void* d_ws, size_t ws_size,
                              hipStream_t stream) {
  (void)in_sizes; (void)n_in; (void)out_size; (void)ws_size;
  (void)hipFuncSetAttribute((const void*)s4_block_kernel, hipFuncAttributeMaxDynamicSharedMemorySize, LDS_BYTES);
  s4_block_kernel<<<256, 256, LDS_BYTES, stream>>>((cptr)d_in[0], (cptr)d_in[1], (cptr)d_in[2], (cptr)d_in[3], (cptr)d_in[4], (cptr)d_in[5], (cptr)d_in[6],
                                                   (cptr)d_in[7], (cptr)d_in[8], (cptr)d_in[10], (cptr)d_in[11], (cptr)d_in[12], (cptr)d_in[13],
                                                   (unsigned char*)d_out, (unsigned char*)d_ws, (cptr)d_in[9]);
}
```

```cpp
#include <hip/hip_runtime.h>

#define GAS __attribute__((address_space(1)))
#define LAS __attribute__((address_space(3)))
#define DI __device__ __forceinline__
typedef unsigned v4u __attribute__((ext_vector_type(4)));
typedef unsigned v2u __attribute__((ext_vector_type(2)));
typedef float f32x2 __attribute__((ext_vector_type(2)));
typedef float f32x4 __attribute__((ext_vector_type(4)));
typedef float f32x16 __attribute__((ext_vector_type(16)));
typedef __bf16 bf16x2 __attribute__((ext_vector_type(2)));
typedef __bf16 bf16x8 __attribute__((ext_vector_type(8)));
typedef short s16x4 __attribute__((ext_vector_type(4)));
typedef unsigned short u16;
typedef unsigned long long u64;

constexpr int SET_BASE = 4096;
constexpr int OFF_MT = 0;
constexpr int OFF_SG = 32768;
constexpr int OFF_MG = 65536;
constexpr int OFF_SX = 524288;
constexpr int OFF_XG = 1 << 20;
constexpr int OFF_Z = OFF_XG + (4 << 20);
constexpr int OFF_HID = OFF_Z + 2097152;
constexpr int OFF_CK = OFF_HID + 7340032;
constexpr int SET_BYTES = OFF_CK + 262144;
static_assert(OFF_MG + 393216 <= OFF_SX, "set size");

constexpr int L_TS = 0, L_TB = 1024, L_TG = 2048;
constexpr int L_UVT = 3072;
constexpr int L_M = 4096;
constexpr int L_UVS = L_M;
constexpr int UVS_ROW = 544, UVS_CH = 57 * UVS_ROW;
constexpr int L_KC = L_M;
constexpr int KC_STRIDE = 4160;
constexpr int L_H = L_KC + 8 * KC_STRIDE;
constexpr int H_STRIDE = 4576;
constexpr int L_XB = L_H + 2 * H_STRIDE;
constexpr int L_RX = L_XB + 8192;
constexpr int L_R = L_M + 65536;
constexpr int L_TOP = L_M + 131072 + 24576;
constexpr int L_KT = L_TOP + 16;
constexpr int L_STAT = L_TOP;
constexpr int L_ETA = L_RX + 12288;
constexpr int L_XR = L_M;
constexpr int L_SP = L_M + 131072;
constexpr int LDS_BYTES = 163840;
static_assert(L_TOP + 4096 <= LDS_BYTES && L_ETA + 16 <= L_R && L_UVS + 2 * UVS_CH <= L_R && L_SP + 24576 <= L_TOP, "LDS map");
static_assert(L_H % 16 == 0 && L_XB % 16 == 0 && L_RX % 16 == 0 && L_KT % 16 == 0, "alignment");

#define ZLAG 30
#define MTK 124
constexpr int EO = 30;
constexpr int CODE_BYTES = 36096;
constexpr int SC1 = 16;
constexpr int OOB = 0x7FFFFF00;
constexpr unsigned SPIN_CAP = 1u << 15;
constexpr unsigned POISON = 0xFFFFFFFFu;
constexpr float LOG2E = 1.4426950408889634f, LN2 = 0.6931471805599453f, INV2PI = 0.15915494309189535f;

#define SB() __builtin_amdgcn_sched_barrier(0)
#define WAITV(n) asm volatile("s_waitcnt vmcnt(" #n ")" ::: "memory")
#define PIN(x) asm volatile("" : "+v"(x))
#define FRESH() asm volatile("" ::: "memory")
#define PAUSE() asm volatile("s_sleep 4" ::: "memory")
#define BAR() do { asm volatile("s_waitcnt lgkmcnt(0)" ::: "memory"); __builtin_amdgcn_s_barrier(); asm volatile("" ::: "memory"); } while (0)
#define STAMP(i)
#define SARG
#define SPASS
#define COUNT(i)

#define STAMPA(i)
#define STAMPB(i) STAMP(i)
DI float u2f(unsigned u) { return __builtin_bit_cast(float, u); }
DI unsigned f2u(float f) { return __builtin_bit_cast(unsigned, f); }
DI float dot2(unsigned u, float c) { const bf16x2 v = __builtin_bit_cast(bf16x2, u); return __builtin_amdgcn_fdot2_f32_bf16(v, v, c, false); }
DI float bf_lo(unsigned u) { return __builtin_bit_cast(float, u << 16); }
DI float bf_hi(unsigned u) { return __builtin_bit_cast(float, u & 0xFFFF0000u); }
DI unsigned pack2(float a, float b) { f32x2 v = {a, b}; return __builtin_bit_cast(unsigned, __builtin_convertvector(v, bf16x2)); }
DI float rbf(float a) { return bf_lo(pack2(a, a)); }
DI unsigned sel(bool c, unsigned a, unsigned b) { asm("" : "+v"(a), "+v"(b)); return c ? a : b; }
DI unsigned guard(unsigned u) { return u < 0xFFFFFFFEu ? u : 0xFFFFFFFEu; }
DI unsigned max4(unsigned m, v4u v) {
  asm("v_max3_u32 %0, %0, %1, %2\n\tv_max3_u32 %0, %0, %3, %4" : "+v"(m) : "v"(v.x), "v"(v.y), "v"(v.z), "v"(v.w));
  return m;
}
DI f32x4 mfma16(v4u a, v4u b, f32x4 c) { return __builtin_amdgcn_mfma_f32_16x16x32_bf16(__builtin_bit_cast(bf16x8, a), __builtin_bit_cast(bf16x8, b), c, 0, 0, 0); }
DI f32x16 mfma32(v4u a, v4u b, f32x16 c) { return __builtin_amdgcn_mfma_f32_32x32x16_bf16(__builtin_bit_cast(bf16x8, a), __builtin_bit_cast(bf16x8, b), c, 0, 0, 0); }
template <bool FIRST> DI void mfma_av(f32x16& acc, const v4u& a, const v4u& b) {
  if (FIRST) asm volatile("v_mfma_f32_32x32x16_bf16 %0, %1, %2, 0" : "=&v"(acc) : "a"(a), "v"(b));
  else asm volatile("v_mfma_f32_32x32x16_bf16 %0, %1, %2, %0" : "+v"(acc) : "a"(a), "v"(b));
}
template <bool FIRST> DI void mfma_va(f32x16& acc, const v4u& a, const v4u& b) {
  if (FIRST) asm volatile("v_mfma_f32_32x32x16_bf16 %0, %1, %2, 0" : "=&v"(acc) : "v"(a), "a"(b));
  else asm volatile("v_mfma_f32_32x32x16_bf16 %0, %1, %2, %0" : "+v"(acc) : "v"(a), "a"(b));
}
template <bool FIRST> DI void mfma16_v(f32x4& acc, const v4u& a, const v4u& b) {
  if (FIRST) asm volatile("v_mfma_f32_16x16x32_bf16 %0, %1, %2, 0" : "=&v"(acc) : "v"(a), "v"(b));
  else asm volatile("v_mfma_f32_16x16x32_bf16 %0, %1, %2, %0" : "+v"(acc) : "v"(a), "v"(b));
}
DI void mfma_done(f32x16& acc) { asm volatile("s_nop 15" : "+v"(acc)); }
DI v2u trd(LAS unsigned char* p) { return __builtin_bit_cast(v2u, __builtin_amdgcn_ds_read_tr16_b64_v4i16((LAS s16x4*)p)); }
DI v4u trd2(LAS unsigned char* p0, LAS unsigned char* p1) { const v2u a = trd(p0), b = trd(p1); return v4u{a.x, a.y, b.x, b.y}; }
DI v4u lds128(LAS unsigned char* p) { return *(LAS v4u*)p; }
DI v4u ldg(__amdgpu_buffer_rsrc_t r, int voff, int soff) { return __builtin_amdgcn_raw_buffer_load_b128(r, voff, soff, SC1); }
DI void stg(__amdgpu_buffer_rsrc_t r, int voff, int soff, v4u v) {
  __builtin_amdgcn_raw_buffer_store_b128(v, r, voff, soff, SC1);
  asm volatile("s_nop 1" :: "v"(v) : "memory");
}
template <int CTRL> DI float dpp_add(float v) {
  return v + __builtin_bit_cast(float, __builtin_amdgcn_update_dpp(0, __builtin_bit_cast(int, v), CTRL, 0xF, 0xF, true));
}
DI float swap16_add(float v) { const unsigned u = __builtin_bit_cast(unsigned, v); const auto r = __builtin_amdgcn_permlane16_swap(u, u, false, false);
  return u2f(r[0]) + u2f(r[1]); }
DI float swap32_add(float v) { const unsigned u = __builtin_bit_cast(unsigned, v); const auto r = __builtin_amdgcn_permlane32_swap(u, u, false, false);
  return u2f(r[0]) + u2f(r[1]); }
DI float quad_sum(float v) { v = dpp_add<0xB1>(v); return dpp_add<0x4E>(v); }
DI float row_sum(float v) { return dpp_add<0x140>(dpp_add<0x141>(quad_sum(v))); }
DI float wave_sum(float v) { return swap32_add(swap16_add(row_sum(v))); }
DI f32x2 cmul(f32x2 a, f32x2 e, f32x2 ep) { const f32x2 t = f32x2{a.x, a.x} * e; return __builtin_elementwise_fma(f32x2{a.y, a.y}, ep, t); }
DI f32x2 cexp(float re2, float rev) {
  const float m = __builtin_amdgcn_exp2f(re2), f = __builtin_amdgcn_fractf(rev);
  return f32x2{m * __builtin_amdgcn_cosf(f), m * __builtin_amdgcn_sinf(f)};
}
DI void dma_w2(const unsigned char* sbase, unsigned v0, unsigned v1, unsigned lds_dst) {
  asm volatile("s_mov_b32 m0, %3\n\ts_nop 0\n\tglobal_load_lds_dwordx4 %0, %2\n\tglobal_load_lds_dwordx4 %1, %2 offset:1024"
               :: "v"(v0), "v"(v1), "s"(sbase), "s"(lds_dst) : "memory");
}
DI void dma_w1(const unsigned char* sbase, unsigned v, unsigned lds_dst) {
  asm volatile("s_mov_b32 m0, %2\n\ts_nop 0\n\tglobal_load_lds_dwordx4 %0, %1" :: "v"(v), "s"(sbase), "s"(lds_dst) : "memory");
}
DI void dma_x1(const unsigned char* sbase, unsigned v, unsigned lds_dst) {
  asm volatile("s_mov_b32 m0, %2\n\ts_nop 0\n\tglobal_load_lds_dwordx4 %0, %1 sc1" :: "v"(v), "s"(sbase), "s"(lds_dst) : "memory");
}
DI void set_m0(unsigned lds_dst) { asm volatile("s_mov_b32 m0, %0\n\ts_nop 0" :: "s"(lds_dst) : "memory"); }
template <int OFF> DI void dma_xn(const unsigned char* sbase, unsigned v) {
  asm volatile("global_load_lds_dwordx4 %0, %1 offset:%2 sc1" :: "v"(v), "s"(sbase), "n"(OFF) : "memory");
}
DI void dma_x2(const unsigned char* sbase, unsigned v, unsigned lds_dst) {
  asm volatile("s_mov_b32 m0, %2\n\ts_nop 0\n\tglobal_load_lds_dwordx4 %0, %1 sc1\n\tglobal_load_lds_dwordx4 %0, %1 offset:1024 sc1"
               :: "v"(v), "s"(sbase), "s"(lds_dst) : "memory");
}
DI void dma_x4(const unsigned char* sbase, unsigned v, unsigned lds_dst) {
  asm volatile("s_mov_b32 m0, %2\n\ts_nop 0\n\tglobal_load_lds_dwordx4 %0, %1 sc1\n\tglobal_load_lds_dwordx4 %0, %1 offset:1024 sc1\n\t"
               "global_load_lds_dwordx4 %0, %1 offset:2048 sc1\n\tglobal_load_lds_dwordx4 %0, %1 offset:3072 sc1"
               :: "v"(v), "s"(sbase), "s"(lds_dst) : "memory");
}
template <int I> struct IC { static constexpr int v = I; };
template <int I, int N, class F> DI void static_for(F&& f) { if constexpr (I < N) { f(IC<I>{}); static_for<I + 1, N>(f); } }
DI unsigned now() { u64 t; asm volatile("s_memrealtime %0\n\ts_waitcnt lgkmcnt(0)" : "=s"(t) :: "memory"); return (unsigned)t; }
template <int N> DI void waitv() { asm volatile("s_waitcnt vmcnt(%0)" :: "n"(N) : "memory"); }
struct ConvOrder { int t[36], u[36]; };
constexpr ConvOrder conv_order() {
  ConvOrder o = {};
  int done[8] = {}, last = -1;
  for (int k = 0; k < 36; ++k) {
    int best = -1;
    for (int t = 0; t < 8; ++t)
      if (t != last && done[t] <= t && (best < 0 || t - done[t] > best - done[best])) best = t;
    o.t[k] = best; o.u[k] = done[best]++; last = best;
  }
  return o;
}
constexpr ConvOrder conv_order_last() {
  ConvOrder o = {};
  int k = 0;
  for (int p = 0; p < 4; ++p)
    for (int u = 0; u <= 2 * p + 1; ++u) {
      o.t[k] = 2 * p + 1; o.u[k] = u; ++k;
      if (u <= 2 * p) { o.t[k] = 2 * p; o.u[k] = u; ++k; }
    }
  return o;
}
constexpr int epi_slot(int v) { return v < 32 ? 2 * EO + v : 2 * 50 - 1 + (v - 32); }
constexpr bool epi_pair(int j) { return (j >= EO && j < EO + 19) || (j >= 50 && j < 61); }
constexpr int hc_upto(int j) { return j < 52 ? 0 : j - 52; }
constexpr int hl_upto(int i) { return i < 9 ? 28 : i < 37 ? 28 + (i - 9) : 56; }
constexpr int c_ops(int j) {
  return (j >= 8 && j < 12 ? 2 : j < 34 || (j >= 35 && j < 51) ? 1 : 0) + hc_upto(j + 1) - hc_upto(j) +
         (j == EO + 6 || j == EO + 14 || j == 56 ? 1 : j == EO + 10 || j == EO + 18 || j == 60 ? 2 : 0);
}
constexpr int c_between(int a, int b) { int n = 0; for (int j = a + 1; j < b; ++j) n += c_ops(j); return n; }
DI unsigned tagbad(unsigned bad, v4u g, unsigned tagA, unsigned tagB) {
  return __builtin_amdgcn_bitop3_b32(__builtin_amdgcn_bitop3_b32(bad, g.y, tagA, 0xF6), g.w, tagB, 0xF6);
}
template <int R0, int R1, int R2, int R3> DI void put4(unsigned base, f32x2 a, f32x2 b, f32x2 c, f32x2 d) {
  asm volatile("s_mov_b32 m0, %4\n\ts_nop 0\n\tds_write_addtid_b32 %0 offset:%5\n\tds_write_addtid_b32 %1 offset:%6\n\t"
               "ds_write_addtid_b32 %2 offset:%7\n\tds_write_addtid_b32 %3 offset:%8"
               :: "v"(pack2(a.x, a.y)), "v"(pack2(b.x, b.y)), "v"(pack2(c.x, c.y)), "v"(pack2(d.x, d.y)), "s"(base),
                  "n"(R0 * UVS_ROW), "n"(R1 * UVS_ROW), "n"(R2 * UVS_ROW), "n"(R3 * UVS_ROW) : "memory");
}
DI unsigned sum4(unsigned m, v4u v) {
  asm("v_add3_u32 %0, %0, %1, %2\n\tv_add3_u32 %0, %0, %3, %4" : "+v"(m) : "v"(v.x), "v"(v.y), "v"(v.z), "v"(v.w));
  return m;
}
DI unsigned sum8(unsigned m, v4u a, v4u b) {
  asm("v_add3_u32 %0, %0, %1, %2\n\tv_add3_u32 %0, %0, %3, %4\n\tv_add3_u32 %0, %0, %5, %6\n\tv_add3_u32 %0, %0, %7, %8"
      : "+v"(m) : "v"(a.x), "v"(a.y), "v"(a.z), "v"(a.w), "v"(b.x), "v"(b.y), "v"(b.z), "v"(b.w));
  return m;
}
template <int I> DI unsigned red_step(unsigned v) {
  constexpr int ctrl[6] = {0xB1, 0x4E, 0x141, 0x140, 0x142, 0x143}, rows[6] = {0xF, 0xF, 0xF, 0xF, 0xA, 0xC};
  return v + (unsigned)__builtin_amdgcn_update_dpp(0, (int)v, ctrl[I], rows[I], 0xF, true);
}
DI unsigned red_all(unsigned v) { return red_step<5>(red_step<4>(red_step<3>(red_step<2>(red_step<1>(red_step<0>(v)))))); }
DI void swap_add32(float& a, float b) { const auto r = __builtin_amdgcn_permlane32_swap(f2u(a), f2u(b), false, false); a = u2f(r[0]) + u2f(r[1]); }
DI void swap_add16(float& a, float b) { const auto r = __builtin_amdgcn_permlane16_swap(f2u(a), f2u(b), false, false); a = u2f(r[0]) + u2f(r[1]); }

typedef const unsigned char* cptr;
typedef unsigned v16u __attribute__((ext_vector_type(16)));
DI cptr mkptr(unsigned lo, unsigned hi) { return (cptr)(((u64)hi << 32) | lo); }

__global__ void __launch_bounds__(256, 1)
s4_block_kernel(cptr, cptr, cptr, cptr, cptr, cptr, cptr, cptr, cptr, cptr, cptr, cptr, cptr, unsigned char*, unsigned char*, cptr) {
  STAMP(0);
  v16u ka0, ka1;
  asm volatile("s_load_dwordx16 %0, %2, 0x0\n\ts_load_dwordx16 %1, %2, 0x40" : "=&s"(ka0), "=&s"(ka1) : "s"(__builtin_amdgcn_kernarg_segment_ptr()));
  SB();
  LAS unsigned char* const L = (LAS unsigned char*)16u - 16;
  const int tid = threadIdx.x, lane = tid & 63, w = __builtin_amdgcn_readfirstlane(tid >> 6);
  const int b = blockIdx.x, xcd = b & 7, slot = b >> 3, id = xcd * 32 + slot;
  const int ks = (id >> 6) * 4 + w, cc = id & 63;
  const int rp = 4 * (xcd >> 1) + (slot >> 3), rg = xcd & 1, dt = slot & 7, si = 8 * rg + dt;
  const int ct = 7 * rg + min(dt, 6);
  const int rot = (0x0340 >> (4 * (2 * (w & 1) + rg))) & 7;

  const unsigned char* pcode;
  asm volatile("s_getpc_b64 %0" : "=s"(pcode));
  const unsigned warm = *(const GAS unsigned*)((const GAS unsigned char*)pcode + (unsigned)min((((slot & 7) * 4 + w) * 9 + min(lane, 8)) * 128, CODE_BYTES));
  const int g16 = lane >> 4, q4 = (lane & 15) >> 2, p4 = lane & 3, hf = lane >> 5, blk = (lane >> 4) & 1, l16 = lane * 16;
  const unsigned lrw = L_R + w * 16384, lsp = L_SP + w * 6144;
#define SLOT(n) ((n) < 16 ? lrw + (n) * 1024 : lsp + ((n) - 16) * 1024)
  unsigned wmo[3], co = (ks * 32 + (lane & 31)) * 2, xo = (id >> 1) * 16384 + (id & 1) * 512 + (tid & 7) * 2048 + (tid >> 3) * 16, ro = id * 8192 + w * 2048 + hf * 1024 + (lane & 31) * 16, po = id * 512 + tid * 2;
  unsigned wto = id * 7168 + (w >> 1) * 3584 + min(lane, 27) * 128;
#pragma unroll
  for (int i = 0; i < 3; ++i) { const unsigned idx = lane + 64 * i; wmo[i] = (ks * 32 * 6144 + cc * 96 - i * 1024) + idx / 6 * (6144 - 96) + idx * 16; PIN(wmo[i]); }
  PIN(co); PIN(xo); PIN(ro); PIN(po); PIN(wto);
  set_m0(lrw);
  SB();
  unsigned w2v0 = (lane >> 3) * 1024 + (((lane & 7) ^ (blk << 2)) * 16), w2v1 = w2v0 + 7168;
  unsigned w1v0 = (lane >> 2) * 3584 + p4 * 16, w1v1 = w1v0 + 57344 - 1024;
  const int zs = OFF_Z + (2 * id + (w >> 1)) * 64;
  int za = (((w & 1) + 2 * (g16 & 1)) * 8 + q4 * 2 + (g16 >> 1)) * 32768 + p4 * 16, zb = za + 32 * 32768;
  int tra = (hf * 8 + q4) * 64 + blk * 32 + p4 * 8;
  LAS unsigned char *w1a = L + lrw + tra, *w1s = L + lsp + tra;
#define W1PULL(step, n) w1f[step] = trd2(((n) < 16 ? w1a : w1s) + ((n) & 15) * 1024, ((n) < 16 ? w1a : w1s) + ((n) & 15) * 1024 + 256)
  LAS unsigned char* ka = L + L_KC + ((w >> 1) * 4 + q4) * KC_STRIDE + 2 * (32 - 8 * g16 + 4 * p4) - 8 + 256 * (w & 1);
  LAS unsigned char* hb = L + L_H + (w >> 1) * H_STRIDE + (lane & 15) * 32 + g16 * 16 + 256 - 256 * (w & 1);
  LAS unsigned char* zp = L + sel(tid < 64, L_KC + ((lane >> 1) & 7) * KC_STRIDE + (lane & 1) * 16, L_H + hf * H_STRIDE + min(lane & 31, 29) * 16);
  LAS unsigned char* ub = L + L_UVS + (lane & 15) * UVS_ROW + g16 * 16;
  int gmo = OFF_MG + (g16 * 256 + id) * 256 + (lane & 15) * 16, gmo2 = OFF_MG + ((4 + blk) * 256 + id) * 256 + (lane & 15) * 16;
  int mgo = sel(lane < 48, (lane >> 1) * 256 + (lane & 1) * 8, OOB), sgo = sel(lane & 31, OOB, hf * 8);
  LAS unsigned char* xbw = L + L_XB + (tid >> 7) * 2048 + (tid & 127) * 4;
  LAS unsigned char* xbr = L + L_XB + w * 2048 + (lane & 15) * 32 + g16 * 8;
  LAS unsigned char *exa = L + L_XB + w * 2048 + l16, *exb = L + L_RX + w * 3072 + l16, *eya = L + L_XB + (w ^ 1) * 2048 + l16, *eyb = L + L_RX + (w ^ 1) * 3072 + l16;
  PIN(xbw); PIN(xbr); PIN(exa); PIN(exb); PIN(eya); PIN(eyb);
  int mto = (tid >> 1) * 128 + (tid & 1) * 16;
  LAS unsigned char* mtw = L + (tid & 1) * L_TG + (tid >> 1) * 4;
  PIN(mto); PIN(mtw);
  PIN(w2v0); PIN(w2v1); PIN(w1v0); PIN(w1v1); PIN(za); PIN(zb); PIN(tra); PIN(w1a); PIN(w1s); PIN(ka); PIN(hb); PIN(zp); PIN(ub); PIN(gmo); PIN(gmo2); PIN(mgo); PIN(sgo);
#define W1DMA(step, lds) dma_w1(w1w + (step) * 57344, w1v0, (lds))
#define W1DMA2(step, lds) dma_w2(w1w + (step) * 57344, w1v0, w1v1, (lds))
#define W2SRC(step) (((step) < 12 ? w2lo : (step) < 16 ? w2c3 : w2hi) + (step) * 16384)
#define W2DMA(step, lds) dma_w2(W2SRC(step), w2v0, w2v1, (lds))
#define W2HALF(n, lds) do { if constexpr ((n) & 1) asm volatile("global_load_lds_dwordx4 %0, %1 offset:1024" :: "v"(w2v1), "s"(W2SRC((n) >> 1)) : "memory"); \
    else dma_w1(W2SRC((n) >> 1), w2v0, (lds)); } while (0)
  SB();
  STAMPA(24);
  asm volatile("s_waitcnt lgkmcnt(0)" : "+s"(ka0), "+s"(ka1));
  STAMPA(25);
  const cptr xin = mkptr(ka0[0], ka0[1]), pAre = mkptr(ka0[2], ka0[3]), pAim = mkptr(ka0[4], ka0[5]), pBre = mkptr(ka0[6], ka0[7]);
  const cptr pBim = mkptr(ka0[8], ka0[9]), pCre = mkptr(ka0[10], ka0[11]), pCim = mkptr(ka0[12], ka0[13]), pldt = mkptr(ka0[14], ka0[15]);
  const cptr pD = mkptr(ka1[0], ka1[1]), w1 = mkptr(ka1[2], ka1[3]), w2 = mkptr(ka1[4], ka1[5]), pc = mkptr(ka1[6], ka1[7]);
  const cptr wmod = mkptr(ka1[8], ka1[9]), out = mkptr(ka1[10], ka1[11]), ws = mkptr(ka1[12], ka1[13]);
  const unsigned epoch_v = __hip_atomic_load((GAS unsigned*)ws, __ATOMIC_RELAXED, __HIP_MEMORY_SCOPE_AGENT);
  asm volatile("global_load_lds_dwordx4 %0, %3\n\tglobal_load_lds_dwordx4 %1, %3 offset:1024\n\tglobal_load_lds_dwordx4 %2, %3 offset:2048"
               :: "v"(wmo[0]), "v"(wmo[1]), "v"(wmo[2]), "s"(wmod) : "memory");
  const unsigned cval = *(const GAS u16*)((const GAS unsigned char*)pc + co);
  SB();
  const v4u xa = *(const GAS v4u*)((const GAS unsigned char*)xin + xo), xb = *(const GAS v4u*)((const GAS unsigned char*)xin + 1024 + xo);
  const v4u ra = *(const GAS v4u*)((const GAS unsigned char*)xin + ro), rb = *(const GAS v4u*)((const GAS unsigned char*)xin + 512 + ro);
  SB();
#define PARAM(p) (unsigned)*(const GAS u16*)((const GAS unsigned char*)(p) + po)
  const unsigned uAre = PARAM(pAre), uAim = PARAM(pAim), uBre = PARAM(pBre), uBim = PARAM(pBim), uCre = PARAM(pCre), uCim = PARAM(pCim);
#undef PARAM
  SB();
  const unsigned char* setp = ws + SET_BASE;
  const __amdgpu_buffer_rsrc_t rs = __builtin_amdgcn_make_buffer_rsrc((void*)setp, 0, SET_BYTES, 0x00020000);
  const unsigned pr0 = __builtin_amdgcn_raw_buffer_load_b32(rs, sel(lane < 48, mgo + OFF_MG + cc * 24 * 256 + ks * 16,
      sel(lane < 56, OFF_CK + rp * 16384 + (ct * 4 + w) * 64 + p4 * 4096, (lane & 1) ? OFF_SG + id * 64 : OFF_MT + id * 128)), 0, 0);
  unsigned uldt, uD;
  asm volatile("s_load_dword %0, %2, %4\n\ts_load_dword %1, %3, %4" : "=&s"(uldt), "=&s"(uD) : "s"(pldt), "s"(pD), "s"(id * 4));
  SB();
  STAMPA(26);
  LAS unsigned char* koff[12];
  {
    float fj = (float)(8 * tid - 3);
    PIN(fj);
    const unsigned lin = (unsigned)(8 * tid - 3 + 0x4300 - 128) * 0x10001u + 0x10000u;
#pragma unroll
    for (int i = 0; i < 12; i += 2) {
      const unsigned r = sel(tid < 32, lin + i * 0x10001u, pack2(fj + (float)i, fj + (float)(i + 1)));
      koff[i] = L + ((r & 0xFFFFu) * 4 + (L_KT + (128 - 0x4300) * 4));
      koff[i + 1] = L + ((r >> 16) * 4 + (L_KT + (128 - 0x4300) * 4));
      PIN(koff[i]);
      if (i < 10) PIN(koff[i + 1]);
    }
  }
  *(LAS v4u*)(L + L_KT - 16) = v4u{0u, 0u, 0u, 0u};
  LAS unsigned char* ktw[4];
  {
    const int row = w * 16 + (lane & 15), sg = max((row >> 3) - 1, 0), rr_ = row - 8 * sg;
    const int idx0 = 128 * sg + ((rr_ >> (3 - sg)) << (7 - sg)) + (rr_ & ((8 >> sg) - 1)) + 1, st = 8 >> sg;
#pragma unroll
    for (int r = 0; r < 4; ++r) {
      const int idx = sel(row < 40, idx0 + st * (4 * g16 + r), tid == 136 && r == 0 ? 0 : 641 + lane);
      ktw[r] = L + L_KT + idx * 4;
      PIN(ktw[r]);
    }
  }
  const unsigned char* w2lo = w2 + (448 * w + 64 * rot) * 1024 + dt * 128;
  const unsigned char* w2c3 = w2lo - (rot >> 2) * (448 * 1024);
  const unsigned char* w2hi = w2lo - ((rot + 1) >> 2) * (448 * 1024);
  const unsigned char* w1w = w1 + ct * 256 + w * 64;
  SB();
  STAMPA(27);
  asm volatile("s_waitcnt lgkmcnt(0)" : "+s"(uldt), "+s"(uD));
  const unsigned epoch = __builtin_amdgcn_readfirstlane(epoch_v);
  asm volatile("" :: "s"(epoch));
  STAMPA(28);
  const unsigned tagA = epoch * 0x9E3779B1u + 0x7F4A7C15u, tagB = ~tagA;
  {
    const float c = bf_lo(cval);
    const float s = c * __builtin_amdgcn_rcpf(1.0f + __builtin_amdgcn_exp2f(-LOG2E * c));
    *(LAS u16*)(L + lrw + 3072 + (lane & 31) * 2) = (u16)pack2(s, s);
    SB();
#define XPUB(i) do { stg(rs, (tid >> 3) * 65536 + (tid & 7) * 16, OFF_XG + ((id & 1) * 128 + (i)) * 16384 + (id >> 1) * 128, v4u{xa[i], tagA, xb[i], tagB}); SB(); } while (0)
    asm volatile("" :: "v"(xb));
    STAMPA(29);
    XPUB(0);
    XPUB(1);
    STAMP(1);
    LAS unsigned char* a = L + lrw + (8 * g16 + q4) * 96 + p4 * 8;
    const v4u sc = lds128(L + lrw + 3072 + g16 * 16);
    const v4u m0 = trd2(a, a + 384), m1 = trd2(a + 32, a + 416), m2 = trd2(a + 64, a + 448);
    SB();
    f32x2 fa[8];
#pragma unroll
    for (int i = 0; i < 4; ++i) { fa[i] = f32x2{bf_lo(ra[i]), bf_hi(ra[i])}; fa[4 + i] = f32x2{bf_lo(rb[i]), bf_hi(rb[i])}; }
    const f32x2 s2 = ((fa[0] + fa[1]) + (fa[2] + fa[3])) + ((fa[4] + fa[5]) + (fa[6] + fa[7]));
    float mean = s2.x + s2.y;
    PIN(mean);
    SB();
    const f32x4 c0 = mfma16(sc, m0, f32x4{}), c1 = mfma16(sc, m1, f32x4{}), c2 = mfma16(sc, m2, f32x4{});
    SB();
    mean = swap16_add(row_sum(mean)) * (1.0f / 512.0f);
    PIN(mean);
    SB();
    XPUB(2);
    __builtin_amdgcn_raw_buffer_store_b64(v2u{sel(lane < 16, f2u(c0[0]), sel(lane < 32, f2u(c1[0]), f2u(c2[0]))), (lane & 1) ? tagB : tagA}, rs, mgo,
                                          OFF_MG + cc * 24 * 256 + ks * 16, SC1);
    SB();
    f32x2 q2 = {0.f, 0.f}, q3 = {0.f, 0.f};
#pragma unroll
    for (int i = 0; i < 8; i += 2) {
      const f32x2 d0 = fa[i] - f32x2{mean, mean}, d1 = fa[i + 1] - f32x2{mean, mean};
      q2 = __builtin_elementwise_fma(d0, d0, q2);
      q3 = __builtin_elementwise_fma(d1, d1, q3);
    }
    q2 += q3;
    const float var = rbf(rbf(swap16_add(row_sum(q2.x + q2.y)) * (1.0f / 512.0f)) + 1e-5f);
    __builtin_amdgcn_raw_buffer_store_b64(v2u{pack2(mean, __builtin_amdgcn_sqrtf(var)), hf ? tagB : tagA}, rs, sgo, OFF_SG + (id * 4 + w) * 16, SC1);
    SB();
    XPUB(3);
#undef XPUB
  }
  unsigned wt;
  SB();
  STAMP(2);
  unsigned dsk = tid == 136 ? uD : 0u;
  PIN(dsk);

  {
    const float dtv = rbf(__builtin_amdgcn_exp2f(LOG2E * (w >> 1 ? bf_hi(uldt) : bf_lo(uldt))));
    const float are = dtv * bf_lo(uAre) * LOG2E, aim = dtv * bf_lo(uAim) * INV2PI;
    const float br = dtv * bf_lo(uBre), bi = dtv * bf_lo(uBim), cr = bf_lo(uCre), ci = bf_lo(uCim);
    const f32x2 wv = {cr * br - ci * bi, cr * bi + ci * br};
    const unsigned row = L_UVS + (w >> 1) * UVS_CH + (w & 1) * 256;
    const f32x2 e1 = cexp(are, aim), e8 = cexp(8.f * are, -8.f * aim), e128 = cexp(128.f * are, 128.f * aim);
    const f32x2 e1p = {-e1.y, e1.x}, e8p = {-e8.y, e8.x}, e128p = {-e128.y, e128.x};
    const v4u pois = {POISON, POISON, POISON, POISON};
    f32x2 z[8], v, v2, y, y2;
#define M1(a) cmul(a, e1, e1p)
#define M8(a) cmul(a, e8, e8p)
#define MK(a) cmul(a, e128, e128p)
    z[0] = M1(wv);
    put4<40, 41, 42, 0>(row, wv, f32x2{1.f, 0.f}, e8, z[0]);
    stg(rs, (tid >> 3) * 32768 + (tid & 7) * 16, OFF_Z + id * 128, pois);
    v = M8(e8); z[1] = M1(z[0]); v2 = M8(v); z[2] = M1(z[1]);
    put4<43, 1, 44, 2>(row, v, z[1], v2, z[2]);
    v = M8(v2); z[3] = M1(z[2]); v2 = M8(v); z[4] = M1(z[3]);
    put4<45, 3, 46, 4>(row, v, z[3], v2, z[4]);
    stg(rs, (tid >> 3) * 32768 + (tid & 7) * 16, OFF_Z + id * 128 + 32 * 32768, pois);
    v = M8(v2); z[5] = M1(z[4]); v2 = M8(v); z[6] = M1(z[5]);
    put4<47, 5, 48, 6>(row, v, z[5], v2, z[6]);
    asm volatile("global_load_dword %0, %1, %2" : "=v"(wt) : "v"(wto), "s"(w1 + (w & 1) * (w2 - w1)), "v"(warm), "v"(pr0) : "memory");
    z[7] = M1(z[6]); z[0] = MK(z[0]); z[1] = MK(z[1]); z[2] = MK(z[2]);
    put4<7, 8, 9, 10>(row, z[7], z[0], z[1], z[2]);
    z[3] = MK(z[3]); z[4] = MK(z[4]); z[5] = MK(z[5]); z[6] = MK(z[6]);
    put4<11, 12, 13, 14>(row, z[3], z[4], z[5], z[6]);
    z[7] = MK(z[7]); z[1] = MK(z[1]); z[3] = MK(z[3]); z[5] = MK(z[5]);
    put4<15, 16, 17, 18>(row, z[7], z[1], z[3], z[5]);
    W1DMA2(0, lrw);
    z[7] = MK(z[7]); z[1] = MK(z[1]); z[3] = MK(z[3]); z[5] = MK(z[5]);
    put4<19, 20, 21, 22>(row, z[7], z[1], z[3], z[5]);
    y = MK(z[7]); z[3] = MK(z[3]); y2 = MK(y); z[4] = MK(z[3]);
    put4<23, 24, 25, 26>(row, y, z[3], y2, z[4]);
    W1DMA2(2, lrw + 2048);
    y = MK(y2); z[3] = MK(z[4]); y2 = MK(y); z[4] = MK(z[3]);
    put4<27, 28, 29, 30>(row, y, z[3], y2, z[4]);
    y = MK(y2); v = M8(v2); y2 = MK(y); v2 = M8(v);
    put4<31, 49, 32, 50>(row, y, v, y2, v2);
    y = MK(y2); v = M8(v2); y2 = MK(y); v2 = M8(v);
    put4<33, 51, 34, 52>(row, y, v, y2, v2);
    W1DMA2(4, lrw + 4096);
    y = MK(y2); v = M8(v2); y2 = MK(y); v2 = M8(v);
    put4<35, 53, 36, 54>(row, y, v, y2, v2);
    y = MK(y2); v = M8(v2); y2 = MK(y); v2 = M8(v);
    put4<37, 55, 38, 56>(row, y, v, y2, v2);
    W1DMA2(6, lrw + 6144);
    y = MK(y2);
    *(LAS unsigned*)(L + row + 39 * UVS_ROW + lane * 4) = pack2(y.x, y.y);
#undef M1
#undef M8
#undef MK
  }
  BAR();
  STAMP(3);
  v4u gx[4], gs[4], gm, gn;
  const int xgo = OFF_XG + id * 16384;
  if (w < 3) {
    LAS unsigned char* ut = ub + w * 16 * UVS_ROW;
    v4u fv[2][8], fu[2][8];
#pragma unroll
    for (int s = 0; s < 8; ++s)
#pragma unroll
      for (int c = 0; c < 2; ++c) { fv[c][s] = lds128(ub + c * UVS_CH + 41 * UVS_ROW + s * 64); fu[c][s] = lds128(ut + c * UVS_CH + s * 64); SB(); }
    f32x4 a0 = {}, a1 = {};
#pragma unroll
    for (int s = 0; s < 8; ++s) { a0 = mfma16(fv[0][s], fu[0][s], a0); SB(); a1 = mfma16(fv[1][s], fu[1][s], a1); SB(); }
    *(LAS unsigned*)ktw[0] = pack2(a0[0] + bf_lo(dsk), a1[0] + bf_hi(dsk));
#pragma unroll
    for (int r = 1; r < 4; ++r) *(LAS unsigned*)ktw[r] = pack2(a0[r], a1[r]);
  }
  SB();
#define LD1(i, so) "buffer_load_dwordx4 %" #i ", %10, %13, " so " offen sc1\n\t"
  asm volatile(LD1(0, "%14") LD1(1, "%15") LD1(2, "%16") LD1(3, "%17") LD1(4, "%18") LD1(5, "%19") LD1(6, "%20") LD1(7, "%21")
               "buffer_load_dwordx4 %8, %11, %13, 0 offen sc1\n\tbuffer_load_dwordx4 %9, %12, %13, 0 offen sc1"
               : "=&v"(gx[0]), "=&v"(gx[1]), "=&v"(gx[2]), "=&v"(gx[3]), "=&v"(gs[0]), "=&v"(gs[1]), "=&v"(gs[2]), "=&v"(gs[3]), "=&v"(gm), "=&v"(gn)
               : "v"(tid * 16), "v"(gmo), "v"(gmo2), "s"(rs), "s"(xgo), "s"(xgo + 4096), "s"(xgo + 8192), "s"(xgo + 12288),
                 "s"(OFF_SG), "s"(OFF_SG + 4096), "s"(OFF_SG + 8192), "s"(OFF_SG + 12288) : "memory");
#undef LD1
  SB();
  BAR();
  {
    unsigned k[11];
#pragma unroll
    for (int i = 0; i < 11; ++i) k[i] = *(LAS unsigned*)koff[i];
    SB();
    if (w < 2) *(LAS v4u*)zp = v4u{0u, 0u, 0u, 0u};
#pragma unroll
    for (int c = 0; c < 2; ++c) {
      unsigned e[5], o[5];
#pragma unroll
      for (int i = 0; i < 5; ++i) {
        o[i] = __builtin_amdgcn_perm(k[2 * i + 1], k[2 * i], c ? 0x07060302u : 0x05040100u);
        e[i] = __builtin_amdgcn_perm(k[2 * i + 2], k[2 * i + 1], c ? 0x07060302u : 0x05040100u);
      }
      LAS unsigned char* d = L + L_KC + c * 4 * KC_STRIDE + 32 + tid * 16;
      *(LAS v4u*)(d) = v4u{e[1], e[2], e[3], e[4]};
      *(LAS v4u*)(d + KC_STRIDE) = v4u{o[1], o[2], o[3], o[4]};
      *(LAS v4u*)(d + 2 * KC_STRIDE) = v4u{e[0], e[1], e[2], e[3]};
      *(LAS v4u*)(d + 3 * KC_STRIDE) = v4u{o[0], o[1], o[2], o[3]};
    }
  }
  SB();
  STAMP(4);
  {
    asm volatile("s_waitcnt vmcnt(0)" : "+v"(gx[0]), "+v"(gx[1]), "+v"(gx[2]), "+v"(gx[3]), "+v"(gs[0]), "+v"(gs[1]), "+v"(gs[2]), "+v"(gs[3]), "+v"(gm), "+v"(gn), "+v"(wt) :: "memory");
    unsigned bad = tagbad(tagbad(0u, gm, tagA, tagB), gn, tagA, tagB);
#pragma unroll
    for (int i = 0; i < 4; ++i) bad = tagbad(tagbad(bad, gx[i], tagA, tagB), gs[i], tagA, tagB);
    if (__builtin_expect(__any(bad != 0u), 0)) {
      _Pragma("nounroll") for (unsigned spin = 0; spin < SPIN_CAP; ++spin) {
        FRESH();
        if (spin) PAUSE();
        COUNT(20);
#pragma unroll
        for (int i = 0; i < 4; ++i) {
          if (tagbad(0u, gx[i], tagA, tagB)) gx[i] = ldg(rs, tid * 16, xgo + i * 4096);
          if (tagbad(0u, gs[i], tagA, tagB)) gs[i] = ldg(rs, tid * 16, OFF_SG + i * 4096);
        }
        if (tagbad(0u, gm, tagA, tagB)) gm = ldg(rs, gmo, 0);
        if (tagbad(0u, gn, tagA, tagB)) gn = ldg(rs, gmo2, 0);
        bad = tagbad(tagbad(0u, gm, tagA, tagB), gn, tagA, tagB);
#pragma unroll
        for (int i = 0; i < 4; ++i) bad = tagbad(tagbad(bad, gx[i], tagA, tagB), gs[i], tagA, tagB);
        if (!__any(bad != 0u)) break;
      }
    }
  }
  STAMP(5);
  W1DMA2(8, lrw + 8192);
  float zsc;
  v4u mtp;
  {
    const int i0 = (int)pack2(row_sum(u2f(gm.x)), row_sum(u2f(gm.z))), i1 = (int)pack2(row_sum(u2f(gn.x)), row_sum(u2f(gn.z)));
    unsigned md[6];
#pragma unroll
    for (int k = 0; k < 6; ++k) md[k] = (unsigned)__builtin_amdgcn_readlane(k < 4 ? i0 : i1, 16 * (k & 3));
#define MOD(k, c) ((c) ? bf_hi(md[k]) : bf_lo(md[k]))
    float s0 = rbf(1.0f + MOD(4, 0)), s1 = rbf(1.0f + MOD(4, 1));
    s0 = s0 == 0.f ? 0x1p-30f : s0;
    s1 = s1 == 0.f ? 0x1p-30f : s1;
    const float G0 = MOD(2, 0) * s0, G1 = MOD(2, 1) * s1;
    const float A0 = rbf(1.0f + MOD(1, 0)) * G0, A1 = rbf(1.0f + MOD(1, 1)) * G1, B0 = MOD(0, 0) * G0, B1 = MOD(0, 1) * G1;
    zsc = w >> 1 ? s1 : s0;
    mtp = v4u{lane ? md[5] : pack2(s0, s1), tagA, md[3], tagB};
#undef MOD
#pragma unroll
    for (int i = 0; i < 4; ++i) {
      const float mA = bf_lo(gs[i].x), rA = __builtin_amdgcn_rcpf(bf_hi(gs[i].x)), mB = bf_lo(gs[i].z), rB = __builtin_amdgcn_rcpf(bf_hi(gs[i].z));
      const float n00 = (bf_lo(gx[i].x) - mA) * rA, n01 = (bf_hi(gx[i].x) - mA) * rA, n10 = (bf_lo(gx[i].z) - mB) * rB, n11 = (bf_hi(gx[i].z) - mB) * rB;
      const int o = (tid + 256 * i) * 4;
      W1DMA2(10 + 2 * i, SLOT(10 + 2 * i));
      *(LAS unsigned*)(L + L_H + 480 + o) = pack2(__builtin_fmaf(n00, A0, B0), __builtin_fmaf(n10, A0, B0));
      *(LAS unsigned*)(L + L_H + H_STRIDE + 480 + o) = pack2(__builtin_fmaf(n01, A1, B1), __builtin_fmaf(n11, A1, B1));
      *(LAS unsigned*)(xbw + i * 512) = __builtin_amdgcn_perm(gx[i].z, gx[i].x, 0x05040100u);
      *(LAS unsigned*)(xbw + 4096 + i * 512) = __builtin_amdgcn_perm(gx[i].z, gx[i].x, 0x07060302u);
    }
  }
  BAR();
  const unsigned tb = now();
  STAMP(6);

  v4u w1f[32], w2f[28][2], mt0, mt1;
  {
    const bool rh = w & 1;
    constexpr ConvOrder O = conv_order(), OL = conv_order_last();
    v2u A[4][8][2], xk[4];
    v4u B[4][8];
    f32x4 acc[8], keep[4];
#pragma unroll
    for (int u = 0; u < 8; ++u) {
      A[0][u][0] = trd(ka + 512 * u + 8);
      A[0][u][1] = trd(ka + 512 * u);
      B[0][7 - u] = lds128(hb + 64 * (8 * (7 - u) + 3));
    }
    SB();
    if (lane) mtp.z = tb;
    stg(rs, tid < 2 ? tid * 16 : OOB, OFF_MT + id * 128, mtp);
    SB();
    auto hand_over = [&](auto Pc) __attribute__((always_inline)) {
      constexpr int i = Pc.v;
      PIN(acc[2 * i]); PIN(acc[2 * i + 1]);
      const f32x4 ev = acc[2 * i], od = acc[2 * i + 1];
      const f32x4 snd = rh ? ev : od, own = rh ? od : ev;
      *(LAS f32x4*)(i < 2 ? exa + i * 1024 : exb + (i - 1) * 1024) = snd;
#pragma unroll
      for (int c = 0; c < 4; ++c) { float t = keep[i][c] + own[c]; PIN(t); keep[i][c] = t; }
    };
    static_for<0, 144>([&](auto Kc) __attribute__((always_inline)) {
      constexpr int K = Kc.v, r = K / 36, k = K % 36, t = r < 3 ? O.t[k] : OL.t[k], u = r < 3 ? O.u[k] : OL.u[k];
      if constexpr (k == 0 && r > 0) asm volatile("" :: "v"(B[r][7]));
      mfma16_v<r == 0 && u == 0>(acc[t], v4u{A[r][u][0].x, A[r][u][0].y, A[r][u][1].x, A[r][u][1].y}, B[r][t - u]);
      SB();
      if constexpr (K == 24) WAITV(13);
      if constexpr (K >= 24 && K < 36) W1PULL(K - 24, K - 24);
      if constexpr (K == 40) asm volatile("" :: "a"(w1f[11]));
      if constexpr (K == 60) WAITV(8);
      if constexpr (K >= 60 && K < 70) W1PULL(K - 48, K - 48);
      if constexpr (K == 76) asm volatile("" :: "a"(w1f[21]));
      if constexpr (r == 2 && k < 4) xk[k] = *(LAS v2u*)(xbr + k * 512);
      constexpr int n = k - (r < 2 ? 0 : 4);
      if constexpr (r < 3 && n >= 0 && n < 24) {
        if constexpr (n % 3 == 2) B[r + 1][n / 3] = lds128(hb + 64 * (8 * (n / 3) - r + 2));
        else A[r + 1][n / 3][n % 3] = trd(ka + 512 * (n / 3) + 64 * (r + 1) + 8 - 8 * (n % 3));
      }
      if constexpr (K >= 100 && K < 116) {
        constexpr int e = K - 100;
        const unsigned xw = (e & 2) ? xk[e >> 2].y : xk[e >> 2].x;
        float t2_ = zsc * ((e & 1) ? bf_hi(xw) : bf_lo(xw));
        PIN(t2_);
        keep[e >> 2][e & 3] = t2_;
      }
      if constexpr (K == 108 + 8) hand_over(IC<0>{});
      if constexpr (K == 108 + 17) hand_over(IC<1>{});
      if constexpr (K == 108 + 30) hand_over(IC<2>{});
      if constexpr (K == 1 || K == 8) W1DMA2(18 + K / 4, SLOT(18 + K / 4));
      if constexpr (K == 15) W1DMA(22, L_RX + w * 3072);
      if constexpr (K == 19) W1DMA(23, L_TOP + w * 1024);
      if constexpr (K >= 41 && K < 69 && K % 7 == 6) W1DMA2(24 + (K - 41) / 7 * 2, lrw + (K - 41) / 7 * 2048);
      if constexpr (K >= 69 && K < 125 && K % 8 == 5) W2DMA((K - 69) / 8, SLOT(8 + (K - 69) / 8 * 2));
      if constexpr (K == MTK)
        asm volatile("buffer_load_dwordx4 %0, %2, %3, 0 offen sc1\n\tbuffer_load_dwordx4 %1, %2, %3, %4 offen sc1"
                     : "=&v"(mt0), "=&v"(mt1) : "v"(mto), "s"(rs), "s"(16384) : "memory");
      SB();
    });
    STAMP(7);
    asm volatile("s_nop 7" : "+v"(acc[6]), "+v"(acc[7]));
    hand_over(IC<3>{});
    BAR();
#pragma unroll
    for (int i = 0; i < 4; ++i) keep[i] += *(LAS f32x4*)(i < 2 ? eya + i * 1024 : eyb + (i - 1) * 1024);
#pragma unroll
    for (int i = 0; i < 2; ++i) {
      const auto s0 = __builtin_amdgcn_permlane16_swap(pack2(keep[2 * i][0], keep[2 * i][1]), pack2(keep[2 * i + 1][0], keep[2 * i + 1][1]), false, false);
      const auto s1 = __builtin_amdgcn_permlane16_swap(pack2(keep[2 * i][2], keep[2 * i][3]), pack2(keep[2 * i + 1][2], keep[2 * i + 1][3]), false, false);
      stg(rs, i ? zb : za, zs, v4u{guard(s0[0]), guard(s1[0]), guard(s0[1]), guard(s1[1])});
    }
    SB();
  }
  STAMP(8);

  static_assert(OFF_MT == 0, "table offset");
  const unsigned te = now();
  asm volatile("s_waitcnt vmcnt(2)" : "+v"(mt0), "+v"(mt1) :: "memory");
  if (__builtin_expect(__any(tagbad(tagbad(0u, mt0, tagA, tagB), mt1, tagA, tagB) != 0u), 0)) {
    _Pragma("nounroll") for (unsigned spin = 0; spin < SPIN_CAP; ++spin) {
      FRESH();
      COUNT(21);
      if (tagbad(0u, mt0, tagA, tagB)) mt0 = ldg(rs, mto, OFF_MT);
      if (tagbad(0u, mt1, tagA, tagB)) mt1 = ldg(rs, mto, OFF_MT + 16384);
      if (!__any(tagbad(tagbad(0u, mt0, tagA, tagB), mt1, tagA, tagB) != 0u)) break;
      PAUSE();
    }
  }
  *(LAS unsigned*)(mtw) = mt0.x;
  *(LAS unsigned*)(mtw + 512) = mt1.x;
  *(LAS unsigned*)(mtw + 1024) = mt0.z;
  *(LAS unsigned*)(mtw + 1536) = mt1.z;
  {
    int late = (lane & 1) ? max((int)(mt0.z - tb), (int)(mt1.z - tb)) : 0;
    late = max(late, __builtin_amdgcn_update_dpp(late, late, 0xB1, 0xF, 0xF, false));
    late = max(late, __builtin_amdgcn_update_dpp(late, late, 0x4E, 0xF, 0xF, false));
    late = max(late, __builtin_amdgcn_update_dpp(late, late, 0x141, 0xF, 0xF, false));
    late = max(late, __builtin_amdgcn_update_dpp(late, late, 0x140, 0xF, 0xF, false));
    late = max(late, __builtin_amdgcn_update_dpp(late, late, 0x142, 0xA, 0xF, false));
    late = max(late, __builtin_amdgcn_update_dpp(late, late, 0x143, 0xC, 0xF, false));
    if (lane == 63) *(LAS int*)(L + L_ETA + w * 4) = late;
  }
  const int w2t = (4 * hf + q4) * 128 + blk * 32 + (((p4 & 1) << 1) | (p4 >> 1)) * 8;
  LAS unsigned char* w2a = L + w2t + (q4 >> 1) * 64;
  LAS unsigned char* w2b = L + w2t + ((q4 >> 1) ^ 1) * 64;
  LAS unsigned char *w2sa = w2a + lsp, *w2sb = w2b + lsp;
  PIN(w2sa); PIN(w2sb);
#define PULL(step, pa, pb, off) do { w2f[step][0] = trd2((pa) + (off), (pa) + (off) + 1024); w2f[step][1] = trd2((pb) + (off), (pb) + (off) + 1024); } while (0)
#pragma unroll
  for (int i = 0; i < 3; ++i) PULL(4 + i, w2sa, w2sb, i * 2048);
  w1f[22] = trd2(L + L_RX + w * 3072 + tra, L + L_RX + w * 3072 + tra + 256);
  w1f[23] = trd2(L + L_TOP + w * 1024 + tra, L + L_TOP + w * 1024 + tra + 256);
  BAR();
  STAMP(9);
  const int par = (0x1C >> (ct % 7)) & 1;
  const unsigned char* xg = setp + OFF_Z + rp * 131072;
  const unsigned xl = L_XR + w * 8192;
#define XSRC(p) (xg + w * 8192 + ((p) ^ par) * 32768)
  const int slo = OFF_Z + (rp * 4 + w) * 32768 + si * 2048;
  v4u hq[56], res[2], sx[8];
  const int c0 = 7 * w + rot, c3 = c0 - 7 * (rot >> 2), c4 = c0 - 7 * ((rot + 1) >> 2);
  const int hb0 = OFF_HID + (rp * 4 + rg) * 114688, kb0 = OFF_CK + (rp * 4 + rg) * 4096 + w * 896, ckv = lane < 56 ? l16 : OOB;
#define CHUNK(n) (((n) % 28 / 4 < 3 ? c0 : (n) % 28 / 4 == 3 ? c3 : c4) + (n) % 28 / 4)
#define HFRAG(n) ldg(rs, l16 + ((n) & 3) * 1024, hb0 + CHUNK(n) * 4096 + ((n) / 28) * (2 * 114688))
#define HLOAD(n) hq[n] = HFRAG(n)
  const int hoff = OFF_HID + (rp * 4 * 112 + ct * 8 + 2 * w) * 1024;
  int hv0 = dt == 7 ? OOB : l16, hv1 = dt == 7 ? OOB : l16 + 1024;
  const int cko = OFF_CK + rp * 16384 + (ct * 4 + w) * 64;
  int ckw = (p4 || dt == 7) ? OOB : lane;
  unsigned ckey = lane ? 0u : tagA;
  PIN(hv0); PIN(hv1); PIN(ckw); PIN(ckey);
#define CKPUT(sum, u) __builtin_amdgcn_raw_buffer_store_b32(ckey - red_step<1>(red_step<0>(sum)), rs, ckw, cko + ((u) ^ par) * 4096, SC1)
  const int sxo = OFF_SX + (rp * 4 + w) * 8192;
  LAS unsigned char* xr[2];
#pragma unroll
  for (int i = 0; i < 2; ++i) { xr[i] = L + L_XR + tra + i * 65536; PIN(xr[i]); }
  LAS unsigned char* rr = L + L_XR + tra + (rg ^ par) * 32768 + (4 * dt + w) * 1024;
  LAS unsigned char* str = L + L_STAT + (lane & 31) * 8;
  LAS unsigned char *pa = w2a + lrw + 8192, *pb = w2b + lrw + 8192;
  w2a += L_XR + w * 8192; w2b += L_XR + w * 8192;
  LAS unsigned char* stw = str + (w ^ par) * 256;
  PIN(str); PIN(stw); PIN(rr); PIN(w2a); PIN(w2b);
  f32x4 uu[4], vv[4];
  v4u sl0, sl1;
  {
    LAS unsigned char* qr = L + ((lane & 31) == 0 ? L_TS : L_TB) + hf * 16;
    PIN(pa); PIN(pb);
#pragma unroll
    for (int i = 0; i < 3; ++i) W2DMA(7 + i, lsp + i * 2048);
    f32x16 uv, uv2;
    v4u qa[32];
    int wait = 0;
    const v4u lt = lds128(L + L_ETA);
    bool sent = false;
    auto send = [&]() __attribute__((always_inline)) {
      STAMPB(26);
      asm volatile("buffer_load_dwordx4 %0, %2, %3, %4 offen sc1\n\tbuffer_load_dwordx4 %1, %2, %3, %4 offen offset:1024 sc1"
                   :: "v"(sl0), "v"(sl1), "v"(l16), "s"(rs), "s"(slo) : "memory");
      dma_x4(XSRC(0), l16, xl);
      dma_x4(XSRC(0) + 4096, l16, xl + 4096);
    };
    asm volatile("" : "=v"(sl0), "=v"(sl1));
#pragma unroll
    for (int i = 0; i < 4; ++i) qa[i] = lds128(qr + i * 32);
    static_for<0, 32>([&](auto Sc) __attribute__((always_inline)) {
      constexpr int s = Sc.v;
      mfma_va<(s < 2)>((s & 1) ? uv2 : uv, qa[s], w1f[s]);
      SB();
      if constexpr (s < 28) qa[s + 4] = lds128(qr + (s + 4) * 32);
      if constexpr (s > 0) asm volatile("" :: "v"(qa[s > 0 ? s - 1 : 0]));
      if constexpr (s < 8) W1PULL(24 + s, s);
      if constexpr (s == 4) { wait = min(__builtin_amdgcn_readfirstlane(max(max((int)lt.x, (int)lt.y), max((int)lt.z, (int)lt.w))), 300) + ZLAG; }
      if constexpr (s >= 8 && s < 16) w2f[(s - 8) >> 1][s & 1] = trd2(((s & 1) ? pb : pa) + ((s - 8) >> 1) * 2048, ((s & 1) ? pb : pa) + ((s - 8) >> 1) * 2048 + 1024);
      if constexpr (s == 4 || s == 13 || s == 22 || s == 31) {
        if (!sent && (int)(now() - te) >= wait) { sent = true; send(); }
      }
      SB();
    });
    mfma_done(uv2);
    PIN(uv);
    LAS unsigned char* uw = L + L_UVT + (32 * w + (lane & 31)) * 4;
    if (lane < 32) { *(LAS float*)(uw) = uv[0] + uv2[0]; *(LAS float*)(uw + 512) = -LOG2E * (uv[1] + uv2[1]); }
    LAS unsigned char* uvp = L + L_UVT + (32 * w + 4 * hf) * 4;
#pragma unroll
    for (int i = 0; i < 4; ++i) { uu[i] = *(LAS f32x4*)(uvp + i * 32); vv[i] = *(LAS f32x4*)(uvp + 512 + i * 32); }
    float i0 = __builtin_amdgcn_rcpf(bf_lo(*(LAS u16*)(L + L_TS + (si * 32 + (lane >> 2)) * 2)));
    float i1 = __builtin_amdgcn_rcpf(bf_lo(*(LAS u16*)(L + L_TS + (si * 32 + 16 + (lane >> 2)) * 2)));
    int sxw = sel(lane & 8, OOB, (p4 * 8 + hf * 4 + blk * 2 + ((lane >> 2) & 1)) * 16);
    PIN(i0); PIN(i1); PIN(sxw);
    SB();
    if (!sent) {
      while ((int)(now() - te) < wait) { }
      send();
    }
    dma_x4(XSRC(1), l16, xl + 32768);
    dma_x4(XSRC(1) + 4096, l16, xl + 32768 + 4096);
    STAMPB(27);
    asm volatile("s_waitcnt vmcnt(16)" : "+v"(sl0), "+v"(sl1) :: "memory");
    STAMPB(28);
    if (__builtin_expect(__any(max4(max4(0u, sl0), sl1) == POISON), 0)) {
      _Pragma("nounroll") for (unsigned spin = 0; spin < SPIN_CAP; ++spin) {
        FRESH();
        if (spin) PAUSE();
        COUNT(22);
        sl0 = ldg(rs, l16, slo);
        sl1 = ldg(rs, l16 + 1024, slo);
        if (!__any(max4(max4(0u, sl0), sl1) == POISON)) break;
      }
    }
    float sm[8], sq[8];
#pragma unroll
    for (int i = 0; i < 4; ++i) {
      const f32x2 a = f32x2{bf_lo(sl0[i]), bf_hi(sl0[i])} * f32x2{i0, i0}, c = f32x2{bf_lo(sl1[i]), bf_hi(sl1[i])} * f32x2{i1, i1};
      const f32x2 s2 = a + c, q2 = __builtin_elementwise_fma(a, a, c * c);
      sm[2 * i] = s2.x; sm[2 * i + 1] = s2.y; sq[2 * i] = q2.x; sq[2 * i + 1] = q2.y;
    }
#pragma unroll
    for (int i = 0; i < 4; ++i) { swap_add32(sm[i], sm[i + 4]); swap_add32(sq[i], sq[i + 4]); }
#pragma unroll
    for (int i = 0; i < 2; ++i) { swap_add16(sm[i], sm[i + 2]); swap_add16(sq[i], sq[i + 2]); }
#pragma unroll
    for (int i = 0; i < 2; ++i) { sm[i] = dpp_add<0x124>(dpp_add<0x128>(sm[i])); sq[i] = dpp_add<0x124>(dpp_add<0x128>(sq[i])); }
    const bool od = lane & 4;
    stg(rs, sxw, OFF_SX + ((rp * 4 + w) * 16 + si) * 512,
        v4u{f2u(od ? sm[1] : sm[0]), tagA, f2u(od ? sq[1] : sq[0]), tagB});
    SB();
  }
  STAMPB(29);
  WAITV(9);
  STAMP(10);
  {
    v4u t[8];
    unsigned m = 0, m2 = 0;
#pragma unroll
    for (int i = 0; i < 8; ++i) t[i] = lds128(L + xl + l16 + i * 1024);
#pragma unroll
    for (int i = 0; i < 8; i += 2) { m = max4(m, t[i]); m2 = max4(m2, t[i + 1]); }
    if (__builtin_expect(__any(max(m, m2) == POISON), 0)) {
      _Pragma("nounroll") for (unsigned spin = 0; spin < SPIN_CAP; ++spin) {
        COUNT(22);
        bool again = false;
#pragma nounroll
        for (int i = 0; i < 8; ++i)
          if (__any(max4(0u, lds128(L + xl + l16 + i * 1024)) == POISON)) {
            again = true;
            dma_x1(XSRC(0) + i * 1024, l16, xl + i * 1024);
            dma_x1(XSRC(1) + i * 1024, l16, xl + 32768 + i * 1024);
          }
        if (!again) break;
        WAITV(0);
        if (spin) PAUSE();
      }
    }
  }
  BAR();
  STAMP(11);

#define XA(t) (xr[(t) >> 6] + ((t) & 63) * 1024)
#define XF(t) trd2(XA(t), XA(t) + 256)
#define XEN
#define REDO(p) \
  if (XEN __builtin_expect(__any(!(acc[(p) % 3][0] == acc[(p) % 3][0])), 0)) { \
    unsigned m0s; \
    asm volatile("s_mov_b32 %0, m0" : "=s"(m0s)); \
    for (unsigned spin = 0; spin < SPIN_CAP / 128; ++spin) { \
      COUNT(23); \
      _Pragma("nounroll") for (int i = 0; i < 8; ++i) dma_x4(xg + ((p) ^ par) * 32768 + i * 4096, l16, L_XR + (p) * 32768 + i * 4096); \
      WAITV(0); \
      _Pragma("unroll") for (int s = 0; s < 32; ++s) { \
        const v4u t = XF(32 * (p) + s); \
        if (s == 0) mfma_av<true>(acc[(p) % 3], w1f[s], t); else mfma_av<false>(acc[(p) % 3], w1f[s], t); \
        SB(); \
      } \
      mfma_done(acc[(p) % 3]); \
      if (!__any(!(acc[(p) % 3][0] == acc[(p) % 3][0]))) break; \
    } \
    set_m0(m0s); \
  }
  f32x16 acc[3];
  f32x2 ec[4];
  float ep[48], ee[48], ey[48];
  unsigned cs = 0, sbad = 0;
  float ssm = 0.f, ssq = 0.f;
  v4u x[128], eo;
#pragma unroll
  for (int t = 0; t < 4; ++t) x[t] = XF(t);
  SB();
  auto eslot = [&](auto Sc) __attribute__((always_inline)) {
    static_for<0, 48>([&](auto Vc) __attribute__((always_inline)) {
      constexpr int V = Vc.v, k = V & 15, st = Sc.v - epi_slot(V);
      if constexpr (st == 6 && (k & 1)) {
        unsigned o = pack2(ey[V - 1], ey[V]);
        PIN(o);
        eo[(k >> 1) & 3] = o;
        if constexpr (k == 7) { stg(rs, hv0, hoff + ((V >> 4) ^ par) * 114688, eo); cs = sum4(0u, eo); PIN(cs); }
        if constexpr (k == 15) { stg(rs, hv1, hoff + ((V >> 4) ^ par) * 114688, eo); CKPUT(sum4(cs, eo), V >> 4); }
      }
      if constexpr (st == 5) { ey[V] = ep[V] * ee[V]; PIN(ey[V]); }
      if constexpr (st == 4) { ee[V] = __builtin_amdgcn_rcpf(ee[V]); PIN(ee[V]); }
      if constexpr (st == 3) { ee[V] = 1.0f + ee[V]; PIN(ee[V]); }
      if constexpr (st == 2) { ee[V] = __builtin_amdgcn_exp2f(ep[V]); PIN(ee[V]); }
      if constexpr (st == 1) { ep[V] = __builtin_fmaf(ec[V >> 4].x, acc[(V >> 4) % 3][k], ep[V]); PIN(ep[V]); }
      if constexpr (st == 0) { ep[V] = __builtin_fmaf(ec[V >> 4].y, uu[k >> 2][k & 3], vv[k >> 2][k & 3]); PIN(ep[V]); }
    });
    SB();
  };
  static_for<0, 64>([&](auto Jc) __attribute__((always_inline)) {
    constexpr int J = Jc.v, p = J >> 4, j = J & 15;
    asm volatile("" :: "v"(x[2 * J + 1]));
    mfma_av<j == 0>(acc[p % 3], w1f[2 * j], x[2 * J]);
    if constexpr (J < 62) { x[2 * J + 4] = XF(2 * J + 4); if constexpr (!epi_pair(J)) x[2 * J + 5] = XF(2 * J + 5); }
    SB();
    if constexpr (epi_pair(J)) eslot(IC<2 * J>{});
    mfma_av<false>(acc[p % 3], w1f[2 * j + 1], x[2 * J + 1]);
    if constexpr (epi_pair(J)) x[2 * J + 5] = XF(2 * J + 5);
    SB();
    if constexpr (J == 10) { waitv<c_between(-1, 10)>(); __builtin_amdgcn_s_barrier(); FRESH(); }
    if constexpr (J == 34) waitv<c_between(19, 34)>();
    if constexpr (J == 38) waitv<c_between(27, 38)>();
    if constexpr (J >= EO - 6 && J < EO - 2) {
      asm volatile("s_waitcnt vmcnt(%2)" : "+v"(sx[2 * (J - EO + 6)]), "+v"(sx[2 * (J - EO + 6) + 1]) : "n"(c_between(8 + J - EO + 6, J)) : "memory");
#pragma unroll
      for (int i = 2 * (J - EO + 6); i < 2 * (J - EO + 6) + 2; ++i) {
        sbad = tagbad(sbad, sx[i], tagA, tagB);
        ssm += u2f(sx[i].x); PIN(ssm);
        ssq += u2f(sx[i].z); PIN(ssq);
      }
      PIN(sbad);
      SB();
    }
    if constexpr (J == EO - 2) {
      if (__builtin_expect(__any(sbad != 0u), 0)) {
        _Pragma("nounroll") for (unsigned spin = 0; spin < SPIN_CAP; ++spin) {
          FRESH();
          if (spin) PAUSE();
          COUNT(23);
          sbad = 0; ssm = 0.f; ssq = 0.f;
#pragma unroll
          for (int i = 0; i < 8; ++i) sx[i] = ldg(rs, l16 + (i & 3) * 1024, sxo + (i >> 2) * 4096);
#pragma unroll
          for (int i = 0; i < 8; ++i) { sbad = tagbad(sbad, sx[i], tagA, tagB); ssm += u2f(sx[i].x); ssq += u2f(sx[i].z); }
          if (!__any(sbad != 0u)) break;
        }
      }
      const float mean = swap32_add(ssm) * (1.0f / 512.0f);
      const float ms = __builtin_fmaf(swap32_add(ssq), 1.0f / 512.0f, 1e-5f);
      const float a = -LOG2E * __builtin_amdgcn_rsqf(fmaxf(__builtin_fmaf(-mean, mean, ms), 1e-5f));
      *(LAS f32x2*)(stw) = f32x2{a, -a * mean};
      SB();
    }
    if constexpr (J == EO - 1) {
      asm volatile("s_waitcnt lgkmcnt(4)" ::: "memory");
      __builtin_amdgcn_s_barrier();
      FRESH();
#pragma unroll
      for (int i = 0; i < 4; ++i) ec[i] = *(LAS f32x2*)(str + i * 256);
      SB();
    }
    if constexpr (!epi_pair(J)) eslot(IC<2 * J>{});
    eslot(IC<2 * J + 1>{});
    if constexpr (j == 1 && p >= 1) {
      if constexpr (p == 2) REDO(1)
      if constexpr (p == 3) REDO(2)
      if (((p - 1) & 1) == (rg ^ par)) res[(p - 1) >> 1] = trd2(rr + ((p - 1) >> 1) * 65536, rr + ((p - 1) >> 1) * 65536 + 256);
    }
    if constexpr (j == 2 && p >= 1) {
      asm volatile("s_waitcnt lgkmcnt(4)" ::: "memory");
      __builtin_amdgcn_s_barrier();
      FRESH();
    }
    if constexpr (J < 8 || (J >= 12 && J < 20)) {
      constexpr int n = J < 8 ? J : J - 4;
      if constexpr ((n & 3) == 0) set_m0(xl + 65536 + (n >> 3) * 32768 + (n & 4) * 1024);
      dma_xn<(n & 3) * 1024>(XSRC(2 + (n >> 3)) + (n & 4) * 1024, l16);
    }
    if constexpr (J >= 8 && J < 12)
      asm volatile("buffer_load_dwordx4 %0, %2, %3, %4 offen offset:%5 sc1\n\tbuffer_load_dwordx4 %1, %2, %3, %4 offen offset:%6 sc1"
                   : "=&v"(sx[2 * (J - 8)]), "=&v"(sx[2 * (J - 8) + 1])
                   : "v"(l16), "s"(rs), "s"(sxo + (J & 2) * 2048), "n"((J & 1) * 2048), "n"((J & 1) * 2048 + 1024) : "memory");
    if constexpr (J >= 19 && J < 22) PULL(7 + J - 19, w2sa, w2sb, (J - 19) * 2048);
    if constexpr (J >= 38 && J < 42) PULL(10 + J - 38, w2a, w2b, (J - 38) * 2048);
    if constexpr (J == 43) asm volatile("" :: "a"(w2f[13][1]));
    if constexpr (J >= 20 && J < 28) W2HALF(20 + J - 20, xl + (J - 20) * 1024);
    if constexpr (J >= 28 && J < 34) W2HALF(28 + J - 28, lsp + (J - 28) * 1024);
    if constexpr (J >= 35 && J < 43) W2HALF(34 + J - 35, xl + 32768 + (J - 35) * 1024);
    if constexpr (J >= 43 && J < 51) W2HALF(42 + J - 43, xl + (J - 43) * 1024);
    static_for<hc_upto(J), hc_upto(J + 1)>([&](auto Nc) __attribute__((always_inline)) { HLOAD(Nc.v); });
    SB();
    if constexpr (J == 15) STAMP(16);
    if constexpr (J == 31) STAMP(17);
    if constexpr (J == 47) STAMP(18);
  });
  STAMP(12);
  static_assert(EO <= 30 && epi_slot(31) < epi_slot(32) && epi_slot(16) + 1 >= 2 * 34 && epi_slot(32) + 1 >= 2 * 50 && epi_slot(47) + 6 < 128, "schedule");
  static_for<12, 20>([&](auto Ic) __attribute__((always_inline)) { HLOAD(Ic.v); });
  SB();
  mfma_done(acc[0]);
  REDO(3)
  if (rg ^ par) res[1] = trd2(rr + 65536, rr + 65536 + 256);
#pragma unroll
  for (int h = 0; h < 2; ++h) {
    v4u o;
#pragma unroll
    for (int i = 0; i < 4; ++i) {
      const int k = 8 * h + 2 * i;
      const f32x2 t = __builtin_elementwise_fma(f32x2{ec[3].y, ec[3].y}, f32x2{uu[k >> 2][k & 3], uu[k >> 2][(k & 3) + 1]}, f32x2{vv[k >> 2][k & 3], vv[k >> 2][(k & 3) + 1]});
      const f32x2 pp = __builtin_elementwise_fma(f32x2{ec[3].x, ec[3].x}, f32x2{acc[0][k], acc[0][k + 1]}, t);
      const f32x2 d = f32x2{__builtin_amdgcn_exp2f(pp.x), __builtin_amdgcn_exp2f(pp.y)} + f32x2{1.0f, 1.0f};
      const f32x2 y = pp * f32x2{__builtin_amdgcn_rcpf(d.x), __builtin_amdgcn_rcpf(d.y)};
      o[i] = pack2(y.x, y.y);
    }
    PIN(o);
    if (h == 0) static_for<20, 24>([&](auto Ic) __attribute__((always_inline)) { HLOAD(Ic.v); });
    if (h == 1) static_for<24, 28>([&](auto Ic) __attribute__((always_inline)) { HLOAD(Ic.v); });
    stg(rs, h ? hv1 : hv0, hoff + (3 ^ par) * 114688, o);
    if (h) CKPUT(sum4(cs, o), 3); else cs = sum4(0u, o);
    SB();
  }
  STAMP(13);

  f32x16 d[2][2];
  LAS unsigned char* redw = L + L_XR + w * 8192 + l16;
  LAS unsigned char* redr = L + L_XR + w * 2048 + l16;
#define REDW(un, q) *(LAS f32x4*)(redw + (un) * 32768 + (q) * 1024) = \
    f32x4{d[un][(q) >> 2][4 * ((q) & 3)], d[un][(q) >> 2][4 * ((q) & 3) + 1], d[un][(q) >> 2][4 * ((q) & 3) + 2], d[un][(q) >> 2][4 * ((q) & 3) + 3]}
  LAS unsigned char *w2ua = w2a + 65536, *w2ub = w2b + 65536;
  PIN(w2ua); PIN(w2ub);
#define CHECK(un) \
  if (__builtin_expect((unsigned)__builtin_amdgcn_readlane((int)m[un], 63) != 14u * tagA, 0)) { \
    _Pragma("nounroll") for (unsigned spin = 0; spin < SPIN_CAP; ++spin) { \
      FRESH(); \
      COUNT(24 + (un)); \
      unsigned t = sum4(0u, ldg(rs, ckv, kb0 + (un) * 8192)); \
      static_for<0, 7>([&](auto Cc) __attribute__((always_inline)) { \
        constexpr int c = Cc.v; \
        v4u q[4]; \
        _Pragma("unroll") for (int i = 0; i < 4; ++i) q[i] = ldg(rs, l16 + i * 1024, hb0 + CHUNK(4 * c) * 4096 + (un) * (2 * 114688)); \
        t = sum8(sum8(t, q[0], q[1]), q[2], q[3]); \
        mfma_av<c == 0>(d[un][0], w2f[4 * c][0], q[0]); mfma_av<c == 0>(d[un][1], w2f[4 * c][1], q[0]); \
        _Pragma("unroll") for (int i = 1; i < 4; ++i) { mfma_av<false>(d[un][0], w2f[4 * c + i][0], q[i]); mfma_av<false>(d[un][1], w2f[4 * c + i][1], q[i]); } \
        SB(); \
      }); \
      mfma_done(d[un][1]); \
      PIN(d[un][0]); \
      if ((unsigned)__builtin_amdgcn_readlane((int)red_all(t), 63) == 14u * tagA) break; \
      PAUSE(); \
    } \
    if (un) { _Pragma("unroll") for (int q = 0; q < 4; ++q) REDW(1, q); } \
  }
  unsigned m[2] = {0u, 0u};
  v4u gbv, scv, ck[2];
  f32x4 pr[8];
  float gb[8], rx[2][8];
  unsigned orow = (rp * 128 + ((lane & 31) >> 3) * 16 + rg * 8 + (lane & 7)) * 1024 + dt * 128 + w * 32 + hf * 16;
  static_for<0, 112>([&](auto Xc) __attribute__((always_inline)) {
    constexpr int X = Xc.v, I = X < 100 ? X >> 1 : 50 + (X - 100) % 6, e = X < 100 ? X & 1 : (X - 100) / 6, un = I / 28;
    mfma_av<I % 28 == 0>(d[un][e], w2f[I % 28][e], hq[I]);
    SB();
    if constexpr (X < 100 && e == 0) {
      if constexpr (I < 3) PULL(14 + I, w2sa, w2sb, I * 2048);
      if constexpr (I >= 3 && I < 7) PULL(14 + I, w2a, w2b, 32768 + (I - 3) * 2048);
      if constexpr (I >= 7 && I < 11) PULL(14 + I, w2a, w2b, (I - 7) * 2048);
      if constexpr (I == 21) waitv<hl_upto(21) - 28 + 1>();
      if constexpr (I >= 21 && I < 24) PULL(4 + I, w2ua, w2ub, (I - 21) * 2048);
      if constexpr (I == 28) { m[0] = sum4(m[0], ck[0]); PIN(m[0]); }
      if constexpr (I >= 29 && I < 35) { m[0] = red_step<I - 29>(m[0]); PIN(m[0]); }
      if constexpr (I == 35) CHECK(0)
      if constexpr (I >= 35 && I < 43) REDW(0, I - 35);
    }
    if constexpr (X < 100 && e == 1) {
      if constexpr (I < 6) W2HALF(50 + I, xl + 65536 + I * 1024);
      static_for<hl_upto(I), hl_upto(I + 1)>([&](auto Nc) __attribute__((always_inline)) { HLOAD(Nc.v); });
      if constexpr (I == 6) ck[0] = ldg(rs, ckv, kb0);
      if constexpr (I == 37) ck[1] = ldg(rs, ckv, kb0 + 8192);
    }
    if constexpr (X < 100 ? e == 1 : e == 0) { m[un] = sum4(m[un], hq[I]); PIN(m[un]); }
    if constexpr (X == 105) { m[1] = sum4(m[1], ck[1]); PIN(m[1]); }
    if constexpr (X >= 106) { m[1] = red_step<X - 106>(m[1]); PIN(m[1]); }
    if constexpr (X == 22) { gbv = lds128(L + L_TG + (64 * dt + 16 * w + 8 * hf) * 2); scv = lds128(L + L_TS + (64 * dt + 16 * w + 8 * hf) * 2); PIN(orow); }
    if constexpr (X >= 26 && X < 42 && (X & 1) == 0) {
      constexpr int q = (X - 26) >> 1;
      const float inv = __builtin_amdgcn_rcpf((q & 1) ? bf_hi(scv[q >> 1]) : bf_lo(scv[q >> 1]));
      gb[q] = -LN2 * ((q & 1) ? bf_hi(gbv[q >> 1]) : bf_lo(gbv[q >> 1]));
      rx[0][q] = inv * ((q & 1) ? bf_hi(res[0][q >> 1]) : bf_lo(res[0][q >> 1]));
      rx[1][q] = inv * ((q & 1) ? bf_hi(res[1][q >> 1]) : bf_lo(res[1][q >> 1]));
      PIN(gb[q]); PIN(rx[0][q]); PIN(rx[1][q]);
    }
    if constexpr (X == 86) BAR();
    if constexpr (X == 86 || X == 88) {
#pragma unroll
      for (int k = X - 86; k < X - 84; ++k) { pr[2 * k] = *(LAS f32x4*)(redr + k * 8192); pr[2 * k + 1] = *(LAS f32x4*)(redr + k * 8192 + 1024); }
    }
    if constexpr (X >= 91 && X < 99) {
      constexpr int q = X - 91;
      float t = (pr[q >> 2][q & 3] + pr[2 + (q >> 2)][q & 3]);
      PIN(t);
      float t2_ = (pr[4 + (q >> 2)][q & 3] + pr[6 + (q >> 2)][q & 3]);
      PIN(t2_);
      rx[0][q] = __builtin_fmaf(gb[q], t + t2_, rx[0][q]);
      PIN(rx[0][q]);
    }
    if constexpr (X == 99)
      *(GAS v4u*)((GAS unsigned char*)out + orow) = v4u{pack2(rx[0][0], rx[0][1]), pack2(rx[0][2], rx[0][3]), pack2(rx[0][4], rx[0][5]), pack2(rx[0][6], rx[0][7])};
    if constexpr (X == 108) asm volatile("s_nop 3" : "+v"(d[1][0]));
    if constexpr (X >= 108) REDW(1, X - 108);
    SB();
    if constexpr (X == 55) STAMP(14);
  });
  STAMP(19);
  asm volatile("s_nop 7" : "+v"(d[1][1]));
  CHECK(1)
#pragma unroll
  for (int q = 4; q < 8; ++q) REDW(1, q);
  BAR();
  {
    f32x4 s0 = {}, s1 = {};
#pragma unroll
    for (int k = 0; k < 4; ++k) { s0 += *(LAS f32x4*)(redr + k * 8192 + 32768); s1 += *(LAS f32x4*)(redr + k * 8192 + 32768 + 1024); }
    float y[8];
#pragma unroll
    for (int q = 0; q < 8; ++q) y[q] = __builtin_fmaf(gb[q], q < 4 ? s0[q & 3] : s1[q & 3], rx[1][q]);
    *(GAS v4u*)((GAS unsigned char*)out + orow + 65536) = v4u{pack2(y[0], y[1]), pack2(y[2], y[3]), pack2(y[4], y[5]), pack2(y[6], y[7])};
  }
  if (b == 0 && tid == 0) __hip_atomic_store((GAS unsigned*)ws, epoch + 1u, __ATOMIC_RELAXED, __HIP_MEMORY_SCOPE_AGENT);
  STAMP(15);
}

extern "C" void kernel_launch(void* const* d_in, const int* in_sizes, int n_in, void* d_out, int out_size, void* d_ws, size_t ws_size,
                              hipStream_t stream) {
  (void)in_sizes; (void)n_in; (void)out_size; (void)ws_size;
  (void)hipFuncSetAttribute((const void*)s4_block_kernel, hipFuncAttributeMaxDynamicSharedMemorySize, LDS_BYTES);
  s4_block_kernel<<<256, 256, LDS_BYTES, stream>>>((cptr)d_in[0], (cptr)d_in[1], (cptr)d_in[2], (cptr)d_in[3], (cptr)d_in[4], (cptr)d_in[5], (cptr)d_in[6],
                                                   (cptr)d_in[7], (cptr)d_in[8], (cptr)d_in[10], (cptr)d_in[11], (cptr)d_in[12], (cptr)d_in[13],
                                                   (unsigned char*)d_out, (unsigned char*)d_ws, (cptr)d_in[9]);
}
```

```cpp
#include <hip/hip_runtime.h>

#define GAS __attribute__((address_space(1)))
#define LAS __attribute__((address_space(3)))
#define DI __device__ __forceinline__
typedef unsigned v4u __attribute__((ext_vector_type(4)));
typedef unsigned v2u __attribute__((ext_vector_type(2)));
typedef float f32x2 __attribute__((ext_vector_type(2)));
typedef float f32x4 __attribute__((ext_vector_type(4)));
typedef float f32x16 __attribute__((ext_vector_type(16)));
typedef __bf16 bf16x2 __attribute__((ext_vector_type(2)));
typedef __bf16 bf16x8 __attribute__((ext_vector_type(8)));
typedef short s16x4 __attribute__((ext_vector_type(4)));
typedef unsigned short u16;
typedef unsigned long long u64;

constexpr int SET_BASE = 4096;
constexpr int OFF_MT = 0;
constexpr int OFF_SG = 32768;
constexpr int OFF_MG = 65536;
constexpr int OFF_SX = 524288;
constexpr int OFF_XG = 1 << 20;
constexpr int OFF_Z = OFF_XG + (4 << 20);
constexpr int OFF_HID = OFF_Z + 2097152;
constexpr int OFF_CK = OFF_HID + 7340032;
constexpr int SET_BYTES = OFF_CK + 262144;
static_assert(OFF_MG + 393216 <= OFF_SX, "set size");

constexpr int L_TS = 0, L_TB = 1024, L_TG = 2048;
constexpr int L_UVT = 3072;
constexpr int L_M = 4096;
constexpr int L_UVS = L_M;
constexpr int UVS_ROW = 544, UVS_CH = 57 * UVS_ROW;
constexpr int L_KC = L_M;
constexpr int KC_STRIDE = 4160;
constexpr int L_H = L_KC + 8 * KC_STRIDE;
constexpr int H_STRIDE = 4576;
constexpr int L_XB = L_H + 2 * H_STRIDE;
constexpr int L_RX = L_XB + 8192;
constexpr int L_R = L_M + 65536;
constexpr int L_TOP = L_M + 131072 + 24576;
constexpr int L_KT = L_TOP + 16;
constexpr int L_STAT = L_TOP;
constexpr int L_ETA = L_RX + 12288;
constexpr int L_XR = L_M;
constexpr int L_SP = L_M + 131072;
constexpr int LDS_BYTES = 163840;
static_assert(L_TOP + 4096 <= LDS_BYTES && L_ETA + 16 <= L_R && L_UVS + 2 * UVS_CH <= L_R && L_SP + 24576 <= L_TOP, "LDS map");
static_assert(L_H % 16 == 0 && L_XB % 16 == 0 && L_RX % 16 == 0 && L_KT % 16 == 0, "alignment");

#define ZLAG 30
#define MTK 124
constexpr int EO = 30;
constexpr int CODE_BYTES = 36096;
constexpr int SC1 = 16;
constexpr int OOB = 0x7FFFFF00;
constexpr unsigned SPIN_CAP = 1u << 15;
constexpr unsigned POISON = 0xFFFFFFFFu;
constexpr float LOG2E = 1.4426950408889634f, LN2 = 0.6931471805599453f, INV2PI = 0.15915494309189535f;

#define SB() __builtin_amdgcn_sched_barrier(0)
#define WAITV(n) asm volatile("s_waitcnt vmcnt(" #n ")" ::: "memory")
#define PIN(x) asm volatile("" : "+v"(x))
#define FRESH() asm volatile("" ::: "memory")
#define PAUSE() asm volatile("s_sleep 4" ::: "memory")
#define BAR() do { asm volatile("s_waitcnt lgkmcnt(0)" ::: "memory"); __builtin_amdgcn_s_barrier(); asm volatile("" ::: "memory"); } while (0)
#define STAMP(i)
#define SARG
#define SPASS
#define COUNT(i)

#define STAMPA(i)
#define STAMPB(i) STAMP(i)
DI float u2f(unsigned u) { return __builtin_bit_cast(float, u); }
DI unsigned f2u(float f) { return __builtin_bit_cast(unsigned, f); }
DI float dot2(unsigned u, float c) { const bf16x2 v = __builtin_bit_cast(bf16x2, u); return __builtin_amdgcn_fdot2_f32_bf16(v, v, c, false); }
DI float bf_lo(unsigned u) { return __builtin_bit_cast(float, u << 16); }
DI float bf_hi(unsigned u) { return __builtin_bit_cast(float, u & 0xFFFF0000u); }
DI unsigned pack2(float a, float b) { f32x2 v = {a, b}; return __builtin_bit_cast(unsigned, __builtin_convertvector(v, bf16x2)); }
DI float rbf(float a) { return bf_lo(pack2(a, a)); }
DI unsigned sel(bool c, unsigned a, unsigned b) { asm("" : "+v"(a), "+v"(b)); return c ? a : b; }
DI unsigned guard(unsigned u) { return u < 0xFFFFFFFEu ? u : 0xFFFFFFFEu; }
DI unsigned max4(unsigned m, v4u v) {
  asm("v_max3_u32 %0, %0, %1, %2\n\tv_max3_u32 %0, %0, %3, %4" : "+v"(m) : "v"(v.x), "v"(v.y), "v"(v.z), "v"(v.w));
  return m;
}
DI f32x4 mfma16(v4u a, v4u b, f32x4 c) { return __builtin_amdgcn_mfma_f32_16x16x32_bf16(__builtin_bit_cast(bf16x8, a), __builtin_bit_cast(bf16x8, b), c, 0, 0, 0); }
DI f32x16 mfma32(v4u a, v4u b, f32x16 c) { return __builtin_amdgcn_mfma_f32_32x32x16_bf16(__builtin_bit_cast(bf16x8, a), __builtin_bit_cast(bf16x8, b), c, 0, 0, 0); }
template <bool FIRST> DI void mfma_av(f32x16& acc, const v4u& a, const v4u& b) {
  if (FIRST) asm volatile("v_mfma_f32_32x32x16_bf16 %0, %1, %2, 0" : "=&v"(acc) : "a"(a), "v"(b));
  else asm volatile("v_mfma_f32_32x32x16_bf16 %0, %1, %2, %0" : "+v"(acc) : "a"(a), "v"(b));
}
template <bool FIRST> DI void mfma_va(f32x16& acc, const v4u& a, const v4u& b) {
  if (FIRST) asm volatile("v_mfma_f32_32x32x16_bf16 %0, %1, %2, 0" : "=&v"(acc) : "v"(a), "a"(b));
  else asm volatile("v_mfma_f32_32x32x16_bf16 %0, %1, %2, %0" : "+v"(acc) : "v"(a), "a"(b));
}
template <bool FIRST> DI void mfma16_v(f32x4& acc, const v4u& a, const v4u& b) {
  if (FIRST) asm volatile("v_mfma_f32_16x16x32_bf16 %0, %1, %2, 0" : "=&v"(acc) : "v"(a), "v"(b));
  else asm volatile("v_mfma_f32_16x16x32_bf16 %0, %1, %2, %0" : "+v"(acc) : "v"(a), "v"(b));
}
DI void mfma_done(f32x16& acc) { asm volatile("s_nop 15" : "+v"(acc)); }
DI v2u trd(LAS unsigned char* p) { return __builtin_bit_cast(v2u, __builtin_amdgcn_ds_read_tr16_b64_v4i16((LAS s16x4*)p)); }
DI v4u trd2(LAS unsigned char* p0, LAS unsigned char* p1) { const v2u a = trd(p0), b = trd(p1); return v4u{a.x, a.y, b.x, b.y}; }
DI v4u lds128(LAS unsigned char* p) { return *(LAS v4u*)p; }
DI v4u ldg(__amdgpu_buffer_rsrc_t r, int voff, int soff) { return __builtin_amdgcn_raw_buffer_load_b128(r, voff, soff, SC1); }
DI void stg(__amdgpu_buffer_rsrc_t r, int voff, int soff, v4u v) {
  __builtin_amdgcn_raw_buffer_store_b128(v, r, voff, soff, SC1);
  asm volatile("s_nop 1" :: "v"(v) : "memory");
}
template <int CTRL> DI float dpp_add(float v) {
  return v + __builtin_bit_cast(float, __builtin_amdgcn_update_dpp(0, __builtin_bit_cast(int, v), CTRL, 0xF, 0xF, true));
}
DI float swap16_add(float v) { const unsigned u = __builtin_bit_cast(unsigned, v); const auto r = __builtin_amdgcn_permlane16_swap(u, u, false, false);
  return u2f(r[0]) + u2f(r[1]); }
DI float swap32_add(float v) { const unsigned u = __builtin_bit_cast(unsigned, v); const auto r = __builtin_amdgcn_permlane32_swap(u, u, false, false);
  return u2f(r[0]) + u2f(r[1]); }
DI float quad_sum(float v) { v = dpp_add<0xB1>(v); return dpp_add<0x4E>(v); }
DI float row_sum(float v) { return dpp_add<0x140>(dpp_add<0x141>(quad_sum(v))); }
DI float wave_sum(float v) { return swap32_add(swap16_add(row_sum(v))); }
DI f32x2 cmul(f32x2 a, f32x2 e, f32x2 ep) { const f32x2 t = f32x2{a.x, a.x} * e; return __builtin_elementwise_fma(f32x2{a.y, a.y}, ep, t); }
DI f32x2 cexp(float re2, float rev) {
  const float m = __builtin_amdgcn_exp2f(re2), f = __builtin_amdgcn_fractf(rev);
  return f32x2{m * __builtin_amdgcn_cosf(f), m * __builtin_amdgcn_sinf(f)};
}
DI void dma_w2(const unsigned char* sbase, unsigned v0, unsigned v1, unsigned lds_dst) {
  asm volatile("s_mov_b32 m0, %3\n\ts_nop 0\n\tglobal_load_lds_dwordx4 %0, %2\n\tglobal_load_lds_dwordx4 %1, %2 offset:1024"
               :: "v"(v0), "v"(v1), "s"(sbase), "s"(lds_dst) : "memory");
}
DI void dma_w1(const unsigned char* sbase, unsigned v, unsigned lds_dst) {
  asm volatile("s_mov_b32 m0, %2\n\ts_nop 0\n\tglobal_load_lds_dwordx4 %0, %1" :: "v"(v), "s"(sbase), "s"(lds_dst) : "memory");
}
DI void dma_x1(const unsigned char* sbase, unsigned v, unsigned lds_dst) {
  asm volatile("s_mov_b32 m0, %2\n\ts_nop 0\n\tglobal_load_lds_dwordx4 %0, %1 sc1" :: "v"(v), "s"(sbase), "s"(lds_dst) : "memory");
}
DI void set_m0(unsigned lds_dst) { asm volatile("s_mov_b32 m0, %0\n\ts_nop 0" :: "s"(lds_dst) : "memory"); }
template <int OFF> DI void dma_xn(const unsigned char* sbase, unsigned v) {
  asm volatile("global_load_lds_dwordx4 %0, %1 offset:%2 sc1" :: "v"(v), "s"(sbase), "n"(OFF) : "memory");
}
DI void dma_x2(const unsigned char* sbase, unsigned v, unsigned lds_dst) {
  asm volatile("s_mov_b32 m0, %2\n\ts_nop 0\n\tglobal_load_lds_dwordx4 %0, %1 sc1\n\tglobal_load_lds_dwordx4 %0, %1 offset:1024 sc1"
               :: "v"(v), "s"(sbase), "s"(lds_dst) : "memory");
}
DI void dma_x4(const unsigned char* sbase, unsigned v, unsigned lds_dst) {
  asm volatile("s_mov_b32 m0, %2\n\ts_nop 0\n\tglobal_load_lds_dwordx4 %0, %1 sc1\n\tglobal_load_lds_dwordx4 %0, %1 offset:1024 sc1\n\t"
               "global_load_lds_dwordx4 %0, %1 offset:2048 sc1\n\tglobal_load_lds_dwordx4 %0, %1 offset:3072 sc1"
               :: "v"(v), "s"(sbase), "s"(lds_dst) : "memory");
}
template <int I> struct IC { static constexpr int v = I; };
template <int I, int N, class F> DI void static_for(F&& f) { if constexpr (I < N) { f(IC<I>{}); static_for<I + 1, N>(f); } }
DI unsigned now() { u64 t; asm volatile("s_memrealtime %0\n\ts_waitcnt lgkmcnt(0)" : "=s"(t) :: "memory"); return (unsigned)t; }
template <int N> DI void waitv() { asm volatile("s_waitcnt vmcnt(%0)" :: "n"(N) : "memory"); }
struct ConvOrder { int t[36], u[36]; };
constexpr ConvOrder conv_order() {
  ConvOrder o = {};
  int done[8] = {}, last = -1;
  for (int k = 0; k < 36; ++k) {
    int best = -1;
    for (int t = 0; t < 8; ++t)
      if (t != last && done[t] <= t && (best < 0 || t - done[t] > best - done[best])) best = t;
    o.t[k] = best; o.u[k] = done[best]++; last = best;
  }
  return o;
}
constexpr ConvOrder conv_order_last() {
  ConvOrder o = {};
  int k = 0;
  for (int p = 0; p < 4; ++p)
    for (int u = 0; u <= 2 * p + 1; ++u) {
      o.t[k] = 2 * p + 1; o.u[k] = u; ++k;
      if (u <= 2 * p) { o.t[k] = 2 * p; o.u[k] = u; ++k; }
    }
  return o;
}
constexpr int epi_slot(int v) { return v < 32 ? 2 * EO + v : 2 * 50 - 1 + (v - 32); }
constexpr bool epi_pair(int j) { return (j >= EO && j < EO + 19) || (j >= 50 && j < 61); }
constexpr int hc_upto(int j) { return j < 52 ? 0 : j - 52; }
constexpr int hl_upto(int i) { return i < 9 ? 28 : i < 37 ? 28 + (i - 9) : 56; }
constexpr int c_ops(int j) {
  return (j >= 8 && j < 12 ? 2 : j < 34 || (j >= 35 && j < 51) ? 1 : 0) + hc_upto(j + 1) - hc_upto(j) +
         (j == EO + 6 || j == EO + 14 || j == 56 ? 1 : j == EO + 10 || j == EO + 18 || j == 60 ? 2 : 0);
}
constexpr int c_between(int a, int b) { int n = 0; for (int j = a + 1; j < b; ++j) n += c_ops(j); return n; }
DI unsigned tagbad(unsigned bad, v4u g, unsigned tagA, unsigned tagB) {
  return __builtin_amdgcn_bitop3_b32(__builtin_amdgcn_bitop3_b32(bad, g.y, tagA, 0xF6), g.w, tagB, 0xF6);
}
template <int R0, int R1, int R2, int R3> DI void put4(unsigned base, f32x2 a, f32x2 b, f32x2 c, f32x2 d) {
  asm volatile("s_mov_b32 m0, %4\n\ts_nop 0\n\tds_write_addtid_b32 %0 offset:%5\n\tds_write_addtid_b32 %1 offset:%6\n\t"
               "ds_write_addtid_b32 %2 offset:%7\n\tds_write_addtid_b32 %3 offset:%8"
               :: "v"(pack2(a.x, a.y)), "v"(pack2(b.x, b.y)), "v"(pack2(c.x, c.y)), "v"(pack2(d.x, d.y)), "s"(base),
                  "n"(R0 * UVS_ROW), "n"(R1 * UVS_ROW), "n"(R2 * UVS_ROW), "n"(R3 * UVS_ROW) : "memory");
}
DI unsigned sum4(unsigned m, v4u v) {
  asm("v_add3_u32 %0, %0, %1, %2\n\tv_add3_u32 %0, %0, %3, %4" : "+v"(m) : "v"(v.x), "v"(v.y), "v"(v.z), "v"(v.w));
  return m;
}
DI unsigned sum8(unsigned m, v4u a, v4u b) {
  asm("v_add3_u32 %0, %0, %1, %2\n\tv_add3_u32 %0, %0, %3, %4\n\tv_add3_u32 %0, %0, %5, %6\n\tv_add3_u32 %0, %0, %7, %8"
      : "+v"(m) : "v"(a.x), "v"(a.y), "v"(a.z), "v"(a.w), "v"(b.x), "v"(b.y), "v"(b.z), "v"(b.w));
  return m;
}
template <int I> DI unsigned red_step(unsigned v) {
  constexpr int ctrl[6] = {0xB1, 0x4E, 0x141, 0x140, 0x142, 0x143}, rows[6] = {0xF, 0xF, 0xF, 0xF, 0xA, 0xC};
  return v + (unsigned)__builtin_amdgcn_update_dpp(0, (int)v, ctrl[I], rows[I], 0xF, true);
}
DI unsigned red_all(unsigned v) { return red_step<5>(red_step<4>(red_step<3>(red_step<2>(red_step<1>(red_step<0>(v)))))); }
DI void swap_add32(float& a, float b) { const auto r = __builtin_amdgcn_permlane32_swap(f2u(a), f2u(b), false, false); a = u2f(r[0]) + u2f(r[1]); }
DI void swap_add16(float& a, float b) { const auto r = __builtin_amdgcn_permlane16_swap(f2u(a), f2u(b), false, false); a = u2f(r[0]) + u2f(r[1]); }

typedef const unsigned char* cptr;
typedef unsigned v16u __attribute__((ext_vector_type(16)));
DI cptr mkptr(unsigned lo, unsigned hi) { return (cptr)(((u64)hi << 32) | lo); }

__global__ void __launch_bounds__(256, 1)
s4_block_kernel(cptr, cptr, cptr, cptr, cptr, cptr, cptr, cptr, cptr, cptr, cptr, cptr, cptr, unsigned char*, unsigned char*, cptr) {
  STAMP(0);
  v16u ka0, ka1;
  asm volatile("s_load_dwordx16 %0, %2, 0x0\n\ts_load_dwordx16 %1, %2, 0x40" : "=&s"(ka0), "=&s"(ka1) : "s"(__builtin_amdgcn_kernarg_segment_ptr()));
  SB();
  LAS unsigned char* const L = (LAS unsigned char*)16u - 16;
  const int tid = threadIdx.x, lane = tid & 63, w = __builtin_amdgcn_readfirstlane(tid >> 6);
  const int b = blockIdx.x, xcd = b & 7, slot = b >> 3, id = xcd * 32 + slot;
  const int ks = (id >> 6) * 4 + w, cc = id & 63;
  const int rp = 4 * (xcd >> 1) + (slot >> 3), rg = xcd & 1, dt = slot & 7, si = 8 * rg + dt;
  const int ct = 7 * rg + min(dt, 6);
  const int rot = (0x0340 >> (4 * (2 * (w & 1) + rg))) & 7;

  const unsigned char* pcode;
  asm volatile("s_getpc_b64 %0" : "=s"(pcode));
  const unsigned warm = *(const GAS unsigned*)((const GAS unsigned char*)pcode + (unsigned)min((((slot & 7) * 4 + w) * 9 + min(lane, 8)) * 128, CODE_BYTES));
  const int g16 = lane >> 4, q4 = (lane & 15) >> 2, p4 = lane & 3, hf = lane >> 5, blk = (lane >> 4) & 1, l16 = lane * 16;
  const unsigned lrw = L_R + w * 16384, lsp = L_SP + w * 6144;
#define SLOT(n) ((n) < 16 ? lrw + (n) * 1024 : lsp + ((n) - 16) * 1024)
  unsigned wmo[3], co = (ks * 32 + (lane & 31)) * 2, xo = (id >> 1) * 16384 + (id & 1) * 512 + (tid & 7) * 2048 + (tid >> 3) * 16, ro = id * 8192 + w * 2048 + hf * 1024 + (lane & 31) * 16, po = id * 512 + tid * 2;
  unsigned wto = id * 7168 + (w >> 1) * 3584 + min(lane, 27) * 128;
#pragma unroll
  for (int i = 0; i < 3; ++i) { const unsigned idx = lane + 64 * i; wmo[i] = (ks * 32 * 6144 + cc * 96 - i * 1024) + idx / 6 * (6144 - 96) + idx * 16; PIN(wmo[i]); }
  PIN(co); PIN(xo); PIN(ro); PIN(po); PIN(wto);
  set_m0(lrw);
  SB();
  unsigned w2v0 = (lane >> 3) * 1024 + (((lane & 7) ^ (blk << 2)) * 16), w2v1 = w2v0 + 7168;
  unsigned w1v0 = (lane >> 2) * 3584 + p4 * 16, w1v1 = w1v0 + 57344 - 1024;
  const int zs = OFF_Z + (2 * id + (w >> 1)) * 64;
  int za = (((w & 1) + 2 * (g16 & 1)) * 8 + q4 * 2 + (g16 >> 1)) * 32768 + p4 * 16, zb = za + 32 * 32768;
  int tra = (hf * 8 + q4) * 64 + blk * 32 + p4 * 8;
  LAS unsigned char *w1a = L + lrw + tra, *w1s = L + lsp + tra;
#define W1PULL(step, n) w1f[step] = trd2(((n) < 16 ? w1a : w1s) + ((n) & 15) * 1024, ((n) < 16 ? w1a : w1s) + ((n) & 15) * 1024 + 256)
  LAS unsigned char* ka = L + L_KC + ((w >> 1) * 4 + q4) * KC_STRIDE + 2 * (32 - 8 * g16 + 4 * p4) - 8 + 256 * (w & 1);
  LAS unsigned char* hb = L + L_H + (w >> 1) * H_STRIDE + (lane & 15) * 32 + g16 * 16 + 256 - 256 * (w & 1);
  LAS unsigned char* zp = L + sel(tid < 64, L_KC + ((lane >> 1) & 7) * KC_STRIDE + (lane & 1) * 16, L_H + hf * H_STRIDE + min(lane & 31, 29) * 16);
  LAS unsigned char* ub = L + L_UVS + (lane & 15) * UVS_ROW + g16 * 16;
  int gmo = OFF_MG + (g16 * 256 + id) * 256 + (lane & 15) * 16, gmo2 = OFF_MG + ((4 + blk) * 256 + id) * 256 + (lane & 15) * 16;
  int mgo = sel(lane < 48, (lane >> 1) * 256 + (lane & 1) * 8, OOB), sgo = sel(lane & 31, OOB, hf * 8);
  LAS unsigned char* xbw = L + L_XB + (tid >> 7) * 2048 + (tid & 127) * 4;
  LAS unsigned char* xbr = L + L_XB + w * 2048 + (lane & 15) * 32 + g16 * 8;
  LAS unsigned char *exa = L + L_XB + w * 2048 + l16, *exb = L + L_RX + w * 3072 + l16, *eya = L + L_XB + (w ^ 1) * 2048 + l16, *eyb = L + L_RX + (w ^ 1) * 3072 + l16;
  PIN(xbw); PIN(xbr); PIN(exa); PIN(exb); PIN(eya); PIN(eyb);
  int mto = (tid >> 1) * 128 + (tid & 1) * 16;
  LAS unsigned char* mtw = L + (tid & 1) * L_TG + (tid >> 1) * 4;
  PIN(mto); PIN(mtw);
  PIN(w2v0); PIN(w2v1); PIN(w1v0); PIN(w1v1); PIN(za); PIN(zb); PIN(tra); PIN(w1a); PIN(w1s); PIN(ka); PIN(hb); PIN(zp); PIN(ub); PIN(gmo); PIN(gmo2); PIN(mgo); PIN(sgo);
#define W1DMA(step, lds) dma_w1(w1w + (step) * 57344, w1v0, (lds))
#define W1DMA2(step, lds) dma_w2(w1w + (step) * 57344, w1v0, w1v1, (lds))
#define W2SRC(step) (((step) < 12 ? w2lo : (step) < 16 ? w2c3 : w2hi) + (step) * 16384)
#define W2DMA(step, lds) dma_w2(W2SRC(step), w2v0, w2v1, (lds))
#define W2HALF(n, lds) do { if constexpr ((n) & 1) asm volatile("global_load_lds_dwordx4 %0, %1 offset:1024" :: "v"(w2v1), "s"(W2SRC((n) >> 1)) : "memory"); \
    else dma_w1(W2SRC((n) >> 1), w2v0, (lds)); } while (0)
  SB();
  STAMPA(24);
  asm volatile("s_waitcnt lgkmcnt(0)" : "+s"(ka0), "+s"(ka1));
  STAMPA(25);
  const cptr xin = mkptr(ka0[0], ka0[1]), pAre = mkptr(ka0[2], ka0[3]), pAim = mkptr(ka0[4], ka0[5]), pBre = mkptr(ka0[6], ka0[7]);
  const cptr pBim = mkptr(ka0[8], ka0[9]), pCre = mkptr(ka0[10], ka0[11]), pCim = mkptr(ka0[12], ka0[13]), pldt = mkptr(ka0[14], ka0[15]);
  const cptr pD = mkptr(ka1[0], ka1[1]), w1 = mkptr(ka1[2], ka1[3]), w2 = mkptr(ka1[4], ka1[5]), pc = mkptr(ka1[6], ka1[7]);
  const cptr wmod = mkptr(ka1[8], ka1[9]), out = mkptr(ka1[10], ka1[11]), ws = mkptr(ka1[12], ka1[13]);
  const unsigned epoch_v = __hip_atomic_load((GAS unsigned*)ws, __ATOMIC_RELAXED, __HIP_MEMORY_SCOPE_AGENT);
  asm volatile("global_load_lds_dwordx4 %0, %3\n\tglobal_load_lds_dwordx4 %1, %3 offset:1024\n\tglobal_load_lds_dwordx4 %2, %3 offset:2048"
               :: "v"(wmo[0]), "v"(wmo[1]), "v"(wmo[2]), "s"(wmod) : "memory");
  const unsigned cval = *(const GAS u16*)((const GAS unsigned char*)pc + co);
  SB();
  const v4u xa = *(const GAS v4u*)((const GAS unsigned char*)xin + xo), xb = *(const GAS v4u*)((const GAS unsigned char*)xin + 1024 + xo);
  const v4u ra = *(const GAS v4u*)((const GAS unsigned char*)xin + ro), rb = *(const GAS v4u*)((const GAS unsigned char*)xin + 512 + ro);
  SB();
#define PARAM(p) (unsigned)*(const GAS u16*)((const GAS unsigned char*)(p) + po)
  const unsigned uAre = PARAM(pAre), uAim = PARAM(pAim), uBre = PARAM(pBre), uBim = PARAM(pBim), uCre = PARAM(pCre), uCim = PARAM(pCim);
#undef PARAM
  SB();
  const unsigned char* setp = ws + SET_BASE;
  const __amdgpu_buffer_rsrc_t rs = __builtin_amdgcn_make_buffer_rsrc((void*)setp, 0, SET_BYTES, 0x00020000);
  unsigned uldt, uD;
  asm volatile("s_load_dword %0, %2, %4\n\ts_load_dword %1, %3, %4" : "=&s"(uldt), "=&s"(uD) : "s"(pldt), "s"(pD), "s"(id * 4));
  SB();
  STAMPA(26);
  LAS unsigned char* koff[12];
  {
    float fj = (float)(8 * tid - 3);
    PIN(fj);
    const unsigned lin = (unsigned)(8 * tid - 3 + 0x4300 - 128) * 0x10001u + 0x10000u;
#pragma unroll
    for (int i = 0; i < 12; i += 2) {
      const unsigned r = sel(tid < 32, lin + i * 0x10001u, pack2(fj + (float)i, fj + (float)(i + 1)));
      koff[i] = L + ((r & 0xFFFFu) * 4 + (L_KT + (128 - 0x4300) * 4));
      koff[i + 1] = L + ((r >> 16) * 4 + (L_KT + (128 - 0x4300) * 4));
      PIN(koff[i]);
      if (i < 10) PIN(koff[i + 1]);
    }
  }
  *(LAS v4u*)(L + L_KT - 16) = v4u{0u, 0u, 0u, 0u};
  LAS unsigned char* ktw[4];
  {
    const int row = w * 16 + (lane & 15), sg = max((row >> 3) - 1, 0), rr_ = row - 8 * sg;
    const int idx0 = 128 * sg + ((rr_ >> (3 - sg)) << (7 - sg)) + (rr_ & ((8 >> sg) - 1)) + 1, st = 8 >> sg;
#pragma unroll
    for (int r = 0; r < 4; ++r) {
      const int idx = sel(row < 40, idx0 + st * (4 * g16 + r), tid == 136 && r == 0 ? 0 : 641 + lane);
      ktw[r] = L + L_KT + idx * 4;
      PIN(ktw[r]);
    }
  }
  const unsigned char* w2lo = w2 + (448 * w + 64 * rot) * 1024 + dt * 128;
  const unsigned char* w2c3 = w2lo - (rot >> 2) * (448 * 1024);
  const unsigned char* w2hi = w2lo - ((rot + 1) >> 2) * (448 * 1024);
  const unsigned char* w1w = w1 + ct * 256 + w * 64;
  SB();
  STAMPA(27);
  asm volatile("s_waitcnt lgkmcnt(0)" : "+s"(uldt), "+s"(uD));
  const unsigned epoch = __builtin_amdgcn_readfirstlane(epoch_v);
  asm volatile("" :: "s"(epoch));
  STAMPA(28);
  const unsigned tagA = epoch * 0x9E3779B1u + 0x7F4A7C15u, tagB = ~tagA;
  {
    const float c = bf_lo(cval);
    const float s = c * __builtin_amdgcn_rcpf(1.0f + __builtin_amdgcn_exp2f(-LOG2E * c));
    *(LAS u16*)(L + lrw + 3072 + (lane & 31) * 2) = (u16)pack2(s, s);
    SB();
#define XPUB(i) do { stg(rs, (tid >> 3) * 65536 + (tid & 7) * 16, OFF_XG + ((id & 1) * 128 + (i)) * 16384 + (id >> 1) * 128, v4u{xa[i], tagA, xb[i], tagB}); SB(); } while (0)
    asm volatile("" :: "v"(xb));
    STAMPA(29);
    XPUB(0);
    XPUB(1);
    STAMP(1);
    LAS unsigned char* a = L + lrw + (8 * g16 + q4) * 96 + p4 * 8;
    const v4u sc = lds128(L + lrw + 3072 + g16 * 16);
    const v4u m0 = trd2(a, a + 384), m1 = trd2(a + 32, a + 416), m2 = trd2(a + 64, a + 448);
    SB();
    f32x2 fa[8];
#pragma unroll
    for (int i = 0; i < 4; ++i) { fa[i] = f32x2{bf_lo(ra[i]), bf_hi(ra[i])}; fa[4 + i] = f32x2{bf_lo(rb[i]), bf_hi(rb[i])}; }
    const f32x2 s2 = ((fa[0] + fa[1]) + (fa[2] + fa[3])) + ((fa[4] + fa[5]) + (fa[6] + fa[7]));
    float mean = s2.x + s2.y;
    PIN(mean);
    SB();
    const f32x4 c0 = mfma16(sc, m0, f32x4{}), c1 = mfma16(sc, m1, f32x4{}), c2 = mfma16(sc, m2, f32x4{});
    SB();
    mean = swap16_add(row_sum(mean)) * (1.0f / 512.0f);
    PIN(mean);
    SB();
    XPUB(2);
    __builtin_amdgcn_raw_buffer_store_b64(v2u{sel(lane < 16, f2u(c0[0]), sel(lane < 32, f2u(c1[0]), f2u(c2[0]))), (lane & 1) ? tagB : tagA}, rs, mgo,
                                          OFF_MG + cc * 24 * 256 + ks * 16, SC1);
    SB();
    f32x2 q2 = {0.f, 0.f}, q3 = {0.f, 0.f};
#pragma unroll
    for (int i = 0; i < 8; i += 2) {
      const f32x2 d0 = fa[i] - f32x2{mean, mean}, d1 = fa[i + 1] - f32x2{mean, mean};
      q2 = __builtin_elementwise_fma(d0, d0, q2);
      q3 = __builtin_elementwise_fma(d1, d1, q3);
    }
    q2 += q3;
    const float var = rbf(rbf(swap16_add(row_sum(q2.x + q2.y)) * (1.0f / 512.0f)) + 1e-5f);
    __builtin_amdgcn_raw_buffer_store_b64(v2u{pack2(mean, __builtin_amdgcn_sqrtf(var)), hf ? tagB : tagA}, rs, sgo, OFF_SG + (id * 4 + w) * 16, SC1);
    SB();
    XPUB(3);
#undef XPUB
  }
  unsigned wt;
  SB();
  STAMP(2);
  unsigned dsk = tid == 136 ? uD : 0u;
  PIN(dsk);

  {
    const float dtv = rbf(__builtin_amdgcn_exp2f(LOG2E * (w >> 1 ? bf_hi(uldt) : bf_lo(uldt))));
    const float are = dtv * bf_lo(uAre) * LOG2E, aim = dtv * bf_lo(uAim) * INV2PI;
    const float br = dtv * bf_lo(uBre), bi = dtv * bf_lo(uBim), cr = bf_lo(uCre), ci = bf_lo(uCim);
    const f32x2 wv = {cr * br - ci * bi, cr * bi + ci * br};
    const unsigned row = L_UVS + (w >> 1) * UVS_CH + (w & 1) * 256;
    const f32x2 e1 = cexp(are, aim), e8 = cexp(8.f * are, -8.f * aim), e128 = cexp(128.f * are, 128.f * aim);
    const f32x2 e1p = {-e1.y, e1.x}, e8p = {-e8.y, e8.x}, e128p = {-e128.y, e128.x};
    const v4u pois = {POISON, POISON, POISON, POISON};
    f32x2 z[8], v, v2, y, y2;
#define M1(a) cmul(a, e1, e1p)
#define M8(a) cmul(a, e8, e8p)
#define MK(a) cmul(a, e128, e128p)
    z[0] = M1(wv);
    put4<40, 41, 42, 0>(row, wv, f32x2{1.f, 0.f}, e8, z[0]);
    stg(rs, za, zs, pois);
    v = M8(e8); z[1] = M1(z[0]); v2 = M8(v); z[2] = M1(z[1]);
    put4<43, 1, 44, 2>(row, v, z[1], v2, z[2]);
    v = M8(v2); z[3] = M1(z[2]); v2 = M8(v); z[4] = M1(z[3]);
    put4<45, 3, 46, 4>(row, v, z[3], v2, z[4]);
    stg(rs, zb, zs, pois);
    v = M8(v2); z[5] = M1(z[4]); v2 = M8(v); z[6] = M1(z[5]);
    put4<47, 5, 48, 6>(row, v, z[5], v2, z[6]);
    asm volatile("global_load_dword %0, %1, %2" : "=v"(wt) : "v"(wto), "s"(w1 + (w & 1) * (w2 - w1)), "v"(warm) : "memory");
    z[7] = M1(z[6]); z[0] = MK(z[0]); z[1] = MK(z[1]); z[2] = MK(z[2]);
    put4<7, 8, 9, 10>(row, z[7], z[0], z[1], z[2]);
    z[3] = MK(z[3]); z[4] = MK(z[4]); z[5] = MK(z[5]); z[6] = MK(z[6]);
    put4<11, 12, 13, 14>(row, z[3], z[4], z[5], z[6]);
    z[7] = MK(z[7]); z[1] = MK(z[1]); z[3] = MK(z[3]); z[5] = MK(z[5]);
    put4<15, 16, 17, 18>(row, z[7], z[1], z[3], z[5]);
    W1DMA2(0, lrw);
    z[7] = MK(z[7]); z[1] = MK(z[1]); z[3] = MK(z[3]); z[5] = MK(z[5]);
    put4<19, 20, 21, 22>(row, z[7], z[1], z[3], z[5]);
    y = MK(z[7]); z[3] = MK(z[3]); y2 = MK(y); z[4] = MK(z[3]);
    put4<23, 24, 25, 26>(row, y, z[3], y2, z[4]);
    W1DMA2(2, lrw + 2048);
    y = MK(y2); z[3] = MK(z[4]); y2 = MK(y); z[4] = MK(z[3]);
    put4<27, 28, 29, 30>(row, y, z[3], y2, z[4]);
    y = MK(y2); v = M8(v2); y2 = MK(y); v2 = M8(v);
    put4<31, 49, 32, 50>(row, y, v, y2, v2);
    y = MK(y2); v = M8(v2); y2 = MK(y); v2 = M8(v);
    put4<33, 51, 34, 52>(row, y, v, y2, v2);
    W1DMA2(4, lrw + 4096);
    y = MK(y2); v = M8(v2); y2 = MK(y); v2 = M8(v);
    put4<35, 53, 36, 54>(row, y, v, y2, v2);
    y = MK(y2); v = M8(v2); y2 = MK(y); v2 = M8(v);
    put4<37, 55, 38, 56>(row, y, v, y2, v2);
    W1DMA2(6, lrw + 6144);
    y = MK(y2);
    *(LAS unsigned*)(L + row + 39 * UVS_ROW + lane * 4) = pack2(y.x, y.y);
#undef M1
#undef M8
#undef MK
  }
  BAR();
  STAMP(3);
  v4u gx[4], gs[4], gm, gn;
  const int xgo = OFF_XG + id * 16384;
  if (w < 3) {
    LAS unsigned char* ut = ub + w * 16 * UVS_ROW;
    v4u fv[2][8], fu[2][8];
#pragma unroll
    for (int s = 0; s < 8; ++s)
#pragma unroll
      for (int c = 0; c < 2; ++c) { fv[c][s] = lds128(ub + c * UVS_CH + 41 * UVS_ROW + s * 64); fu[c][s] = lds128(ut + c * UVS_CH + s * 64); SB(); }
    f32x4 a0 = {}, a1 = {};
#pragma unroll
    for (int s = 0; s < 8; ++s) { a0 = mfma16(fv[0][s], fu[0][s], a0); SB(); a1 = mfma16(fv[1][s], fu[1][s], a1); SB(); }
    *(LAS unsigned*)ktw[0] = pack2(a0[0] + bf_lo(dsk), a1[0] + bf_hi(dsk));
#pragma unroll
    for (int r = 1; r < 4; ++r) *(LAS unsigned*)ktw[r] = pack2(a0[r], a1[r]);
  }
  SB();
#define LD1(i, so) "buffer_load_dwordx4 %" #i ", %10, %13, " so " offen sc1\n\t"
  asm volatile(LD1(0, "%14") LD1(1, "%15") LD1(2, "%16") LD1(3, "%17") LD1(4, "%18") LD1(5, "%19") LD1(6, "%20") LD1(7, "%21")
               "buffer_load_dwordx4 %8, %11, %13, 0 offen sc1\n\tbuffer_load_dwordx4 %9, %12, %13, 0 offen sc1"
               : "=&v"(gx[0]), "=&v"(gx[1]), "=&v"(gx[2]), "=&v"(gx[3]), "=&v"(gs[0]), "=&v"(gs[1]), "=&v"(gs[2]), "=&v"(gs[3]), "=&v"(gm), "=&v"(gn)
               : "v"(tid * 16), "v"(gmo), "v"(gmo2), "s"(rs), "s"(xgo), "s"(xgo + 4096), "s"(xgo + 8192), "s"(xgo + 12288),
                 "s"(OFF_SG), "s"(OFF_SG + 4096), "s"(OFF_SG + 8192), "s"(OFF_SG + 12288) : "memory");
#undef LD1
  SB();
  BAR();
  {
    unsigned k[11];
#pragma unroll
    for (int i = 0; i < 11; ++i) k[i] = *(LAS unsigned*)koff[i];
    SB();
    if (w < 2) *(LAS v4u*)zp = v4u{0u, 0u, 0u, 0u};
#pragma unroll
    for (int c = 0; c < 2; ++c) {
      unsigned e[5], o[5];
#pragma unroll
      for (int i = 0; i < 5; ++i) {
        o[i] = __builtin_amdgcn_perm(k[2 * i + 1], k[2 * i], c ? 0x07060302u : 0x05040100u);
        e[i] = __builtin_amdgcn_perm(k[2 * i + 2], k[2 * i + 1], c ? 0x07060302u : 0x05040100u);
      }
      LAS unsigned char* d = L + L_KC + c * 4 * KC_STRIDE + 32 + tid * 16;
      *(LAS v4u*)(d) = v4u{e[1], e[2], e[3], e[4]};
      *(LAS v4u*)(d + KC_STRIDE) = v4u{o[1], o[2], o[3], o[4]};
      *(LAS v4u*)(d + 2 * KC_STRIDE) = v4u{e[0], e[1], e[2], e[3]};
      *(LAS v4u*)(d + 3 * KC_STRIDE) = v4u{o[0], o[1], o[2], o[3]};
    }
  }
  SB();
  STAMP(4);
  {
    asm volatile("s_waitcnt vmcnt(0)" : "+v"(gx[0]), "+v"(gx[1]), "+v"(gx[2]), "+v"(gx[3]), "+v"(gs[0]), "+v"(gs[1]), "+v"(gs[2]), "+v"(gs[3]), "+v"(gm), "+v"(gn), "+v"(wt) :: "memory");
    unsigned bad = tagbad(tagbad(0u, gm, tagA, tagB), gn, tagA, tagB);
#pragma unroll
    for (int i = 0; i < 4; ++i) bad = tagbad(tagbad(bad, gx[i], tagA, tagB), gs[i], tagA, tagB);
    if (__builtin_expect(__any(bad != 0u), 0)) {
      _Pragma("nounroll") for (unsigned spin = 0; spin < SPIN_CAP; ++spin) {
        FRESH();
        if (spin) PAUSE();
        COUNT(20);
#pragma unroll
        for (int i = 0; i < 4; ++i) {
          if (tagbad(0u, gx[i], tagA, tagB)) gx[i] = ldg(rs, tid * 16, xgo + i * 4096);
          if (tagbad(0u, gs[i], tagA, tagB)) gs[i] = ldg(rs, tid * 16, OFF_SG + i * 4096);
        }
        if (tagbad(0u, gm, tagA, tagB)) gm = ldg(rs, gmo, 0);
        if (tagbad(0u, gn, tagA, tagB)) gn = ldg(rs, gmo2, 0);
        bad = tagbad(tagbad(0u, gm, tagA, tagB), gn, tagA, tagB);
#pragma unroll
        for (int i = 0; i < 4; ++i) bad = tagbad(tagbad(bad, gx[i], tagA, tagB), gs[i], tagA, tagB);
        if (!__any(bad != 0u)) break;
      }
    }
  }
  STAMP(5);
  W1DMA2(8, lrw + 8192);
  float zsc;
  v4u mtp;
  {
    const int i0 = (int)pack2(row_sum(u2f(gm.x)), row_sum(u2f(gm.z))), i1 = (int)pack2(row_sum(u2f(gn.x)), row_sum(u2f(gn.z)));
    unsigned md[6];
#pragma unroll
    for (int k = 0; k < 6; ++k) md[k] = (unsigned)__builtin_amdgcn_readlane(k < 4 ? i0 : i1, 16 * (k & 3));
#define MOD(k, c) ((c) ? bf_hi(md[k]) : bf_lo(md[k]))
    float s0 = rbf(1.0f + MOD(4, 0)), s1 = rbf(1.0f + MOD(4, 1));
    s0 = s0 == 0.f ? 0x1p-30f : s0;
    s1 = s1 == 0.f ? 0x1p-30f : s1;
    const float G0 = MOD(2, 0) * s0, G1 = MOD(2, 1) * s1;
    const float A0 = rbf(1.0f + MOD(1, 0)) * G0, A1 = rbf(1.0f + MOD(1, 1)) * G1, B0 = MOD(0, 0) * G0, B1 = MOD(0, 1) * G1;
    zsc = w >> 1 ? s1 : s0;
    mtp = v4u{lane ? md[5] : pack2(s0, s1), tagA, md[3], tagB};
#undef MOD
#pragma unroll
    for (int i = 0; i < 4; ++i) {
      const float mA = bf_lo(gs[i].x), rA = __builtin_amdgcn_rcpf(bf_hi(gs[i].x)), mB = bf_lo(gs[i].z), rB = __builtin_amdgcn_rcpf(bf_hi(gs[i].z));
      const float n00 = (bf_lo(gx[i].x) - mA) * rA, n01 = (bf_hi(gx[i].x) - mA) * rA, n10 = (bf_lo(gx[i].z) - mB) * rB, n11 = (bf_hi(gx[i].z) - mB) * rB;
      const int o = (tid + 256 * i) * 4;
      W1DMA2(10 + 2 * i, SLOT(10 + 2 * i));
      *(LAS unsigned*)(L + L_H + 480 + o) = pack2(__builtin_fmaf(n00, A0, B0), __builtin_fmaf(n10, A0, B0));
      *(LAS unsigned*)(L + L_H + H_STRIDE + 480 + o) = pack2(__builtin_fmaf(n01, A1, B1), __builtin_fmaf(n11, A1, B1));
      *(LAS unsigned*)(xbw + i * 512) = __builtin_amdgcn_perm(gx[i].z, gx[i].x, 0x05040100u);
      *(LAS unsigned*)(xbw + 4096 + i * 512) = __builtin_amdgcn_perm(gx[i].z, gx[i].x, 0x07060302u);
    }
  }
  BAR();
  const unsigned tb = now();
  STAMP(6);

  v4u w1f[32], w2f[28][2], mt0, mt1;
  {
    const bool rh = w & 1;
    constexpr ConvOrder O = conv_order(), OL = conv_order_last();
    v2u A[4][8][2], xk[4];
    v4u B[4][8];
    f32x4 acc[8], keep[4];
#pragma unroll
    for (int u = 0; u < 8; ++u) {
      A[0][u][0] = trd(ka + 512 * u + 8);
      A[0][u][1] = trd(ka + 512 * u);
      B[0][7 - u] = lds128(hb + 64 * (8 * (7 - u) + 3));
    }
    SB();
    if (lane) mtp.z = tb;
    stg(rs, tid < 2 ? tid * 16 : OOB, OFF_MT + id * 128, mtp);
    SB();
    auto hand_over = [&](auto Pc) __attribute__((always_inline)) {
      constexpr int i = Pc.v;
      PIN(acc[2 * i]); PIN(acc[2 * i + 1]);
      const f32x4 ev = acc[2 * i], od = acc[2 * i + 1];
      const f32x4 snd = rh ? ev : od, own = rh ? od : ev;
      *(LAS f32x4*)(i < 2 ? exa + i * 1024 : exb + (i - 1) * 1024) = snd;
#pragma unroll
      for (int c = 0; c < 4; ++c) { float t = keep[i][c] + own[c]; PIN(t); keep[i][c] = t; }
    };
    static_for<0, 144>([&](auto Kc) __attribute__((always_inline)) {
      constexpr int K = Kc.v, r = K / 36, k = K % 36, t = r < 3 ? O.t[k] : OL.t[k], u = r < 3 ? O.u[k] : OL.u[k];
      if constexpr (k == 0 && r > 0) asm volatile("" :: "v"(B[r][7]));
      mfma16_v<r == 0 && u == 0>(acc[t], v4u{A[r][u][0].x, A[r][u][0].y, A[r][u][1].x, A[r][u][1].y}, B[r][t - u]);
      SB();
      if constexpr (K == 24) WAITV(13);
      if constexpr (K >= 24 && K < 36) W1PULL(K - 24, K - 24);
      if constexpr (K == 40) asm volatile("" :: "a"(w1f[11]));
      if constexpr (K == 60) WAITV(8);
      if constexpr (K >= 60 && K < 70) W1PULL(K - 48, K - 48);
      if constexpr (K == 76) asm volatile("" :: "a"(w1f[21]));
      if constexpr (r == 2 && k < 4) xk[k] = *(LAS v2u*)(xbr + k * 512);
      constexpr int n = k - (r < 2 ? 0 : 4);
      if constexpr (r < 3 && n >= 0 && n < 24) {
        if constexpr (n % 3 == 2) B[r + 1][n / 3] = lds128(hb + 64 * (8 * (n / 3) - r + 2));
        else A[r + 1][n / 3][n % 3] = trd(ka + 512 * (n / 3) + 64 * (r + 1) + 8 - 8 * (n % 3));
      }
      if constexpr (K >= 100 && K < 116) {
        constexpr int e = K - 100;
        const unsigned xw = (e & 2) ? xk[e >> 2].y : xk[e >> 2].x;
        float t2_ = zsc * ((e & 1) ? bf_hi(xw) : bf_lo(xw));
        PIN(t2_);
        keep[e >> 2][e & 3] = t2_;
      }
      if constexpr (K == 108 + 8) hand_over(IC<0>{});
      if constexpr (K == 108 + 17) hand_over(IC<1>{});
      if constexpr (K == 108 + 30) hand_over(IC<2>{});
      if constexpr (K == 1 || K == 8) W1DMA2(18 + K / 4, SLOT(18 + K / 4));
      if constexpr (K == 15) W1DMA(22, L_RX + w * 3072);
      if constexpr (K == 19) W1DMA(23, L_TOP + w * 1024);
      if constexpr (K >= 41 && K < 69 && K % 7 == 6) W1DMA2(24 + (K - 41) / 7 * 2, lrw + (K - 41) / 7 * 2048);
      if constexpr (K >= 69 && K < 125 && K % 8 == 5) W2DMA((K - 69) / 8, SLOT(8 + (K - 69) / 8 * 2));
      if constexpr (K == MTK)
        asm volatile("buffer_load_dwordx4 %0, %2, %3, 0 offen sc1\n\tbuffer_load_dwordx4 %1, %2, %3, %4 offen sc1"
                     : "=&v"(mt0), "=&v"(mt1) : "v"(mto), "s"(rs), "s"(16384) : "memory");
      SB();
    });
    STAMP(7);
    asm volatile("s_nop 7" : "+v"(acc[6]), "+v"(acc[7]));
    hand_over(IC<3>{});
    BAR();
#pragma unroll
    for (int i = 0; i < 4; ++i) keep[i] += *(LAS f32x4*)(i < 2 ? eya + i * 1024 : eyb + (i - 1) * 1024);
#pragma unroll
    for (int i = 0; i < 2; ++i) {
      const auto s0 = __builtin_amdgcn_permlane16_swap(pack2(keep[2 * i][0], keep[2 * i][1]), pack2(keep[2 * i + 1][0], keep[2 * i + 1][1]), false, false);
      const auto s1 = __builtin_amdgcn_permlane16_swap(pack2(keep[2 * i][2], keep[2 * i][3]), pack2(keep[2 * i + 1][2], keep[2 * i + 1][3]), false, false);
      stg(rs, i ? zb : za, zs, v4u{guard(s0[0]), guard(s1[0]), guard(s0[1]), guard(s1[1])});
    }
    SB();
  }
  STAMP(8);

  static_assert(OFF_MT == 0, "table offset");
  const unsigned te = now();
  asm volatile("s_waitcnt vmcnt(2)" : "+v"(mt0), "+v"(mt1) :: "memory");
  if (__builtin_expect(__any(tagbad(tagbad(0u, mt0, tagA, tagB), mt1, tagA, tagB) != 0u), 0)) {
    _Pragma("nounroll") for (unsigned spin = 0; spin < SPIN_CAP; ++spin) {
      FRESH();
      COUNT(21);
      if (tagbad(0u, mt0, tagA, tagB)) mt0 = ldg(rs, mto, OFF_MT);
      if (tagbad(0u, mt1, tagA, tagB)) mt1 = ldg(rs, mto, OFF_MT + 16384);
      if (!__any(tagbad(tagbad(0u, mt0, tagA, tagB), mt1, tagA, tagB) != 0u)) break;
      PAUSE();
    }
  }
  *(LAS unsigned*)(mtw) = mt0.x;
  *(LAS unsigned*)(mtw + 512) = mt1.x;
  *(LAS unsigned*)(mtw + 1024) = mt0.z;
  *(LAS unsigned*)(mtw + 1536) = mt1.z;
  {
    int late = (lane & 1) ? max((int)(mt0.z - tb), (int)(mt1.z - tb)) : 0;
    late = max(late, __builtin_amdgcn_update_dpp(late, late, 0xB1, 0xF, 0xF, false));
    late = max(late, __builtin_amdgcn_update_dpp(late, late, 0x4E, 0xF, 0xF, false));
    late = max(late, __builtin_amdgcn_update_dpp(late, late, 0x141, 0xF, 0xF, false));
    late = max(late, __builtin_amdgcn_update_dpp(late, late, 0x140, 0xF, 0xF, false));
    late = max(late, __builtin_amdgcn_update_dpp(late, late, 0x142, 0xA, 0xF, false));
    late = max(late, __builtin_amdgcn_update_dpp(late, late, 0x143, 0xC, 0xF, false));
    if (lane == 63) *(LAS int*)(L + L_ETA + w * 4) = late;
  }
  const int w2t = (4 * hf + q4) * 128 + blk * 32 + (((p4 & 1) << 1) | (p4 >> 1)) * 8;
  LAS unsigned char* w2a = L + w2t + (q4 >> 1) * 64;
  LAS unsigned char* w2b = L + w2t + ((q4 >> 1) ^ 1) * 64;
  LAS unsigned char *w2sa = w2a + lsp, *w2sb = w2b + lsp;
  PIN(w2sa); PIN(w2sb);
#define PULL(step, pa, pb, off) do { w2f[step][0] = trd2((pa) + (off), (pa) + (off) + 1024); w2f[step][1] = trd2((pb) + (off), (pb) + (off) + 1024); } while (0)
#pragma unroll
  for (int i = 0; i < 3; ++i) PULL(4 + i, w2sa, w2sb, i * 2048);
  w1f[22] = trd2(L + L_RX + w * 3072 + tra, L + L_RX + w * 3072 + tra + 256);
  w1f[23] = trd2(L + L_TOP + w * 1024 + tra, L + L_TOP + w * 1024 + tra + 256);
  BAR();
  STAMP(9);
  const int par = (0x1C >> (ct % 7)) & 1;
  const unsigned char* xg = setp + OFF_Z + rp * 131072;
  const unsigned xl = L_XR + w * 8192;
#define XSRC(p) (xg + w * 8192 + ((p) ^ par) * 32768)
  const int slo = OFF_Z + (rp * 4 + w) * 32768 + si * 2048;
  v4u hq[56], res[2], sx[8];
  const int c0 = 7 * w + rot, c3 = c0 - 7 * (rot >> 2), c4 = c0 - 7 * ((rot + 1) >> 2);
  const int hb0 = OFF_HID + (rp * 4 + rg) * 114688, kb0 = OFF_CK + (rp * 4 + rg) * 4096 + w * 896, ckv = lane < 56 ? l16 : OOB;
#define CHUNK(n) (((n) % 28 / 4 < 3 ? c0 : (n) % 28 / 4 == 3 ? c3 : c4) + (n) % 28 / 4)
#define HFRAG(n) ldg(rs, l16 + ((n) & 3) * 1024, hb0 + CHUNK(n) * 4096 + ((n) / 28) * (2 * 114688))
#define HLOAD(n) hq[n] = HFRAG(n)
  const int hoff = OFF_HID + (rp * 4 * 112 + ct * 8 + 2 * w) * 1024;
  int hv0 = dt == 7 ? OOB : l16, hv1 = dt == 7 ? OOB : l16 + 1024;
  const int cko = OFF_CK + rp * 16384 + (ct * 4 + w) * 64;
  int ckw = (p4 || dt == 7) ? OOB : lane;
  unsigned ckey = lane ? 0u : tagA;
  PIN(hv0); PIN(hv1); PIN(ckw); PIN(ckey);
#define CKPUT(sum, u) __builtin_amdgcn_raw_buffer_store_b32(ckey - red_step<1>(red_step<0>(sum)), rs, ckw, cko + ((u) ^ par) * 4096, SC1)
  const int sxo = OFF_SX + (rp * 4 + w) * 8192;
  LAS unsigned char* xr[2];
#pragma unroll
  for (int i = 0; i < 2; ++i) { xr[i] = L + L_XR + tra + i * 65536; PIN(xr[i]); }
  LAS unsigned char* rr = L + L_XR + tra + (rg ^ par) * 32768 + (4 * dt + w) * 1024;
  LAS unsigned char* str = L + L_STAT + (lane & 31) * 8;
  LAS unsigned char *pa = w2a + lrw + 8192, *pb = w2b + lrw + 8192;
  w2a += L_XR + w * 8192; w2b += L_XR + w * 8192;
  LAS unsigned char* stw = str + (w ^ par) * 256;
  PIN(str); PIN(stw); PIN(rr); PIN(w2a); PIN(w2b);
  f32x4 uu[4], vv[4];
  v4u sl0, sl1;
  {
    LAS unsigned char* qr = L + ((lane & 31) == 0 ? L_TS : L_TB) + hf * 16;
    PIN(pa); PIN(pb);
#pragma unroll
    for (int i = 0; i < 3; ++i) W2DMA(7 + i, lsp + i * 2048);
    f32x16 uv, uv2;
    v4u qa[32];
    int wait = 0;
    const v4u lt = lds128(L + L_ETA);
    bool sent = false;
    auto send = [&]() __attribute__((always_inline)) {
      STAMPB(26);
      asm volatile("buffer_load_dwordx4 %0, %2, %3, %4 offen sc1\n\tbuffer_load_dwordx4 %1, %2, %3, %4 offen offset:1024 sc1"
                   :: "v"(sl0), "v"(sl1), "v"(l16), "s"(rs), "s"(slo) : "memory");
      dma_x4(XSRC(0), l16, xl);
      dma_x4(XSRC(0) + 4096, l16, xl + 4096);
    };
    asm volatile("" : "=v"(sl0), "=v"(sl1));
#pragma unroll
    for (int i = 0; i < 4; ++i) qa[i] = lds128(qr + i * 32);
    static_for<0, 32>([&](auto Sc) __attribute__((always_inline)) {
      constexpr int s = Sc.v;
      mfma_va<(s < 2)>((s & 1) ? uv2 : uv, qa[s], w1f[s]);
      SB();
      if constexpr (s < 28) qa[s + 4] = lds128(qr + (s + 4) * 32);
      if constexpr (s > 0) asm volatile("" :: "v"(qa[s > 0 ? s - 1 : 0]));
      if constexpr (s < 8) W1PULL(24 + s, s);
      if constexpr (s == 4) { wait = min(__builtin_amdgcn_readfirstlane(max(max((int)lt.x, (int)lt.y), max((int)lt.z, (int)lt.w))), 300) + ZLAG; }
      if constexpr (s >= 8 && s < 16) w2f[(s - 8) >> 1][s & 1] = trd2(((s & 1) ? pb : pa) + ((s - 8) >> 1) * 2048, ((s & 1) ? pb : pa) + ((s - 8) >> 1) * 2048 + 1024);
      if constexpr (s == 4 || s == 13 || s == 22 || s == 31) {
        if (!sent && (int)(now() - te) >= wait) { sent = true; send(); }
      }
      SB();
    });
    mfma_done(uv2);
    PIN(uv);
    LAS unsigned char* uw = L + L_UVT + (32 * w + (lane & 31)) * 4;
    if (lane < 32) { *(LAS float*)(uw) = uv[0] + uv2[0]; *(LAS float*)(uw + 512) = -LOG2E * (uv[1] + uv2[1]); }
    LAS unsigned char* uvp = L + L_UVT + (32 * w + 4 * hf) * 4;
#pragma unroll
    for (int i = 0; i < 4; ++i) { uu[i] = *(LAS f32x4*)(uvp + i * 32); vv[i] = *(LAS f32x4*)(uvp + 512 + i * 32); }
    float i0 = __builtin_amdgcn_rcpf(bf_lo(*(LAS u16*)(L + L_TS + (si * 32 + (lane >> 2)) * 2)));
    float i1 = __builtin_amdgcn_rcpf(bf_lo(*(LAS u16*)(L + L_TS + (si * 32 + 16 + (lane >> 2)) * 2)));
    int sxw = sel(lane & 8, OOB, (p4 * 8 + hf * 4 + blk * 2 + ((lane >> 2) & 1)) * 16);
    PIN(i0); PIN(i1); PIN(sxw);
    SB();
    if (!sent) {
      while ((int)(now() - te) < wait) { }
      send();
    }
    dma_x4(XSRC(1), l16, xl + 32768);
    dma_x4(XSRC(1) + 4096, l16, xl + 32768 + 4096);
    STAMPB(27);
    asm volatile("s_waitcnt vmcnt(16)" : "+v"(sl0), "+v"(sl1) :: "memory");
    STAMPB(28);
    if (__builtin_expect(__any(max4(max4(0u, sl0), sl1) == POISON), 0)) {
      _Pragma("nounroll") for (unsigned spin = 0; spin < SPIN_CAP; ++spin) {
        FRESH();
        if (spin) PAUSE();
        COUNT(22);
        sl0 = ldg(rs, l16, slo);
        sl1 = ldg(rs, l16 + 1024, slo);
        if (!__any(max4(max4(0u, sl0), sl1) == POISON)) break;
      }
    }
    float sm[8], sq[8];
#pragma unroll
    for (int i = 0; i < 4; ++i) {
      const f32x2 a = f32x2{bf_lo(sl0[i]), bf_hi(sl0[i])} * f32x2{i0, i0}, c = f32x2{bf_lo(sl1[i]), bf_hi(sl1[i])} * f32x2{i1, i1};
      const f32x2 s2 = a + c, q2 = __builtin_elementwise_fma(a, a, c * c);
      sm[2 * i] = s2.x; sm[2 * i + 1] = s2.y; sq[2 * i] = q2.x; sq[2 * i + 1] = q2.y;
    }
#pragma unroll
    for (int i = 0; i < 4; ++i) { swap_add32(sm[i], sm[i + 4]); swap_add32(sq[i], sq[i + 4]); }
#pragma unroll
    for (int i = 0; i < 2; ++i) { swap_add16(sm[i], sm[i + 2]); swap_add16(sq[i], sq[i + 2]); }
#pragma unroll
    for (int i = 0; i < 2; ++i) { sm[i] = dpp_add<0x124>(dpp_add<0x128>(sm[i])); sq[i] = dpp_add<0x124>(dpp_add<0x128>(sq[i])); }
    const bool od = lane & 4;
    stg(rs, sxw, OFF_SX + ((rp * 4 + w) * 16 + si) * 512,
        v4u{f2u(od ? sm[1] : sm[0]), tagA, f2u(od ? sq[1] : sq[0]), tagB});
    SB();
  }
  STAMPB(29);
  WAITV(9);
  STAMP(10);
  {
    v4u t[8];
    unsigned m = 0, m2 = 0;
#pragma unroll
    for (int i = 0; i < 8; ++i) t[i] = lds128(L + xl + l16 + i * 1024);
#pragma unroll
    for (int i = 0; i < 8; i += 2) { m = max4(m, t[i]); m2 = max4(m2, t[i + 1]); }
    if (__builtin_expect(__any(max(m, m2) == POISON), 0)) {
      _Pragma("nounroll") for (unsigned spin = 0; spin < SPIN_CAP; ++spin) {
        COUNT(22);
        bool again = false;
#pragma nounroll
        for (int i = 0; i < 8; ++i)
          if (__any(max4(0u, lds128(L + xl + l16 + i * 1024)) == POISON)) {
            again = true;
            dma_x1(XSRC(0) + i * 1024, l16, xl + i * 1024);
            dma_x1(XSRC(1) + i * 1024, l16, xl + 32768 + i * 1024);
          }
        if (!again) break;
        WAITV(0);
        if (spin) PAUSE();
      }
    }
  }
  BAR();
  STAMP(11);

#define XA(t) (xr[(t) >> 6] + ((t) & 63) * 1024)
#define XF(t) trd2(XA(t), XA(t) + 256)
#define XEN
#define REDO(p) \
  if (XEN __builtin_expect(__any(!(acc[(p) % 3][0] == acc[(p) % 3][0])), 0)) { \
    unsigned m0s; \
    asm volatile("s_mov_b32 %0, m0" : "=s"(m0s)); \
    for (unsigned spin = 0; spin < SPIN_CAP / 128; ++spin) { \
      COUNT(23); \
      _Pragma("nounroll") for (int i = 0; i < 8; ++i) dma_x4(xg + ((p) ^ par) * 32768 + i * 4096, l16, L_XR + (p) * 32768 + i * 4096); \
      WAITV(0); \
      _Pragma("unroll") for (int s = 0; s < 32; ++s) { \
        const v4u t = XF(32 * (p) + s); \
        if (s == 0) mfma_av<true>(acc[(p) % 3], w1f[s], t); else mfma_av<false>(acc[(p) % 3], w1f[s], t); \
        SB(); \
      } \
      mfma_done(acc[(p) % 3]); \
      if (!__any(!(acc[(p) % 3][0] == acc[(p) % 3][0]))) break; \
    } \
    set_m0(m0s); \
  }
  f32x16 acc[3];
  f32x2 ec[4];
  float ep[48], ee[48], ey[48];
  unsigned cs = 0, sbad = 0;
  float ssm = 0.f, ssq = 0.f;
  v4u x[128], eo;
#pragma unroll
  for (int t = 0; t < 4; ++t) x[t] = XF(t);
  SB();
  auto eslot = [&](auto Sc) __attribute__((always_inline)) {
    static_for<0, 48>([&](auto Vc) __attribute__((always_inline)) {
      constexpr int V = Vc.v, k = V & 15, st = Sc.v - epi_slot(V);
      if constexpr (st == 6 && (k & 1)) {
        unsigned o = pack2(ey[V - 1], ey[V]);
        PIN(o);
        eo[(k >> 1) & 3] = o;
        if constexpr (k == 7) { stg(rs, hv0, hoff + ((V >> 4) ^ par) * 114688, eo); cs = sum4(0u, eo); PIN(cs); }
        if constexpr (k == 15) { stg(rs, hv1, hoff + ((V >> 4) ^ par) * 114688, eo); CKPUT(sum4(cs, eo), V >> 4); }
      }
      if constexpr (st == 5) { ey[V] = ep[V] * ee[V]; PIN(ey[V]); }
      if constexpr (st == 4) { ee[V] = __builtin_amdgcn_rcpf(ee[V]); PIN(ee[V]); }
      if constexpr (st == 3) { ee[V] = 1.0f + ee[V]; PIN(ee[V]); }
      if constexpr (st == 2) { ee[V] = __builtin_amdgcn_exp2f(ep[V]); PIN(ee[V]); }
      if constexpr (st == 1) { ep[V] = __builtin_fmaf(ec[V >> 4].x, acc[(V >> 4) % 3][k], ep[V]); PIN(ep[V]); }
      if constexpr (st == 0) { ep[V] = __builtin_fmaf(ec[V >> 4].y, uu[k >> 2][k & 3], vv[k >> 2][k & 3]); PIN(ep[V]); }
    });
    SB();
  };
  static_for<0, 64>([&](auto Jc) __attribute__((always_inline)) {
    constexpr int J = Jc.v, p = J >> 4, j = J & 15;
    asm volatile("" :: "v"(x[2 * J + 1]));
    mfma_av<j == 0>(acc[p % 3], w1f[2 * j], x[2 * J]);
    if constexpr (J < 62) { x[2 * J + 4] = XF(2 * J + 4); if constexpr (!epi_pair(J)) x[2 * J + 5] = XF(2 * J + 5); }
    SB();
    if constexpr (epi_pair(J)) eslot(IC<2 * J>{});
    mfma_av<false>(acc[p % 3], w1f[2 * j + 1], x[2 * J + 1]);
    if constexpr (epi_pair(J)) x[2 * J + 5] = XF(2 * J + 5);
    SB();
    if constexpr (J == 10) { waitv<c_between(-1, 10)>(); __builtin_amdgcn_s_barrier(); FRESH(); }
    if constexpr (J == 34) waitv<c_between(19, 34)>();
    if constexpr (J == 38) waitv<c_between(27, 38)>();
    if constexpr (J >= EO - 6 && J < EO - 2) {
      asm volatile("s_waitcnt vmcnt(%2)" : "+v"(sx[2 * (J - EO + 6)]), "+v"(sx[2 * (J - EO + 6) + 1]) : "n"(c_between(8 + J - EO + 6, J)) : "memory");
#pragma unroll
      for (int i = 2 * (J - EO + 6); i < 2 * (J - EO + 6) + 2; ++i) {
        sbad = tagbad(sbad, sx[i], tagA, tagB);
        ssm += u2f(sx[i].x); PIN(ssm);
        ssq += u2f(sx[i].z); PIN(ssq);
      }
      PIN(sbad);
      SB();
    }
    if constexpr (J == EO - 2) {
      if (__builtin_expect(__any(sbad != 0u), 0)) {
        _Pragma("nounroll") for (unsigned spin = 0; spin < SPIN_CAP; ++spin) {
          FRESH();
          if (spin) PAUSE();
          COUNT(23);
          sbad = 0; ssm = 0.f; ssq = 0.f;
#pragma unroll
          for (int i = 0; i < 8; ++i) sx[i] = ldg(rs, l16 + (i & 3) * 1024, sxo + (i >> 2) * 4096);
#pragma unroll
          for (int i = 0; i < 8; ++i) { sbad = tagbad(sbad, sx[i], tagA, tagB); ssm += u2f(sx[i].x); ssq += u2f(sx[i].z); }
          if (!__any(sbad != 0u)) break;
        }
      }
      const float mean = swap32_add(ssm) * (1.0f / 512.0f);
      const float ms = __builtin_fmaf(swap32_add(ssq), 1.0f / 512.0f, 1e-5f);
      const float a = -LOG2E * __builtin_amdgcn_rsqf(fmaxf(__builtin_fmaf(-mean, mean, ms), 1e-5f));
      *(LAS f32x2*)(stw) = f32x2{a, -a * mean};
      SB();
    }
    if constexpr (J == EO - 1) {
      asm volatile("s_waitcnt lgkmcnt(4)" ::: "memory");
      __builtin_amdgcn_s_barrier();
      FRESH();
#pragma unroll
      for (int i = 0; i < 4; ++i) ec[i] = *(LAS f32x2*)(str + i * 256);
      SB();
    }
    if constexpr (!epi_pair(J)) eslot(IC<2 * J>{});
    eslot(IC<2 * J + 1>{});
    if constexpr (j == 1 && p >= 1) {
      if constexpr (p == 2) REDO(1)
      if constexpr (p == 3) REDO(2)
      if (((p - 1) & 1) == (rg ^ par)) res[(p - 1) >> 1] = trd2(rr + ((p - 1) >> 1) * 65536, rr + ((p - 1) >> 1) * 65536 + 256);
    }
    if constexpr (j == 2 && p >= 1) {
      asm volatile("s_waitcnt lgkmcnt(4)" ::: "memory");
      __builtin_amdgcn_s_barrier();
      FRESH();
    }
    if constexpr (J < 8 || (J >= 12 && J < 20)) {
      constexpr int n = J < 8 ? J : J - 4;
      if constexpr ((n & 3) == 0) set_m0(xl + 65536 + (n >> 3) * 32768 + (n & 4) * 1024);
      dma_xn<(n & 3) * 1024>(XSRC(2 + (n >> 3)) + (n & 4) * 1024, l16);
    }
    if constexpr (J >= 8 && J < 12)
      asm volatile("buffer_load_dwordx4 %0, %2, %3, %4 offen offset:%5 sc1\n\tbuffer_load_dwordx4 %1, %2, %3, %4 offen offset:%6 sc1"
                   : "=&v"(sx[2 * (J - 8)]), "=&v"(sx[2 * (J - 8) + 1])
                   : "v"(l16), "s"(rs), "s"(sxo + (J & 2) * 2048), "n"((J & 1) * 2048), "n"((J & 1) * 2048 + 1024) : "memory");
    if constexpr (J >= 19 && J < 22) PULL(7 + J - 19, w2sa, w2sb, (J - 19) * 2048);
    if constexpr (J >= 38 && J < 42) PULL(10 + J - 38, w2a, w2b, (J - 38) * 2048);
    if constexpr (J == 43) asm volatile("" :: "a"(w2f[13][1]));
    if constexpr (J >= 20 && J < 28) W2HALF(20 + J - 20, xl + (J - 20) * 1024);
    if constexpr (J >= 28 && J < 34) W2HALF(28 + J - 28, lsp + (J - 28) * 1024);
    if constexpr (J >= 35 && J < 43) W2HALF(34 + J - 35, xl + 32768 + (J - 35) * 1024);
    if constexpr (J >= 43 && J < 51) W2HALF(42 + J - 43, xl + (J - 43) * 1024);
    static_for<hc_upto(J), hc_upto(J + 1)>([&](auto Nc) __attribute__((always_inline)) { HLOAD(Nc.v); });
    SB();
    if constexpr (J == 15) STAMP(16);
    if constexpr (J == 31) STAMP(17);
    if constexpr (J == 47) STAMP(18);
  });
  STAMP(12);
  static_assert(EO <= 30 && epi_slot(31) < epi_slot(32) && epi_slot(16) + 1 >= 2 * 34 && epi_slot(32) + 1 >= 2 * 50 && epi_slot(47) + 6 < 128, "schedule");
  static_for<12, 20>([&](auto Ic) __attribute__((always_inline)) { HLOAD(Ic.v); });
  SB();
  mfma_done(acc[0]);
  REDO(3)
  if (rg ^ par) res[1] = trd2(rr + 65536, rr + 65536 + 256);
#pragma unroll
  for (int h = 0; h < 2; ++h) {
    v4u o;
#pragma unroll
    for (int i = 0; i < 4; ++i) {
      const int k = 8 * h + 2 * i;
      const f32x2 t = __builtin_elementwise_fma(f32x2{ec[3].y, ec[3].y}, f32x2{uu[k >> 2][k & 3], uu[k >> 2][(k & 3) + 1]}, f32x2{vv[k >> 2][k & 3], vv[k >> 2][(k & 3) + 1]});
      const f32x2 pp = __builtin_elementwise_fma(f32x2{ec[3].x, ec[3].x}, f32x2{acc[0][k], acc[0][k + 1]}, t);
      const f32x2 d = f32x2{__builtin_amdgcn_exp2f(pp.x), __builtin_amdgcn_exp2f(pp.y)} + f32x2{1.0f, 1.0f};
      const f32x2 y = pp * f32x2{__builtin_amdgcn_rcpf(d.x), __builtin_amdgcn_rcpf(d.y)};
      o[i] = pack2(y.x, y.y);
    }
    PIN(o);
    if (h == 0) static_for<20, 24>([&](auto Ic) __attribute__((always_inline)) { HLOAD(Ic.v); });
    if (h == 1) static_for<24, 28>([&](auto Ic) __attribute__((always_inline)) { HLOAD(Ic.v); });
    stg(rs, h ? hv1 : hv0, hoff + (3 ^ par) * 114688, o);
    if (h) CKPUT(sum4(cs, o), 3); else cs = sum4(0u, o);
    SB();
  }
  STAMP(13);

  f32x16 d[2][2];
  LAS unsigned char* redw = L + L_XR + w * 8192 + l16;
  LAS unsigned char* redr = L + L_XR + w * 2048 + l16;
#define REDW(un, q) *(LAS f32x4*)(redw + (un) * 32768 + (q) * 1024) = \
    f32x4{d[un][(q) >> 2][4 * ((q) & 3)], d[un][(q) >> 2][4 * ((q) & 3) + 1], d[un][(q) >> 2][4 * ((q) & 3) + 2], d[un][(q) >> 2][4 * ((q) & 3) + 3]}
  LAS unsigned char *w2ua = w2a + 65536, *w2ub = w2b + 65536;
  PIN(w2ua); PIN(w2ub);
#define CHECK(un) \
  if (__builtin_expect((unsigned)__builtin_amdgcn_readlane((int)m[un], 63) != 14u * tagA, 0)) { \
    _Pragma("nounroll") for (unsigned spin = 0; spin < SPIN_CAP; ++spin) { \
      FRESH(); \
      COUNT(24 + (un)); \
      unsigned t = sum4(0u, ldg(rs, ckv, kb0 + (un) * 8192)); \
      static_for<0, 7>([&](auto Cc) __attribute__((always_inline)) { \
        constexpr int c = Cc.v; \
        v4u q[4]; \
        _Pragma("unroll") for (int i = 0; i < 4; ++i) q[i] = ldg(rs, l16 + i * 1024, hb0 + CHUNK(4 * c) * 4096 + (un) * (2 * 114688)); \
        t = sum8(sum8(t, q[0], q[1]), q[2], q[3]); \
        mfma_av<c == 0>(d[un][0], w2f[4 * c][0], q[0]); mfma_av<c == 0>(d[un][1], w2f[4 * c][1], q[0]); \
        _Pragma("unroll") for (int i = 1; i < 4; ++i) { mfma_av<false>(d[un][0], w2f[4 * c + i][0], q[i]); mfma_av<false>(d[un][1], w2f[4 * c + i][1], q[i]); } \
        SB(); \
      }); \
      mfma_done(d[un][1]); \
      PIN(d[un][0]); \
      if ((unsigned)__builtin_amdgcn_readlane((int)red_all(t), 63) == 14u * tagA) break; \
      PAUSE(); \
    } \
    if (un) { _Pragma("unroll") for (int q = 0; q < 4; ++q) REDW(1, q); } \
  }
  unsigned m[2] = {0u, 0u};
  v4u gbv, scv, ck[2];
  f32x4 pr[8];
  float gb[8], rx[2][8];
  unsigned orow = (rp * 128 + ((lane & 31) >> 3) * 16 + rg * 8 + (lane & 7)) * 1024 + dt * 128 + w * 32 + hf * 16;
  static_for<0, 112>([&](auto Xc) __attribute__((always_inline)) {
    constexpr int X = Xc.v, I = X < 100 ? X >> 1 : 50 + (X - 100) % 6, e = X < 100 ? X & 1 : (X - 100) / 6, un = I / 28;
    mfma_av<I % 28 == 0>(d[un][e], w2f[I % 28][e], hq[I]);
    SB();
    if constexpr (X < 100 && e == 0) {
      if constexpr (I < 3) PULL(14 + I, w2sa, w2sb, I * 2048);
      if constexpr (I >= 3 && I < 7) PULL(14 + I, w2a, w2b, 32768 + (I - 3) * 2048);
      if constexpr (I >= 7 && I < 11) PULL(14 + I, w2a, w2b, (I - 7) * 2048);
      if constexpr (I == 21) waitv<hl_upto(21) - 28 + 1>();
      if constexpr (I >= 21 && I < 24) PULL(4 + I, w2ua, w2ub, (I - 21) * 2048);
      if constexpr (I == 28) { m[0] = sum4(m[0], ck[0]); PIN(m[0]); }
      if constexpr (I >= 29 && I < 35) { m[0] = red_step<I - 29>(m[0]); PIN(m[0]); }
      if constexpr (I == 35) CHECK(0)
      if constexpr (I >= 35 && I < 43) REDW(0, I - 35);
    }
    if constexpr (X < 100 && e == 1) {
      if constexpr (I < 6) W2HALF(50 + I, xl + 65536 + I * 1024);
      static_for<hl_upto(I), hl_upto(I + 1)>([&](auto Nc) __attribute__((always_inline)) { HLOAD(Nc.v); });
      if constexpr (I == 6) ck[0] = ldg(rs, ckv, kb0);
      if constexpr (I == 37) ck[1] = ldg(rs, ckv, kb0 + 8192);
    }
    if constexpr (X < 100 ? e == 1 : e == 0) { m[un] = sum4(m[un], hq[I]); PIN(m[un]); }
    if constexpr (X == 105) { m[1] = sum4(m[1], ck[1]); PIN(m[1]); }
    if constexpr (X >= 106) { m[1] = red_step<X - 106>(m[1]); PIN(m[1]); }
    if constexpr (X == 22) { gbv = lds128(L + L_TG + (64 * dt + 16 * w + 8 * hf) * 2); scv = lds128(L + L_TS + (64 * dt + 16 * w + 8 * hf) * 2); PIN(orow); }
    if constexpr (X >= 26 && X < 42 && (X & 1) == 0) {
      constexpr int q = (X - 26) >> 1;
      const float inv = __builtin_amdgcn_rcpf((q & 1) ? bf_hi(scv[q >> 1]) : bf_lo(scv[q >> 1]));
      gb[q] = -LN2 * ((q & 1) ? bf_hi(gbv[q >> 1]) : bf_lo(gbv[q >> 1]));
      rx[0][q] = inv * ((q & 1) ? bf_hi(res[0][q >> 1]) : bf_lo(res[0][q >> 1]));
      rx[1][q] = inv * ((q & 1) ? bf_hi(res[1][q >> 1]) : bf_lo(res[1][q >> 1]));
      PIN(gb[q]); PIN(rx[0][q]); PIN(rx[1][q]);
    }
    if constexpr (X == 86) BAR();
    if constexpr (X == 86 || X == 88) {
#pragma unroll
      for (int k = X - 86; k < X - 84; ++k) { pr[2 * k] = *(LAS f32x4*)(redr + k * 8192); pr[2 * k + 1] = *(LAS f32x4*)(redr + k * 8192 + 1024); }
    }
    if constexpr (X >= 91 && X < 99) {
      constexpr int q = X - 91;
      float t = (pr[q >> 2][q & 3] + pr[2 + (q >> 2)][q & 3]);
      PIN(t);
      float t2_ = (pr[4 + (q >> 2)][q & 3] + pr[6 + (q >> 2)][q & 3]);
      PIN(t2_);
      rx[0][q] = __builtin_fmaf(gb[q], t + t2_, rx[0][q]);
      PIN(rx[0][q]);
    }
    if constexpr (X == 99)
      *(GAS v4u*)((GAS unsigned char*)out + orow) = v4u{pack2(rx[0][0], rx[0][1]), pack2(rx[0][2], rx[0][3]), pack2(rx[0][4], rx[0][5]), pack2(rx[0][6], rx[0][7])};
    if constexpr (X == 108) asm volatile("s_nop 3" : "+v"(d[1][0]));
    if constexpr (X >= 108) REDW(1, X - 108);
    SB();
    if constexpr (X == 55) STAMP(14);
  });
  STAMP(19);
  asm volatile("s_nop 7" : "+v"(d[1][1]));
  CHECK(1)
#pragma unroll
  for (int q = 4; q < 8; ++q) REDW(1, q);
  BAR();
  {
    f32x4 s0 = {}, s1 = {};
#pragma unroll
    for (int k = 0; k < 4; ++k) { s0 += *(LAS f32x4*)(redr + k * 8192 + 32768); s1 += *(LAS f32x4*)(redr + k * 8192 + 32768 + 1024); }
    float y[8];
#pragma unroll
    for (int q = 0; q < 8; ++q) y[q] = __builtin_fmaf(gb[q], q < 4 ? s0[q & 3] : s1[q & 3], rx[1][q]);
    *(GAS v4u*)((GAS unsigned char*)out + orow + 65536) = v4u{pack2(y[0], y[1]), pack2(y[2], y[3]), pack2(y[4], y[5]), pack2(y[6], y[7])};
  }
  if (b == 0 && tid == 0) __hip_atomic_store((GAS unsigned*)ws, epoch + 1u, __ATOMIC_RELAXED, __HIP_MEMORY_SCOPE_AGENT);
  STAMP(15);
}

extern "C" void kernel_launch(void* const* d_in, const int* in_sizes, int n_in, void* d_out, int out_size, void* d_ws, size_t ws_size,
                              hipStream_t stream) {
  (void)in_sizes; (void)n_in; (void)out_size; (void)ws_size;
  (void)hipFuncSetAttribute((const void*)s4_block_kernel, hipFuncAttributeMaxDynamicSharedMemorySize, LDS_BYTES);
  s4_block_kernel<<<256, 256, LDS_BYTES, stream>>>((cptr)d_in[0], (cptr)d_in[1], (cptr)d_in[2], (cptr)d_in[3], (cptr)d_in[4], (cptr)d_in[5], (cptr)d_in[6],
                                                   (cptr)d_in[7], (cptr)d_in[8], (cptr)d_in[10], (cptr)d_in[11], (cptr)d_in[12], (cptr)d_in[13],
                                                   (unsigned char*)d_out, (unsigned char*)d_ws, (cptr)d_in[9]);
}
```

```cpp
#include <hip/hip_runtime.h>

#define GAS __attribute__((address_space(1)))
#define LAS __attribute__((address_space(3)))
#define DI __device__ __forceinline__
typedef unsigned v4u __attribute__((ext_vector_type(4)));
typedef unsigned v2u __attribute__((ext_vector_type(2)));
typedef float f32x2 __attribute__((ext_vector_type(2)));
typedef float f32x4 __attribute__((ext_vector_type(4)));
typedef float f32x16 __attribute__((ext_vector_type(16)));
typedef __bf16 bf16x2 __attribute__((ext_vector_type(2)));
typedef __bf16 bf16x8 __attribute__((ext_vector_type(8)));
typedef short s16x4 __attribute__((ext_vector_type(4)));
typedef unsigned short u16;
typedef unsigned long long u64;

constexpr int SET_BASE = 4096;
constexpr int OFF_MT = 0;
constexpr int OFF_SG = 32768;
constexpr int OFF_MG = 65536;
constexpr int OFF_SX = 524288;
constexpr int OFF_XG = 1 << 20;
constexpr int OFF_Z = OFF_XG + (4 << 20);
constexpr int OFF_HID = OFF_Z + 2097152;
constexpr int OFF_CK = OFF_HID + 7340032;
constexpr int SET_BYTES = OFF_CK + 262144;
static_assert(OFF_MG + 393216 <= OFF_SX, "set size");

constexpr int L_TS = 0, L_TB = 1024, L_TG = 2048;
constexpr int L_UVT = 3072;
constexpr int L_M = 4096;
constexpr int L_UVS = L_M;
constexpr int UVS_ROW = 544, UVS_CH = 57 * UVS_ROW;
constexpr int L_KC = L_M;
constexpr int KC_STRIDE = 4160;
constexpr int L_H = L_KC + 8 * KC_STRIDE;
constexpr int H_STRIDE = 4576;
constexpr int L_XB = L_H + 2 * H_STRIDE;
constexpr int L_RX = L_XB + 8192;
constexpr int L_R = L_M + 65536;
constexpr int L_TOP = L_M + 131072 + 24576;
constexpr int L_KT = L_TOP + 16;
constexpr int L_STAT = L_TOP;
constexpr int L_ETA = L_RX + 12288;
constexpr int L_XR = L_M;
constexpr int L_SP = L_M + 131072;
constexpr int LDS_BYTES = 163840;
static_assert(L_TOP + 4096 <= LDS_BYTES && L_ETA + 16 <= L_R && L_UVS + 2 * UVS_CH <= L_R && L_SP + 24576 <= L_TOP, "LDS map");
static_assert(L_H % 16 == 0 && L_XB % 16 == 0 && L_RX % 16 == 0 && L_KT % 16 == 0, "alignment");

#define ZLAG 30
#define MTK 124
constexpr int EO = 30;
constexpr int CODE_BYTES = 36096;
constexpr int SC1 = 16;
constexpr int OOB = 0x7FFFFF00;
constexpr unsigned SPIN_CAP = 1u << 15;
constexpr unsigned POISON = 0xFFFFFFFFu;
constexpr float LOG2E = 1.4426950408889634f, LN2 = 0.6931471805599453f, INV2PI = 0.15915494309189535f;

#define SB() __builtin_amdgcn_sched_barrier(0)
#define WAITV(n) asm volatile("s_waitcnt vmcnt(" #n ")" ::: "memory")
#define PIN(x) asm volatile("" : "+v"(x))
#define FRESH() asm volatile("" ::: "memory")
#define PAUSE() asm volatile("s_sleep 4" ::: "memory")
#define BAR() do { asm volatile("s_waitcnt lgkmcnt(0)" ::: "memory"); __builtin_amdgcn_s_barrier(); asm volatile("" ::: "memory"); } while (0)
#define STAMP(i)
#define SARG
#define SPASS
#define COUNT(i)

#define STAMPA(i)
#define STAMPB(i) STAMP(i)
DI float u2f(unsigned u) { return __builtin_bit_cast(float, u); }
DI unsigned f2u(float f) { return __builtin_bit_cast(unsigned, f); }
DI float dot2(unsigned u, float c) { const bf16x2 v = __builtin_bit_cast(bf16x2, u); return __builtin_amdgcn_fdot2_f32_bf16(v, v, c, false); }
DI float bf_lo(unsigned u) { return __builtin_bit_cast(float, u << 16); }
DI float bf_hi(unsigned u) { return __builtin_bit_cast(float, u & 0xFFFF0000u); }
DI unsigned pack2(float a, float b) { f32x2 v = {a, b}; return __builtin_bit_cast(unsigned, __builtin_convertvector(v, bf16x2)); }
DI float rbf(float a) { return bf_lo(pack2(a, a)); }
DI unsigned sel(bool c, unsigned a, unsigned b) { asm("" : "+v"(a), "+v"(b)); return c ? a : b; }
DI unsigned guard(unsigned u) { return u < 0xFFFFFFFEu ? u : 0xFFFFFFFEu; }
DI unsigned max4(unsigned m, v4u v) {
  asm("v_max3_u32 %0, %0, %1, %2\n\tv_max3_u32 %0, %0, %3, %4" : "+v"(m) : "v"(v.x), "v"(v.y), "v"(v.z), "v"(v.w));
  return m;
}
DI f32x4 mfma16(v4u a, v4u b, f32x4 c) { return __builtin_amdgcn_mfma_f32_16x16x32_bf16(__builtin_bit_cast(bf16x8, a), __builtin_bit_cast(bf16x8, b), c, 0, 0, 0); }
DI f32x16 mfma32(v4u a, v4u b, f32x16 c) { return __builtin_amdgcn_mfma_f32_32x32x16_bf16(__builtin_bit_cast(bf16x8, a), __builtin_bit_cast(bf16x8, b), c, 0, 0, 0); }
template <bool FIRST> DI void mfma_av(f32x16& acc, const v4u& a, const v4u& b) {
  if (FIRST) asm volatile("v_mfma_f32_32x32x16_bf16 %0, %1, %2, 0" : "=&v"(acc) : "a"(a), "v"(b));
  else asm volatile("v_mfma_f32_32x32x16_bf16 %0, %1, %2, %0" : "+v"(acc) : "a"(a), "v"(b));
}
template <bool FIRST> DI void mfma_va(f32x16& acc, const v4u& a, const v4u& b) {
  if (FIRST) asm volatile("v_mfma_f32_32x32x16_bf16 %0, %1, %2, 0" : "=&v"(acc) : "v"(a), "a"(b));
  else asm volatile("v_mfma_f32_32x32x16_bf16 %0, %1, %2, %0" : "+v"(acc) : "v"(a), "a"(b));
}
template <bool FIRST> DI void mfma16_v(f32x4& acc, const v4u& a, const v4u& b) {
  if (FIRST) asm volatile("v_mfma_f32_16x16x32_bf16 %0, %1, %2, 0" : "=&v"(acc) : "v"(a), "v"(b));
  else asm volatile("v_mfma_f32_16x16x32_bf16 %0, %1, %2, %0" : "+v"(acc) : "v"(a), "v"(b));
}
DI void mfma_done(f32x16& acc) { asm volatile("s_nop 15" : "+v"(acc)); }
DI v2u trd(LAS unsigned char* p) { return __builtin_bit_cast(v2u, __builtin_amdgcn_ds_read_tr16_b64_v4i16((LAS s16x4*)p)); }
DI v4u trd2(LAS unsigned char* p0, LAS unsigned char* p1) { const v2u a = trd(p0), b = trd(p1); return v4u{a.x, a.y, b.x, b.y}; }
DI v4u lds128(LAS unsigned char* p) { return *(LAS v4u*)p; }
DI v4u ldg(__amdgpu_buffer_rsrc_t r, int voff, int soff) { return __builtin_amdgcn_raw_buffer_load_b128(r, voff, soff, SC1); }
DI void stg(__amdgpu_buffer_rsrc_t r, int voff, int soff, v4u v) {
  __builtin_amdgcn_raw_buffer_store_b128(v, r, voff, soff, SC1);
  asm volatile("s_nop 1" :: "v"(v) : "memory");
}
template <int CTRL> DI float dpp_add(float v) {
  return v + __builtin_bit_cast(float, __builtin_amdgcn_update_dpp(0, __builtin_bit_cast(int, v), CTRL, 0xF, 0xF, true));
}
DI float swap16_add(float v) { const unsigned u = __builtin_bit_cast(unsigned, v); const auto r = __builtin_amdgcn_permlane16_swap(u, u, false, false);
  return u2f(r[0]) + u2f(r[1]); }
DI float swap32_add(float v) { const unsigned u = __builtin_bit_cast(unsigned, v); const auto r = __builtin_amdgcn_permlane32_swap(u, u, false, false);
  return u2f(r[0]) + u2f(r[1]); }
DI float quad_sum(float v) { v = dpp_add<0xB1>(v); return dpp_add<0x4E>(v); }
DI float row_sum(float v) { return dpp_add<0x140>(dpp_add<0x141>(quad_sum(v))); }
DI float wave_sum(float v) { return swap32_add(swap16_add(row_sum(v))); }
DI f32x2 cmul(f32x2 a, f32x2 e, f32x2 ep) { const f32x2 t = f32x2{a.x, a.x} * e; return __builtin_elementwise_fma(f32x2{a.y, a.y}, ep, t); }
DI f32x2 cexp(float re2, float rev) {
  const float m = __builtin_amdgcn_exp2f(re2), f = __builtin_amdgcn_fractf(rev);
  return f32x2{m * __builtin_amdgcn_cosf(f), m * __builtin_amdgcn_sinf(f)};
}
DI void dma_w2(const unsigned char* sbase, unsigned v0, unsigned v1, unsigned lds_dst) {
  asm volatile("s_mov_b32 m0, %3\n\ts_nop 0\n\tglobal_load_lds_dwordx4 %0, %2\n\tglobal_load_lds_dwordx4 %1, %2 offset:1024"
               :: "v"(v0), "v"(v1), "s"(sbase), "s"(lds_dst) : "memory");
}
DI void dma_w1(const unsigned char* sbase, unsigned v, unsigned lds_dst) {
  asm volatile("s_mov_b32 m0, %2\n\ts_nop 0\n\tglobal_load_lds_dwordx4 %0, %1" :: "v"(v), "s"(sbase), "s"(lds_dst) : "memory");
}
DI void dma_x1(const unsigned char* sbase, unsigned v, unsigned lds_dst) {
  asm volatile("s_mov_b32 m0, %2\n\ts_nop 0\n\tglobal_load_lds_dwordx4 %0, %1 sc1" :: "v"(v), "s"(sbase), "s"(lds_dst) : "memory");
}
DI void set_m0(unsigned lds_dst) { asm volatile("s_mov_b32 m0, %0\n\ts_nop 0" :: "s"(lds_dst) : "memory"); }
template <int OFF> DI void dma_xn(const unsigned char* sbase, unsigned v) {
  asm volatile("global_load_lds_dwordx4 %0, %1 offset:%2 sc1" :: "v"(v), "s"(sbase), "n"(OFF) : "memory");
}
DI void dma_x2(const unsigned char* sbase, unsigned v, unsigned lds_dst) {
  asm volatile("s_mov_b32 m0, %2\n\ts_nop 0\n\tglobal_load_lds_dwordx4 %0, %1 sc1\n\tglobal_load_lds_dwordx4 %0, %1 offset:1024 sc1"
               :: "v"(v), "s"(sbase), "s"(lds_dst) : "memory");
}
DI void dma_x4(const unsigned char* sbase, unsigned v, unsigned lds_dst) {
  asm volatile("s_mov_b32 m0, %2\n\ts_nop 0\n\tglobal_load_lds_dwordx4 %0, %1 sc1\n\tglobal_load_lds_dwordx4 %0, %1 offset:1024 sc1\n\t"
               "global_load_lds_dwordx4 %0, %1 offset:2048 sc1\n\tglobal_load_lds_dwordx4 %0, %1 offset:3072 sc1"
               :: "v"(v), "s"(sbase), "s"(lds_dst) : "memory");
}
template <int I> struct IC { static constexpr int v = I; };
template <int I, int N, class F> DI void static_for(F&& f) { if constexpr (I < N) { f(IC<I>{}); static_for<I + 1, N>(f); } }
DI unsigned now() { u64 t; asm volatile("s_memrealtime %0\n\ts_waitcnt lgkmcnt(0)" : "=s"(t) :: "memory"); return (unsigned)t; }
template <int N> DI void waitv() { asm volatile("s_waitcnt vmcnt(%0)" :: "n"(N) : "memory"); }
struct ConvOrder { int t[36], u[36]; };
constexpr ConvOrder conv_order() {
  ConvOrder o = {};
  int done[8] = {}, last = -1;
  for (int k = 0; k < 36; ++k) {
    int best = -1;
    for (int t = 0; t < 8; ++t)
      if (t != last && done[t] <= t && (best < 0 || t - done[t] > best - done[best])) best = t;
    o.t[k] = best; o.u[k] = done[best]++; last = best;
  }
  return o;
}
constexpr ConvOrder conv_order_last() {
  ConvOrder o = {};
  int k = 0;
  for (int p = 0; p < 4; ++p)
    for (int u = 0; u <= 2 * p + 1; ++u) {
      o.t[k] = 2 * p + 1; o.u[k] = u; ++k;
      if (u <= 2 * p) { o.t[k] = 2 * p; o.u[k] = u; ++k; }
    }
  return o;
}
constexpr int epi_slot(int v) { return v < 32 ? 2 * EO + v : 2 * 50 - 1 + (v - 32); }
constexpr bool epi_pair(int j) { return (j >= EO && j < EO + 19) || (j >= 50 && j < 61); }
constexpr int hc_upto(int j) { return j < 52 ? 0 : j - 52; }
constexpr int hl_upto(int i) { return i < 9 ? 28 : i < 37 ? 28 + (i - 9) : 56; }
constexpr int c_ops(int j) {
  return (j >= 8 && j < 12 ? 2 : j < 34 || (j >= 35 && j < 51) ? 1 : 0) + hc_upto(j + 1) - hc_upto(j) +
         (j == EO + 6 || j == EO + 14 || j == 56 ? 1 : j == EO + 10 || j == EO + 18 || j == 60 ? 2 : 0);
}
constexpr int c_between(int a, int b) { int n = 0; for (int j = a + 1; j < b; ++j) n += c_ops(j); return n; }
DI unsigned tagbad(unsigned bad, v4u g, unsigned tagA, unsigned tagB) {
  return __builtin_amdgcn_bitop3_b32(__builtin_amdgcn_bitop3_b32(bad, g.y, tagA, 0xF6), g.w, tagB, 0xF6);
}
template <int R0, int R1, int R2, int R3> DI void put4(unsigned base, f32x2 a, f32x2 b, f32x2 c, f32x2 d) {
  asm volatile("s_mov_b32 m0, %4\n\ts_nop 0\n\tds_write_addtid_b32 %0 offset:%5\n\tds_write_addtid_b32 %1 offset:%6\n\t"
               "ds_write_addtid_b32 %2 offset:%7\n\tds_write_addtid_b32 %3 offset:%8"
               :: "v"(pack2(a.x, a.y)), "v"(pack2(b.x, b.y)), "v"(pack2(c.x, c.y)), "v"(pack2(d.x, d.y)), "s"(base),
                  "n"(R0 * UVS_ROW), "n"(R1 * UVS_ROW), "n"(R2 * UVS_ROW), "n"(R3 * UVS_ROW) : "memory");
}
DI unsigned sum4(unsigned m, v4u v) {
  asm("v_add3_u32 %0, %0, %1, %2\n\tv_add3_u32 %0, %0, %3, %4" : "+v"(m) : "v"(v.x), "v"(v.y), "v"(v.z), "v"(v.w));
  return m;
}
DI unsigned sum8(unsigned m, v4u a, v4u b) {
  asm("v_add3_u32 %0, %0, %1, %2\n\tv_add3_u32 %0, %0, %3, %4\n\tv_add3_u32 %0, %0, %5, %6\n\tv_add3_u32 %0, %0, %7, %8"
      : "+v"(m) : "v"(a.x), "v"(a.y), "v"(a.z), "v"(a.w), "v"(b.x), "v"(b.y), "v"(b.z), "v"(b.w));
  return m;
}
template <int I> DI unsigned red_step(unsigned v) {
  constexpr int ctrl[6] = {0xB1, 0x4E, 0x141, 0x140, 0x142, 0x143}, rows[6] = {0xF, 0xF, 0xF, 0xF, 0xA, 0xC};
  return v + (unsigned)__builtin_amdgcn_update_dpp(0, (int)v, ctrl[I], rows[I], 0xF, true);
}
DI unsigned red_all(unsigned v) { return red_step<5>(red_step<4>(red_step<3>(red_step<2>(red_step<1>(red_step<0>(v)))))); }
DI void swap_add32(float& a, float b) { const auto r = __builtin_amdgcn_permlane32_swap(f2u(a), f2u(b), false, false); a = u2f(r[0]) + u2f(r[1]); }
DI void swap_add16(float& a, float b) { const auto r = __builtin_amdgcn_permlane16_swap(f2u(a), f2u(b), false, false); a = u2f(r[0]) + u2f(r[1]); }

extern "C" __device__ u64 dispatch_id() __asm("llvm.amdgcn.dispatch.id");
typedef const unsigned char* cptr;
typedef unsigned v16u __attribute__((ext_vector_type(16)));
DI cptr mkptr(unsigned lo, unsigned hi) { return (cptr)(((u64)hi << 32) | lo); }

__global__ void __launch_bounds__(256, 1)
s4_block_kernel(cptr, cptr, cptr, cptr, cptr, cptr, cptr, cptr, cptr, cptr, cptr, cptr, cptr, unsigned char*, unsigned char*, cptr) {
  STAMP(0);
  v16u ka0, ka1;
  asm volatile("s_load_dwordx16 %0, %2, 0x0\n\ts_load_dwordx16 %1, %2, 0x40" : "=&s"(ka0), "=&s"(ka1) : "s"(__builtin_amdgcn_kernarg_segment_ptr()));
  SB();
  LAS unsigned char* const L = (LAS unsigned char*)16u - 16;
  const int tid = threadIdx.x, lane = tid & 63, w = __builtin_amdgcn_readfirstlane(tid >> 6);
  const int b = blockIdx.x, xcd = b & 7, slot = b >> 3, id = xcd * 32 + slot;
  const int ks = (id >> 6) * 4 + w, cc = id & 63;
  const int rp = 4 * (xcd >> 1) + (slot >> 3), rg = xcd & 1, dt = slot & 7, si = 8 * rg + dt;
  const int ct = 7 * rg + min(dt, 6);
  const int rot = (0x0340 >> (4 * (2 * (w & 1) + rg))) & 7;

  const unsigned char* pcode;
  asm volatile("s_getpc_b64 %0" : "=s"(pcode));
  const unsigned warm = *(const GAS unsigned*)((const GAS unsigned char*)pcode + (unsigned)min((((slot & 7) * 4 + w) * 9 + min(lane, 8)) * 128, CODE_BYTES));
  const int g16 = lane >> 4, q4 = (lane & 15) >> 2, p4 = lane & 3, hf = lane >> 5, blk = (lane >> 4) & 1, l16 = lane * 16;
  const unsigned lrw = L_R + w * 16384, lsp = L_SP + w * 6144;
#define SLOT(n) ((n) < 16 ? lrw + (n) * 1024 : lsp + ((n) - 16) * 1024)
  unsigned wmo[3], co = (ks * 32 + (lane & 31)) * 2, xo = (id >> 1) * 16384 + (id & 1) * 512 + (tid & 7) * 2048 + (tid >> 3) * 16, ro = id * 8192 + w * 2048 + hf * 1024 + (lane & 31) * 16, po = id * 512 + tid * 2;
  unsigned wto = id * 7168 + (w >> 1) * 3584 + min(lane, 27) * 128;
#pragma unroll
  for (int i = 0; i < 3; ++i) { const unsigned idx = lane + 64 * i; wmo[i] = (ks * 32 * 6144 + cc * 96 - i * 1024) + idx / 6 * (6144 - 96) + idx * 16; PIN(wmo[i]); }
  PIN(co); PIN(xo); PIN(ro); PIN(po); PIN(wto);
  set_m0(lrw);
  SB();
  unsigned w2v0 = (lane >> 3) * 1024 + (((lane & 7) ^ (blk << 2)) * 16), w2v1 = w2v0 + 7168;
  unsigned w1v0 = (lane >> 2) * 3584 + p4 * 16, w1v1 = w1v0 + 57344 - 1024;
  const int zs = OFF_Z + (2 * id + (w >> 1)) * 64;
  int za = (((w & 1) + 2 * (g16 & 1)) * 8 + q4 * 2 + (g16 >> 1)) * 32768 + p4 * 16, zb = za + 32 * 32768;
  int tra = (hf * 8 + q4) * 64 + blk * 32 + p4 * 8;
  LAS unsigned char *w1a = L + lrw + tra, *w1s = L + lsp + tra;
#define W1PULL(step, n) w1f[step] = trd2(((n) < 16 ? w1a : w1s) + ((n) & 15) * 1024, ((n) < 16 ? w1a : w1s) + ((n) & 15) * 1024 + 256)
  LAS unsigned char* ka = L + L_KC + ((w >> 1) * 4 + q4) * KC_STRIDE + 2 * (32 - 8 * g16 + 4 * p4) - 8 + 256 * (w & 1);
  LAS unsigned char* hb = L + L_H + (w >> 1) * H_STRIDE + (lane & 15) * 32 + g16 * 16 + 256 - 256 * (w & 1);
  LAS unsigned char* zp = L + sel(tid < 64, L_KC + ((lane >> 1) & 7) * KC_STRIDE + (lane & 1) * 16, L_H + hf * H_STRIDE + min(lane & 31, 29) * 16);
  LAS unsigned char* ub = L + L_UVS + (lane & 15) * UVS_ROW + g16 * 16;
  int gmo = OFF_MG + (g16 * 256 + id) * 256 + (lane & 15) * 16, gmo2 = OFF_MG + ((4 + blk) * 256 + id) * 256 + (lane & 15) * 16;
  int mgo = sel(lane < 48, (lane >> 1) * 256 + (lane & 1) * 8, OOB), sgo = sel(lane & 31, OOB, hf * 8);
  LAS unsigned char* xbw = L + L_XB + (tid >> 7) * 2048 + (tid & 127) * 4;
  LAS unsigned char* xbr = L + L_XB + w * 2048 + (lane & 15) * 32 + g16 * 8;
  LAS unsigned char *exa = L + L_XB + w * 2048 + l16, *exb = L + L_RX + w * 3072 + l16, *eya = L + L_XB + (w ^ 1) * 2048 + l16, *eyb = L + L_RX + (w ^ 1) * 3072 + l16;
  PIN(xbw); PIN(xbr); PIN(exa); PIN(exb); PIN(eya); PIN(eyb);
  int mto = (tid >> 1) * 128 + (tid & 1) * 16;
  LAS unsigned char* mtw = L + (tid & 1) * L_TG + (tid >> 1) * 4;
  PIN(mto); PIN(mtw);
  PIN(w2v0); PIN(w2v1); PIN(w1v0); PIN(w1v1); PIN(za); PIN(zb); PIN(tra); PIN(w1a); PIN(w1s); PIN(ka); PIN(hb); PIN(zp); PIN(ub); PIN(gmo); PIN(gmo2); PIN(mgo); PIN(sgo);
#define W1DMA(step, lds) dma_w1(w1w + (step) * 57344, w1v0, (lds))
#define W1DMA2(step, lds) dma_w2(w1w + (step) * 57344, w1v0, w1v1, (lds))
#define W2SRC(step) (((step) < 12 ? w2lo : (step) < 16 ? w2c3 : w2hi) + (step) * 16384)
#define W2DMA(step, lds) dma_w2(W2SRC(step), w2v0, w2v1, (lds))
#define W2HALF(n, lds) do { if constexpr ((n) & 1) asm volatile("global_load_lds_dwordx4 %0, %1 offset:1024" :: "v"(w2v1), "s"(W2SRC((n) >> 1)) : "memory"); \
    else dma_w1(W2SRC((n) >> 1), w2v0, (lds)); } while (0)
  SB();
  STAMPA(24);
  asm volatile("s_waitcnt lgkmcnt(0)" : "+s"(ka0), "+s"(ka1));
  STAMPA(25);
  const cptr xin = mkptr(ka0[0], ka0[1]), pAre = mkptr(ka0[2], ka0[3]), pAim = mkptr(ka0[4], ka0[5]), pBre = mkptr(ka0[6], ka0[7]);
  const cptr pBim = mkptr(ka0[8], ka0[9]), pCre = mkptr(ka0[10], ka0[11]), pCim = mkptr(ka0[12], ka0[13]), pldt = mkptr(ka0[14], ka0[15]);
  const cptr pD = mkptr(ka1[0], ka1[1]), w1 = mkptr(ka1[2], ka1[3]), w2 = mkptr(ka1[4], ka1[5]), pc = mkptr(ka1[6], ka1[7]);
  const cptr wmod = mkptr(ka1[8], ka1[9]), out = mkptr(ka1[10], ka1[11]), ws = mkptr(ka1[12], ka1[13]);
  asm volatile("global_load_lds_dwordx4 %0, %3\n\tglobal_load_lds_dwordx4 %1, %3 offset:1024\n\tglobal_load_lds_dwordx4 %2, %3 offset:2048"
               :: "v"(wmo[0]), "v"(wmo[1]), "v"(wmo[2]), "s"(wmod) : "memory");
  const unsigned cval = *(const GAS u16*)((const GAS unsigned char*)pc + co);
  SB();
  const v4u xa = *(const GAS v4u*)((const GAS unsigned char*)xin + xo), xb = *(const GAS v4u*)((const GAS unsigned char*)xin + 1024 + xo);
  const v4u ra = *(const GAS v4u*)((const GAS unsigned char*)xin + ro), rb = *(const GAS v4u*)((const GAS unsigned char*)xin + 512 + ro);
  SB();
#define PARAM(p) (unsigned)*(const GAS u16*)((const GAS unsigned char*)(p) + po)
  const unsigned uAre = PARAM(pAre), uAim = PARAM(pAim), uBre = PARAM(pBre), uBim = PARAM(pBim), uCre = PARAM(pCre), uCim = PARAM(pCim);
#undef PARAM
  SB();
  const unsigned char* setp = ws + SET_BASE;
  const __amdgpu_buffer_rsrc_t rs = __builtin_amdgcn_make_buffer_rsrc((void*)setp, 0, SET_BYTES, 0x00020000);
  unsigned uldt, uD;
  asm volatile("s_load_dword %0, %2, %4\n\ts_load_dword %1, %3, %4" : "=&s"(uldt), "=&s"(uD) : "s"(pldt), "s"(pD), "s"(id * 4));
  SB();
  STAMPA(26);
  LAS unsigned char* koff[12];
  {
    float fj = (float)(8 * tid - 3);
    PIN(fj);
    const unsigned lin = (unsigned)(8 * tid - 3 + 0x4300 - 128) * 0x10001u + 0x10000u;
#pragma unroll
    for (int i = 0; i < 12; i += 2) {
      const unsigned r = sel(tid < 32, lin + i * 0x10001u, pack2(fj + (float)i, fj + (float)(i + 1)));
      koff[i] = L + ((r & 0xFFFFu) * 4 + (L_KT + (128 - 0x4300) * 4));
      koff[i + 1] = L + ((r >> 16) * 4 + (L_KT + (128 - 0x4300) * 4));
      PIN(koff[i]);
      if (i < 10) PIN(koff[i + 1]);
    }
  }
  *(LAS v4u*)(L + L_KT - 16) = v4u{0u, 0u, 0u, 0u};
  LAS unsigned char* ktw[4];
  {
    const int row = w * 16 + (lane & 15), sg = max((row >> 3) - 1, 0), rr_ = row - 8 * sg;
    const int idx0 = 128 * sg + ((rr_ >> (3 - sg)) << (7 - sg)) + (rr_ & ((8 >> sg) - 1)) + 1, st = 8 >> sg;
#pragma unroll
    for (int r = 0; r < 4; ++r) {
      const int idx = sel(row < 40, idx0 + st * (4 * g16 + r), tid == 136 && r == 0 ? 0 : 641 + lane);
      ktw[r] = L + L_KT + idx * 4;
      PIN(ktw[r]);
    }
  }
  const unsigned char* w2lo = w2 + (448 * w + 64 * rot) * 1024 + dt * 128;
  const unsigned char* w2c3 = w2lo - (rot >> 2) * (448 * 1024);
  const unsigned char* w2hi = w2lo - ((rot + 1) >> 2) * (448 * 1024);
  const unsigned char* w1w = w1 + ct * 256 + w * 64;
  SB();
  STAMPA(27);
  asm volatile("s_waitcnt lgkmcnt(0)" : "+s"(uldt), "+s"(uD));
  STAMPA(28);
  unsigned tagA = ((unsigned)dispatch_id() + (unsigned)((u64)__builtin_amdgcn_dispatch_ptr() >> 6) * 0x85EBCA77u) * 0x9E3779B1u + 0x7F4A7C15u, tagB = ~tagA;
  unsigned pmod, pstat;
  {
    const float c = bf_lo(cval);
    const float s = c * __builtin_amdgcn_rcpf(1.0f + __builtin_amdgcn_exp2f(-LOG2E * c));
    *(LAS u16*)(L + lrw + 3072 + (lane & 31) * 2) = (u16)pack2(s, s);
    SB();
#define XPUB(i) do { stg(rs, (tid >> 3) * 65536 + (tid & 7) * 16, OFF_XG + ((id & 1) * 128 + (i)) * 16384 + (id >> 1) * 128, v4u{xa[i], tagA, xb[i], tagB}); SB(); } while (0)
    asm volatile("" :: "v"(xb));
    STAMPA(29);
    XPUB(0);
    XPUB(1);
    STAMP(1);
    LAS unsigned char* a = L + lrw + (8 * g16 + q4) * 96 + p4 * 8;
    const v4u sc = lds128(L + lrw + 3072 + g16 * 16);
    const v4u m0 = trd2(a, a + 384), m1 = trd2(a + 32, a + 416), m2 = trd2(a + 64, a + 448);
    SB();
    f32x2 fa[8];
#pragma unroll
    for (int i = 0; i < 4; ++i) { fa[i] = f32x2{bf_lo(ra[i]), bf_hi(ra[i])}; fa[4 + i] = f32x2{bf_lo(rb[i]), bf_hi(rb[i])}; }
    const f32x2 s2 = ((fa[0] + fa[1]) + (fa[2] + fa[3])) + ((fa[4] + fa[5]) + (fa[6] + fa[7]));
    float mean = s2.x + s2.y;
    PIN(mean);
    SB();
    const f32x4 c0 = mfma16(sc, m0, f32x4{}), c1 = mfma16(sc, m1, f32x4{}), c2 = mfma16(sc, m2, f32x4{});
    SB();
    mean = swap16_add(row_sum(mean)) * (1.0f / 512.0f);
    PIN(mean);
    SB();
    XPUB(2);
    pmod = sel(lane < 16, f2u(c0[0]), sel(lane < 32, f2u(c1[0]), f2u(c2[0])));
#define MPUB() __builtin_amdgcn_raw_buffer_store_b64(v2u{pmod, (lane & 1) ? tagB : tagA}, rs, mgo, OFF_MG + cc * 24 * 256 + ks * 16, SC1)
    MPUB();
    SB();
    f32x2 q2 = {0.f, 0.f}, q3 = {0.f, 0.f};
#pragma unroll
    for (int i = 0; i < 8; i += 2) {
      const f32x2 d0 = fa[i] - f32x2{mean, mean}, d1 = fa[i + 1] - f32x2{mean, mean};
      q2 = __builtin_elementwise_fma(d0, d0, q2);
      q3 = __builtin_elementwise_fma(d1, d1, q3);
    }
    q2 += q3;
    const float var = rbf(rbf(swap16_add(row_sum(q2.x + q2.y)) * (1.0f / 512.0f)) + 1e-5f);
    pstat = pack2(mean, __builtin_amdgcn_sqrtf(var));
#define SPUB() __builtin_amdgcn_raw_buffer_store_b64(v2u{pstat, hf ? tagB : tagA}, rs, sgo, OFF_SG + (id * 4 + w) * 16, SC1)
    SPUB();
    SB();
    XPUB(3);
  }
  unsigned wt, lastv;
  SB();
  STAMP(2);
  unsigned dsk = tid == 136 ? uD : 0u;
  PIN(dsk);

  {
    const float dtv = rbf(__builtin_amdgcn_exp2f(LOG2E * (w >> 1 ? bf_hi(uldt) : bf_lo(uldt))));
    const float are = dtv * bf_lo(uAre) * LOG2E, aim = dtv * bf_lo(uAim) * INV2PI;
    const float br = dtv * bf_lo(uBre), bi = dtv * bf_lo(uBim), cr = bf_lo(uCre), ci = bf_lo(uCim);
    const f32x2 wv = {cr * br - ci * bi, cr * bi + ci * br};
    const unsigned row = L_UVS + (w >> 1) * UVS_CH + (w & 1) * 256;
    const f32x2 e1 = cexp(are, aim), e8 = cexp(8.f * are, -8.f * aim), e128 = cexp(128.f * are, 128.f * aim);
    const f32x2 e1p = {-e1.y, e1.x}, e8p = {-e8.y, e8.x}, e128p = {-e128.y, e128.x};
    const v4u pois = {POISON, POISON, POISON, POISON};
    f32x2 z[8], v, v2, y, y2;
#define M1(a) cmul(a, e1, e1p)
#define M8(a) cmul(a, e8, e8p)
#define MK(a) cmul(a, e128, e128p)
    z[0] = M1(wv);
    put4<40, 41, 42, 0>(row, wv, f32x2{1.f, 0.f}, e8, z[0]);
    stg(rs, (tid >> 3) * 32768 + (tid & 7) * 16, OFF_Z + id * 128, pois);
    v = M8(e8); z[1] = M1(z[0]); v2 = M8(v); z[2] = M1(z[1]);
    put4<43, 1, 44, 2>(row, v, z[1], v2, z[2]);
    v = M8(v2); z[3] = M1(z[2]); v2 = M8(v); z[4] = M1(z[3]);
    put4<45, 3, 46, 4>(row, v, z[3], v2, z[4]);
    stg(rs, (tid >> 3) * 32768 + (tid & 7) * 16, OFF_Z + id * 128 + 32 * 32768, pois);
    v = M8(v2); z[5] = M1(z[4]); v2 = M8(v); z[6] = M1(z[5]);
    put4<47, 5, 48, 6>(row, v, z[5], v2, z[6]);
    asm volatile("global_load_dword %0, %1, %2" : "=v"(wt) : "v"(wto), "s"(w1 + (w & 1) * (w2 - w1)), "v"(warm) : "memory");
    asm volatile("global_load_dword %0, %1, %2 sc1" : "=v"(lastv) : "v"(0u), "s"(ws) : "memory");
    z[7] = M1(z[6]); z[0] = MK(z[0]); z[1] = MK(z[1]); z[2] = MK(z[2]);
    put4<7, 8, 9, 10>(row, z[7], z[0], z[1], z[2]);
    z[3] = MK(z[3]); z[4] = MK(z[4]); z[5] = MK(z[5]); z[6] = MK(z[6]);
    put4<11, 12, 13, 14>(row, z[3], z[4], z[5], z[6]);
    z[7] = MK(z[7]); z[1] = MK(z[1]); z[3] = MK(z[3]); z[5] = MK(z[5]);
    put4<15, 16, 17, 18>(row, z[7], z[1], z[3], z[5]);
    W1DMA2(0, lrw);
    z[7] = MK(z[7]); z[1] = MK(z[1]); z[3] = MK(z[3]); z[5] = MK(z[5]);
    put4<19, 20, 21, 22>(row, z[7], z[1], z[3], z[5]);
    y = MK(z[7]); z[3] = MK(z[3]); y2 = MK(y); z[4] = MK(z[3]);
    put4<23, 24, 25, 26>(row, y, z[3], y2, z[4]);
    W1DMA2(2, lrw + 2048);
    y = MK(y2); z[3] = MK(z[4]); y2 = MK(y); z[4] = MK(z[3]);
    put4<27, 28, 29, 30>(row, y, z[3], y2, z[4]);
    y = MK(y2); v = M8(v2); y2 = MK(y); v2 = M8(v);
    put4<31, 49, 32, 50>(row, y, v, y2, v2);
    y = MK(y2); v = M8(v2); y2 = MK(y); v2 = M8(v);
    put4<33, 51, 34, 52>(row, y, v, y2, v2);
    W1DMA2(4, lrw + 4096);
    y = MK(y2); v = M8(v2); y2 = MK(y); v2 = M8(v);
    put4<35, 53, 36, 54>(row, y, v, y2, v2);
    y = MK(y2); v = M8(v2); y2 = MK(y); v2 = M8(v);
    put4<37, 55, 38, 56>(row, y, v, y2, v2);
    W1DMA2(6, lrw + 6144);
    y = MK(y2);
    *(LAS unsigned*)(L + row + 39 * UVS_ROW + lane * 4) = pack2(y.x, y.y);
#undef M1
#undef M8
#undef MK
  }
  BAR();
  STAMP(3);
  v4u gx[4], gs[4], gm, gn;
  const int xgo = OFF_XG + id * 16384;
  if (w < 3) {
    LAS unsigned char* ut = ub + w * 16 * UVS_ROW;
    v4u fv[2][8], fu[2][8];
#pragma unroll
    for (int s = 0; s < 8; ++s)
#pragma unroll
      for (int c = 0; c < 2; ++c) { fv[c][s] = lds128(ub + c * UVS_CH + 41 * UVS_ROW + s * 64); fu[c][s] = lds128(ut + c * UVS_CH + s * 64); SB(); }
    f32x4 a0 = {}, a1 = {};
#pragma unroll
    for (int s = 0; s < 8; ++s) { a0 = mfma16(fv[0][s], fu[0][s], a0); SB(); a1 = mfma16(fv[1][s], fu[1][s], a1); SB(); }
    *(LAS unsigned*)ktw[0] = pack2(a0[0] + bf_lo(dsk), a1[0] + bf_hi(dsk));
#pragma unroll
    for (int r = 1; r < 4; ++r) *(LAS unsigned*)ktw[r] = pack2(a0[r], a1[r]);
  }
  SB();
#define LD1(i, so) "buffer_load_dwordx4 %" #i ", %10, %13, " so " offen sc1\n\t"
  asm volatile(LD1(0, "%14") LD1(1, "%15") LD1(2, "%16") LD1(3, "%17") LD1(4, "%18") LD1(5, "%19") LD1(6, "%20") LD1(7, "%21")
               "buffer_load_dwordx4 %8, %11, %13, 0 offen sc1\n\tbuffer_load_dwordx4 %9, %12, %13, 0 offen sc1"
               : "=&v"(gx[0]), "=&v"(gx[1]), "=&v"(gx[2]), "=&v"(gx[3]), "=&v"(gs[0]), "=&v"(gs[1]), "=&v"(gs[2]), "=&v"(gs[3]), "=&v"(gm), "=&v"(gn)
               : "v"(tid * 16), "v"(gmo), "v"(gmo2), "s"(rs), "s"(xgo), "s"(xgo + 4096), "s"(xgo + 8192), "s"(xgo + 12288),
                 "s"(OFF_SG), "s"(OFF_SG + 4096), "s"(OFF_SG + 8192), "s"(OFF_SG + 12288) : "memory");
#undef LD1
  SB();
  BAR();
  {
    unsigned k[11];
#pragma unroll
    for (int i = 0; i < 11; ++i) k[i] = *(LAS unsigned*)koff[i];
    SB();
    if (w < 2) *(LAS v4u*)zp = v4u{0u, 0u, 0u, 0u};
#pragma unroll
    for (int c = 0; c < 2; ++c) {
      unsigned e[5], o[5];
#pragma unroll
      for (int i = 0; i < 5; ++i) {
        o[i] = __builtin_amdgcn_perm(k[2 * i + 1], k[2 * i], c ? 0x07060302u : 0x05040100u);
        e[i] = __builtin_amdgcn_perm(k[2 * i + 2], k[2 * i + 1], c ? 0x07060302u : 0x05040100u);
      }
      LAS unsigned char* d = L + L_KC + c * 4 * KC_STRIDE + 32 + tid * 16;
      *(LAS v4u*)(d) = v4u{e[1], e[2], e[3], e[4]};
      *(LAS v4u*)(d + KC_STRIDE) = v4u{o[1], o[2], o[3], o[4]};
      *(LAS v4u*)(d + 2 * KC_STRIDE) = v4u{e[0], e[1], e[2], e[3]};
      *(LAS v4u*)(d + 3 * KC_STRIDE) = v4u{o[0], o[1], o[2], o[3]};
    }
  }
  SB();
  STAMP(4);
  {
    asm volatile("s_waitcnt vmcnt(0)" : "+v"(gx[0]), "+v"(gx[1]), "+v"(gx[2]), "+v"(gx[3]), "+v"(gs[0]), "+v"(gs[1]), "+v"(gs[2]), "+v"(gs[3]), "+v"(gm), "+v"(gn), "+v"(wt), "+v"(lastv) :: "memory");
    if (__builtin_expect((unsigned)__builtin_amdgcn_readfirstlane(lastv) == tagA, 0)) {
      tagA += 0x632BE5ABu;
      tagB = ~tagA;
      XPUB(0); XPUB(1); XPUB(2); XPUB(3);
      MPUB();
      SPUB();
    }
    unsigned bad = tagbad(tagbad(0u, gm, tagA, tagB), gn, tagA, tagB);
#pragma unroll
    for (int i = 0; i < 4; ++i) bad = tagbad(tagbad(bad, gx[i], tagA, tagB), gs[i], tagA, tagB);
    if (__builtin_expect(__any(bad != 0u), 0)) {
      _Pragma("nounroll") for (unsigned spin = 0; spin < SPIN_CAP; ++spin) {
        FRESH();
        if (spin) PAUSE();
        COUNT(20);
#pragma unroll
        for (int i = 0; i < 4; ++i) {
          if (tagbad(0u, gx[i], tagA, tagB)) gx[i] = ldg(rs, tid * 16, xgo + i * 4096);
          if (tagbad(0u, gs[i], tagA, tagB)) gs[i] = ldg(rs, tid * 16, OFF_SG + i * 4096);
        }
        if (tagbad(0u, gm, tagA, tagB)) gm = ldg(rs, gmo, 0);
        if (tagbad(0u, gn, tagA, tagB)) gn = ldg(rs, gmo2, 0);
        bad = tagbad(tagbad(0u, gm, tagA, tagB), gn, tagA, tagB);
#pragma unroll
        for (int i = 0; i < 4; ++i) bad = tagbad(tagbad(bad, gx[i], tagA, tagB), gs[i], tagA, tagB);
        if (!__any(bad != 0u)) break;
      }
    }
  }
  STAMP(5);
  W1DMA2(8, lrw + 8192);
  float zsc;
  v4u mtp;
  {
    const int i0 = (int)pack2(row_sum(u2f(gm.x)), row_sum(u2f(gm.z))), i1 = (int)pack2(row_sum(u2f(gn.x)), row_sum(u2f(gn.z)));
    unsigned md[6];
#pragma unroll
    for (int k = 0; k < 6; ++k) md[k] = (unsigned)__builtin_amdgcn_readlane(k < 4 ? i0 : i1, 16 * (k & 3));
#define MOD(k, c) ((c) ? bf_hi(md[k]) : bf_lo(md[k]))
    float s0 = rbf(1.0f + MOD(4, 0)), s1 = rbf(1.0f + MOD(4, 1));
    s0 = s0 == 0.f ? 0x1p-30f : s0;
    s1 = s1 == 0.f ? 0x1p-30f : s1;
    const float G0 = MOD(2, 0) * s0, G1 = MOD(2, 1) * s1;
    const float A0 = rbf(1.0f + MOD(1, 0)) * G0, A1 = rbf(1.0f + MOD(1, 1)) * G1, B0 = MOD(0, 0) * G0, B1 = MOD(0, 1) * G1;
    zsc = w >> 1 ? s1 : s0;
    mtp = v4u{lane ? md[5] : pack2(s0, s1), tagA, md[3], tagB};
#undef MOD
#pragma unroll
    for (int i = 0; i < 4; ++i) {
      const float mA = bf_lo(gs[i].x), rA = __builtin_amdgcn_rcpf(bf_hi(gs[i].x)), mB = bf_lo(gs[i].z), rB = __builtin_amdgcn_rcpf(bf_hi(gs[i].z));
      const float n00 = (bf_lo(gx[i].x) - mA) * rA, n01 = (bf_hi(gx[i].x) - mA) * rA, n10 = (bf_lo(gx[i].z) - mB) * rB, n11 = (bf_hi(gx[i].z) - mB) * rB;
      const int o = (tid + 256 * i) * 4;
      W1DMA2(10 + 2 * i, SLOT(10 + 2 * i));
      *(LAS unsigned*)(L + L_H + 480 + o) = pack2(__builtin_fmaf(n00, A0, B0), __builtin_fmaf(n10, A0, B0));
      *(LAS unsigned*)(L + L_H + H_STRIDE + 480 + o) = pack2(__builtin_fmaf(n01, A1, B1), __builtin_fmaf(n11, A1, B1));
      *(LAS unsigned*)(xbw + i * 512) = __builtin_amdgcn_perm(gx[i].z, gx[i].x, 0x05040100u);
      *(LAS unsigned*)(xbw + 4096 + i * 512) = __builtin_amdgcn_perm(gx[i].z, gx[i].x, 0x07060302u);
    }
  }
  BAR();
  const unsigned tb = now();
  STAMP(6);

  v4u w1f[32], w2f[28][2], mt0, mt1;
  {
    const bool rh = w & 1;
    constexpr ConvOrder O = conv_order(), OL = conv_order_last();
    v2u A[4][8][2], xk[4];
    v4u B[4][8];
    f32x4 acc[8], keep[4];
#pragma unroll
    for (int u = 0; u < 8; ++u) {
      A[0][u][0] = trd(ka + 512 * u + 8);
      A[0][u][1] = trd(ka + 512 * u);
      B[0][7 - u] = lds128(hb + 64 * (8 * (7 - u) + 3));
    }
    SB();
    if (lane) mtp.z = tb;
    stg(rs, tid < 2 ? tid * 16 : OOB, OFF_MT + id * 128, mtp);
    SB();
    auto hand_over = [&](auto Pc) __attribute__((always_inline)) {
      constexpr int i = Pc.v;
      PIN(acc[2 * i]); PIN(acc[2 * i + 1]);
      const f32x4 ev = acc[2 * i], od = acc[2 * i + 1];
      const f32x4 snd = rh ? ev : od, own = rh ? od : ev;
      *(LAS f32x4*)(i < 2 ? exa + i * 1024 : exb + (i - 1) * 1024) = snd;
#pragma unroll
      for (int c = 0; c < 4; ++c) { float t = keep[i][c] + own[c]; PIN(t); keep[i][c] = t; }
    };
    static_for<0, 144>([&](auto Kc) __attribute__((always_inline)) {
      constexpr int K = Kc.v, r = K / 36, k = K % 36, t = r < 3 ? O.t[k] : OL.t[k], u = r < 3 ? O.u[k] : OL.u[k];
      if constexpr (k == 0 && r > 0) asm volatile("" :: "v"(B[r][7]));
      mfma16_v<r == 0 && u == 0>(acc[t], v4u{A[r][u][0].x, A[r][u][0].y, A[r][u][1].x, A[r][u][1].y}, B[r][t - u]);
      SB();
      if constexpr (K == 24) WAITV(13);
      if constexpr (K >= 24 && K < 36) W1PULL(K - 24, K - 24);
      if constexpr (K == 40) asm volatile("" :: "a"(w1f[11]));
      if constexpr (K == 60) WAITV(8);
      if constexpr (K >= 60 && K < 70) W1PULL(K - 48, K - 48);
      if constexpr (K == 76) asm volatile("" :: "a"(w1f[21]));
      if constexpr (r == 2 && k < 4) xk[k] = *(LAS v2u*)(xbr + k * 512);
      constexpr int n = k - (r < 2 ? 0 : 4);
      if constexpr (r < 3 && n >= 0 && n < 24) {
        if constexpr (n % 3 == 2) B[r + 1][n / 3] = lds128(hb + 64 * (8 * (n / 3) - r + 2));
        else A[r + 1][n / 3][n % 3] = trd(ka + 512 * (n / 3) + 64 * (r + 1) + 8 - 8 * (n % 3));
      }
      if constexpr (K >= 100 && K < 116) {
        constexpr int e = K - 100;
        const unsigned xw = (e & 2) ? xk[e >> 2].y : xk[e >> 2].x;
        float t2_ = zsc * ((e & 1) ? bf_hi(xw) : bf_lo(xw));
        PIN(t2_);
        keep[e >> 2][e & 3] = t2_;
      }
      if constexpr (K == 108 + 8) hand_over(IC<0>{});
      if constexpr (K == 108 + 17) hand_over(IC<1>{});
      if constexpr (K == 108 + 30) hand_over(IC<2>{});
      if constexpr (K == 1 || K == 8) W1DMA2(18 + K / 4, SLOT(18 + K / 4));
      if constexpr (K == 15) W1DMA(22, L_RX + w * 3072);
      if constexpr (K == 19) W1DMA(23, L_TOP + w * 1024);
      if constexpr (K >= 41 && K < 69 && K % 7 == 6) W1DMA2(24 + (K - 41) / 7 * 2, lrw + (K - 41) / 7 * 2048);
      if constexpr (K >= 69 && K < 125 && K % 8 == 5) W2DMA((K - 69) / 8, SLOT(8 + (K - 69) / 8 * 2));
      if constexpr (K == MTK)
        asm volatile("buffer_load_dwordx4 %0, %2, %3, 0 offen sc1\n\tbuffer_load_dwordx4 %1, %2, %3, %4 offen sc1"
                     : "=&v"(mt0), "=&v"(mt1) : "v"(mto), "s"(rs), "s"(16384) : "memory");
      SB();
    });
    STAMP(7);
    asm volatile("s_nop 7" : "+v"(acc[6]), "+v"(acc[7]));
    hand_over(IC<3>{});
    BAR();
#pragma unroll
    for (int i = 0; i < 4; ++i) keep[i] += *(LAS f32x4*)(i < 2 ? eya + i * 1024 : eyb + (i - 1) * 1024);
#pragma unroll
    for (int i = 0; i < 2; ++i) {
      const auto s0 = __builtin_amdgcn_permlane16_swap(pack2(keep[2 * i][0], keep[2 * i][1]), pack2(keep[2 * i + 1][0], keep[2 * i + 1][1]), false, false);
      const auto s1 = __builtin_amdgcn_permlane16_swap(pack2(keep[2 * i][2], keep[2 * i][3]), pack2(keep[2 * i + 1][2], keep[2 * i + 1][3]), false, false);
      stg(rs, i ? zb : za, zs, v4u{guard(s0[0]), guard(s1[0]), guard(s0[1]), guard(s1[1])});
    }
    SB();
  }
  STAMP(8);

  static_assert(OFF_MT == 0, "table offset");
  const unsigned te = now();
  asm volatile("s_waitcnt vmcnt(2)" : "+v"(mt0), "+v"(mt1) :: "memory");
  if (__builtin_expect(__any(tagbad(tagbad(0u, mt0, tagA, tagB), mt1, tagA, tagB) != 0u), 0)) {
    _Pragma("nounroll") for (unsigned spin = 0; spin < SPIN_CAP; ++spin) {
      FRESH();
      COUNT(21);
      if (tagbad(0u, mt0, tagA, tagB)) mt0 = ldg(rs, mto, OFF_MT);
      if (tagbad(0u, mt1, tagA, tagB)) mt1 = ldg(rs, mto, OFF_MT + 16384);
      if (!__any(tagbad(tagbad(0u, mt0, tagA, tagB), mt1, tagA, tagB) != 0u)) break;
      PAUSE();
    }
  }
  *(LAS unsigned*)(mtw) = mt0.x;
  *(LAS unsigned*)(mtw + 512) = mt1.x;
  *(LAS unsigned*)(mtw + 1024) = mt0.z;
  *(LAS unsigned*)(mtw + 1536) = mt1.z;
  {
    int late = (lane & 1) ? max((int)(mt0.z - tb), (int)(mt1.z - tb)) : 0;
    late = max(late, __builtin_amdgcn_update_dpp(late, late, 0xB1, 0xF, 0xF, false));
    late = max(late, __builtin_amdgcn_update_dpp(late, late, 0x4E, 0xF, 0xF, false));
    late = max(late, __builtin_amdgcn_update_dpp(late, late, 0x141, 0xF, 0xF, false));
    late = max(late, __builtin_amdgcn_update_dpp(late, late, 0x140, 0xF, 0xF, false));
    late = max(late, __builtin_amdgcn_update_dpp(late, late, 0x142, 0xA, 0xF, false));
    late = max(late, __builtin_amdgcn_update_dpp(late, late, 0x143, 0xC, 0xF, false));
    if (lane == 63) *(LAS int*)(L + L_ETA + w * 4) = late;
  }
  const int w2t = (4 * hf + q4) * 128 + blk * 32 + (((p4 & 1) << 1) | (p4 >> 1)) * 8;
  LAS unsigned char* w2a = L + w2t + (q4 >> 1) * 64;
  LAS unsigned char* w2b = L + w2t + ((q4 >> 1) ^ 1) * 64;
  LAS unsigned char *w2sa = w2a + lsp, *w2sb = w2b + lsp;
  PIN(w2sa); PIN(w2sb);
#define PULL(step, pa, pb, off) do { w2f[step][0] = trd2((pa) + (off), (pa) + (off) + 1024); w2f[step][1] = trd2((pb) + (off), (pb) + (off) + 1024); } while (0)
#pragma unroll
  for (int i = 0; i < 3; ++i) PULL(4 + i, w2sa, w2sb, i * 2048);
  w1f[22] = trd2(L + L_RX + w * 3072 + tra, L + L_RX + w * 3072 + tra + 256);
  w1f[23] = trd2(L + L_TOP + w * 1024 + tra, L + L_TOP + w * 1024 + tra + 256);
  BAR();
  STAMP(9);
  const int par = (0x1C >> (ct % 7)) & 1;
  const unsigned char* xg = setp + OFF_Z + rp * 131072;
  const unsigned xl = L_XR + w * 8192;
#define XSRC(p) (xg + w * 8192 + ((p) ^ par) * 32768)
  const int slo = OFF_Z + (rp * 4 + w) * 32768 + si * 2048;
  v4u hq[56], res[2], sx[8];
  const int c0 = 7 * w + rot, c3 = c0 - 7 * (rot >> 2), c4 = c0 - 7 * ((rot + 1) >> 2);
  const int hb0 = OFF_HID + (rp * 4 + rg) * 114688, kb0 = OFF_CK + (rp * 4 + rg) * 4096 + w * 896, ckv = lane < 56 ? l16 : OOB;
#define CHUNK(n) (((n) % 28 / 4 < 3 ? c0 : (n) % 28 / 4 == 3 ? c3 : c4) + (n) % 28 / 4)
#define HFRAG(n) ldg(rs, l16 + ((n) & 3) * 1024, hb0 + CHUNK(n) * 4096 + ((n) / 28) * (2 * 114688))
#define HLOAD(n) hq[n] = HFRAG(n)
  const int hoff = OFF_HID + (rp * 4 * 112 + ct * 8 + 2 * w) * 1024;
  int hv0 = dt == 7 ? OOB : l16, hv1 = dt == 7 ? OOB : l16 + 1024;
  const int cko = OFF_CK + rp * 16384 + (ct * 4 + w) * 64;
  int ckw = (p4 || dt == 7) ? OOB : lane;
  unsigned ckey = lane ? 0u : tagA;
  PIN(hv0); PIN(hv1); PIN(ckw); PIN(ckey);
#define CKPUT(sum, u) __builtin_amdgcn_raw_buffer_store_b32(ckey - red_step<1>(red_step<0>(sum)), rs, ckw, cko + ((u) ^ par) * 4096, SC1)
  const int sxo = OFF_SX + (rp * 4 + w) * 8192;
  LAS unsigned char* xr[2];
#pragma unroll
  for (int i = 0; i < 2; ++i) { xr[i] = L + L_XR + tra + i * 65536; PIN(xr[i]); }
  LAS unsigned char* rr = L + L_XR + tra + (rg ^ par) * 32768 + (4 * dt + w) * 1024;
  LAS unsigned char* str = L + L_STAT + (lane & 31) * 8;
  LAS unsigned char *pa = w2a + lrw + 8192, *pb = w2b + lrw + 8192;
  w2a += L_XR + w * 8192; w2b += L_XR + w * 8192;
  LAS unsigned char* stw = str + (w ^ par) * 256;
  PIN(str); PIN(stw); PIN(rr); PIN(w2a); PIN(w2b);
  f32x4 uu[4], vv[4];
  v4u sl0, sl1;
  {
    LAS unsigned char* qr = L + ((lane & 31) == 0 ? L_TS : L_TB) + hf * 16;
    PIN(pa); PIN(pb);
#pragma unroll
    for (int i = 0; i < 3; ++i) W2DMA(7 + i, lsp + i * 2048);
    f32x16 uv, uv2;
    v4u qa[32];
    int wait = 0;
    const v4u lt = lds128(L + L_ETA);
    bool sent = false;
    auto send = [&]() __attribute__((always_inline)) {
      STAMPB(26);
      asm volatile("buffer_load_dwordx4 %0, %2, %3, %4 offen sc1\n\tbuffer_load_dwordx4 %1, %2, %3, %4 offen offset:1024 sc1"
                   :: "v"(sl0), "v"(sl1), "v"(l16), "s"(rs), "s"(slo) : "memory");
      dma_x4(XSRC(0), l16, xl);
      dma_x4(XSRC(0) + 4096, l16, xl + 4096);
    };
    asm volatile("" : "=v"(sl0), "=v"(sl1));
#pragma unroll
    for (int i = 0; i < 4; ++i) qa[i] = lds128(qr + i * 32);
    static_for<0, 32>([&](auto Sc) __attribute__((always_inline)) {
      constexpr int s = Sc.v;
      mfma_va<(s < 2)>((s & 1) ? uv2 : uv, qa[s], w1f[s]);
      SB();
      if constexpr (s < 28) qa[s + 4] = lds128(qr + (s + 4) * 32);
      if constexpr (s > 0) asm volatile("" :: "v"(qa[s > 0 ? s - 1 : 0]));
      if constexpr (s < 8) W1PULL(24 + s, s);
      if constexpr (s == 4) { wait = min(__builtin_amdgcn_readfirstlane(max(max((int)lt.x, (int)lt.y), max((int)lt.z, (int)lt.w))), 300) + ZLAG; }
      if constexpr (s >= 8 && s < 16) w2f[(s - 8) >> 1][s & 1] = trd2(((s & 1) ? pb : pa) + ((s - 8) >> 1) * 2048, ((s & 1) ? pb : pa) + ((s - 8) >> 1) * 2048 + 1024);
      if constexpr (s == 4 || s == 13 || s == 22 || s == 31) {
        if (!sent && (int)(now() - te) >= wait) { sent = true; send(); }
      }
      SB();
    });
    mfma_done(uv2);
    PIN(uv);
    LAS unsigned char* uw = L + L_UVT + (32 * w + (lane & 31)) * 4;
    if (lane < 32) { *(LAS float*)(uw) = uv[0] + uv2[0]; *(LAS float*)(uw + 512) = -LOG2E * (uv[1] + uv2[1]); }
    LAS unsigned char* uvp = L + L_UVT + (32 * w + 4 * hf) * 4;
#pragma unroll
    for (int i = 0; i < 4; ++i) { uu[i] = *(LAS f32x4*)(uvp + i * 32); vv[i] = *(LAS f32x4*)(uvp + 512 + i * 32); }
    float i0 = __builtin_amdgcn_rcpf(bf_lo(*(LAS u16*)(L + L_TS + (si * 32 + (lane >> 2)) * 2)));
    float i1 = __builtin_amdgcn_rcpf(bf_lo(*(LAS u16*)(L + L_TS + (si * 32 + 16 + (lane >> 2)) * 2)));
    int sxw = sel(lane & 8, OOB, (p4 * 8 + hf * 4 + blk * 2 + ((lane >> 2) & 1)) * 16);
    PIN(i0); PIN(i1); PIN(sxw);
    SB();
    if (!sent) {
      while ((int)(now() - te) < wait) { }
      send();
    }
    dma_x4(XSRC(1), l16, xl + 32768);
    dma_x4(XSRC(1) + 4096, l16, xl + 32768 + 4096);
    STAMPB(27);
    asm volatile("s_waitcnt vmcnt(16)" : "+v"(sl0), "+v"(sl1) :: "memory");
    STAMPB(28);
    if (__builtin_expect(__any(max4(max4(0u, sl0), sl1) == POISON), 0)) {
      _Pragma("nounroll") for (unsigned spin = 0; spin < SPIN_CAP; ++spin) {
        FRESH();
        if (spin) PAUSE();
        COUNT(22);
        sl0 = ldg(rs, l16, slo);
        sl1 = ldg(rs, l16 + 1024, slo);
        if (!__any(max4(max4(0u, sl0), sl1) == POISON)) break;
      }
    }
    float sm[8], sq[8];
#pragma unroll
    for (int i = 0; i < 4; ++i) {
      const f32x2 a = f32x2{bf_lo(sl0[i]), bf_hi(sl0[i])} * f32x2{i0, i0}, c = f32x2{bf_lo(sl1[i]), bf_hi(sl1[i])} * f32x2{i1, i1};
      const f32x2 s2 = a + c, q2 = __builtin_elementwise_fma(a, a, c * c);
      sm[2 * i] = s2.x; sm[2 * i + 1] = s2.y; sq[2 * i] = q2.x; sq[2 * i + 1] = q2.y;
    }
#pragma unroll
    for (int i = 0; i < 4; ++i) { swap_add32(sm[i], sm[i + 4]); swap_add32(sq[i], sq[i + 4]); }
#pragma unroll
    for (int i = 0; i < 2; ++i) { swap_add16(sm[i], sm[i + 2]); swap_add16(sq[i], sq[i + 2]); }
#pragma unroll
    for (int i = 0; i < 2; ++i) { sm[i] = dpp_add<0x124>(dpp_add<0x128>(sm[i])); sq[i] = dpp_add<0x124>(dpp_add<0x128>(sq[i])); }
    const bool od = lane & 4;
    stg(rs, sxw, OFF_SX + ((rp * 4 + w) * 16 + si) * 512,
        v4u{f2u(od ? sm[1] : sm[0]), tagA, f2u(od ? sq[1] : sq[0]), tagB});
    SB();
  }
  STAMPB(29);
  WAITV(9);
  STAMP(10);
  {
    v4u t[8];
    unsigned m = 0, m2 = 0;
#pragma unroll
    for (int i = 0; i < 8; ++i) t[i] = lds128(L + xl + l16 + i * 1024);
#pragma unroll
    for (int i = 0; i < 8; i += 2) { m = max4(m, t[i]); m2 = max4(m2, t[i + 1]); }
    if (__builtin_expect(__any(max(m, m2) == POISON), 0)) {
      _Pragma("nounroll") for (unsigned spin = 0; spin < SPIN_CAP; ++spin) {
        COUNT(22);
        bool again = false;
#pragma nounroll
        for (int i = 0; i < 8; ++i)
          if (__any(max4(0u, lds128(L + xl + l16 + i * 1024)) == POISON)) {
            again = true;
            dma_x1(XSRC(0) + i * 1024, l16, xl + i * 1024);
            dma_x1(XSRC(1) + i * 1024, l16, xl + 32768 + i * 1024);
          }
        if (!again) break;
        WAITV(0);
        if (spin) PAUSE();
      }
    }
  }
  BAR();
  STAMP(11);

#define XA(t) (xr[(t) >> 6] + ((t) & 63) * 1024)
#define XF(t) trd2(XA(t), XA(t) + 256)
#define XEN
#define REDO(p) \
  if (XEN __builtin_expect(__any(!(acc[(p) % 3][0] == acc[(p) % 3][0])), 0)) { \
    unsigned m0s; \
    asm volatile("s_mov_b32 %0, m0" : "=s"(m0s)); \
    for (unsigned spin = 0; spin < SPIN_CAP / 128; ++spin) { \
      COUNT(23); \
      _Pragma("nounroll") for (int i = 0; i < 8; ++i) dma_x4(xg + ((p) ^ par) * 32768 + i * 4096, l16, L_XR + (p) * 32768 + i * 4096); \
      WAITV(0); \
      _Pragma("unroll") for (int s = 0; s < 32; ++s) { \
        const v4u t = XF(32 * (p) + s); \
        if (s == 0) mfma_av<true>(acc[(p) % 3], w1f[s], t); else mfma_av<false>(acc[(p) % 3], w1f[s], t); \
        SB(); \
      } \
      mfma_done(acc[(p) % 3]); \
      if (!__any(!(acc[(p) % 3][0] == acc[(p) % 3][0]))) break; \
    } \
    set_m0(m0s); \
  }
  f32x16 acc[3];
  f32x2 ec[4];
  float ep[48], ee[48], ey[48];
  unsigned cs = 0, sbad = 0;
  float ssm = 0.f, ssq = 0.f;
  v4u x[128], eo;
#pragma unroll
  for (int t = 0; t < 4; ++t) x[t] = XF(t);
  SB();
  auto eslot = [&](auto Sc) __attribute__((always_inline)) {
    static_for<0, 48>([&](auto Vc) __attribute__((always_inline)) {
      constexpr int V = Vc.v, k = V & 15, st = Sc.v - epi_slot(V);
      if constexpr (st == 6 && (k & 1)) {
        unsigned o = pack2(ey[V - 1], ey[V]);
        PIN(o);
        eo[(k >> 1) & 3] = o;
        if constexpr (k == 7) { stg(rs, hv0, hoff + ((V >> 4) ^ par) * 114688, eo); cs = sum4(0u, eo); PIN(cs); }
        if constexpr (k == 15) { stg(rs, hv1, hoff + ((V >> 4) ^ par) * 114688, eo); CKPUT(sum4(cs, eo), V >> 4); }
      }
      if constexpr (st == 5) { ey[V] = ep[V] * ee[V]; PIN(ey[V]); }
      if constexpr (st == 4) { ee[V] = __builtin_amdgcn_rcpf(ee[V]); PIN(ee[V]); }
      if constexpr (st == 3) { ee[V] = 1.0f + ee[V]; PIN(ee[V]); }
      if constexpr (st == 2) { ee[V] = __builtin_amdgcn_exp2f(ep[V]); PIN(ee[V]); }
      if constexpr (st == 1) { ep[V] = __builtin_fmaf(ec[V >> 4].x, acc[(V >> 4) % 3][k], ep[V]); PIN(ep[V]); }
      if constexpr (st == 0) { ep[V] = __builtin_fmaf(ec[V >> 4].y, uu[k >> 2][k & 3], vv[k >> 2][k & 3]); PIN(ep[V]); }
    });
    SB();
  };
  static_for<0, 64>([&](auto Jc) __attribute__((always_inline)) {
    constexpr int J = Jc.v, p = J >> 4, j = J & 15;
    asm volatile("" :: "v"(x[2 * J + 1]));
    mfma_av<j == 0>(acc[p % 3], w1f[2 * j], x[2 * J]);
    if constexpr (J < 62) { x[2 * J + 4] = XF(2 * J + 4); if constexpr (!epi_pair(J)) x[2 * J + 5] = XF(2 * J + 5); }
    SB();
    if constexpr (epi_pair(J)) eslot(IC<2 * J>{});
    mfma_av<false>(acc[p % 3], w1f[2 * j + 1], x[2 * J + 1]);
    if constexpr (epi_pair(J)) x[2 * J + 5] = XF(2 * J + 5);
    SB();
    if constexpr (J == 10) { waitv<c_between(-1, 10)>(); __builtin_amdgcn_s_barrier(); FRESH(); }
    if constexpr (J == 34) waitv<c_between(19, 34)>();
    if constexpr (J == 38) waitv<c_between(27, 38)>();
    if constexpr (J >= EO - 6 && J < EO - 2) {
      asm volatile("s_waitcnt vmcnt(%2)" : "+v"(sx[2 * (J - EO + 6)]), "+v"(sx[2 * (J - EO + 6) + 1]) : "n"(c_between(8 + J - EO + 6, J)) : "memory");
#pragma unroll
      for (int i = 2 * (J - EO + 6); i < 2 * (J - EO + 6) + 2; ++i) {
        sbad = tagbad(sbad, sx[i], tagA, tagB);
        ssm += u2f(sx[i].x); PIN(ssm);
        ssq += u2f(sx[i].z); PIN(ssq);
      }
      PIN(sbad);
      SB();
    }
    if constexpr (J == EO - 2) {
      if (__builtin_expect(__any(sbad != 0u), 0)) {
        _Pragma("nounroll") for (unsigned spin = 0; spin < SPIN_CAP; ++spin) {
          FRESH();
          if (spin) PAUSE();
          COUNT(23);
          sbad = 0; ssm = 0.f; ssq = 0.f;
#pragma unroll
          for (int i = 0; i < 8; ++i) sx[i] = ldg(rs, l16 + (i & 3) * 1024, sxo + (i >> 2) * 4096);
#pragma unroll
          for (int i = 0; i < 8; ++i) { sbad = tagbad(sbad, sx[i], tagA, tagB); ssm += u2f(sx[i].x); ssq += u2f(sx[i].z); }
          if (!__any(sbad != 0u)) break;
        }
      }
      const float mean = swap32_add(ssm) * (1.0f / 512.0f);
      const float ms = __builtin_fmaf(swap32_add(ssq), 1.0f / 512.0f, 1e-5f);
      const float a = -LOG2E * __builtin_amdgcn_rsqf(fmaxf(__builtin_fmaf(-mean, mean, ms), 1e-5f));
      *(LAS f32x2*)(stw) = f32x2{a, -a * mean};
      SB();
    }
    if constexpr (J == EO - 1) {
      asm volatile("s_waitcnt lgkmcnt(4)" ::: "memory");
      __builtin_amdgcn_s_barrier();
      FRESH();
#pragma unroll
      for (int i = 0; i < 4; ++i) ec[i] = *(LAS f32x2*)(str + i * 256);
      SB();
    }
    if constexpr (!epi_pair(J)) eslot(IC<2 * J>{});
    eslot(IC<2 * J + 1>{});
    if constexpr (j == 1 && p >= 1) {
      if constexpr (p == 2) REDO(1)
      if constexpr (p == 3) REDO(2)
      if (((p - 1) & 1) == (rg ^ par)) res[(p - 1) >> 1] = trd2(rr + ((p - 1) >> 1) * 65536, rr + ((p - 1) >> 1) * 65536 + 256);
    }
    if constexpr (j == 2 && p >= 1) {
      asm volatile("s_waitcnt lgkmcnt(4)" ::: "memory");
      __builtin_amdgcn_s_barrier();
      FRESH();
    }
    if constexpr (J < 8 || (J >= 12 && J < 20)) {
      constexpr int n = J < 8 ? J : J - 4;
      if constexpr ((n & 3) == 0) set_m0(xl + 65536 + (n >> 3) * 32768 + (n & 4) * 1024);
      dma_xn<(n & 3) * 1024>(XSRC(2 + (n >> 3)) + (n & 4) * 1024, l16);
    }
    if constexpr (J >= 8 && J < 12)
      asm volatile("buffer_load_dwordx4 %0, %2, %3, %4 offen offset:%5 sc1\n\tbuffer_load_dwordx4 %1, %2, %3, %4 offen offset:%6 sc1"
                   : "=&v"(sx[2 * (J - 8)]), "=&v"(sx[2 * (J - 8) + 1])
                   : "v"(l16), "s"(rs), "s"(sxo + (J & 2) * 2048), "n"((J & 1) * 2048), "n"((J & 1) * 2048 + 1024) : "memory");
    if constexpr (J >= 19 && J < 22) PULL(7 + J - 19, w2sa, w2sb, (J - 19) * 2048);
    if constexpr (J >= 38 && J < 42) PULL(10 + J - 38, w2a, w2b, (J - 38) * 2048);
    if constexpr (J == 43) asm volatile("" :: "a"(w2f[13][1]));
    if constexpr (J >= 20 && J < 28) W2HALF(20 + J - 20, xl + (J - 20) * 1024);
    if constexpr (J >= 28 && J < 34) W2HALF(28 + J - 28, lsp + (J - 28) * 1024);
    if constexpr (J >= 35 && J < 43) W2HALF(34 + J - 35, xl + 32768 + (J - 35) * 1024);
    if constexpr (J >= 43 && J < 51) W2HALF(42 + J - 43, xl + (J - 43) * 1024);
    static_for<hc_upto(J), hc_upto(J + 1)>([&](auto Nc) __attribute__((always_inline)) { HLOAD(Nc.v); });
    SB();
    if constexpr (J == 15) STAMP(16);
    if constexpr (J == 31) STAMP(17);
    if constexpr (J == 47) STAMP(18);
  });
  STAMP(12);
  static_assert(EO <= 30 && epi_slot(31) < epi_slot(32) && epi_slot(16) + 1 >= 2 * 34 && epi_slot(32) + 1 >= 2 * 50 && epi_slot(47) + 6 < 128, "schedule");
  static_for<12, 20>([&](auto Ic) __attribute__((always_inline)) { HLOAD(Ic.v); });
  SB();
  mfma_done(acc[0]);
  REDO(3)
  if (rg ^ par) res[1] = trd2(rr + 65536, rr + 65536 + 256);
#pragma unroll
  for (int h = 0; h < 2; ++h) {
    v4u o;
#pragma unroll
    for (int i = 0; i < 4; ++i) {
      const int k = 8 * h + 2 * i;
      const f32x2 t = __builtin_elementwise_fma(f32x2{ec[3].y, ec[3].y}, f32x2{uu[k >> 2][k & 3], uu[k >> 2][(k & 3) + 1]}, f32x2{vv[k >> 2][k & 3], vv[k >> 2][(k & 3) + 1]});
      const f32x2 pp = __builtin_elementwise_fma(f32x2{ec[3].x, ec[3].x}, f32x2{acc[0][k], acc[0][k + 1]}, t);
      const f32x2 d = f32x2{__builtin_amdgcn_exp2f(pp.x), __builtin_amdgcn_exp2f(pp.y)} + f32x2{1.0f, 1.0f};
      const f32x2 y = pp * f32x2{__builtin_amdgcn_rcpf(d.x), __builtin_amdgcn_rcpf(d.y)};
      o[i] = pack2(y.x, y.y);
    }
    PIN(o);
    if (h == 0) static_for<20, 24>([&](auto Ic) __attribute__((always_inline)) { HLOAD(Ic.v); });
    if (h == 1) static_for<24, 28>([&](auto Ic) __attribute__((always_inline)) { HLOAD(Ic.v); });
    stg(rs, h ? hv1 : hv0, hoff + (3 ^ par) * 114688, o);
    if (h) CKPUT(sum4(cs, o), 3); else cs = sum4(0u, o);
    SB();
  }
  STAMP(13);

  f32x16 d[2][2];
  LAS unsigned char* redw = L + L_XR + w * 8192 + l16;
  LAS unsigned char* redr = L + L_XR + w * 2048 + l16;
#define REDW(un, q) *(LAS f32x4*)(redw + (un) * 32768 + (q) * 1024) = \
    f32x4{d[un][(q) >> 2][4 * ((q) & 3)], d[un][(q) >> 2][4 * ((q) & 3) + 1], d[un][(q) >> 2][4 * ((q) & 3) + 2], d[un][(q) >> 2][4 * ((q) & 3) + 3]}
  LAS unsigned char *w2ua = w2a + 65536, *w2ub = w2b + 65536;
  PIN(w2ua); PIN(w2ub);
#define CHECK(un) \
  if (__builtin_expect((unsigned)__builtin_amdgcn_readlane((int)m[un], 63) != 14u * tagA, 0)) { \
    _Pragma("nounroll") for (unsigned spin = 0; spin < SPIN_CAP; ++spin) { \
      FRESH(); \
      COUNT(24 + (un)); \
      unsigned t = sum4(0u, ldg(rs, ckv, kb0 + (un) * 8192)); \
      static_for<0, 7>([&](auto Cc) __attribute__((always_inline)) { \
        constexpr int c = Cc.v; \
        v4u q[4]; \
        _Pragma("unroll") for (int i = 0; i < 4; ++i) q[i] = ldg(rs, l16 + i * 1024, hb0 + CHUNK(4 * c) * 4096 + (un) * (2 * 114688)); \
        t = sum8(sum8(t, q[0], q[1]), q[2], q[3]); \
        mfma_av<c == 0>(d[un][0], w2f[4 * c][0], q[0]); mfma_av<c == 0>(d[un][1], w2f[4 * c][1], q[0]); \
        _Pragma("unroll") for (int i = 1; i < 4; ++i) { mfma_av<false>(d[un][0], w2f[4 * c + i][0], q[i]); mfma_av<false>(d[un][1], w2f[4 * c + i][1], q[i]); } \
        SB(); \
      }); \
      mfma_done(d[un][1]); \
      PIN(d[un][0]); \
      if ((unsigned)__builtin_amdgcn_readlane((int)red_all(t), 63) == 14u * tagA) break; \
      PAUSE(); \
    } \
    if (un) { _Pragma("unroll") for (int q = 0; q < 4; ++q) REDW(1, q); } \
  }
  unsigned m[2] = {0u, 0u};
  v4u gbv, scv, ck[2];
  f32x4 pr[8];
  float gb[8], rx[2][8];
  unsigned orow = (rp * 128 + ((lane & 31) >> 3) * 16 + rg * 8 + (lane & 7)) * 1024 + dt * 128 + w * 32 + hf * 16;
  static_for<0, 112>([&](auto Xc) __attribute__((always_inline)) {
    constexpr int X = Xc.v, I = X < 100 ? X >> 1 : 50 + (X - 100) % 6, e = X < 100 ? X & 1 : (X - 100) / 6, un = I / 28;
    mfma_av<I % 28 == 0>(d[un][e], w2f[I % 28][e], hq[I]);
    SB();
    if constexpr (X < 100 && e == 0) {
      if constexpr (I < 3) PULL(14 + I, w2sa, w2sb, I * 2048);
      if constexpr (I >= 3 && I < 7) PULL(14 + I, w2a, w2b, 32768 + (I - 3) * 2048);
      if constexpr (I >= 7 && I < 11) PULL(14 + I, w2a, w2b, (I - 7) * 2048);
      if constexpr (I == 21) waitv<hl_upto(21) - 28 + 1>();
      if constexpr (I >= 21 && I < 24) PULL(4 + I, w2ua, w2ub, (I - 21) * 2048);
      if constexpr (I == 28) { m[0] = sum4(m[0], ck[0]); PIN(m[0]); }
      if constexpr (I >= 29 && I < 35) { m[0] = red_step<I - 29>(m[0]); PIN(m[0]); }
      if constexpr (I == 35) CHECK(0)
      if constexpr (I >= 35 && I < 43) REDW(0, I - 35);
    }
    if constexpr (X < 100 && e == 1) {
      if constexpr (I < 6) W2HALF(50 + I, xl + 65536 + I * 1024);
      static_for<hl_upto(I), hl_upto(I + 1)>([&](auto Nc) __attribute__((always_inline)) { HLOAD(Nc.v); });
      if constexpr (I == 6) ck[0] = ldg(rs, ckv, kb0);
      if constexpr (I == 37) ck[1] = ldg(rs, ckv, kb0 + 8192);
    }
    if constexpr (X < 100 ? e == 1 : e == 0) { m[un] = sum4(m[un], hq[I]); PIN(m[un]); }
    if constexpr (X == 105) { m[1] = sum4(m[1], ck[1]); PIN(m[1]); }
    if constexpr (X >= 106) { m[1] = red_step<X - 106>(m[1]); PIN(m[1]); }
    if constexpr (X == 22) { gbv = lds128(L + L_TG + (64 * dt + 16 * w + 8 * hf) * 2); scv = lds128(L + L_TS + (64 * dt + 16 * w + 8 * hf) * 2); PIN(orow); }
    if constexpr (X >= 26 && X < 42 && (X & 1) == 0) {
      constexpr int q = (X - 26) >> 1;
      const float inv = __builtin_amdgcn_rcpf((q & 1) ? bf_hi(scv[q >> 1]) : bf_lo(scv[q >> 1]));
      gb[q] = -LN2 * ((q & 1) ? bf_hi(gbv[q >> 1]) : bf_lo(gbv[q >> 1]));
      rx[0][q] = inv * ((q & 1) ? bf_hi(res[0][q >> 1]) : bf_lo(res[0][q >> 1]));
      rx[1][q] = inv * ((q & 1) ? bf_hi(res[1][q >> 1]) : bf_lo(res[1][q >> 1]));
      PIN(gb[q]); PIN(rx[0][q]); PIN(rx[1][q]);
    }
    if constexpr (X == 86) BAR();
    if constexpr (X == 86 || X == 88) {
#pragma unroll
      for (int k = X - 86; k < X - 84; ++k) { pr[2 * k] = *(LAS f32x4*)(redr + k * 8192); pr[2 * k + 1] = *(LAS f32x4*)(redr + k * 8192 + 1024); }
    }
    if constexpr (X >= 91 && X < 99) {
      constexpr int q = X - 91;
      float t = (pr[q >> 2][q & 3] + pr[2 + (q >> 2)][q & 3]);
      PIN(t);
      float t2_ = (pr[4 + (q >> 2)][q & 3] + pr[6 + (q >> 2)][q & 3]);
      PIN(t2_);
      rx[0][q] = __builtin_fmaf(gb[q], t + t2_, rx[0][q]);
      PIN(rx[0][q]);
    }
    if constexpr (X == 99)
      *(GAS v4u*)((GAS unsigned char*)out + orow) = v4u{pack2(rx[0][0], rx[0][1]), pack2(rx[0][2], rx[0][3]), pack2(rx[0][4], rx[0][5]), pack2(rx[0][6], rx[0][7])};
    if constexpr (X == 108) asm volatile("s_nop 3" : "+v"(d[1][0]));
    if constexpr (X >= 108) REDW(1, X - 108);
    SB();
    if constexpr (X == 55) STAMP(14);
  });
  STAMP(19);
  asm volatile("s_nop 7" : "+v"(d[1][1]));
  CHECK(1)
#pragma unroll
  for (int q = 4; q < 8; ++q) REDW(1, q);
  BAR();
  {
    f32x4 s0 = {}, s1 = {};
#pragma unroll
    for (int k = 0; k < 4; ++k) { s0 += *(LAS f32x4*)(redr + k * 8192 + 32768); s1 += *(LAS f32x4*)(redr + k * 8192 + 32768 + 1024); }
    float y[8];
#pragma unroll
    for (int q = 0; q < 8; ++q) y[q] = __builtin_fmaf(gb[q], q < 4 ? s0[q & 3] : s1[q & 3], rx[1][q]);
    *(GAS v4u*)((GAS unsigned char*)out + orow + 65536) = v4u{pack2(y[0], y[1]), pack2(y[2], y[3]), pack2(y[4], y[5]), pack2(y[6], y[7])};
  }
  if (b == 0 && tid == 0) __hip_atomic_store((GAS unsigned*)ws, tagA, __ATOMIC_RELAXED, __HIP_MEMORY_SCOPE_AGENT);
  STAMP(15);
}

extern "C" void kernel_launch(void* const* d_in, const int* in_sizes, int n_in, void* d_out, int out_size, void* d_ws, size_t ws_size,
                              hipStream_t stream) {
  (void)in_sizes; (void)n_in; (void)out_size; (void)ws_size;
  (void)hipFuncSetAttribute((const void*)s4_block_kernel, hipFuncAttributeMaxDynamicSharedMemorySize, LDS_BYTES);
  s4_block_kernel<<<256, 256, LDS_BYTES, stream>>>((cptr)d_in[0], (cptr)d_in[1], (cptr)d_in[2], (cptr)d_in[3], (cptr)d_in[4], (cptr)d_in[5], (cptr)d_in[6],
                                                   (cptr)d_in[7], (cptr)d_in[8], (cptr)d_in[10], (cptr)d_in[11], (cptr)d_in[12], (cptr)d_in[13],
                                                   (unsigned char*)d_out, (unsigned char*)d_ws, (cptr)d_in[9]);
}
```

```cpp
#include <hip/hip_runtime.h>

#define GAS __attribute__((address_space(1)))
#define LAS __attribute__((address_space(3)))
#define DI __device__ __forceinline__
typedef unsigned v4u __attribute__((ext_vector_type(4)));
typedef unsigned v2u __attribute__((ext_vector_type(2)));
typedef float f32x2 __attribute__((ext_vector_type(2)));
typedef float f32x4 __attribute__((ext_vector_type(4)));
typedef float f32x16 __attribute__((ext_vector_type(16)));
typedef __bf16 bf16x2 __attribute__((ext_vector_type(2)));
typedef __bf16 bf16x8 __attribute__((ext_vector_type(8)));
typedef short s16x4 __attribute__((ext_vector_type(4)));
typedef unsigned short u16;
typedef unsigned long long u64;

constexpr int SET_BASE = 4096;
constexpr int OFF_MT = 0;
constexpr int OFF_SG = 32768;
constexpr int OFF_MG = 65536;
constexpr int OFF_SX = 524288;
constexpr int OFF_XC = 458752;
constexpr int OFF_XG = 1 << 20;
constexpr int OFF_Z = OFF_XG + (2 << 20);
constexpr int OFF_HID = OFF_Z + 2097152;
constexpr int OFF_CK = OFF_HID + 7340032;
constexpr int SET_BYTES = OFF_CK + 262144;
static_assert(OFF_MG + 393216 <= OFF_XC && OFF_XC + 65536 <= OFF_SX, "set size");

constexpr int L_TS = 0, L_TB = 1024, L_TG = 2048;
constexpr int L_UVT = 3072;
constexpr int L_M = 4096;
constexpr int L_UVS = L_M;
constexpr int UVS_ROW = 544, UVS_CH = 57 * UVS_ROW;
constexpr int L_KC = L_M;
constexpr int KC_STRIDE = 4160;
constexpr int L_H = L_KC + 8 * KC_STRIDE;
constexpr int H_STRIDE = 4576;
constexpr int L_XB = L_H + 2 * H_STRIDE;
constexpr int L_RX = L_XB + 8192;
constexpr int L_R = L_M + 65536;
constexpr int L_TOP = L_M + 131072 + 24576;
constexpr int L_KT = L_TOP + 16;
constexpr int L_STAT = L_TOP;
constexpr int L_ETA = L_RX + 12288;
constexpr int L_XR = L_M;
constexpr int L_SP = L_M + 131072;
constexpr int LDS_BYTES = 163840;
static_assert(L_TOP + 4096 <= LDS_BYTES && L_ETA + 16 <= L_R && L_UVS + 2 * UVS_CH <= L_R && L_SP + 24576 <= L_TOP, "LDS map");
static_assert(L_H % 16 == 0 && L_XB % 16 == 0 && L_RX % 16 == 0 && L_KT % 16 == 0, "alignment");

#define ZLAG 30
#define MTK 124
constexpr int EO = 30;
constexpr int CODE_BYTES = 35328;
constexpr int SC1 = 16;
constexpr int OOB = 0x7FFFFF00;
constexpr unsigned SPIN_CAP = 1u << 15;
constexpr unsigned POISON = 0xFFFFFFFFu;
constexpr float LOG2E = 1.4426950408889634f, LN2 = 0.6931471805599453f, INV2PI = 0.15915494309189535f;

#define SB() __builtin_amdgcn_sched_barrier(0)
#define WAITV(n) asm volatile("s_waitcnt vmcnt(" #n ")" ::: "memory")
#define PIN(x) asm volatile("" : "+v"(x))
#define FRESH() asm volatile("" ::: "memory")
#define PAUSE() asm volatile("s_sleep 4" ::: "memory")
#define BAR() do { asm volatile("s_waitcnt lgkmcnt(0)" ::: "memory"); __builtin_amdgcn_s_barrier(); asm volatile("" ::: "memory"); } while (0)
#define STAMP(i)
#define SARG
#define SPASS
#define COUNT(i)

#define STAMPC(i)
#define STAMPA(i)
#define STAMPB(i) STAMP(i)
DI float u2f(unsigned u) { return __builtin_bit_cast(float, u); }
DI unsigned f2u(float f) { return __builtin_bit_cast(unsigned, f); }
DI float dot2(unsigned u, float c) { const bf16x2 v = __builtin_bit_cast(bf16x2, u); return __builtin_amdgcn_fdot2_f32_bf16(v, v, c, false); }
DI float bf_lo(unsigned u) { return __builtin_bit_cast(float, u << 16); }
DI float bf_hi(unsigned u) { return __builtin_bit_cast(float, u & 0xFFFF0000u); }
DI unsigned pack2(float a, float b) { f32x2 v = {a, b}; return __builtin_bit_cast(unsigned, __builtin_convertvector(v, bf16x2)); }
DI float rbf(float a) { return bf_lo(pack2(a, a)); }
DI unsigned sel(bool c, unsigned a, unsigned b) { asm("" : "+v"(a), "+v"(b)); return c ? a : b; }
DI unsigned guard(unsigned u) { return u < 0xFFFFFFFEu ? u : 0xFFFFFFFEu; }
DI unsigned max4(unsigned m, v4u v) {
  asm("v_max3_u32 %0, %0, %1, %2\n\tv_max3_u32 %0, %0, %3, %4" : "+v"(m) : "v"(v.x), "v"(v.y), "v"(v.z), "v"(v.w));
  return m;
}
DI f32x4 mfma16(v4u a, v4u b, f32x4 c) { return __builtin_amdgcn_mfma_f32_16x16x32_bf16(__builtin_bit_cast(bf16x8, a), __builtin_bit_cast(bf16x8, b), c, 0, 0, 0); }
DI f32x16 mfma32(v4u a, v4u b, f32x16 c) { return __builtin_amdgcn_mfma_f32_32x32x16_bf16(__builtin_bit_cast(bf16x8, a), __builtin_bit_cast(bf16x8, b), c, 0, 0, 0); }
template <bool FIRST> DI void mfma_av(f32x16& acc, const v4u& a, const v4u& b) {
  if (FIRST) asm volatile("v_mfma_f32_32x32x16_bf16 %0, %1, %2, 0" : "=&v"(acc) : "a"(a), "v"(b));
  else asm volatile("v_mfma_f32_32x32x16_bf16 %0, %1, %2, %0" : "+v"(acc) : "a"(a), "v"(b));
}
template <bool FIRST> DI void mfma4_va(f32x4& c0, f32x4& c1, const v4u& a, const v4u& b) {
  const v2u al = {a.x, a.y}, ah = {a.z, a.w}, bl = {b.x, b.y}, bh = {b.z, b.w};
  if (FIRST) asm volatile("v_mfma_f32_4x4x4_16b_bf16 %0, %2, %4, 0\n\tv_mfma_f32_4x4x4_16b_bf16 %1, %3, %5, 0" : "=&v"(c0), "=&v"(c1) : "v"(al), "v"(ah), "a"(bl), "a"(bh));
  else asm volatile("s_nop 0\n\tv_mfma_f32_4x4x4_16b_bf16 %0, %2, %4, %0\n\tv_mfma_f32_4x4x4_16b_bf16 %1, %3, %5, %1" : "+v"(c0), "+v"(c1) : "v"(al), "v"(ah), "a"(bl), "a"(bh));
}
template <bool FIRST> DI void mfma16_v(f32x4& acc, const v4u& a, const v4u& b) {
  if (FIRST) asm volatile("v_mfma_f32_16x16x32_bf16 %0, %1, %2, 0" : "=&v"(acc) : "v"(a), "v"(b));
  else asm volatile("v_mfma_f32_16x16x32_bf16 %0, %1, %2, %0" : "+v"(acc) : "v"(a), "v"(b));
}
DI void mfma_done(f32x16& acc) { asm volatile("s_nop 15" : "+v"(acc)); }
DI v2u trd(LAS unsigned char* p) { return __builtin_bit_cast(v2u, __builtin_amdgcn_ds_read_tr16_b64_v4i16((LAS s16x4*)p)); }
DI v4u trd2(LAS unsigned char* p0, LAS unsigned char* p1) { const v2u a = trd(p0), b = trd(p1); return v4u{a.x, a.y, b.x, b.y}; }
DI v4u lds128(LAS unsigned char* p) { return *(LAS v4u*)p; }
DI v4u ldg(__amdgpu_buffer_rsrc_t r, int voff, int soff) { return __builtin_amdgcn_raw_buffer_load_b128(r, voff, soff, SC1); }
DI void stg(__amdgpu_buffer_rsrc_t r, int voff, int soff, v4u v) {
  __builtin_amdgcn_raw_buffer_store_b128(v, r, voff, soff, SC1);
  asm volatile("s_nop 1" :: "v"(v) : "memory");
}
template <int CTRL> DI float dpp_add(float v) {
  return v + __builtin_bit_cast(float, __builtin_amdgcn_update_dpp(0, __builtin_bit_cast(int, v), CTRL, 0xF, 0xF, true));
}
DI float swap16_add(float v) { const unsigned u = __builtin_bit_cast(unsigned, v); const auto r = __builtin_amdgcn_permlane16_swap(u, u, false, false);
  return u2f(r[0]) + u2f(r[1]); }
DI float swap32_add(float v) { const unsigned u = __builtin_bit_cast(unsigned, v); const auto r = __builtin_amdgcn_permlane32_swap(u, u, false, false);
  return u2f(r[0]) + u2f(r[1]); }
DI float quad_sum(float v) { v = dpp_add<0xB1>(v); return dpp_add<0x4E>(v); }
DI float row_sum(float v) { return dpp_add<0x140>(dpp_add<0x141>(quad_sum(v))); }
DI float wave_sum(float v) { return swap32_add(swap16_add(row_sum(v))); }
DI f32x2 cmul(f32x2 a, f32x2 e, f32x2 ep) { const f32x2 t = f32x2{a.x, a.x} * e; return __builtin_elementwise_fma(f32x2{a.y, a.y}, ep, t); }
DI f32x2 cexp(float re2, float rev) {
  const float m = __builtin_amdgcn_exp2f(re2), f = __builtin_amdgcn_fractf(rev);
  return f32x2{m * __builtin_amdgcn_cosf(f), m * __builtin_amdgcn_sinf(f)};
}
template <int LI, int SI> DI void grp(int& so, unsigned lbase, int sbase) {
  asm volatile("s_add_i32 m0, %1, %3\n\ts_add_i32 %0, %2, %4" : "=s"(so) : "s"(lbase), "s"(sbase), "n"(LI), "n"(SI) : "scc", "memory");
}
template <int K, bool COH> DI void xfer(unsigned v, __amdgpu_buffer_rsrc_t r, int so) {
  if (COH) asm volatile("buffer_load_dwordx4 %0, %1, %2 offen offset:%3 sc1 lds" :: "v"(v), "s"(r), "s"(so), "n"(K * 1024) : "memory");
  else asm volatile("buffer_load_dwordx4 %0, %1, %2 offen offset:%3 lds" :: "v"(v), "s"(r), "s"(so), "n"(K * 1024) : "memory");
}
DI void set_m0(unsigned lds_dst) { asm volatile("s_mov_b32 m0, %0\n\ts_nop 0" :: "s"(lds_dst) : "memory"); }
DI void dma_x4(const unsigned char* sbase, unsigned v, unsigned lds_dst) {
  asm volatile("s_mov_b32 m0, %2\n\ts_nop 0\n\tglobal_load_lds_dwordx4 %0, %1 sc1\n\tglobal_load_lds_dwordx4 %0, %1 offset:1024 sc1\n\t"
               "global_load_lds_dwordx4 %0, %1 offset:2048 sc1\n\tglobal_load_lds_dwordx4 %0, %1 offset:3072 sc1"
               :: "v"(v), "s"(sbase), "s"(lds_dst) : "memory");
}
template <int I> struct IC { static constexpr int v = I; };
template <int I, int N, class F> DI void static_for(F&& f) { if constexpr (I < N) { f(IC<I>{}); static_for<I + 1, N>(f); } }
DI unsigned now() { u64 t; asm volatile("s_memrealtime %0\n\ts_waitcnt lgkmcnt(0)" : "=s"(t) :: "memory"); return (unsigned)t; }
DI void reload(v4u& d, __amdgpu_buffer_rsrc_t r, int voff, int soff) { d = ldg(r, voff, soff); FRESH(); }
DI void reload1(unsigned& d, __amdgpu_buffer_rsrc_t r, int voff, int soff) { d = __builtin_amdgcn_raw_buffer_load_b32(r, voff, soff, SC1); FRESH(); }
template <int N> DI void waitv() { asm volatile("s_waitcnt vmcnt(%0)" :: "n"(N) : "memory"); }
struct ConvOrder { int t[36], u[36]; };
constexpr ConvOrder conv_order() {
  ConvOrder o = {};
  int done[8] = {}, last = -1;
  for (int k = 0; k < 36; ++k) {
    int best = -1;
    for (int t = 0; t < 8; ++t)
      if (t != last && done[t] <= t && (best < 0 || t - done[t] > best - done[best])) best = t;
    o.t[k] = best; o.u[k] = done[best]++; last = best;
  }
  return o;
}
constexpr ConvOrder conv_order_last() {
  ConvOrder o = {};
  int k = 0;
  for (int p = 0; p < 4; ++p)
    for (int u = 0; u <= 2 * p + 1; ++u) {
      o.t[k] = 2 * p + 1; o.u[k] = u; ++k;
      if (u <= 2 * p) { o.t[k] = 2 * p; o.u[k] = u; ++k; }
    }
  return o;
}
constexpr int epi_slot(int v) { return v < 32 ? 2 * EO + v : 2 * 50 - 1 + (v - 32); }
constexpr bool epi_pair(int j) { return (j >= EO && j < EO + 19) || (j >= 50 && j < 61); }
constexpr int hc_upto(int j) { return j < 50 ? 0 : j < 60 ? j - 50 : 10 + 2 * (j - 60); }
constexpr int hl_upto(int i) { return i < 9 ? 28 : i < 37 ? 28 + (i - 9) : 56; }
constexpr int c_ops(int j) {
  return (j >= 10 && j < 14 ? 2 : j < 34 || (j >= 35 && j < 51) ? 1 : 0) + hc_upto(j + 1) - hc_upto(j) +
         (j == EO + 6 || j == EO + 14 || j == 56 ? 1 : j == EO + 10 || j == EO + 18 || j == 60 ? 2 : 0);
}
constexpr int c_between(int a, int b) { int n = 0; for (int j = a + 1; j < b; ++j) n += c_ops(j); return n; }
DI unsigned tagbad(unsigned bad, v4u g, unsigned tagA, unsigned tagB) {
  return __builtin_amdgcn_bitop3_b32(__builtin_amdgcn_bitop3_b32(bad, g.y, tagA, 0xF6), g.w, tagB, 0xF6);
}
template <int R0, int R1, int R2, int R3> DI void put4(unsigned base, f32x2 a, f32x2 b, f32x2 c, f32x2 d) {
  asm volatile("s_mov_b32 m0, %4\n\ts_nop 0\n\tds_write_addtid_b32 %0 offset:%5\n\tds_write_addtid_b32 %1 offset:%6\n\t"
               "ds_write_addtid_b32 %2 offset:%7\n\tds_write_addtid_b32 %3 offset:%8"
               :: "v"(pack2(a.x, a.y)), "v"(pack2(b.x, b.y)), "v"(pack2(c.x, c.y)), "v"(pack2(d.x, d.y)), "s"(base),
                  "n"(R0 * UVS_ROW), "n"(R1 * UVS_ROW), "n"(R2 * UVS_ROW), "n"(R3 * UVS_ROW) : "memory");
}
DI unsigned sum4(unsigned m, v4u v) {
  asm("v_add3_u32 %0, %0, %1, %2\n\tv_add3_u32 %0, %0, %3, %4" : "+v"(m) : "v"(v.x), "v"(v.y), "v"(v.z), "v"(v.w));
  return m;
}
DI unsigned sum8(unsigned m, v4u a, v4u b) {
  asm("v_add3_u32 %0, %0, %1, %2\n\tv_add3_u32 %0, %0, %3, %4\n\tv_add3_u32 %0, %0, %5, %6\n\tv_add3_u32 %0, %0, %7, %8"
      : "+v"(m) : "v"(a.x), "v"(a.y), "v"(a.z), "v"(a.w), "v"(b.x), "v"(b.y), "v"(b.z), "v"(b.w));
  return m;
}
template <int I> DI unsigned red_step(unsigned v) {
  constexpr int ctrl[6] = {0xB1, 0x4E, 0x141, 0x140, 0x142, 0x143}, rows[6] = {0xF, 0xF, 0xF, 0xF, 0xA, 0xC};
  return v + (unsigned)__builtin_amdgcn_update_dpp(0, (int)v, ctrl[I], rows[I], 0xF, true);
}
DI unsigned red_all(unsigned v) { return red_step<5>(red_step<4>(red_step<3>(red_step<2>(red_step<1>(red_step<0>(v)))))); }
DI void swap_add32(float& a, float b) { const auto r = __builtin_amdgcn_permlane32_swap(f2u(a), f2u(b), false, false); a = u2f(r[0]) + u2f(r[1]); }
DI void swap_add16(float& a, float b) { const auto r = __builtin_amdgcn_permlane16_swap(f2u(a), f2u(b), false, false); a = u2f(r[0]) + u2f(r[1]); }

typedef const unsigned char* cptr;
typedef unsigned v16u __attribute__((ext_vector_type(16)));
DI cptr mkptr(unsigned lo, unsigned hi) { return (cptr)(((u64)hi << 32) | lo); }

__global__ void __launch_bounds__(256, 1)
s4_block_kernel(cptr, cptr, cptr, cptr, cptr, cptr, cptr, cptr, cptr, cptr, cptr, cptr, cptr, unsigned char*, unsigned char*, cptr) {
  STAMP(0);
  v16u ka0, ka1;
  asm volatile("s_load_dwordx16 %0, %2, 0x0\n\ts_load_dwordx16 %1, %2, 0x40" : "=&s"(ka0), "=&s"(ka1) : "s"(__builtin_amdgcn_kernarg_segment_ptr()));
  SB();
  LAS unsigned char* const L = (LAS unsigned char*)16u - 16;
  const int tid = threadIdx.x, lane = tid & 63, w = __builtin_amdgcn_readfirstlane(tid >> 6);
  const int b = blockIdx.x, xcd = b & 7, slot = b >> 3, id = xcd * 32 + slot;
  const int ks = (id >> 6) * 4 + w, cc = id & 63;
  const int rp = 4 * (xcd >> 1) + (slot >> 3), rg = xcd & 1, dt = slot & 7, si = 8 * rg + dt;
  const int ct = 7 * rg + min(dt, 6);
  const int rot = (0x0340 >> (4 * (2 * (w & 1) + rg))) & 7;

  const unsigned char* pcode;
  asm volatile("s_getpc_b64 %0" : "=s"(pcode));
  const unsigned warm = *(const GAS unsigned*)((const GAS unsigned char*)pcode + (unsigned)min((((slot & 7) * 4 + w) * 9 + min(lane, 8)) * 128, CODE_BYTES));
  const int g16 = lane >> 4, q4 = (lane & 15) >> 2, p4 = lane & 3, hf = lane >> 5, blk = (lane >> 4) & 1, l16 = lane * 16;
  const unsigned lrw = L_R + w * 16384;
  unsigned lsp = L_SP + w * 6144;
  unsigned wmo[3], co = (ks * 32 + (lane & 31)) * 2, xo = (id >> 2) * 32768 + (id & 3) * 256 + (tid & 7) * 4096 + (tid >> 3) * 8, ro = id * 8192 + w * 2048 + hf * 1024 + (lane & 31) * 16, po = id * 512 + tid * 2;
#pragma unroll
  for (int i = 0; i < 3; ++i) { const unsigned idx = lane + 64 * i; wmo[i] = (ks * 32 * 6144 + cc * 96 - i * 1024) + idx / 6 * (6144 - 96) + idx * 16; PIN(wmo[i]); }
  PIN(co); PIN(xo); PIN(ro); PIN(po);
  set_m0(lrw);
  SB();
  asm volatile("s_waitcnt lgkmcnt(0)" : "+s"(ka0), "+s"(ka1));
  const cptr xin = mkptr(ka0[0], ka0[1]), pAre = mkptr(ka0[2], ka0[3]), pAim = mkptr(ka0[4], ka0[5]), pBre = mkptr(ka0[6], ka0[7]);
  const cptr pBim = mkptr(ka0[8], ka0[9]), pCre = mkptr(ka0[10], ka0[11]), pCim = mkptr(ka0[12], ka0[13]), pldt = mkptr(ka0[14], ka0[15]);
  const cptr pD = mkptr(ka1[0], ka1[1]), w1 = mkptr(ka1[2], ka1[3]), w2 = mkptr(ka1[4], ka1[5]), pc = mkptr(ka1[6], ka1[7]);
  const cptr wmod = mkptr(ka1[8], ka1[9]), out = mkptr(ka1[10], ka1[11]), ws = mkptr(ka1[12], ka1[13]);
  const unsigned epoch_v = __hip_atomic_load((GAS unsigned*)ws, __ATOMIC_RELAXED, __HIP_MEMORY_SCOPE_AGENT);
  asm volatile("global_load_lds_dwordx4 %0, %3\n\tglobal_load_lds_dwordx4 %1, %3 offset:1024\n\tglobal_load_lds_dwordx4 %2, %3 offset:2048"
               :: "v"(wmo[0]), "v"(wmo[1]), "v"(wmo[2]), "s"(wmod) : "memory");
  const unsigned cval = *(const GAS u16*)((const GAS unsigned char*)pc + co);
  SB();
  v2u xq[4];
#pragma unroll
  for (int i = 0; i < 4; ++i) xq[i] = *(const GAS v2u*)((const GAS unsigned char*)xin + 1024 * i + xo);
  const v4u ra = *(const GAS v4u*)((const GAS unsigned char*)xin + ro), rb = *(const GAS v4u*)((const GAS unsigned char*)xin + 512 + ro);
  SB();
#define PARAM(p) (unsigned)*(const GAS u16*)((const GAS unsigned char*)(p) + po)
  const unsigned uAre = PARAM(pAre), uAim = PARAM(pAim), uBre = PARAM(pBre), uBim = PARAM(pBim), uCre = PARAM(pCre), uCim = PARAM(pCim);
#undef PARAM
  SB();
  const unsigned char* setp = ws + SET_BASE;
  const __amdgpu_buffer_rsrc_t rs = __builtin_amdgcn_make_buffer_rsrc((void*)setp, 0, SET_BYTES, 0x00020000);
  unsigned uldt, uD;
  asm volatile("s_load_dword %0, %2, %4\n\ts_load_dword %1, %3, %4" : "=&s"(uldt), "=&s"(uD) : "s"(pldt), "s"(pD), "s"(id * 4));
  SB();
  STAMPA(26);
  unsigned wto = id * 7168 + (w >> 1) * 3584 + min(lane, 27) * 128;
  PIN(wto);
  unsigned w1v[4], w2v[4];
#pragma unroll
  for (int i = 0; i < 4; ++i) {
    w1v[i] = (lane >> 3) * 3584 + (((lane & 7) ^ (blk << 2)) * 16) + i * (57344 - 1024);
    w2v[i] = (lane >> 3) * 1024 + (((lane & 7) ^ (blk << 2)) * 16) + i * (8192 - 1024);
    PIN(w1v[i]); PIN(w2v[i]);
  }
  int w1b = ct * 256 + (w >> 1) * 128 + (w & 1) * (8 * 3584), w2lo = (448 * w + 64 * rot) * 1024 + dt * 128;
  int w2c3 = w2lo - (rot >> 2) * (448 * 1024), w2hi = w2lo - ((rot + 1) >> 2) * (448 * 1024), so = 0;
  unsigned lrx = L_RX + w * 3072, lto = L_TOP + w * 1024;
  int zs = OFF_Z + (2 * id + (w >> 1)) * 64;
  int za = (((w & 1) + 2 * (g16 & 1)) * 8 + q4 * 2 + (g16 >> 1)) * 32768 + p4 * 16, zb = za + 32 * 32768;
  int tra = (hf * 8 + q4) * 64 + blk * 32 + p4 * 8;
  const int wsrc = (w & 2) + hf;
  int w1t = q4 * 128 + (((w & 1) ^ (q4 >> 1)) * 64) + blk * 32 + p4 * 8;
  LAS unsigned char *w1a = L + L_R + wsrc * 16384 + w1t, *w1s = L + L_SP + wsrc * 6144 + w1t;
  LAS unsigned char *w1x = L + L_RX + wsrc * 3072 + w1t, *w1y = L + L_TOP + wsrc * 1024 + w1t;
#define W1PULL(step, n) w1f[step] = trd2(((n) < 16 ? w1a : w1s) + ((n) & 15) * 1024, ((n) < 16 ? w1a : w1s) + ((n) & 15) * 1024 + 512)
  LAS unsigned char* ka = L + L_KC + ((w >> 1) * 4 + q4) * KC_STRIDE + 2 * (32 - 8 * g16 + 4 * p4) - 8 + 256 * (w & 1);
  LAS unsigned char* hb = L + L_H + (w >> 1) * H_STRIDE + (lane & 15) * 32 + g16 * 16 + 256 - 256 * (w & 1);
  LAS unsigned char* zp = L + sel(tid < 64, L_KC + ((lane >> 1) & 7) * KC_STRIDE + (lane & 1) * 16, L_H + hf * H_STRIDE + min(lane & 31, 29) * 16);
  LAS unsigned char* ub = L + L_UVS + (lane & 15) * UVS_ROW + g16 * 16;
  int gmo = OFF_MG + (g16 * 256 + id) * 256 + (lane & 15) * 16, gmo2 = OFF_MG + ((4 + blk) * 256 + id) * 256 + (lane & 15) * 16;
  int mgo = sel(lane < 48, (lane >> 1) * 256 + (lane & 1) * 8, OOB), sgo = sel(lane & 31, OOB, hf * 8);
  LAS unsigned char* xbw = L + L_XB + ((w & 1) * 4 + (w >> 1)) * 512 + lane * 8;
  int xcw = sel(lane & 7, OOB, tid), xcr = sel(lane & 7, OOB, (tid >> 3) * 1024), xgw = (tid >> 3) * 16384 + (tid & 7) * 16;
  PIN(xcw); PIN(xcr); PIN(xgw);
  LAS unsigned char* xbr = L + L_XB + w * 2048 + (lane & 15) * 32 + g16 * 8;
  LAS unsigned char *exa = L + L_XB + w * 2048 + l16, *exb = L + L_RX + w * 3072 + l16, *eya = L + L_XB + (w ^ 1) * 2048 + l16, *eyb = L + L_RX + (w ^ 1) * 3072 + l16;
  PIN(xbw); PIN(xbr); PIN(exa); PIN(exb); PIN(eya); PIN(eyb);
  int mto = (tid >> 1) * 128 + (tid & 1) * 16;
  LAS unsigned char* mtw = L + (tid & 1) * L_TG + (tid >> 1) * 4;
  PIN(mto); PIN(mtw);
  PIN(za); PIN(zb); PIN(tra); PIN(w1a); PIN(w1s); PIN(w1x); PIN(w1y); PIN(ka); PIN(hb); PIN(zp); PIN(ub); PIN(gmo); PIN(gmo2); PIN(mgo); PIN(sgo);
  asm volatile("" : "+s"(w1b), "+s"(w2lo), "+s"(w2c3), "+s"(w2hi), "+s"(lsp), "+s"(lrx), "+s"(lto), "+s"(zs));
#define W1P(n, k, lbase, li) do { if constexpr ((k) == 0) grp<(li), (n) * 57344>(so, lbase, w1b); xfer<(k), false>(w1v[k], rw1, so); } while (0)
#define W2P(n, k, lbase, li) do { if constexpr ((k) == 0) grp<(li), (n) * 8192>(so, lbase, (n) < 24 ? w2lo : (n) < 32 ? w2c3 : w2hi); xfer<(k), false>(w2v[k], rw2, so); } while (0)
  SB();
  LAS unsigned char* koff[12];
  {
    float fj = (float)(8 * tid - 3);
    PIN(fj);
    const unsigned lin = (unsigned)(8 * tid - 3 + 0x4300 - 128) * 0x10001u + 0x10000u;
#pragma unroll
    for (int i = 0; i < 12; i += 2) {
      const unsigned r = sel(tid < 32, lin + i * 0x10001u, pack2(fj + (float)i, fj + (float)(i + 1)));
      koff[i] = L + ((r & 0xFFFFu) * 4 + (L_KT + (128 - 0x4300) * 4));
      koff[i + 1] = L + ((r >> 16) * 4 + (L_KT + (128 - 0x4300) * 4));
      PIN(koff[i]);
      if (i < 10) PIN(koff[i + 1]);
    }
  }
  *(LAS v4u*)(L + L_KT - 16) = v4u{0u, 0u, 0u, 0u};
  LAS unsigned char* ktw[4];
  {
    const int row = w * 16 + (lane & 15), sg = max((row >> 3) - 1, 0), rr_ = row - 8 * sg;
    const int idx0 = 128 * sg + ((rr_ >> (3 - sg)) << (7 - sg)) + (rr_ & ((8 >> sg) - 1)) + 1, st = 8 >> sg;
#pragma unroll
    for (int r = 0; r < 4; ++r) {
      const int idx = sel(row < 40, idx0 + st * (4 * g16 + r), tid == 136 && r == 0 ? 0 : 641 + lane);
      ktw[r] = L + L_KT + idx * 4;
      PIN(ktw[r]);
    }
  }
  const __amdgpu_buffer_rsrc_t rw1 = __builtin_amdgcn_make_buffer_rsrc((void*)w1, 0, 1835008, 0x00020000);
  const __amdgpu_buffer_rsrc_t rw2 = __builtin_amdgcn_make_buffer_rsrc((void*)w2, 0, 1835008, 0x00020000);
  SB();
  STAMPA(27);
  asm volatile("s_waitcnt lgkmcnt(0)" : "+s"(uldt), "+s"(uD));
  const unsigned epoch = __builtin_amdgcn_readfirstlane(epoch_v);
  asm volatile("" :: "s"(epoch));
  STAMPA(28);
  const unsigned tagA = epoch * 0x9E3779B1u + 0x7F4A7C15u, tagB = ~tagA;
  {
    const float c = bf_lo(cval);
    const float s = c * __builtin_amdgcn_rcpf(1.0f + __builtin_amdgcn_exp2f(-LOG2E * c));
    *(LAS u16*)(L + lrw + 3072 + (lane & 31) * 2) = (u16)pack2(s, s);
    SB();
    asm volatile("" :: "v"(xq[3]));
    STAMPA(29);
    const v4u xc0 = {xq[0].x, xq[1].x, xq[2].x, xq[3].x}, xc1 = {xq[0].y, xq[1].y, xq[2].y, xq[3].y};
    stg(rs, xgw, OFF_XG + (id & 3) * 524288 + (id >> 2) * 128, xc0);
    stg(rs, xgw, OFF_XG + (id & 3) * 524288 + (id >> 2) * 128 + 8192, xc1);
    SB();
    STAMP(1);
    LAS unsigned char* a = L + lrw + (8 * g16 + q4) * 96 + p4 * 8;
    const v4u sc = lds128(L + lrw + 3072 + g16 * 16);
    const v4u m0 = trd2(a, a + 384), m1 = trd2(a + 32, a + 416), m2 = trd2(a + 64, a + 448);
    SB();
    f32x2 fa[8];
#pragma unroll
    for (int i = 0; i < 4; ++i) { fa[i] = f32x2{bf_lo(ra[i]), bf_hi(ra[i])}; fa[4 + i] = f32x2{bf_lo(rb[i]), bf_hi(rb[i])}; }
    const f32x2 s2 = ((fa[0] + fa[1]) + (fa[2] + fa[3])) + ((fa[4] + fa[5]) + (fa[6] + fa[7]));
    float mean = s2.x + s2.y;
    PIN(mean);
    SB();
    const f32x4 c0 = mfma16(sc, m0, f32x4{}), c1 = mfma16(sc, m1, f32x4{}), c2 = mfma16(sc, m2, f32x4{});
    SB();
    mean = swap16_add(row_sum(mean)) * (1.0f / 512.0f);
    const unsigned k0 = red_step<2>(red_step<1>(red_step<0>(sum4(0u, xc0)))), k1 = red_step<2>(red_step<1>(red_step<0>(sum4(0u, xc1))));
    __builtin_amdgcn_raw_buffer_store_b64(v2u{tagA - k0, tagA - k1}, rs, xcw, OFF_XC + id * 256, SC1);
    PIN(mean);
    SB();
    __builtin_amdgcn_raw_buffer_store_b64(v2u{sel(lane < 16, f2u(c0[0]), sel(lane < 32, f2u(c1[0]), f2u(c2[0]))), (lane & 1) ? tagB : tagA}, rs, mgo,
                                          OFF_MG + cc * 24 * 256 + ks * 16, SC1);
    SB();
    f32x2 q2 = {0.f, 0.f}, q3 = {0.f, 0.f};
#pragma unroll
    for (int i = 0; i < 8; i += 2) {
      const f32x2 d0 = fa[i] - f32x2{mean, mean}, d1 = fa[i + 1] - f32x2{mean, mean};
      q2 = __builtin_elementwise_fma(d0, d0, q2);
      q3 = __builtin_elementwise_fma(d1, d1, q3);
    }
    q2 += q3;
    const float var = rbf(rbf(swap16_add(row_sum(q2.x + q2.y)) * (1.0f / 512.0f)) + 1e-5f);
    __builtin_amdgcn_raw_buffer_store_b64(v2u{pack2(mean, __builtin_amdgcn_sqrtf(var)), hf ? tagB : tagA}, rs, sgo, OFF_SG + ((w & 1) * 512 + id * 2 + (w >> 1)) * 16, SC1);
    SB();
  }
  unsigned wt;
  SB();
  STAMP(2);
  unsigned dsk = tid == 136 ? uD : 0u;
  PIN(dsk);

  {
    const float dtv = rbf(__builtin_amdgcn_exp2f(LOG2E * (w >> 1 ? bf_hi(uldt) : bf_lo(uldt))));
    const float are = dtv * bf_lo(uAre) * LOG2E, aim = dtv * bf_lo(uAim) * INV2PI;
    const float br = dtv * bf_lo(uBre), bi = dtv * bf_lo(uBim), cr = bf_lo(uCre), ci = bf_lo(uCim);
    const f32x2 wv = {cr * br - ci * bi, cr * bi + ci * br};
    const unsigned row = L_UVS + (w >> 1) * UVS_CH + (w & 1) * 256;
    const f32x2 e1 = cexp(are, aim), e8 = cexp(8.f * are, -8.f * aim), e128 = cexp(128.f * are, 128.f * aim);
    const f32x2 e1p = {-e1.y, e1.x}, e8p = {-e8.y, e8.x}, e128p = {-e128.y, e128.x};
    const v4u pois = {POISON, POISON, POISON, POISON};
    f32x2 z[8], v, v2, y, y2;
#define M1(a) cmul(a, e1, e1p)
#define M8(a) cmul(a, e8, e8p)
#define MK(a) cmul(a, e128, e128p)
    z[0] = M1(wv);
    put4<40, 41, 42, 0>(row, wv, f32x2{1.f, 0.f}, e8, z[0]);
    asm volatile("global_load_dword %0, %1, %2" : "=v"(wt) : "v"(wto), "s"(w1 + (w & 1) * (w2 - w1)), "v"(warm) : "memory");
    v = M8(e8); z[1] = M1(z[0]); v2 = M8(v); z[2] = M1(z[1]);
    put4<43, 1, 44, 2>(row, v, z[1], v2, z[2]);
    W1P(0, 0, lrw, 0); W1P(1, 1, lrw, 0); W1P(2, 2, lrw, 0); W1P(3, 3, lrw, 0);
    v = M8(v2); z[3] = M1(z[2]); v2 = M8(v); z[4] = M1(z[3]);
    put4<45, 3, 46, 4>(row, v, z[3], v2, z[4]);
    v = M8(v2); z[5] = M1(z[4]); v2 = M8(v); z[6] = M1(z[5]);
    put4<47, 5, 48, 6>(row, v, z[5], v2, z[6]);
    z[7] = M1(z[6]); z[0] = MK(z[0]); z[1] = MK(z[1]); z[2] = MK(z[2]);
    put4<7, 8, 9, 10>(row, z[7], z[0], z[1], z[2]);
    STAMPC(26);
    stg(rs, (tid >> 3) * 32768 + (tid & 7) * 16, OFF_Z + id * 128, pois);
    STAMPC(27);
    z[3] = MK(z[3]); z[4] = MK(z[4]); z[5] = MK(z[5]); z[6] = MK(z[6]);
    put4<11, 12, 13, 14>(row, z[3], z[4], z[5], z[6]);
    z[7] = MK(z[7]); z[1] = MK(z[1]); z[3] = MK(z[3]); z[5] = MK(z[5]);
    put4<15, 16, 17, 18>(row, z[7], z[1], z[3], z[5]);
    z[7] = MK(z[7]); z[1] = MK(z[1]); z[3] = MK(z[3]); z[5] = MK(z[5]);
    put4<19, 20, 21, 22>(row, z[7], z[1], z[3], z[5]);
    stg(rs, (tid >> 3) * 32768 + (tid & 7) * 16, OFF_Z + id * 128 + 32 * 32768, pois);
    STAMPC(28);
    y = MK(z[7]); z[3] = MK(z[3]); y2 = MK(y); z[4] = MK(z[3]);
    put4<23, 24, 25, 26>(row, y, z[3], y2, z[4]);
    y = MK(y2); z[3] = MK(z[4]); y2 = MK(y); z[4] = MK(z[3]);
    put4<27, 28, 29, 30>(row, y, z[3], y2, z[4]);
    y = MK(y2); v = M8(v2); y2 = MK(y); v2 = M8(v);
    put4<31, 49, 32, 50>(row, y, v, y2, v2);
    y = MK(y2); v = M8(v2); y2 = MK(y); v2 = M8(v);
    put4<33, 51, 34, 52>(row, y, v, y2, v2);
    y = MK(y2); v = M8(v2); y2 = MK(y); v2 = M8(v);
    put4<35, 53, 36, 54>(row, y, v, y2, v2);
    y = MK(y2); v = M8(v2); y2 = MK(y); v2 = M8(v);
    put4<37, 55, 38, 56>(row, y, v, y2, v2);
    y = MK(y2);
    *(LAS unsigned*)(L + row + 39 * UVS_ROW + lane * 4) = pack2(y.x, y.y);
    STAMPC(29);
    W1P(4, 0, lrw, 4096); W1P(5, 1, lrw, 0); W1P(6, 2, lrw, 0); W1P(7, 3, lrw, 0);
#undef M1
#undef M8
#undef MK
  }
  BAR();
  STAMP(3);
  v4u gx0, gx1, gs0, gs1, gs2, gs3, gm, gn;
  unsigned gc0, gc1;
  const int xgo = OFF_XG + id * 8192, xco = OFF_XC + id * 4;
  v4u fv[2][8], fu[2][8];
  if (w < 3) {
    LAS unsigned char* ut = ub + w * 16 * UVS_ROW;
#pragma unroll
    for (int s = 0; s < 8; ++s)
#pragma unroll
      for (int c = 0; c < 2; ++c) { fv[c][s] = lds128(ub + c * UVS_CH + 41 * UVS_ROW + s * 64); fu[c][s] = lds128(ut + c * UVS_CH + s * 64); SB(); }
    f32x4 a0 = {}, a1 = {};
#pragma unroll
    for (int s = 0; s < 8; ++s) { a0 = mfma16(fv[0][s], fu[0][s], a0); SB(); a1 = mfma16(fv[1][s], fu[1][s], a1); SB(); }
    *(LAS unsigned*)ktw[0] = pack2(a0[0] + bf_lo(dsk), a1[0] + bf_hi(dsk));
#pragma unroll
    for (int r = 1; r < 4; ++r) *(LAS unsigned*)ktw[r] = pack2(a0[r], a1[r]);
  }
  SB();
#define LD1(i, so) "buffer_load_dwordx4 %" #i ", %10, %14, " so " offen sc1\n\t"
  asm volatile("buffer_load_dwordx4 %6, %11, %14, 0 offen sc1\n\tbuffer_load_dwordx4 %7, %12, %14, 0 offen sc1\n\t"
               LD1(0, "%15") LD1(1, "%16") "buffer_load_dword %8, %13, %14, %21 offen sc1\n\tbuffer_load_dword %9, %13, %14, %22 offen sc1\n\t"
               LD1(2, "%17") LD1(3, "%18") LD1(4, "%19") LD1(5, "%20")
               : "=&v"(gx0), "=&v"(gx1), "=&v"(gs0), "=&v"(gs1), "=&v"(gs2), "=&v"(gs3), "=&v"(gm), "=&v"(gn), "=&v"(gc0), "=&v"(gc1)
               : "v"(tid * 16), "v"(gmo), "v"(gmo2), "v"(xcr), "s"(rs), "s"(xgo), "s"(xgo + 4096),
                 "s"(OFF_SG), "s"(OFF_SG + 8192), "s"(OFF_SG + 4096), "s"(OFF_SG + 12288), "s"(xco), "s"(xco + 32768) : "memory");
#undef LD1
  SB();
  BAR();
  {
    unsigned k[11];
#pragma unroll
    for (int i = 0; i < 11; ++i) k[i] = *(LAS unsigned*)koff[i];
    SB();
    if (w < 2) *(LAS v4u*)zp = v4u{0u, 0u, 0u, 0u};
#pragma unroll
    for (int c = 0; c < 2; ++c) {
      unsigned e[5], o[5];
#pragma unroll
      for (int i = 0; i < 5; ++i) {
        o[i] = __builtin_amdgcn_perm(k[2 * i + 1], k[2 * i], c ? 0x07060302u : 0x05040100u);
        e[i] = __builtin_amdgcn_perm(k[2 * i + 2], k[2 * i + 1], c ? 0x07060302u : 0x05040100u);
      }
      LAS unsigned char* d = L + L_KC + c * 4 * KC_STRIDE + 32 + tid * 16;
      *(LAS v4u*)(d) = v4u{e[1], e[2], e[3], e[4]};
      *(LAS v4u*)(d + KC_STRIDE) = v4u{o[1], o[2], o[3], o[4]};
      *(LAS v4u*)(d + 2 * KC_STRIDE) = v4u{e[0], e[1], e[2], e[3]};
      *(LAS v4u*)(d + 3 * KC_STRIDE) = v4u{o[0], o[1], o[2], o[3]};
    }
  }
  SB();
  STAMP(4);
  {
    asm volatile("s_waitcnt vmcnt(0)" : "+v"(gx0), "+v"(gx1), "+v"(gs0), "+v"(gs1), "+v"(gs2), "+v"(gs3), "+v"(gm), "+v"(gn), "+v"(gc0), "+v"(gc1), "+v"(wt) :: "memory");
#define XBAD() (red_step<2>(red_step<1>(red_step<0>(sum8(gc0 + gc1, gx0, gx1)))) ^ (2u * tagA))
#define TB(a, c) tagbad(tagbad(0u, a, tagA, tagB), c, tagA, tagB)
    if (__builtin_expect(__any((XBAD() | TB(gm, gn) | TB(gs0, gs1) | TB(gs2, gs3)) != 0u), 0)) {
      _Pragma("nounroll") for (unsigned spin = 0; spin < SPIN_CAP; ++spin) {
        if (spin) PAUSE();
        COUNT(20);
        if (XBAD()) { reload(gx0, rs, tid * 16, xgo); reload(gx1, rs, tid * 16, xgo + 4096); reload1(gc0, rs, xcr, xco); reload1(gc1, rs, xcr, xco + 32768); }
        if (TB(gs0, gs1)) { reload(gs0, rs, tid * 16, OFF_SG); reload(gs1, rs, tid * 16, OFF_SG + 8192); }
        if (TB(gs2, gs3)) { reload(gs2, rs, tid * 16, OFF_SG + 4096); reload(gs3, rs, tid * 16, OFF_SG + 12288); }
        if (TB(gm, gn)) { reload(gm, rs, gmo, 0); reload(gn, rs, gmo2, 0); }
        if (!__any((XBAD() | TB(gm, gn) | TB(gs0, gs1) | TB(gs2, gs3)) != 0u)) break;
      }
    }
#undef XBAD
#undef TB
  }
  STAMP(5);
  W1P(8, 0, lrw, 8192); W1P(9, 1, lrw, 0);
  float zsc;
  v4u mtp;
  {
    float r0 = u2f(gm.x), r1 = u2f(gm.z), r2 = u2f(gn.x), r3 = u2f(gn.z);
#define RSTEP(c) r0 = dpp_add<c>(r0); PIN(r0); r1 = dpp_add<c>(r1); PIN(r1); r2 = dpp_add<c>(r2); PIN(r2); r3 = dpp_add<c>(r3); PIN(r3)
    RSTEP(0xB1); RSTEP(0x4E); RSTEP(0x141); RSTEP(0x140);
#undef RSTEP
    const int i0 = (int)pack2(r0, r1), i1 = (int)pack2(r2, r3);
    unsigned md[6];
#pragma unroll
    for (int k = 0; k < 6; ++k) md[k] = (unsigned)__builtin_amdgcn_readlane(k < 4 ? i0 : i1, 16 * (k & 3));
#define MOD(k) f32x2{bf_lo(md[k]), bf_hi(md[k])}
    const f32x2 one = {1.0f, 1.0f}, sr = one + MOD(4), cr = one + MOD(1);
    unsigned sp = pack2(sr.x, sr.y);
    const unsigned cp = pack2(cr.x, cr.y);
    sp |= ((sp & 0x7FFFu) ? 0u : 0x3080u) | ((sp & 0x7FFF0000u) ? 0u : 0x30800000u);
    const f32x2 S = {bf_lo(sp), bf_hi(sp)}, G = MOD(2) * S, A = f32x2{bf_lo(cp), bf_hi(cp)} * G, B = MOD(0) * G;
#undef MOD
    zsc = w >> 1 ? S.y : S.x;
    mtp = v4u{sel(lane, md[5], sp), tagA, md[3], tagB};
    auto rows4 = [&](const v4u& gx, const v4u& ga, const v4u& gb_, int i) __attribute__((always_inline)) {
      const f32x2 ma = {bf_lo(ga.x), bf_lo(ga.z)}, mb = {bf_lo(gb_.x), bf_lo(gb_.z)};
      const f32x2 ra_ = {__builtin_amdgcn_rcpf(bf_hi(ga.x)), __builtin_amdgcn_rcpf(bf_hi(ga.z))}, rb_ = {__builtin_amdgcn_rcpf(bf_hi(gb_.x)), __builtin_amdgcn_rcpf(bf_hi(gb_.z))};
      const f32x2 h0 = __builtin_elementwise_fma((f32x2{bf_lo(gx.x), bf_lo(gx.y)} - ma) * ra_, f32x2{A.x, A.x}, f32x2{B.x, B.x});
      const f32x2 h1 = __builtin_elementwise_fma((f32x2{bf_lo(gx.z), bf_lo(gx.w)} - mb) * rb_, f32x2{A.x, A.x}, f32x2{B.x, B.x});
      const f32x2 h2 = __builtin_elementwise_fma((f32x2{bf_hi(gx.x), bf_hi(gx.y)} - ma) * ra_, f32x2{A.y, A.y}, f32x2{B.y, B.y});
      const f32x2 h3 = __builtin_elementwise_fma((f32x2{bf_hi(gx.z), bf_hi(gx.w)} - mb) * rb_, f32x2{A.y, A.y}, f32x2{B.y, B.y});
      *(LAS v2u*)(L + L_H + 480 + tid * 8 + i * 2048) = v2u{pack2(h0.x, h0.y), pack2(h1.x, h1.y)};
      *(LAS v2u*)(L + L_H + H_STRIDE + 480 + tid * 8 + i * 2048) = v2u{pack2(h2.x, h2.y), pack2(h3.x, h3.y)};
      *(LAS v2u*)(xbw + i * 1024) = v2u{__builtin_amdgcn_perm(gx.y, gx.x, 0x05040100u), __builtin_amdgcn_perm(gx.w, gx.z, 0x05040100u)};
      *(LAS v2u*)(xbw + 4096 + i * 1024) = v2u{__builtin_amdgcn_perm(gx.y, gx.x, 0x07060302u), __builtin_amdgcn_perm(gx.w, gx.z, 0x07060302u)};
    };
    W1P(10, 2, lrw, 0); W1P(11, 3, lrw, 0);
    rows4(gx0, gs0, gs1, 0);
    W1P(12, 0, lrw, 12288); W1P(13, 1, lrw, 0); W1P(14, 2, lrw, 0); W1P(15, 3, lrw, 0);
    rows4(gx1, gs2, gs3, 1);
    W1P(16, 0, lsp, 0); W1P(17, 1, lsp, 0);
  }
  BAR();
  const unsigned tb = now();
  STAMP(6);

  v4u w1f[32], w2f[28][2], mt0, mt1;
  {
    const bool rh = w & 1;
    constexpr ConvOrder O = conv_order(), OL = conv_order_last();
    v2u A[4][8][2], xk[4];
    v4u B[4][8];
    f32x4 acc[8], keep[4];
#pragma unroll
    for (int u = 0; u < 8; ++u) {
      A[0][u][0] = trd(ka + 512 * u + 8);
      A[0][u][1] = trd(ka + 512 * u);
      B[0][7 - u] = lds128(hb + 64 * (8 * (7 - u) + 3));
    }
    SB();
    if (lane) mtp.z = tb;
    stg(rs, tid < 2 ? tid * 16 : OOB, OFF_MT + id * 128, mtp);
    SB();
    auto hand_over = [&](auto Pc) __attribute__((always_inline)) {
      constexpr int i = Pc.v;
      PIN(acc[2 * i]); PIN(acc[2 * i + 1]);
      const f32x4 ev = acc[2 * i], od = acc[2 * i + 1];
      const f32x4 snd = rh ? ev : od, own = rh ? od : ev;
      *(LAS f32x4*)(i < 2 ? exa + i * 1024 : exb + (i - 1) * 1024) = snd;
#pragma unroll
      for (int c = 0; c < 4; ++c) { float t = keep[i][c] + own[c]; PIN(t); keep[i][c] = t; }
    };
    static_for<0, 144>([&](auto Kc) __attribute__((always_inline)) {
      constexpr int K = Kc.v, r = K / 36, k = K % 36, t = r < 3 ? O.t[k] : OL.t[k], u = r < 3 ? O.u[k] : OL.u[k];
      if constexpr (k == 0 && r > 0) asm volatile("" :: "v"(B[r][7]));
      mfma16_v<r == 0 && u == 0>(acc[t], v4u{A[r][u][0].x, A[r][u][0].y, A[r][u][1].x, A[r][u][1].y}, B[r][t - u]);
      SB();
      if constexpr (K == 24) { WAITV(13); __builtin_amdgcn_s_barrier(); FRESH(); }
      if constexpr (K >= 24 && K < 36) W1PULL(K - 24, K - 24);
      if constexpr (K == 40) { asm volatile("" :: "a"(w1f[11])); __builtin_amdgcn_s_barrier(); FRESH(); }
      if constexpr (K == 60) { WAITV(9); __builtin_amdgcn_s_barrier(); FRESH(); }
      if constexpr (K >= 60 && K < 70) W1PULL(K - 48, K - 48);
      if constexpr (K == 76) { asm volatile("" :: "a"(w1f[21])); __builtin_amdgcn_s_barrier(); FRESH(); }
      if constexpr (K == 142) WAITV(16);
      if constexpr (r == 2 && k < 4) xk[k] = *(LAS v2u*)(xbr + k * 512);
      constexpr int n = k - (r < 2 ? 0 : 4);
      if constexpr (r < 3 && n >= 0 && n < 24) {
        if constexpr (n % 3 == 2) B[r + 1][n / 3] = lds128(hb + 64 * (8 * (n / 3) - r + 2));
        else A[r + 1][n / 3][n % 3] = trd(ka + 512 * (n / 3) + 64 * (r + 1) + 8 - 8 * (n % 3));
      }
      if constexpr (K >= 100 && K < 116) {
        constexpr int e = K - 100;
        const unsigned xw = (e & 2) ? xk[e >> 2].y : xk[e >> 2].x;
        float t2_ = zsc * ((e & 1) ? bf_hi(xw) : bf_lo(xw));
        PIN(t2_);
        keep[e >> 2][e & 3] = t2_;
      }
      if constexpr (K == 108 + 8) hand_over(IC<0>{});
      if constexpr (K == 108 + 17) hand_over(IC<1>{});
      if constexpr (K == 108 + 30) hand_over(IC<2>{});
      if constexpr (K < 14 && K % 4 == 1) W1P(18 + K / 4, (2 + K / 4) & 3, lsp, 4096);
      if constexpr (K == 17) W1P(22, 0, lrx, 0);
      if constexpr (K == 21) W1P(23, 0, lto, 0);
      if constexpr (K >= 41 && K < 65 && K % 3 == 2) W1P(24 + (K - 41) / 3, ((K - 41) / 3) & 3, lrw, (K - 41) / 3 * 1024);
      if constexpr (K >= 66 && K < 98 && K % 4 == 2) W2P((K - 66) / 4, ((K - 66) / 4) & 3, lrw, 8192 + (K - 66) / 4 * 1024);
      if constexpr (K >= 98 && K < 122 && K % 4 == 2) W2P((K - 66) / 4, ((K - 98) / 4) & 3, lsp, (K - 98) / 4 * 1024);
      if constexpr (K == MTK)
        asm volatile("buffer_load_dwordx4 %0, %2, %3, 0 offen sc1\n\tbuffer_load_dwordx4 %1, %2, %3, %4 offen sc1"
                     : "=&v"(mt0), "=&v"(mt1) : "v"(mto), "s"(rs), "s"(16384) : "memory");
      SB();
    });
    STAMP(7);
    asm volatile("s_nop 7" : "+v"(acc[6]), "+v"(acc[7]));
    hand_over(IC<3>{});
    BAR();
#pragma unroll
    for (int i = 0; i < 4; ++i) keep[i] += *(LAS f32x4*)(i < 2 ? eya + i * 1024 : eyb + (i - 1) * 1024);
#pragma unroll
    for (int i = 0; i < 2; ++i) {
      const auto s0 = __builtin_amdgcn_permlane16_swap(pack2(keep[2 * i][0], keep[2 * i][1]), pack2(keep[2 * i + 1][0], keep[2 * i + 1][1]), false, false);
      const auto s1 = __builtin_amdgcn_permlane16_swap(pack2(keep[2 * i][2], keep[2 * i][3]), pack2(keep[2 * i + 1][2], keep[2 * i + 1][3]), false, false);
      stg(rs, i ? zb : za, zs, v4u{guard(s0[0]), guard(s1[0]), guard(s0[1]), guard(s1[1])});
    }
    SB();
  }
  STAMP(8);

  static_assert(OFF_MT == 0, "table offset");
  const unsigned te = now();
  const int w2t = (4 * hf + q4) * 128 + blk * 32 + (((p4 & 1) << 1) | (p4 >> 1)) * 8;
  LAS unsigned char* w2a = L + w2t + (q4 >> 1) * 64;
  LAS unsigned char* w2b = L + w2t + ((q4 >> 1) ^ 1) * 64;
  LAS unsigned char *w2sa = w2a + lsp, *w2sb = w2b + lsp;
  PIN(w2sa); PIN(w2sb);
#define PULL(step, pa, pb, off) do { w2f[step][0] = trd2((pa) + (off), (pa) + (off) + 1024); w2f[step][1] = trd2((pb) + (off), (pb) + (off) + 1024); } while (0)
  const int par = (0x1C >> (ct % 7)) & 1;
  const unsigned char* xg = setp + OFF_Z + rp * 131072;
  unsigned xl = L_XR + w * 8192;
  int xs2 = OFF_Z + rp * 131072 + w * 8192 + (2 ^ par) * 32768, xs3 = xs2 + 32768 - 65536 * par;
  asm volatile("" : "+s"(xl), "+s"(xs2), "+s"(xs3));
  int slo = OFF_Z + (rp * 4 + w) * 32768 + si * 2048;
  v4u hq[56], rc[4], res[2], sx[8];
  u64 nanv = 0;
  const int c0 = 7 * w + rot, c3 = c0 - 7 * (rot >> 2), c4 = c0 - 7 * ((rot + 1) >> 2);
  const int hb0 = OFF_HID + (rp * 4 + rg) * 114688, kb0 = OFF_CK + (rp * 4 + rg) * 4096 + w * 896, ckv = lane < 56 ? l16 : OOB;
#define CHUNK(n) (((n) % 28 / 4 < 3 ? c0 : (n) % 28 / 4 == 3 ? c3 : c4) + (n) % 28 / 4)
#define HFRAG(n) ldg(rs, l16 + ((n) & 3) * 1024, hb0 + CHUNK(n) * 4096 + ((n) / 28) * (2 * 114688))
#define HLOAD(n) hq[n] = HFRAG(n)
  const int hoff = OFF_HID + (rp * 4 * 112 + ct * 8 + 2 * w) * 1024;
  int hv0 = dt == 7 ? OOB : l16, hv1 = dt == 7 ? OOB : l16 + 1024;
  const int cko = OFF_CK + rp * 16384 + (ct * 4 + w) * 64;
  int ckw = (p4 || dt == 7) ? OOB : lane;
  PIN(hv0); PIN(hv1); PIN(ckw);
#define CKPUT(sum, u) __builtin_amdgcn_raw_buffer_store_b32(ckey - red_step<1>(red_step<0>(sum)), rs, ckw, cko + ((u) ^ par) * 4096, SC1)
  const int sxo = OFF_SX + (rp * 4 + w) * 8192;
  LAS unsigned char* xr[2];
#pragma unroll
  for (int i = 0; i < 2; ++i) { xr[i] = L + L_XR + tra + i * 65536; PIN(xr[i]); }
  LAS unsigned char *rr = L + L_XR + tra + (4 * dt + w) * 1024, *rr2 = rr + 65536;
  LAS unsigned char* str = L + L_STAT + (lane & 31) * 8;
  LAS unsigned char *pa = w2a + lrw + 8192, *pb = w2b + lrw + 8192;
  w2a += L_XR + w * 8192; w2b += L_XR + w * 8192;
  LAS unsigned char* stw = str + (w ^ par) * 256;
  PIN(str); PIN(stw); PIN(rr); PIN(rr2); PIN(w2a); PIN(w2b);
  LAS unsigned char* qr = L + (p4 ? L_TB : L_TS) + hf * 16;
  LAS unsigned char* uw = L + L_UVT + hf * 512 + (32 * w + (lane & 31)) * 4;
  LAS unsigned char* uvp = L + L_UVT + (32 * w + 4 * hf) * 4;
  LAS unsigned char* ivp = L + L_TS + (si * 32 + (lane >> 2)) * 2;
  int sxw = sel(lane & 8, OOB, (p4 * 8 + hf * 4 + blk * 2 + ((lane >> 2) & 1)) * 16);
  PIN(pa); PIN(pb); PIN(qr); PIN(uw); PIN(uvp); PIN(ivp); PIN(sxw);
  asm volatile("" : "+s"(slo));
  SB();
  asm volatile("s_waitcnt vmcnt(2)" : "+v"(mt0), "+v"(mt1) :: "memory");
  if (__builtin_expect(__any(tagbad(tagbad(0u, mt0, tagA, tagB), mt1, tagA, tagB) != 0u), 0)) {
    _Pragma("nounroll") for (unsigned spin = 0; spin < SPIN_CAP; ++spin) {
      FRESH();
      COUNT(21);
      if (tagbad(0u, mt0, tagA, tagB)) mt0 = ldg(rs, mto, OFF_MT);
      if (tagbad(0u, mt1, tagA, tagB)) mt1 = ldg(rs, mto, OFF_MT + 16384);
      if (!__any(tagbad(tagbad(0u, mt0, tagA, tagB), mt1, tagA, tagB) != 0u)) break;
      PAUSE();
    }
  }
  *(LAS unsigned*)(mtw) = mt0.x;
  *(LAS unsigned*)(mtw + 512) = mt1.x;
  *(LAS unsigned*)(mtw + 1024) = mt0.z;
  *(LAS unsigned*)(mtw + 1536) = mt1.z;
  {
    int late = (lane & 1) ? max((int)(mt0.z - tb), (int)(mt1.z - tb)) : 0;
    late = max(late, __builtin_amdgcn_update_dpp(late, late, 0xB1, 0xF, 0xF, false));
    late = max(late, __builtin_amdgcn_update_dpp(late, late, 0x4E, 0xF, 0xF, false));
    late = max(late, __builtin_amdgcn_update_dpp(late, late, 0x141, 0xF, 0xF, false));
    late = max(late, __builtin_amdgcn_update_dpp(late, late, 0x140, 0xF, 0xF, false));
    late = max(late, __builtin_amdgcn_update_dpp(late, late, 0x142, 0xA, 0xF, false));
    late = max(late, __builtin_amdgcn_update_dpp(late, late, 0x143, 0xC, 0xF, false));
    if (lane == 63) *(LAS int*)(L + L_ETA + w * 4) = late;
  }
  w1f[22] = trd2(w1x, w1x + 512);
  w1f[23] = trd2(w1y, w1y + 512);
  BAR();
  STAMP(9);
  f32x4 uu[4], vv[4];
  v4u sl0, sl1;
  unsigned ckey = lane ? 0u : tagA;
  {
    const v4u lt = lds128(L + L_ETA);
    const unsigned j0 = *(LAS u16*)ivp, j1 = *(LAS u16*)(ivp + 32);
    v4u qa[32];
#pragma unroll
    for (int i = 0; i < 4; ++i) qa[i] = lds128(qr + i * 32);
    SB();
    bool sent = false;
    auto send = [&]() __attribute__((always_inline)) {
      STAMPB(26);
      asm volatile("buffer_load_dwordx4 %0, %2, %3, %4 offen sc1\n\tbuffer_load_dwordx4 %1, %2, %3, %4 offen offset:1024 sc1"
                   :: "v"(sl0), "v"(sl1), "v"(l16), "s"(rs), "s"(slo) : "memory");
      static_for<0, 8>([&](auto Nc) __attribute__((always_inline)) {
        if constexpr ((Nc.v & 3) == 0) grp<Nc.v * 1024, Nc.v * 1024 - 65536>(so, xl, xs2);
        xfer<Nc.v & 3, true>(l16, rs, so);
      });
    };
    asm volatile("" : "=v"(sl0), "=v"(sl1));
    const int wait = min(__builtin_amdgcn_readfirstlane(max(max((int)lt.x, (int)lt.y), max((int)lt.z, (int)lt.w))), 300) + ZLAG;
    f32x4 c0_, c1_;
    static_for<0, 32>([&](auto Sc) __attribute__((always_inline)) {
      constexpr int s = Sc.v;
      if constexpr (s % 8 == 0) {
        if (!sent && (int)(now() - te) >= wait) { sent = true; send(); }
        SB();
      }
      mfma4_va<s == 0>(c0_, c1_, qa[s], w1f[s]);
      SB();
      if constexpr (s < 28) qa[s + 4] = lds128(qr + (s + 4) * 32);
      if constexpr (s < 8) W1PULL(24 + s, s);
      if constexpr (s >= 8 && s < 16) w2f[(s - 8) >> 1][s & 1] = trd2(((s & 1) ? pb : pa) + ((s - 8) >> 1) * 2048, ((s & 1) ? pb : pa) + ((s - 8) >> 1) * 2048 + 1024);
      if constexpr (s >= 16 && s < 22) w2f[4 + ((s - 16) >> 1)][s & 1] = trd2(((s & 1) ? w2sb : w2sa) + ((s - 16) >> 1) * 2048, ((s & 1) ? w2sb : w2sa) + ((s - 16) >> 1) * 2048 + 1024);
      SB();
    });
    float i0 = __builtin_amdgcn_rcpf(bf_lo(j0)), i1 = __builtin_amdgcn_rcpf(bf_lo(j1));
    PIN(i0); PIN(i1); PIN(ckey);
    asm volatile("s_nop 1" : "+v"(c0_), "+v"(c1_));
    float un_ = c0_[0] + c1_[0];
    swap_add32(un_, -LOG2E * (c0_[1] + c1_[1]));
    *(LAS float*)uw = un_;
#pragma unroll
    for (int i = 0; i < 4; ++i) { uu[i] = *(LAS f32x4*)(uvp + i * 32); vv[i] = *(LAS f32x4*)(uvp + 512 + i * 32); }
    SB();
    if (!sent) {
      while ((int)(now() - te) < wait) { }
      send();
    }
    static_for<0, 8>([&](auto Nc) __attribute__((always_inline)) {
      if constexpr ((Nc.v & 3) == 0) grp<32768 + Nc.v * 1024, Nc.v * 1024 - 65536>(so, xl, xs3);
      xfer<Nc.v & 3, true>(l16, rs, so);
    });
    STAMPB(27);
    asm volatile("s_waitcnt vmcnt(16)" : "+v"(sl0), "+v"(sl1) :: "memory");
    STAMPB(28);
    if (__builtin_expect(__any(max4(max4(0u, sl0), sl1) == POISON), 0)) {
      _Pragma("nounroll") for (unsigned spin = 0; spin < SPIN_CAP; ++spin) {
        FRESH();
        if (spin) PAUSE();
        COUNT(22);
        sl0 = ldg(rs, l16, slo);
        sl1 = ldg(rs, l16 + 1024, slo);
        if (!__any(max4(max4(0u, sl0), sl1) == POISON)) break;
      }
    }
    float sm[8], sq[8];
#pragma unroll
    for (int i = 0; i < 4; ++i) {
      const f32x2 a = f32x2{bf_lo(sl0[i]), bf_hi(sl0[i])} * f32x2{i0, i0}, c = f32x2{bf_lo(sl1[i]), bf_hi(sl1[i])} * f32x2{i1, i1};
      const f32x2 s2 = a + c, q2 = __builtin_elementwise_fma(a, a, c * c);
      sm[2 * i] = s2.x; sm[2 * i + 1] = s2.y; sq[2 * i] = q2.x; sq[2 * i + 1] = q2.y;
    }
#pragma unroll
    for (int i = 0; i < 4; ++i) { swap_add32(sm[i], sm[i + 4]); swap_add32(sq[i], sq[i + 4]); }
#pragma unroll
    for (int i = 0; i < 2; ++i) { swap_add16(sm[i], sm[i + 2]); swap_add16(sq[i], sq[i + 2]); }
#pragma unroll
    for (int i = 0; i < 2; ++i) { sm[i] = dpp_add<0x124>(dpp_add<0x128>(sm[i])); sq[i] = dpp_add<0x124>(dpp_add<0x128>(sq[i])); }
    const bool od = lane & 4;
    stg(rs, sxw, OFF_SX + ((rp * 4 + w) * 16 + si) * 512,
        v4u{f2u(od ? sm[1] : sm[0]), tagA, f2u(od ? sq[1] : sq[0]), tagB});
    SB();
  }
  STAMPB(29);
  W2P(14, 0, lsp, 0); W2P(15, 1, lsp, 0); W2P(16, 2, lsp, 0); W2P(17, 3, lsp, 0); W2P(18, 0, lsp, 4096); W2P(19, 1, lsp, 0);
  WAITV(15);
  STAMP(10);
  BAR();
  STAMP(11);

#define XA(t) (xr[(t) >> 6] + ((t) & 63) * 1024)
#define XF(t) trd2(XA(t), XA(t) + 256)
#define XEN
#define REDO(p) \
  if (XEN __builtin_expect(nanv != 0, 0)) { \
    unsigned m0s; \
    asm volatile("s_mov_b32 %0, m0" : "=s"(m0s)); \
    for (unsigned spin = 0; spin < SPIN_CAP / 16; ++spin) { \
      COUNT(23); \
      _Pragma("nounroll") for (int i = 0; i < 8; ++i) dma_x4(xg + ((p) ^ par) * 32768 + i * 4096, l16, L_XR + (p) * 32768 + i * 4096); \
      WAITV(0); \
      _Pragma("unroll") for (int s = 0; s < 32; ++s) { \
        const v4u t = XF(32 * (p) + s); \
        if (s == 0) mfma_av<true>(acc[(p) % 3], w1f[s], t); else mfma_av<false>(acc[(p) % 3], w1f[s], t); \
        SB(); \
      } \
      mfma_done(acc[(p) % 3]); \
      if (!__any(!(acc[(p) % 3][0] == acc[(p) % 3][0]))) break; \
    } \
    set_m0(m0s); \
  }
  f32x16 acc[3];
  f32x2 ec[4];
  float ep[48], ee[48], ey[48];
  unsigned cs = 0, sbad = 0;
  float ssm = 0.f, ssq = 0.f;
  v4u x[128], eo;
#pragma unroll
  for (int t = 0; t < 4; ++t) x[t] = XF(t);
  SB();
  auto eslot = [&](auto Sc) __attribute__((always_inline)) {
    static_for<0, 48>([&](auto Vc) __attribute__((always_inline)) {
      constexpr int V = Vc.v, k = V & 15, st = Sc.v - epi_slot(V);
      if constexpr (st == 6 && (k & 1)) {
        unsigned o = pack2(ey[V - 1], ey[V]);
        PIN(o);
        eo[(k >> 1) & 3] = o;
        if constexpr (k == 7) { stg(rs, hv0, hoff + ((V >> 4) ^ par) * 114688, eo); cs = sum4(0u, eo); PIN(cs); }
        if constexpr (k == 15) { stg(rs, hv1, hoff + ((V >> 4) ^ par) * 114688, eo); CKPUT(sum4(cs, eo), V >> 4); }
      }
      if constexpr (st == 5) { ey[V] = ep[V] * ee[V]; PIN(ey[V]); }
      if constexpr (st == 4) { ee[V] = __builtin_amdgcn_rcpf(ee[V]); PIN(ee[V]); }
      if constexpr (st == 3) { ee[V] = 1.0f + ee[V]; PIN(ee[V]); }
      if constexpr (st == 2) { ee[V] = __builtin_amdgcn_exp2f(ep[V]); PIN(ee[V]); }
      if constexpr (st == 1) { ep[V] = __builtin_fmaf(ec[V >> 4].x, acc[(V >> 4) % 3][k], ep[V]); PIN(ep[V]); }
      if constexpr (st == 0) { ep[V] = __builtin_fmaf(ec[V >> 4].y, uu[k >> 2][k & 3], vv[k >> 2][k & 3]); PIN(ep[V]); }
    });
    SB();
  };
  static_for<0, 64>([&](auto Jc) __attribute__((always_inline)) {
    constexpr int J = Jc.v, p = J >> 4, j = J & 15;
    asm volatile("" :: "v"(x[2 * J + 1]));
    mfma_av<j == 0>(acc[p % 3], w1f[2 * j], x[2 * J]);
    if constexpr (J < 10 || (J >= 14 && J < 20)) {
      constexpr int n = J < 10 ? J : J - 4;
      if constexpr ((n & 3) == 0) grp<65536 + (n >> 3) * 32768 + (n & 4) * 1024, (n & 4) * 1024>(so, xl, n < 8 ? xs2 : xs3);
      xfer<n & 3, true>(l16, rs, so);
    }
    if constexpr (J >= 10 && J < 14)
      asm volatile("buffer_load_dwordx4 %0, %2, %3, %4 offen offset:%5 sc1\n\tbuffer_load_dwordx4 %1, %2, %3, %4 offen offset:%6 sc1"
                   : "=&v"(sx[2 * (J - 10)]), "=&v"(sx[2 * (J - 10) + 1])
                   : "v"(l16), "s"(rs), "s"(sxo + ((J - 10) & 2) * 2048), "n"((J & 1) * 2048), "n"((J & 1) * 2048 + 1024) : "memory");
    if constexpr (J == 43) asm volatile("" :: "a"(w2f[13][1]));
    if constexpr (J >= 20 && J < 28) W2P(J, J & 3, xl, (J - 20) * 1024);
    if constexpr (J >= 28 && J < 34) W2P(J, J & 3, lsp, (J - 28) * 1024);
    if constexpr (J >= 35 && J < 43) W2P(J - 1, (J - 35) & 3, xl, 32768 + (J - 35) * 1024);
    if constexpr (J >= 43 && J < 51) W2P(J - 1, (J - 43) & 3, xl, (J - 43) * 1024);
    static_for<hc_upto(J), hc_upto(J + 1)>([&](auto Nc) __attribute__((always_inline)) { HLOAD(Nc.v); });
    SB();
    constexpr bool sym = epi_pair(J) || J < 19 || J > 60;
    if constexpr (J < 62) { x[2 * J + 4] = XF(2 * J + 4); if constexpr (!sym) x[2 * J + 5] = XF(2 * J + 5); }
    if constexpr (J >= 38 && J < 42) w2f[10 + J - 38][0] = trd2(w2a + (J - 38) * 2048, w2a + (J - 38) * 2048 + 1024);
    SB();
    if constexpr (epi_pair(J)) eslot(IC<2 * J>{});
    mfma_av<false>(acc[p % 3], w1f[2 * j + 1], x[2 * J + 1]);
    if constexpr (sym && J < 62) x[2 * J + 5] = XF(2 * J + 5);
    if constexpr (J >= 38 && J < 42) w2f[10 + J - 38][1] = trd2(w2b + (J - 38) * 2048, w2b + (J - 38) * 2048 + 1024);
    if constexpr (J >= 19 && J < 25) w2f[7 + ((J - 19) >> 1)][(J - 19) & 1] = trd2((((J - 19) & 1) ? w2sb : w2sa) + ((J - 19) >> 1) * 2048, (((J - 19) & 1) ? w2sb : w2sa) + ((J - 19) >> 1) * 2048 + 1024);
    SB();
    if constexpr (J == 10) { waitv<c_between(-1, 10)>(); __builtin_amdgcn_s_barrier(); FRESH(); }
    if constexpr (J == 34) waitv<c_between(19, 34)>();
    if constexpr (J == 37) waitv<c_between(27, 37)>();
    if constexpr (J == EO - 4 || J == EO - 3) {
      constexpr int i = 4 * (J - EO + 4);
      asm volatile("s_waitcnt vmcnt(%4)" : "+v"(sx[i]), "+v"(sx[i + 1]), "+v"(sx[i + 2]), "+v"(sx[i + 3]) : "n"(c_between(11 + i / 2, J)) : "memory");
      unsigned b0 = tagbad(0u, sx[i], tagA, tagB), b1 = tagbad(0u, sx[i + 1], tagA, tagB);
      float m0 = u2f(sx[i].x) + u2f(sx[i + 1].x), m1 = u2f(sx[i + 2].x) + u2f(sx[i + 3].x);
      PIN(m0); PIN(m1);
      float q0 = u2f(sx[i].z) + u2f(sx[i + 1].z), q1 = u2f(sx[i + 2].z) + u2f(sx[i + 3].z);
      PIN(q0); PIN(q1);
      sbad |= tagbad(b0, sx[i + 2], tagA, tagB) | tagbad(b1, sx[i + 3], tagA, tagB);
      ssm += m0 + m1; PIN(ssm);
      ssq += q0 + q1; PIN(ssq);
      PIN(sbad);
      SB();
    }
    if constexpr (J == EO - 2) {
      if (__builtin_expect(__any(sbad != 0u), 0)) {
        _Pragma("nounroll") for (unsigned spin = 0; spin < SPIN_CAP; ++spin) {
          FRESH();
          if (spin) PAUSE();
          COUNT(23);
          sbad = 0; ssm = 0.f; ssq = 0.f;
#pragma unroll
          for (int i = 0; i < 8; ++i) sx[i] = ldg(rs, l16 + (i & 3) * 1024, sxo + (i >> 2) * 4096);
#pragma unroll
          for (int i = 0; i < 8; i += 4) {
            sbad = tagbad(tagbad(tagbad(tagbad(sbad, sx[i], tagA, tagB), sx[i + 1], tagA, tagB), sx[i + 2], tagA, tagB), sx[i + 3], tagA, tagB);
            ssm += (u2f(sx[i].x) + u2f(sx[i + 1].x)) + (u2f(sx[i + 2].x) + u2f(sx[i + 3].x));
            ssq += (u2f(sx[i].z) + u2f(sx[i + 1].z)) + (u2f(sx[i + 2].z) + u2f(sx[i + 3].z));
          }
          if (!__any(sbad != 0u)) break;
        }
      }
      const float mean = swap32_add(ssm) * (1.0f / 512.0f);
      const float ms = __builtin_fmaf(swap32_add(ssq), 1.0f / 512.0f, 1e-5f);
      const float a = -LOG2E * __builtin_amdgcn_rsqf(fmaxf(__builtin_fmaf(-mean, mean, ms), 1e-5f));
      *(LAS f32x2*)(stw) = f32x2{a, -a * mean};
      SB();
    }
    if constexpr (J == EO - 1) {
      asm volatile("s_waitcnt lgkmcnt(4)" ::: "memory");
      __builtin_amdgcn_s_barrier();
      FRESH();
#pragma unroll
      for (int i = 0; i < 4; ++i) ec[i] = *(LAS f32x2*)(str + i * 256);
      SB();
    }
    if constexpr (!epi_pair(J)) eslot(IC<2 * J>{});
    eslot(IC<2 * J + 1>{});
    if constexpr (J == 16) asm volatile("s_nop 4");
    if constexpr (j == 0 && p >= 1) { PIN(acc[(p - 1) % 3]); nanv = __builtin_amdgcn_ballot_w64(!(acc[(p - 1) % 3][0] == acc[(p - 1) % 3][0])); asm volatile("" : "+s"(nanv)); }
    if constexpr (j == 1 && p >= 1) {
      if constexpr (p == 1) REDO(0)
      if constexpr (p == 2) REDO(1)
      if constexpr (p == 3) REDO(2)
      rc[p - 1] = trd2((p == 3 ? rr2 : rr) + ((p - 1) & 1) * 32768, (p == 3 ? rr2 : rr) + ((p - 1) & 1) * 32768 + 256);
    }
    if constexpr (j == 2 && p >= 1) {
      asm volatile("s_waitcnt lgkmcnt(4)" ::: "memory");
      __builtin_amdgcn_s_barrier();
      FRESH();
    }
    SB();
    if constexpr (J == 15) STAMP(16);
    if constexpr (J == 31) STAMP(17);
    if constexpr (J == 47) STAMP(18);
  });
  STAMP(12);
  static_assert(EO <= 30 && epi_slot(31) < epi_slot(32) && epi_slot(16) + 1 >= 2 * 34 && epi_slot(32) + 1 >= 2 * 50 && epi_slot(47) + 6 < 128, "schedule");
  static_for<18, 24>([&](auto Ic) __attribute__((always_inline)) { HLOAD(Ic.v); });
  SB();
  asm volatile("s_nop 3" : "+v"(acc[0]));
  nanv = __builtin_amdgcn_ballot_w64(!(acc[0][0] == acc[0][0]));
  REDO(3)
  rc[3] = trd2(rr2 + 32768, rr2 + 32768 + 256);
#pragma unroll
  for (int h = 0; h < 2; ++h) {
    v4u o;
#pragma unroll
    for (int i = 0; i < 4; ++i) {
      const int k = 8 * h + 2 * i;
      const f32x2 t = __builtin_elementwise_fma(f32x2{ec[3].y, ec[3].y}, f32x2{uu[k >> 2][k & 3], uu[k >> 2][(k & 3) + 1]}, f32x2{vv[k >> 2][k & 3], vv[k >> 2][(k & 3) + 1]});
      const f32x2 pp = __builtin_elementwise_fma(f32x2{ec[3].x, ec[3].x}, f32x2{acc[0][k], acc[0][k + 1]}, t);
      const f32x2 d = f32x2{__builtin_amdgcn_exp2f(pp.x), __builtin_amdgcn_exp2f(pp.y)} + f32x2{1.0f, 1.0f};
      const f32x2 y = pp * f32x2{__builtin_amdgcn_rcpf(d.x), __builtin_amdgcn_rcpf(d.y)};
      o[i] = pack2(y.x, y.y);
    }
    stg(rs, h ? hv1 : hv0, hoff + (3 ^ par) * 114688, o);
    if (h) CKPUT(sum4(cs, o), 3); else cs = sum4(0u, o);
    SB();
  }
  static_for<24, 28>([&](auto Ic) __attribute__((always_inline)) { HLOAD(Ic.v); });
  SB();
  STAMP(13);

  f32x16 d[2][2];
  LAS unsigned char* redw = L + L_XR + w * 8192 + l16;
  LAS unsigned char* redr = L + L_XR + w * 2048 + l16;
#define REDW(un, q) *(LAS f32x4*)(redw + (un) * 32768 + (q) * 1024) = \
    f32x4{d[un][(q) >> 2][4 * ((q) & 3)], d[un][(q) >> 2][4 * ((q) & 3) + 1], d[un][(q) >> 2][4 * ((q) & 3) + 2], d[un][(q) >> 2][4 * ((q) & 3) + 3]}
  LAS unsigned char *w2ua = w2a + 65536, *w2ub = w2b + 65536;
  PIN(w2ua); PIN(w2ub);
#define CHECK(un) \
  if (__builtin_expect((unsigned)__builtin_amdgcn_readlane((int)m[un], 63) != 14u * tagA, 0)) { \
    _Pragma("nounroll") for (unsigned spin = 0; spin < SPIN_CAP; ++spin) { \
      FRESH(); \
      COUNT(24 + (un)); \
      unsigned t = sum4(0u, ldg(rs, ckv, kb0 + (un) * 8192)); \
      static_for<0, 7>([&](auto Cc) __attribute__((always_inline)) { \
        constexpr int c = Cc.v; \
        v4u q[4]; \
        _Pragma("unroll") for (int i = 0; i < 4; ++i) q[i] = ldg(rs, l16 + i * 1024, hb0 + CHUNK(4 * c) * 4096 + (un) * (2 * 114688)); \
        t = sum8(sum8(t, q[0], q[1]), q[2], q[3]); \
        mfma_av<c == 0>(d[un][0], w2f[4 * c][0], q[0]); mfma_av<c == 0>(d[un][1], w2f[4 * c][1], q[0]); \
        _Pragma("unroll") for (int i = 1; i < 4; ++i) { mfma_av<false>(d[un][0], w2f[4 * c + i][0], q[i]); mfma_av<false>(d[un][1], w2f[4 * c + i][1], q[i]); } \
        SB(); \
      }); \
      mfma_done(d[un][1]); \
      PIN(d[un][0]); \
      if ((unsigned)__builtin_amdgcn_readlane((int)red_all(t), 63) == 14u * tagA) break; \
      PAUSE(); \
    } \
    if (un) { _Pragma("unroll") for (int q = 0; q < 4; ++q) REDW(1, q); } \
  }
  unsigned m[2] = {0u, 0u};
  v4u gbv, scv, ck[2];
  f32x4 pr[8];
  float gb[8], rx[2][8];
  unsigned orow = (rp * 128 + ((lane & 31) >> 3) * 16 + rg * 8 + (lane & 7)) * 1024 + dt * 128 + w * 32 + hf * 16;
  static_for<0, 112>([&](auto Xc) __attribute__((always_inline)) {
    constexpr int X = Xc.v, I = X < 100 ? X >> 1 : 50 + (X - 100) % 6, e = X < 100 ? X & 1 : (X - 100) / 6, un = I / 28;
    mfma_av<I % 28 == 0>(d[un][e], w2f[I % 28][e], hq[I]);
    SB();
    if constexpr (X < 100 && e == 0) {
      if constexpr (I < 3) PULL(14 + I, w2sa, w2sb, I * 2048);
      if constexpr (I >= 3 && I < 7) PULL(14 + I, w2a, w2b, 32768 + (I - 3) * 2048);
      if constexpr (I >= 7 && I < 11) PULL(14 + I, w2a, w2b, (I - 7) * 2048);
      if constexpr (I == 21) waitv<hl_upto(21) - 28 + 1>();
      if constexpr (I >= 21 && I < 24) PULL(4 + I, w2ua, w2ub, (I - 21) * 2048);
      if constexpr (I == 28) { m[0] = sum4(m[0], ck[0]); PIN(m[0]); }
      if constexpr (I >= 29 && I < 35) { m[0] = red_step<I - 29>(m[0]); PIN(m[0]); }
      if constexpr (I == 35) CHECK(0)
      if constexpr (I >= 35 && I < 43) REDW(0, I - 35);
    }
    if constexpr (X < 100 && e == 1) {
      if constexpr (I < 6) W2P(50 + I, I & 3, xl, 65536 + I * 1024);
      static_for<hl_upto(I), hl_upto(I + 1)>([&](auto Nc) __attribute__((always_inline)) { HLOAD(Nc.v); });
      if constexpr (I == 6) ck[0] = ldg(rs, ckv, kb0);
      if constexpr (I == 37) ck[1] = ldg(rs, ckv, kb0 + 8192);
    }
    if constexpr (X < 100 ? e == 1 : e == 0) { m[un] = sum4(m[un], hq[I]); PIN(m[un]); }
    if constexpr (X == 105) { m[1] = sum4(m[1], ck[1]); PIN(m[1]); }
    if constexpr (X >= 106) { m[1] = red_step<X - 106>(m[1]); PIN(m[1]); }
    if constexpr (X == 23 || X == 24) { const v4u ev = rc[2 * (X - 23)], od = rc[2 * (X - 23) + 1]; res[X - 23] = (rg ^ par) ? od : ev; PIN(res[X - 23]); }
    if constexpr (X == 22) { gbv = lds128(L + L_TG + (64 * dt + 16 * w + 8 * hf) * 2); scv = lds128(L + L_TS + (64 * dt + 16 * w + 8 * hf) * 2); PIN(orow); }
    if constexpr (X >= 26 && X < 42 && (X & 1) == 0) {
      constexpr int q = (X - 26) >> 1;
      const float inv = __builtin_amdgcn_rcpf((q & 1) ? bf_hi(scv[q >> 1]) : bf_lo(scv[q >> 1]));
      gb[q] = -LN2 * ((q & 1) ? bf_hi(gbv[q >> 1]) : bf_lo(gbv[q >> 1]));
      rx[0][q] = inv * ((q & 1) ? bf_hi(res[0][q >> 1]) : bf_lo(res[0][q >> 1]));
      rx[1][q] = inv * ((q & 1) ? bf_hi(res[1][q >> 1]) : bf_lo(res[1][q >> 1]));
      PIN(gb[q]); PIN(rx[0][q]); PIN(rx[1][q]);
    }
    if constexpr (X == 86) BAR();
    if constexpr (X == 86 || X == 88) {
#pragma unroll
      for (int k = X - 86; k < X - 84; ++k) { pr[2 * k] = *(LAS f32x4*)(redr + k * 8192); pr[2 * k + 1] = *(LAS f32x4*)(redr + k * 8192 + 1024); }
    }
    if constexpr (X >= 91 && X < 99) {
      constexpr int q = X - 91;
      float t = (pr[q >> 2][q & 3] + pr[2 + (q >> 2)][q & 3]);
      PIN(t);
      float t2_ = (pr[4 + (q >> 2)][q & 3] + pr[6 + (q >> 2)][q & 3]);
      PIN(t2_);
      rx[0][q] = __builtin_fmaf(gb[q], t + t2_, rx[0][q]);
      PIN(rx[0][q]);
    }
    if constexpr (X == 99)
      *(GAS v4u*)((GAS unsigned char*)out + orow) = v4u{pack2(rx[0][0], rx[0][1]), pack2(rx[0][2], rx[0][3]), pack2(rx[0][4], rx[0][5]), pack2(rx[0][6], rx[0][7])};
    if constexpr (X == 108) asm volatile("s_nop 3" : "+v"(d[1][0]));
    if constexpr (X >= 108) REDW(1, X - 108);
    SB();
    if constexpr (X == 55) STAMP(14);
  });
  STAMP(19);
  asm volatile("s_nop 7" : "+v"(d[1][1]));
  CHECK(1)
#pragma unroll
  for (int q = 4; q < 8; ++q) REDW(1, q);
  BAR();
  {
    f32x4 s0 = {}, s1 = {};
#pragma unroll
    for (int k = 0; k < 4; ++k) { s0 += *(LAS f32x4*)(redr + k * 8192 + 32768); s1 += *(LAS f32x4*)(redr + k * 8192 + 32768 + 1024); }
    float y[8];
#pragma unroll
    for (int q = 0; q < 8; ++q) y[q] = __builtin_fmaf(gb[q], q < 4 ? s0[q & 3] : s1[q & 3], rx[1][q]);
    *(GAS v4u*)((GAS unsigned char*)out + orow + 65536) = v4u{pack2(y[0], y[1]), pack2(y[2], y[3]), pack2(y[4], y[5]), pack2(y[6], y[7])};
  }
  if (b == 0 && tid == 0) __hip_atomic_store((GAS unsigned*)ws, epoch + 1u, __ATOMIC_RELAXED, __HIP_MEMORY_SCOPE_AGENT);
  STAMP(15);
}

extern "C" void kernel_launch(void* const* d_in, const int* in_sizes, int n_in, void* d_out, int out_size, void* d_ws, size_t ws_size,
                              hipStream_t stream) {
  (void)in_sizes; (void)n_in; (void)out_size; (void)ws_size;
  (void)hipFuncSetAttribute((const void*)s4_block_kernel, hipFuncAttributeMaxDynamicSharedMemorySize, LDS_BYTES);
  s4_block_kernel<<<256, 256, LDS_BYTES, stream>>>((cptr)d_in[0], (cptr)d_in[1], (cptr)d_in[2], (cptr)d_in[3], (cptr)d_in[4], (cptr)d_in[5], (cptr)d_in[6],
                                                   (cptr)d_in[7], (cptr)d_in[8], (cptr)d_in[10], (cptr)d_in[11], (cptr)d_in[12], (cptr)d_in[13],
                                                   (unsigned char*)d_out, (unsigned char*)d_ws, (cptr)d_in[9]);
}
```

```cpp
#define PROBE 22
#include <hip/hip_runtime.h>

#define GAS __attribute__((address_space(1)))
#define LAS __attribute__((address_space(3)))
#define DI __device__ __forceinline__
typedef unsigned v4u __attribute__((ext_vector_type(4)));
typedef unsigned v2u __attribute__((ext_vector_type(2)));
typedef float f32x2 __attribute__((ext_vector_type(2)));
typedef float f32x4 __attribute__((ext_vector_type(4)));
typedef float f32x16 __attribute__((ext_vector_type(16)));
typedef __bf16 bf16x2 __attribute__((ext_vector_type(2)));
typedef __bf16 bf16x8 __attribute__((ext_vector_type(8)));
typedef short s16x4 __attribute__((ext_vector_type(4)));
typedef unsigned short u16;
typedef unsigned long long u64;

constexpr int SET_BASE = 4096;
constexpr int OFF_MT = 0;
constexpr int OFF_SG = 32768;
constexpr int OFF_MG = 65536;
constexpr int OFF_SX = 524288;
constexpr int OFF_XC = 458752;
constexpr int OFF_XG = 1 << 20;
constexpr int OFF_Z = OFF_XG + (2 << 20);
constexpr int OFF_HID = OFF_Z + 2097152;
constexpr int OFF_CK = OFF_HID + 7340032;
constexpr int SET_BYTES = OFF_CK + 262144;
static_assert(OFF_MG + 393216 <= OFF_XC && OFF_XC + 65536 <= OFF_SX, "set size");

constexpr int L_TS = 0, L_TB = 1024, L_TG = 2048;
constexpr int L_UVT = 3072;
constexpr int L_M = 4096;
constexpr int L_UVS = L_M;
constexpr int UVS_ROW = 544, UVS_CH = 57 * UVS_ROW;
constexpr int L_KC = L_M;
constexpr int KC_STRIDE = 4160;
constexpr int L_H = L_KC + 8 * KC_STRIDE;
constexpr int H_STRIDE = 4576;
constexpr int L_XB = L_H + 2 * H_STRIDE;
constexpr int L_RX = L_XB + 8192;
constexpr int L_R = L_M + 65536;
constexpr int L_TOP = L_M + 131072 + 24576;
constexpr int L_KT = L_TOP + 16;
constexpr int L_STAT = L_TOP;
constexpr int L_ETA = L_RX + 12288;
constexpr int L_XR = L_M;
constexpr int L_SP = L_M + 131072;
constexpr int LDS_BYTES = 163840;
static_assert(L_TOP + 4096 <= LDS_BYTES && L_ETA + 16 <= L_R && L_UVS + 2 * UVS_CH <= L_R && L_SP + 24576 <= L_TOP, "LDS map");
static_assert(L_H % 16 == 0 && L_XB % 16 == 0 && L_RX % 16 == 0 && L_KT % 16 == 0, "alignment");

#define ZLAG 30
#define MTK 124
constexpr int EO = 30;
constexpr int CODE_BYTES = 35328;
constexpr int SC1 = 16;
constexpr int OOB = 0x7FFFFF00;
constexpr unsigned SPIN_CAP = 1u << 15;
constexpr unsigned POISON = 0xFFFFFFFFu;
constexpr float LOG2E = 1.4426950408889634f, LN2 = 0.6931471805599453f, INV2PI = 0.15915494309189535f;

#define SB() __builtin_amdgcn_sched_barrier(0)
#define WAITV(n) asm volatile("s_waitcnt vmcnt(" #n ")" ::: "memory")
#define PIN(x) asm volatile("" : "+v"(x))
#define FRESH() asm volatile("" ::: "memory")
#define PAUSE() asm volatile("s_sleep 4" ::: "memory")
#define BAR() do { asm volatile("s_waitcnt lgkmcnt(0)" ::: "memory"); __builtin_amdgcn_s_barrier(); asm volatile("" ::: "memory"); } while (0)
#define STAMP(i) do { if ((i) == 15 && probe) { const unsigned t_ = now(); while (now() - t_ < 2000u) { } } } while (0)
#define COUNT(i) do { if ((i) == PROBE) probe = 1; } while (0)

#define STAMPC(i)
#define STAMPA(i)
#define STAMPB(i) STAMP(i)
DI float u2f(unsigned u) { return __builtin_bit_cast(float, u); }
DI unsigned f2u(float f) { return __builtin_bit_cast(unsigned, f); }
DI float dot2(unsigned u, float c) { const bf16x2 v = __builtin_bit_cast(bf16x2, u); return __builtin_amdgcn_fdot2_f32_bf16(v, v, c, false); }
DI float bf_lo(unsigned u) { return __builtin_bit_cast(float, u << 16); }
DI float bf_hi(unsigned u) { return __builtin_bit_cast(float, u & 0xFFFF0000u); }
DI unsigned pack2(float a, float b) { f32x2 v = {a, b}; return __builtin_bit_cast(unsigned, __builtin_convertvector(v, bf16x2)); }
DI float rbf(float a) { return bf_lo(pack2(a, a)); }
DI unsigned sel(bool c, unsigned a, unsigned b) { asm("" : "+v"(a), "+v"(b)); return c ? a : b; }
DI unsigned guard(unsigned u) { return u < 0xFFFFFFFEu ? u : 0xFFFFFFFEu; }
DI unsigned max4(unsigned m, v4u v) {
  asm("v_max3_u32 %0, %0, %1, %2\n\tv_max3_u32 %0, %0, %3, %4" : "+v"(m) : "v"(v.x), "v"(v.y), "v"(v.z), "v"(v.w));
  return m;
}
DI f32x4 mfma16(v4u a, v4u b, f32x4 c) { return __builtin_amdgcn_mfma_f32_16x16x32_bf16(__builtin_bit_cast(bf16x8, a), __builtin_bit_cast(bf16x8, b), c, 0, 0, 0); }
DI f32x16 mfma32(v4u a, v4u b, f32x16 c) { return __builtin_amdgcn_mfma_f32_32x32x16_bf16(__builtin_bit_cast(bf16x8, a), __builtin_bit_cast(bf16x8, b), c, 0, 0, 0); }
template <bool FIRST> DI void mfma_av(f32x16& acc, const v4u& a, const v4u& b) {
  if (FIRST) asm volatile("v_mfma_f32_32x32x16_bf16 %0, %1, %2, 0" : "=&v"(acc) : "a"(a), "v"(b));
  else asm volatile("v_mfma_f32_32x32x16_bf16 %0, %1, %2, %0" : "+v"(acc) : "a"(a), "v"(b));
}
template <bool FIRST> DI void mfma4_va(f32x4& c0, f32x4& c1, const v4u& a, const v4u& b) {
  const v2u al = {a.x, a.y}, ah = {a.z, a.w}, bl = {b.x, b.y}, bh = {b.z, b.w};
  if (FIRST) asm volatile("v_mfma_f32_4x4x4_16b_bf16 %0, %2, %4, 0\n\tv_mfma_f32_4x4x4_16b_bf16 %1, %3, %5, 0" : "=&v"(c0), "=&v"(c1) : "v"(al), "v"(ah), "a"(bl), "a"(bh));
  else asm volatile("s_nop 0\n\tv_mfma_f32_4x4x4_16b_bf16 %0, %2, %4, %0\n\tv_mfma_f32_4x4x4_16b_bf16 %1, %3, %5, %1" : "+v"(c0), "+v"(c1) : "v"(al), "v"(ah), "a"(bl), "a"(bh));
}
template <bool FIRST> DI void mfma16_v(f32x4& acc, const v4u& a, const v4u& b) {
  if (FIRST) asm volatile("v_mfma_f32_16x16x32_bf16 %0, %1, %2, 0" : "=&v"(acc) : "v"(a), "v"(b));
  else asm volatile("v_mfma_f32_16x16x32_bf16 %0, %1, %2, %0" : "+v"(acc) : "v"(a), "v"(b));
}
DI void mfma_done(f32x16& acc) { asm volatile("s_nop 15" : "+v"(acc)); }
DI v2u trd(LAS unsigned char* p) { return __builtin_bit_cast(v2u, __builtin_amdgcn_ds_read_tr16_b64_v4i16((LAS s16x4*)p)); }
DI v4u trd2(LAS unsigned char* p0, LAS unsigned char* p1) { const v2u a = trd(p0), b = trd(p1); return v4u{a.x, a.y, b.x, b.y}; }
DI v4u lds128(LAS unsigned char* p) { return *(LAS v4u*)p; }
DI v4u ldg(__amdgpu_buffer_rsrc_t r, int voff, int soff) { return __builtin_amdgcn_raw_buffer_load_b128(r, voff, soff, SC1); }
DI void stg(__amdgpu_buffer_rsrc_t r, int voff, int soff, v4u v) {
  __builtin_amdgcn_raw_buffer_store_b128(v, r, voff, soff, SC1);
  asm volatile("s_nop 1" :: "v"(v) : "memory");
}
template <int CTRL> DI float dpp_add(float v) {
  return v + __builtin_bit_cast(float, __builtin_amdgcn_update_dpp(0, __builtin_bit_cast(int, v), CTRL, 0xF, 0xF, true));
}
DI float swap16_add(float v) { const unsigned u = __builtin_bit_cast(unsigned, v); const auto r = __builtin_amdgcn_permlane16_swap(u, u, false, false);
  return u2f(r[0]) + u2f(r[1]); }
DI float swap32_add(float v) { const unsigned u = __builtin_bit_cast(unsigned, v); const auto r = __builtin_amdgcn_permlane32_swap(u, u, false, false);
  return u2f(r[0]) + u2f(r[1]); }
DI float quad_sum(float v) { v = dpp_add<0xB1>(v); return dpp_add<0x4E>(v); }
DI float row_sum(float v) { return dpp_add<0x140>(dpp_add<0x141>(quad_sum(v))); }
DI float wave_sum(float v) { return swap32_add(swap16_add(row_sum(v))); }
DI f32x2 cmul(f32x2 a, f32x2 e, f32x2 ep) { const f32x2 t = f32x2{a.x, a.x} * e; return __builtin_elementwise_fma(f32x2{a.y, a.y}, ep, t); }
DI f32x2 cexp(float re2, float rev) {
  const float m = __builtin_amdgcn_exp2f(re2), f = __builtin_amdgcn_fractf(rev);
  return f32x2{m * __builtin_amdgcn_cosf(f), m * __builtin_amdgcn_sinf(f)};
}
template <int LI, int SI> DI void grp(int& so, unsigned lbase, int sbase) {
  asm volatile("s_add_i32 m0, %1, %3\n\ts_add_i32 %0, %2, %4" : "=s"(so) : "s"(lbase), "s"(sbase), "n"(LI), "n"(SI) : "scc", "memory");
}
template <int K, bool COH> DI void xfer(unsigned v, __amdgpu_buffer_rsrc_t r, int so) {
  if (COH) asm volatile("buffer_load_dwordx4 %0, %1, %2 offen offset:%3 sc1 lds" :: "v"(v), "s"(r), "s"(so), "n"(K * 1024) : "memory");
  else asm volatile("buffer_load_dwordx4 %0, %1, %2 offen offset:%3 lds" :: "v"(v), "s"(r), "s"(so), "n"(K * 1024) : "memory");
}
DI void set_m0(unsigned lds_dst) { asm volatile("s_mov_b32 m0, %0\n\ts_nop 0" :: "s"(lds_dst) : "memory"); }
DI void dma_x4(const unsigned char* sbase, unsigned v, unsigned lds_dst) {
  asm volatile("s_mov_b32 m0, %2\n\ts_nop 0\n\tglobal_load_lds_dwordx4 %0, %1 sc1\n\tglobal_load_lds_dwordx4 %0, %1 offset:1024 sc1\n\t"
               "global_load_lds_dwordx4 %0, %1 offset:2048 sc1\n\tglobal_load_lds_dwordx4 %0, %1 offset:3072 sc1"
               :: "v"(v), "s"(sbase), "s"(lds_dst) : "memory");
}
template <int I> struct IC { static constexpr int v = I; };
template <int I, int N, class F> DI void static_for(F&& f) { if constexpr (I < N) { f(IC<I>{}); static_for<I + 1, N>(f); } }
DI unsigned now() { u64 t; asm volatile("s_memrealtime %0\n\ts_waitcnt lgkmcnt(0)" : "=s"(t) :: "memory"); return (unsigned)t; }
DI void reload(v4u& d, __amdgpu_buffer_rsrc_t r, int voff, int soff) { d = ldg(r, voff, soff); FRESH(); }
DI void reload1(unsigned& d, __amdgpu_buffer_rsrc_t r, int voff, int soff) { d = __builtin_amdgcn_raw_buffer_load_b32(r, voff, soff, SC1); FRESH(); }
template <int N> DI void waitv() { asm volatile("s_waitcnt vmcnt(%0)" :: "n"(N) : "memory"); }
struct ConvOrder { int t[36], u[36]; };
constexpr ConvOrder conv_order() {
  ConvOrder o = {};
  int done[8] = {}, last = -1;
  for (int k = 0; k < 36; ++k) {
    int best = -1;
    for (int t = 0; t < 8; ++t)
      if (t != last && done[t] <= t && (best < 0 || t - done[t] > best - done[best])) best = t;
    o.t[k] = best; o.u[k] = done[best]++; last = best;
  }
  return o;
}
constexpr ConvOrder conv_order_last() {
  ConvOrder o = {};
  int k = 0;
  for (int p = 0; p < 4; ++p)
    for (int u = 0; u <= 2 * p + 1; ++u) {
      o.t[k] = 2 * p + 1; o.u[k] = u; ++k;
      if (u <= 2 * p) { o.t[k] = 2 * p; o.u[k] = u; ++k; }
    }
  return o;
}
constexpr int epi_slot(int v) { return v < 32 ? 2 * EO + v : 2 * 50 - 1 + (v - 32); }
constexpr bool epi_pair(int j) { return (j >= EO && j < EO + 19) || (j >= 50 && j < 61); }
constexpr int hc_upto(int j) { return j < 50 ? 0 : j < 60 ? j - 50 : 10 + 2 * (j - 60); }
constexpr int hl_upto(int i) { return i < 9 ? 28 : i < 37 ? 28 + (i - 9) : 56; }
constexpr int c_ops(int j) {
  return (j >= 10 && j < 14 ? 2 : j < 34 || (j >= 35 && j < 51) ? 1 : 0) + hc_upto(j + 1) - hc_upto(j) +
         (j == EO + 6 || j == EO + 14 || j == 56 ? 1 : j == EO + 10 || j == EO + 18 || j == 60 ? 2 : 0);
}
constexpr int c_between(int a, int b) { int n = 0; for (int j = a + 1; j < b; ++j) n += c_ops(j); return n; }
DI unsigned tagbad(unsigned bad, v4u g, unsigned tagA, unsigned tagB) {
  return __builtin_amdgcn_bitop3_b32(__builtin_amdgcn_bitop3_b32(bad, g.y, tagA, 0xF6), g.w, tagB, 0xF6);
}
template <int R0, int R1, int R2, int R3> DI void put4(unsigned base, f32x2 a, f32x2 b, f32x2 c, f32x2 d) {
  asm volatile("s_mov_b32 m0, %4\n\ts_nop 0\n\tds_write_addtid_b32 %0 offset:%5\n\tds_write_addtid_b32 %1 offset:%6\n\t"
               "ds_write_addtid_b32 %2 offset:%7\n\tds_write_addtid_b32 %3 offset:%8"
               :: "v"(pack2(a.x, a.y)), "v"(pack2(b.x, b.y)), "v"(pack2(c.x, c.y)), "v"(pack2(d.x, d.y)), "s"(base),
                  "n"(R0 * UVS_ROW), "n"(R1 * UVS_ROW), "n"(R2 * UVS_ROW), "n"(R3 * UVS_ROW) : "memory");
}
DI unsigned sum4(unsigned m, v4u v) {
  asm("v_add3_u32 %0, %0, %1, %2\n\tv_add3_u32 %0, %0, %3, %4" : "+v"(m) : "v"(v.x), "v"(v.y), "v"(v.z), "v"(v.w));
  return m;
}
DI unsigned sum8(unsigned m, v4u a, v4u b) {
  asm("v_add3_u32 %0, %0, %1, %2\n\tv_add3_u32 %0, %0, %3, %4\n\tv_add3_u32 %0, %0, %5, %6\n\tv_add3_u32 %0, %0, %7, %8"
      : "+v"(m) : "v"(a.x), "v"(a.y), "v"(a.z), "v"(a.w), "v"(b.x), "v"(b.y), "v"(b.z), "v"(b.w));
  return m;
}
template <int I> DI unsigned red_step(unsigned v) {
  constexpr int ctrl[6] = {0xB1, 0x4E, 0x141, 0x140, 0x142, 0x143}, rows[6] = {0xF, 0xF, 0xF, 0xF, 0xA, 0xC};
  return v + (unsigned)__builtin_amdgcn_update_dpp(0, (int)v, ctrl[I], rows[I], 0xF, true);
}
DI unsigned red_all(unsigned v) { return red_step<5>(red_step<4>(red_step<3>(red_step<2>(red_step<1>(red_step<0>(v)))))); }
DI void swap_add32(float& a, float b) { const auto r = __builtin_amdgcn_permlane32_swap(f2u(a), f2u(b), false, false); a = u2f(r[0]) + u2f(r[1]); }
DI void swap_add16(float& a, float b) { const auto r = __builtin_amdgcn_permlane16_swap(f2u(a), f2u(b), false, false); a = u2f(r[0]) + u2f(r[1]); }

typedef const unsigned char* cptr;
typedef unsigned v16u __attribute__((ext_vector_type(16)));
DI cptr mkptr(unsigned lo, unsigned hi) { return (cptr)(((u64)hi << 32) | lo); }

__global__ void __launch_bounds__(256, 1)
s4_block_kernel(cptr, cptr, cptr, cptr, cptr, cptr, cptr, cptr, cptr, cptr, cptr, cptr, cptr, unsigned char*, unsigned char*, cptr) {
  unsigned probe = 0;
  STAMP(0);
  v16u ka0, ka1;
  asm volatile("s_load_dwordx16 %0, %2, 0x0\n\ts_load_dwordx16 %1, %2, 0x40" : "=&s"(ka0), "=&s"(ka1) : "s"(__builtin_amdgcn_kernarg_segment_ptr()));
  SB();
  LAS unsigned char* const L = (LAS unsigned char*)16u - 16;
  const int tid = threadIdx.x, lane = tid & 63, w = __builtin_amdgcn_readfirstlane(tid >> 6);
  const int b = blockIdx.x, xcd = b & 7, slot = b >> 3, id = xcd * 32 + slot;
  const int ks = (id >> 6) * 4 + w, cc = id & 63;
  const int rp = 4 * (xcd >> 1) + (slot >> 3), rg = xcd & 1, dt = slot & 7, si = 8 * rg + dt;
  const int ct = 7 * rg + min(dt, 6);
  const int rot = (0x0340 >> (4 * (2 * (w & 1) + rg))) & 7;

  const unsigned char* pcode;
  asm volatile("s_getpc_b64 %0" : "=s"(pcode));
  const unsigned warm = *(const GAS unsigned*)((const GAS unsigned char*)pcode + (unsigned)min((((slot & 7) * 4 + w) * 9 + min(lane, 8)) * 128, CODE_BYTES));
  const int g16 = lane >> 4, q4 = (lane & 15) >> 2, p4 = lane & 3, hf = lane >> 5, blk = (lane >> 4) & 1, l16 = lane * 16;
  const unsigned lrw = L_R + w * 16384;
  unsigned lsp = L_SP + w * 6144;
  unsigned wmo[3], co = (ks * 32 + (lane & 31)) * 2, xo = (id >> 2) * 32768 + (id & 3) * 256 + (tid & 7) * 4096 + (tid >> 3) * 8, ro = id * 8192 + w * 2048 + hf * 1024 + (lane & 31) * 16, po = id * 512 + tid * 2;
#pragma unroll
  for (int i = 0; i < 3; ++i) { const unsigned idx = lane + 64 * i; wmo[i] = (ks * 32 * 6144 + cc * 96 - i * 1024) + idx / 6 * (6144 - 96) + idx * 16; PIN(wmo[i]); }
  PIN(co); PIN(xo); PIN(ro); PIN(po);
  set_m0(lrw);
  SB();
  asm volatile("s_waitcnt lgkmcnt(0)" : "+s"(ka0), "+s"(ka1));
  const cptr xin = mkptr(ka0[0], ka0[1]), pAre = mkptr(ka0[2], ka0[3]), pAim = mkptr(ka0[4], ka0[5]), pBre = mkptr(ka0[6], ka0[7]);
  const cptr pBim = mkptr(ka0[8], ka0[9]), pCre = mkptr(ka0[10], ka0[11]), pCim = mkptr(ka0[12], ka0[13]), pldt = mkptr(ka0[14], ka0[15]);
  const cptr pD = mkptr(ka1[0], ka1[1]), w1 = mkptr(ka1[2], ka1[3]), w2 = mkptr(ka1[4], ka1[5]), pc = mkptr(ka1[6], ka1[7]);
  const cptr wmod = mkptr(ka1[8], ka1[9]), out = mkptr(ka1[10], ka1[11]), ws = mkptr(ka1[12], ka1[13]);
  const unsigned epoch_v = __hip_atomic_load((GAS unsigned*)ws, __ATOMIC_RELAXED, __HIP_MEMORY_SCOPE_AGENT);
  asm volatile("global_load_lds_dwordx4 %0, %3\n\tglobal_load_lds_dwordx4 %1, %3 offset:1024\n\tglobal_load_lds_dwordx4 %2, %3 offset:2048"
               :: "v"(wmo[0]), "v"(wmo[1]), "v"(wmo[2]), "s"(wmod) : "memory");
  const unsigned cval = *(const GAS u16*)((const GAS unsigned char*)pc + co);
  SB();
  v2u xq[4];
#pragma unroll
  for (int i = 0; i < 4; ++i) xq[i] = *(const GAS v2u*)((const GAS unsigned char*)xin + 1024 * i + xo);
  const v4u ra = *(const GAS v4u*)((const GAS unsigned char*)xin + ro), rb = *(const GAS v4u*)((const GAS unsigned char*)xin + 512 + ro);
  SB();
#define PARAM(p) (unsigned)*(const GAS u16*)((const GAS unsigned char*)(p) + po)
  const unsigned uAre = PARAM(pAre), uAim = PARAM(pAim), uBre = PARAM(pBre), uBim = PARAM(pBim), uCre = PARAM(pCre), uCim = PARAM(pCim);
#undef PARAM
  SB();
  const unsigned char* setp = ws + SET_BASE;
  const __amdgpu_buffer_rsrc_t rs = __builtin_amdgcn_make_buffer_rsrc((void*)setp, 0, SET_BYTES, 0x00020000);
  unsigned uldt, uD;
  asm volatile("s_load_dword %0, %2, %4\n\ts_load_dword %1, %3, %4" : "=&s"(uldt), "=&s"(uD) : "s"(pldt), "s"(pD), "s"(id * 4));
  SB();
  STAMPA(26);
  unsigned wto = id * 7168 + (w >> 1) * 3584 + min(lane, 27) * 128;
  PIN(wto);
  unsigned w1v[4], w2v[4];
#pragma unroll
  for (int i = 0; i < 4; ++i) {
    w1v[i] = (lane >> 3) * 3584 + (((lane & 7) ^ (blk << 2)) * 16) + i * (57344 - 1024);
    w2v[i] = (lane >> 3) * 1024 + (((lane & 7) ^ (blk << 2)) * 16) + i * (8192 - 1024);
    PIN(w1v[i]); PIN(w2v[i]);
  }
  int w1b = ct * 256 + (w >> 1) * 128 + (w & 1) * (8 * 3584), w2lo = (448 * w + 64 * rot) * 1024 + dt * 128;
  int w2c3 = w2lo - (rot >> 2) * (448 * 1024), w2hi = w2lo - ((rot + 1) >> 2) * (448 * 1024), so = 0;
  unsigned lrx = L_RX + w * 3072, lto = L_TOP + w * 1024;
  int zs = OFF_Z + (2 * id + (w >> 1)) * 64;
  int za = (((w & 1) + 2 * (g16 & 1)) * 8 + q4 * 2 + (g16 >> 1)) * 32768 + p4 * 16, zb = za + 32 * 32768;
  int tra = (hf * 8 + q4) * 64 + blk * 32 + p4 * 8;
  const int wsrc = (w & 2) + hf;
  int w1t = q4 * 128 + (((w & 1) ^ (q4 >> 1)) * 64) + blk * 32 + p4 * 8;
  LAS unsigned char *w1a = L + L_R + wsrc * 16384 + w1t, *w1s = L + L_SP + wsrc * 6144 + w1t;
  LAS unsigned char *w1x = L + L_RX + wsrc * 3072 + w1t, *w1y = L + L_TOP + wsrc * 1024 + w1t;
#define W1PULL(step, n) w1f[step] = trd2(((n) < 16 ? w1a : w1s) + ((n) & 15) * 1024, ((n) < 16 ? w1a : w1s) + ((n) & 15) * 1024 + 512)
  LAS unsigned char* ka = L + L_KC + ((w >> 1) * 4 + q4) * KC_STRIDE + 2 * (32 - 8 * g16 + 4 * p4) - 8 + 256 * (w & 1);
  LAS unsigned char* hb = L + L_H + (w >> 1) * H_STRIDE + (lane & 15) * 32 + g16 * 16 + 256 - 256 * (w & 1);
  LAS unsigned char* zp = L + sel(tid < 64, L_KC + ((lane >> 1) & 7) * KC_STRIDE + (lane & 1) * 16, L_H + hf * H_STRIDE + min(lane & 31, 29) * 16);
  LAS unsigned char* ub = L + L_UVS + (lane & 15) * UVS_ROW + g16 * 16;
  int gmo = OFF_MG + (g16 * 256 + id) * 256 + (lane & 15) * 16, gmo2 = OFF_MG + ((4 + blk) * 256 + id) * 256 + (lane & 15) * 16;
  int mgo = sel(lane < 48, (lane >> 1) * 256 + (lane & 1) * 8, OOB), sgo = sel(lane & 31, OOB, hf * 8);
  LAS unsigned char* xbw = L + L_XB + ((w & 1) * 4 + (w >> 1)) * 512 + lane * 8;
  int xcw = sel(lane & 7, OOB, tid), xcr = sel(lane & 7, OOB, (tid >> 3) * 1024), xgw = (tid >> 3) * 16384 + (tid & 7) * 16;
  PIN(xcw); PIN(xcr); PIN(xgw);
  LAS unsigned char* xbr = L + L_XB + w * 2048 + (lane & 15) * 32 + g16 * 8;
  LAS unsigned char *exa = L + L_XB + w * 2048 + l16, *exb = L + L_RX + w * 3072 + l16, *eya = L + L_XB + (w ^ 1) * 2048 + l16, *eyb = L + L_RX + (w ^ 1) * 3072 + l16;
  PIN(xbw); PIN(xbr); PIN(exa); PIN(exb); PIN(eya); PIN(eyb);
  int mto = (tid >> 1) * 128 + (tid & 1) * 16;
  LAS unsigned char* mtw = L + (tid & 1) * L_TG + (tid >> 1) * 4;
  PIN(mto); PIN(mtw);
  PIN(za); PIN(zb); PIN(tra); PIN(w1a); PIN(w1s); PIN(w1x); PIN(w1y); PIN(ka); PIN(hb); PIN(zp); PIN(ub); PIN(gmo); PIN(gmo2); PIN(mgo); PIN(sgo);
  asm volatile("" : "+s"(w1b), "+s"(w2lo), "+s"(w2c3), "+s"(w2hi), "+s"(lsp), "+s"(lrx), "+s"(lto), "+s"(zs));
#define W1P(n, k, lbase, li) do { if constexpr ((k) == 0) grp<(li), (n) * 57344>(so, lbase, w1b); xfer<(k), false>(w1v[k], rw1, so); } while (0)
#define W2P(n, k, lbase, li) do { if constexpr ((k) == 0) grp<(li), (n) * 8192>(so, lbase, (n) < 24 ? w2lo : (n) < 32 ? w2c3 : w2hi); xfer<(k), false>(w2v[k], rw2, so); } while (0)
  SB();
  LAS unsigned char* koff[12];
  {
    float fj = (float)(8 * tid - 3);
    PIN(fj);
    const unsigned lin = (unsigned)(8 * tid - 3 + 0x4300 - 128) * 0x10001u + 0x10000u;
#pragma unroll
    for (int i = 0; i < 12; i += 2) {
      const unsigned r = sel(tid < 32, lin + i * 0x10001u, pack2(fj + (float)i, fj + (float)(i + 1)));
      koff[i] = L + ((r & 0xFFFFu) * 4 + (L_KT + (128 - 0x4300) * 4));
      koff[i + 1] = L + ((r >> 16) * 4 + (L_KT + (128 - 0x4300) * 4));
      PIN(koff[i]);
      if (i < 10) PIN(koff[i + 1]);
    }
  }
  *(LAS v4u*)(L + L_KT - 16) = v4u{0u, 0u, 0u, 0u};
  LAS unsigned char* ktw[4];
  {
    const int row = w * 16 + (lane & 15), sg = max((row >> 3) - 1, 0), rr_ = row - 8 * sg;
    const int idx0 = 128 * sg + ((rr_ >> (3 - sg)) << (7 - sg)) + (rr_ & ((8 >> sg) - 1)) + 1, st = 8 >> sg;
#pragma unroll
    for (int r = 0; r < 4; ++r) {
      const int idx = sel(row < 40, idx0 + st * (4 * g16 + r), tid == 136 && r == 0 ? 0 : 641 + lane);
      ktw[r] = L + L_KT + idx * 4;
      PIN(ktw[r]);
    }
  }
  const __amdgpu_buffer_rsrc_t rw1 = __builtin_amdgcn_make_buffer_rsrc((void*)w1, 0, 1835008, 0x00020000);
  const __amdgpu_buffer_rsrc_t rw2 = __builtin_amdgcn_make_buffer_rsrc((void*)w2, 0, 1835008, 0x00020000);
  SB();
  STAMPA(27);
  asm volatile("s_waitcnt lgkmcnt(0)" : "+s"(uldt), "+s"(uD));
  const unsigned epoch = __builtin_amdgcn_readfirstlane(epoch_v);
  asm volatile("" :: "s"(epoch));
  STAMPA(28);
  const unsigned tagA = epoch * 0x9E3779B1u + 0x7F4A7C15u, tagB = ~tagA;
  {
    const float c = bf_lo(cval);
    const float s = c * __builtin_amdgcn_rcpf(1.0f + __builtin_amdgcn_exp2f(-LOG2E * c));
    *(LAS u16*)(L + lrw + 3072 + (lane & 31) * 2) = (u16)pack2(s, s);
    SB();
    asm volatile("" :: "v"(xq[3]));
    STAMPA(29);
    const v4u xc0 = {xq[0].x, xq[1].x, xq[2].x, xq[3].x}, xc1 = {xq[0].y, xq[1].y, xq[2].y, xq[3].y};
    stg(rs, xgw, OFF_XG + (id & 3) * 524288 + (id >> 2) * 128, xc0);
    stg(rs, xgw, OFF_XG + (id & 3) * 524288 + (id >> 2) * 128 + 8192, xc1);
    SB();
    STAMP(1);
    LAS unsigned char* a = L + lrw + (8 * g16 + q4) * 96 + p4 * 8;
    const v4u sc = lds128(L + lrw + 3072 + g16 * 16);
    const v4u m0 = trd2(a, a + 384), m1 = trd2(a + 32, a + 416), m2 = trd2(a + 64, a + 448);
    SB();
    f32x2 fa[8];
#pragma unroll
    for (int i = 0; i < 4; ++i) { fa[i] = f32x2{bf_lo(ra[i]), bf_hi(ra[i])}; fa[4 + i] = f32x2{bf_lo(rb[i]), bf_hi(rb[i])}; }
    const f32x2 s2 = ((fa[0] + fa[1]) + (fa[2] + fa[3])) + ((fa[4] + fa[5]) + (fa[6] + fa[7]));
    float mean = s2.x + s2.y;
    PIN(mean);
    SB();
    const f32x4 c0 = mfma16(sc, m0, f32x4{}), c1 = mfma16(sc, m1, f32x4{}), c2 = mfma16(sc, m2, f32x4{});
    SB();
    mean = swap16_add(row_sum(mean)) * (1.0f / 512.0f);
    const unsigned k0 = red_step<2>(red_step<1>(red_step<0>(sum4(0u, xc0)))), k1 = red_step<2>(red_step<1>(red_step<0>(sum4(0u, xc1))));
    __builtin_amdgcn_raw_buffer_store_b64(v2u{tagA - k0, tagA - k1}, rs, xcw, OFF_XC + id * 256, SC1);
    PIN(mean);
    SB();
    __builtin_amdgcn_raw_buffer_store_b64(v2u{sel(lane < 16, f2u(c0[0]), sel(lane < 32, f2u(c1[0]), f2u(c2[0]))), (lane & 1) ? tagB : tagA}, rs, mgo,
                                          OFF_MG + cc * 24 * 256 + ks * 16, SC1);
    SB();
    f32x2 q2 = {0.f, 0.f}, q3 = {0.f, 0.f};
#pragma unroll
    for (int i = 0; i < 8; i += 2) {
      const f32x2 d0 = fa[i] - f32x2{mean, mean}, d1 = fa[i + 1] - f32x2{mean, mean};
      q2 = __builtin_elementwise_fma(d0, d0, q2);
      q3 = __builtin_elementwise_fma(d1, d1, q3);
    }
    q2 += q3;
    const float var = rbf(rbf(swap16_add(row_sum(q2.x + q2.y)) * (1.0f / 512.0f)) + 1e-5f);
    __builtin_amdgcn_raw_buffer_store_b64(v2u{pack2(mean, __builtin_amdgcn_sqrtf(var)), hf ? tagB : tagA}, rs, sgo, OFF_SG + ((w & 1) * 512 + id * 2 + (w >> 1)) * 16, SC1);
    SB();
  }
  unsigned wt;
  SB();
  STAMP(2);
  unsigned dsk = tid == 136 ? uD : 0u;
  PIN(dsk);

  {
    const float dtv = rbf(__builtin_amdgcn_exp2f(LOG2E * (w >> 1 ? bf_hi(uldt) : bf_lo(uldt))));
    const float are = dtv * bf_lo(uAre) * LOG2E, aim = dtv * bf_lo(uAim) * INV2PI;
    const float br = dtv * bf_lo(uBre), bi = dtv * bf_lo(uBim), cr = bf_lo(uCre), ci = bf_lo(uCim);
    const f32x2 wv = {cr * br - ci * bi, cr * bi + ci * br};
    const unsigned row = L_UVS + (w >> 1) * UVS_CH + (w & 1) * 256;
    const f32x2 e1 = cexp(are, aim), e8 = cexp(8.f * are, -8.f * aim), e128 = cexp(128.f * are, 128.f * aim);
    const f32x2 e1p = {-e1.y, e1.x}, e8p = {-e8.y, e8.x}, e128p = {-e128.y, e128.x};
    const v4u pois = {POISON, POISON, POISON, POISON};
    f32x2 z[8], v, v2, y, y2;
#define M1(a) cmul(a, e1, e1p)
#define M8(a) cmul(a, e8, e8p)
#define MK(a) cmul(a, e128, e128p)
    z[0] = M1(wv);
    put4<40, 41, 42, 0>(row, wv, f32x2{1.f, 0.f}, e8, z[0]);
    asm volatile("global_load_dword %0, %1, %2" : "=v"(wt) : "v"(wto), "s"(w1 + (w & 1) * (w2 - w1)), "v"(warm) : "memory");
    v = M8(e8); z[1] = M1(z[0]); v2 = M8(v); z[2] = M1(z[1]);
    put4<43, 1, 44, 2>(row, v, z[1], v2, z[2]);
    W1P(0, 0, lrw, 0); W1P(1, 1, lrw, 0); W1P(2, 2, lrw, 0); W1P(3, 3, lrw, 0);
    v = M8(v2); z[3] = M1(z[2]); v2 = M8(v); z[4] = M1(z[3]);
    put4<45, 3, 46, 4>(row, v, z[3], v2, z[4]);
    v = M8(v2); z[5] = M1(z[4]); v2 = M8(v); z[6] = M1(z[5]);
    put4<47, 5, 48, 6>(row, v, z[5], v2, z[6]);
    z[7] = M1(z[6]); z[0] = MK(z[0]); z[1] = MK(z[1]); z[2] = MK(z[2]);
    put4<7, 8, 9, 10>(row, z[7], z[0], z[1], z[2]);
    STAMPC(26);
    stg(rs, (tid >> 3) * 32768 + (tid & 7) * 16, OFF_Z + id * 128, pois);
    STAMPC(27);
    z[3] = MK(z[3]); z[4] = MK(z[4]); z[5] = MK(z[5]); z[6] = MK(z[6]);
    put4<11, 12, 13, 14>(row, z[3], z[4], z[5], z[6]);
    z[7] = MK(z[7]); z[1] = MK(z[1]); z[3] = MK(z[3]); z[5] = MK(z[5]);
    put4<15, 16, 17, 18>(row, z[7], z[1], z[3], z[5]);
    z[7] = MK(z[7]); z[1] = MK(z[1]); z[3] = MK(z[3]); z[5] = MK(z[5]);
    put4<19, 20, 21, 22>(row, z[7], z[1], z[3], z[5]);
    stg(rs, (tid >> 3) * 32768 + (tid & 7) * 16, OFF_Z + id * 128 + 32 * 32768, pois);
    STAMPC(28);
    y = MK(z[7]); z[3] = MK(z[3]); y2 = MK(y); z[4] = MK(z[3]);
    put4<23, 24, 25, 26>(row, y, z[3], y2, z[4]);
    y = MK(y2); z[3] = MK(z[4]); y2 = MK(y); z[4] = MK(z[3]);
    put4<27, 28, 29, 30>(row, y, z[3], y2, z[4]);
    y = MK(y2); v = M8(v2); y2 = MK(y); v2 = M8(v);
    put4<31, 49, 32, 50>(row, y, v, y2, v2);
    y = MK(y2); v = M8(v2); y2 = MK(y); v2 = M8(v);
    put4<33, 51, 34, 52>(row, y, v, y2, v2);
    y = MK(y2); v = M8(v2); y2 = MK(y); v2 = M8(v);
    put4<35, 53, 36, 54>(row, y, v, y2, v2);
    y = MK(y2); v = M8(v2); y2 = MK(y); v2 = M8(v);
    put4<37, 55, 38, 56>(row, y, v, y2, v2);
    y = MK(y2);
    *(LAS unsigned*)(L + row + 39 * UVS_ROW + lane * 4) = pack2(y.x, y.y);
    STAMPC(29);
    W1P(4, 0, lrw, 4096); W1P(5, 1, lrw, 0); W1P(6, 2, lrw, 0); W1P(7, 3, lrw, 0);
#undef M1
#undef M8
#undef MK
  }
  BAR();
  STAMP(3);
  v4u gx0, gx1, gs0, gs1, gs2, gs3, gm, gn;
  unsigned gc0, gc1;
  const int xgo = OFF_XG + id * 8192, xco = OFF_XC + id * 4;
  v4u fv[2][8], fu[2][8];
  if (w < 3) {
    LAS unsigned char* ut = ub + w * 16 * UVS_ROW;
#pragma unroll
    for (int s = 0; s < 8; ++s)
#pragma unroll
      for (int c = 0; c < 2; ++c) { fv[c][s] = lds128(ub + c * UVS_CH + 41 * UVS_ROW + s * 64); fu[c][s] = lds128(ut + c * UVS_CH + s * 64); SB(); }
    f32x4 a0 = {}, a1 = {};
#pragma unroll
    for (int s = 0; s < 8; ++s) { a0 = mfma16(fv[0][s], fu[0][s], a0); SB(); a1 = mfma16(fv[1][s], fu[1][s], a1); SB(); }
    *(LAS unsigned*)ktw[0] = pack2(a0[0] + bf_lo(dsk), a1[0] + bf_hi(dsk));
#pragma unroll
    for (int r = 1; r < 4; ++r) *(LAS unsigned*)ktw[r] = pack2(a0[r], a1[r]);
  }
  SB();
#define LD1(i, so) "buffer_load_dwordx4 %" #i ", %10, %14, " so " offen sc1\n\t"
  asm volatile("buffer_load_dwordx4 %6, %11, %14, 0 offen sc1\n\tbuffer_load_dwordx4 %7, %12, %14, 0 offen sc1\n\t"
               LD1(0, "%15") LD1(1, "%16") "buffer_load_dword %8, %13, %14, %21 offen sc1\n\tbuffer_load_dword %9, %13, %14, %22 offen sc1\n\t"
               LD1(2, "%17") LD1(3, "%18") LD1(4, "%19") LD1(5, "%20")
               : "=&v"(gx0), "=&v"(gx1), "=&v"(gs0), "=&v"(gs1), "=&v"(gs2), "=&v"(gs3), "=&v"(gm), "=&v"(gn), "=&v"(gc0), "=&v"(gc1)
               : "v"(tid * 16), "v"(gmo), "v"(gmo2), "v"(xcr), "s"(rs), "s"(xgo), "s"(xgo + 4096),
                 "s"(OFF_SG), "s"(OFF_SG + 8192), "s"(OFF_SG + 4096), "s"(OFF_SG + 12288), "s"(xco), "s"(xco + 32768) : "memory");
#undef LD1
  SB();
  BAR();
  {
    unsigned k[11];
#pragma unroll
    for (int i = 0; i < 11; ++i) k[i] = *(LAS unsigned*)koff[i];
    SB();
    if (w < 2) *(LAS v4u*)zp = v4u{0u, 0u, 0u, 0u};
#pragma unroll
    for (int c = 0; c < 2; ++c) {
      unsigned e[5], o[5];
#pragma unroll
      for (int i = 0; i < 5; ++i) {
        o[i] = __builtin_amdgcn_perm(k[2 * i + 1], k[2 * i], c ? 0x07060302u : 0x05040100u);
        e[i] = __builtin_amdgcn_perm(k[2 * i + 2], k[2 * i + 1], c ? 0x07060302u : 0x05040100u);
      }
      LAS unsigned char* d = L + L_KC + c * 4 * KC_STRIDE + 32 + tid * 16;
      *(LAS v4u*)(d) = v4u{e[1], e[2], e[3], e[4]};
      *(LAS v4u*)(d + KC_STRIDE) = v4u{o[1], o[2], o[3], o[4]};
      *(LAS v4u*)(d + 2 * KC_STRIDE) = v4u{e[0], e[1], e[2], e[3]};
      *(LAS v4u*)(d + 3 * KC_STRIDE) = v4u{o[0], o[1], o[2], o[3]};
    }
  }
  SB();
  STAMP(4);
  {
    asm volatile("s_waitcnt vmcnt(0)" : "+v"(gx0), "+v"(gx1), "+v"(gs0), "+v"(gs1), "+v"(gs2), "+v"(gs3), "+v"(gm), "+v"(gn), "+v"(gc0), "+v"(gc1), "+v"(wt) :: "memory");
#define XBAD() (red_step<2>(red_step<1>(red_step<0>(sum8(gc0 + gc1, gx0, gx1)))) ^ (2u * tagA))
#define TB(a, c) tagbad(tagbad(0u, a, tagA, tagB), c, tagA, tagB)
    if (__builtin_expect(__any((XBAD() | TB(gm, gn) | TB(gs0, gs1) | TB(gs2, gs3)) != 0u), 0)) {
      _Pragma("nounroll") for (unsigned spin = 0; spin < SPIN_CAP; ++spin) {
        if (spin) PAUSE();
        COUNT(20);
        if (XBAD()) { reload(gx0, rs, tid * 16, xgo); reload(gx1, rs, tid * 16, xgo + 4096); reload1(gc0, rs, xcr, xco); reload1(gc1, rs, xcr, xco + 32768); }
        if (TB(gs0, gs1)) { reload(gs0, rs, tid * 16, OFF_SG); reload(gs1, rs, tid * 16, OFF_SG + 8192); }
        if (TB(gs2, gs3)) { reload(gs2, rs, tid * 16, OFF_SG + 4096); reload(gs3, rs, tid * 16, OFF_SG + 12288); }
        if (TB(gm, gn)) { reload(gm, rs, gmo, 0); reload(gn, rs, gmo2, 0); }
        if (!__any((XBAD() | TB(gm, gn) | TB(gs0, gs1) | TB(gs2, gs3)) != 0u)) break;
      }
    }
#undef XBAD
#undef TB
  }
  STAMP(5);
  W1P(8, 0, lrw, 8192); W1P(9, 1, lrw, 0);
  float zsc;
  v4u mtp;
  {
    float r0 = u2f(gm.x), r1 = u2f(gm.z), r2 = u2f(gn.x), r3 = u2f(gn.z);
#define RSTEP(c) r0 = dpp_add<c>(r0); PIN(r0); r1 = dpp_add<c>(r1); PIN(r1); r2 = dpp_add<c>(r2); PIN(r2); r3 = dpp_add<c>(r3); PIN(r3)
    RSTEP(0xB1); RSTEP(0x4E); RSTEP(0x141); RSTEP(0x140);
#undef RSTEP
    const int i0 = (int)pack2(r0, r1), i1 = (int)pack2(r2, r3);
    unsigned md[6];
#pragma unroll
    for (int k = 0; k < 6; ++k) md[k] = (unsigned)__builtin_amdgcn_readlane(k < 4 ? i0 : i1, 16 * (k & 3));
#define MOD(k) f32x2{bf_lo(md[k]), bf_hi(md[k])}
    const f32x2 one = {1.0f, 1.0f}, sr = one + MOD(4), cr = one + MOD(1);
    unsigned sp = pack2(sr.x, sr.y);
    const unsigned cp = pack2(cr.x, cr.y);
    sp |= ((sp & 0x7FFFu) ? 0u : 0x3080u) | ((sp & 0x7FFF0000u) ? 0u : 0x30800000u);
    const f32x2 S = {bf_lo(sp), bf_hi(sp)}, G = MOD(2) * S, A = f32x2{bf_lo(cp), bf_hi(cp)} * G, B = MOD(0) * G;
#undef MOD
    zsc = w >> 1 ? S.y : S.x;
    mtp = v4u{sel(lane, md[5], sp), tagA, md[3], tagB};
    auto rows4 = [&](const v4u& gx, const v4u& ga, const v4u& gb_, int i) __attribute__((always_inline)) {
      const f32x2 ma = {bf_lo(ga.x), bf_lo(ga.z)}, mb = {bf_lo(gb_.x), bf_lo(gb_.z)};
      const f32x2 ra_ = {__builtin_amdgcn_rcpf(bf_hi(ga.x)), __builtin_amdgcn_rcpf(bf_hi(ga.z))}, rb_ = {__builtin_amdgcn_rcpf(bf_hi(gb_.x)), __builtin_amdgcn_rcpf(bf_hi(gb_.z))};
      const f32x2 h0 = __builtin_elementwise_fma((f32x2{bf_lo(gx.x), bf_lo(gx.y)} - ma) * ra_, f32x2{A.x, A.x}, f32x2{B.x, B.x});
      const f32x2 h1 = __builtin_elementwise_fma((f32x2{bf_lo(gx.z), bf_lo(gx.w)} - mb) * rb_, f32x2{A.x, A.x}, f32x2{B.x, B.x});
      const f32x2 h2 = __builtin_elementwise_fma((f32x2{bf_hi(gx.x), bf_hi(gx.y)} - ma) * ra_, f32x2{A.y, A.y}, f32x2{B.y, B.y});
      const f32x2 h3 = __builtin_elementwise_fma((f32x2{bf_hi(gx.z), bf_hi(gx.w)} - mb) * rb_, f32x2{A.y, A.y}, f32x2{B.y, B.y});
      *(LAS v2u*)(L + L_H + 480 + tid * 8 + i * 2048) = v2u{pack2(h0.x, h0.y), pack2(h1.x, h1.y)};
      *(LAS v2u*)(L + L_H + H_STRIDE + 480 + tid * 8 + i * 2048) = v2u{pack2(h2.x, h2.y), pack2(h3.x, h3.y)};
      *(LAS v2u*)(xbw + i * 1024) = v2u{__builtin_amdgcn_perm(gx.y, gx.x, 0x05040100u), __builtin_amdgcn_perm(gx.w, gx.z, 0x05040100u)};
      *(LAS v2u*)(xbw + 4096 + i * 1024) = v2u{__builtin_amdgcn_perm(gx.y, gx.x, 0x07060302u), __builtin_amdgcn_perm(gx.w, gx.z, 0x07060302u)};
    };
    W1P(10, 2, lrw, 0); W1P(11, 3, lrw, 0);
    rows4(gx0, gs0, gs1, 0);
    W1P(12, 0, lrw, 12288); W1P(13, 1, lrw, 0); W1P(14, 2, lrw, 0); W1P(15, 3, lrw, 0);
    rows4(gx1, gs2, gs3, 1);
    W1P(16, 0, lsp, 0); W1P(17, 1, lsp, 0);
  }
  BAR();
  const unsigned tb = now();
  STAMP(6);

  v4u w1f[32], w2f[28][2], mt0, mt1;
  {
    const bool rh = w & 1;
    constexpr ConvOrder O = conv_order(), OL = conv_order_last();
    v2u A[4][8][2], xk[4];
    v4u B[4][8];
    f32x4 acc[8], keep[4];
#pragma unroll
    for (int u = 0; u < 8; ++u) {
      A[0][u][0] = trd(ka + 512 * u + 8);
      A[0][u][1] = trd(ka + 512 * u);
      B[0][7 - u] = lds128(hb + 64 * (8 * (7 - u) + 3));
    }
    SB();
    if (lane) mtp.z = tb;
    stg(rs, tid < 2 ? tid * 16 : OOB, OFF_MT + id * 128, mtp);
    SB();
    auto hand_over = [&](auto Pc) __attribute__((always_inline)) {
      constexpr int i = Pc.v;
      PIN(acc[2 * i]); PIN(acc[2 * i + 1]);
      const f32x4 ev = acc[2 * i], od = acc[2 * i + 1];
      const f32x4 snd = rh ? ev : od, own = rh ? od : ev;
      *(LAS f32x4*)(i < 2 ? exa + i * 1024 : exb + (i - 1) * 1024) = snd;
#pragma unroll
      for (int c = 0; c < 4; ++c) { float t = keep[i][c] + own[c]; PIN(t); keep[i][c] = t; }
    };
    static_for<0, 144>([&](auto Kc) __attribute__((always_inline)) {
      constexpr int K = Kc.v, r = K / 36, k = K % 36, t = r < 3 ? O.t[k] : OL.t[k], u = r < 3 ? O.u[k] : OL.u[k];
      if constexpr (k == 0 && r > 0) asm volatile("" :: "v"(B[r][7]));
      mfma16_v<r == 0 && u == 0>(acc[t], v4u{A[r][u][0].x, A[r][u][0].y, A[r][u][1].x, A[r][u][1].y}, B[r][t - u]);
      SB();
      if constexpr (K == 24) { WAITV(13); __builtin_amdgcn_s_barrier(); FRESH(); }
      if constexpr (K >= 24 && K < 36) W1PULL(K - 24, K - 24);
      if constexpr (K == 40) { asm volatile("" :: "a"(w1f[11])); __builtin_amdgcn_s_barrier(); FRESH(); }
      if constexpr (K == 60) { WAITV(9); __builtin_amdgcn_s_barrier(); FRESH(); }
      if constexpr (K >= 60 && K < 70) W1PULL(K - 48, K - 48);
      if constexpr (K == 76) { asm volatile("" :: "a"(w1f[21])); __builtin_amdgcn_s_barrier(); FRESH(); }
      if constexpr (K == 142) WAITV(16);
      if constexpr (r == 2 && k < 4) xk[k] = *(LAS v2u*)(xbr + k * 512);
      constexpr int n = k - (r < 2 ? 0 : 4);
      if constexpr (r < 3 && n >= 0 && n < 24) {
        if constexpr (n % 3 == 2) B[r + 1][n / 3] = lds128(hb + 64 * (8 * (n / 3) - r + 2));
        else A[r + 1][n / 3][n % 3] = trd(ka + 512 * (n / 3) + 64 * (r + 1) + 8 - 8 * (n % 3));
      }
      if constexpr (K >= 100 && K < 116) {
        constexpr int e = K - 100;
        const unsigned xw = (e & 2) ? xk[e >> 2].y : xk[e >> 2].x;
        float t2_ = zsc * ((e & 1) ? bf_hi(xw) : bf_lo(xw));
        PIN(t2_);
        keep[e >> 2][e & 3] = t2_;
      }
      if constexpr (K == 108 + 8) hand_over(IC<0>{});
      if constexpr (K == 108 + 17) hand_over(IC<1>{});
      if constexpr (K == 108 + 30) hand_over(IC<2>{});
      if constexpr (K < 14 && K % 4 == 1) W1P(18 + K / 4, (2 + K / 4) & 3, lsp, 4096);
      if constexpr (K == 17) W1P(22, 0, lrx, 0);
      if constexpr (K == 21) W1P(23, 0, lto, 0);
      if constexpr (K >= 41 && K < 65 && K % 3 == 2) W1P(24 + (K - 41) / 3, ((K - 41) / 3) & 3, lrw, (K - 41) / 3 * 1024);
      if constexpr (K >= 66 && K < 98 && K % 4 == 2) W2P((K - 66) / 4, ((K - 66) / 4) & 3, lrw, 8192 + (K - 66) / 4 * 1024);
      if constexpr (K >= 98 && K < 122 && K % 4 == 2) W2P((K - 66) / 4, ((K - 98) / 4) & 3, lsp, (K - 98) / 4 * 1024);
      if constexpr (K == MTK)
        asm volatile("buffer_load_dwordx4 %0, %2, %3, 0 offen sc1\n\tbuffer_load_dwordx4 %1, %2, %3, %4 offen sc1"
                     : "=&v"(mt0), "=&v"(mt1) : "v"(mto), "s"(rs), "s"(16384) : "memory");
      SB();
    });
    STAMP(7);
    asm volatile("s_nop 7" : "+v"(acc[6]), "+v"(acc[7]));
    hand_over(IC<3>{});
    BAR();
#pragma unroll
    for (int i = 0; i < 4; ++i) keep[i] += *(LAS f32x4*)(i < 2 ? eya + i * 1024 : eyb + (i - 1) * 1024);
#pragma unroll
    for (int i = 0; i < 2; ++i) {
      const auto s0 = __builtin_amdgcn_permlane16_swap(pack2(keep[2 * i][0], keep[2 * i][1]), pack2(keep[2 * i + 1][0], keep[2 * i + 1][1]), false, false);
      const auto s1 = __builtin_amdgcn_permlane16_swap(pack2(keep[2 * i][2], keep[2 * i][3]), pack2(keep[2 * i + 1][2], keep[2 * i + 1][3]), false, false);
      stg(rs, i ? zb : za, zs, v4u{guard(s0[0]), guard(s1[0]), guard(s0[1]), guard(s1[1])});
    }
    SB();
  }
  STAMP(8);

  static_assert(OFF_MT == 0, "table offset");
  const unsigned te = now();
  const int w2t = (4 * hf + q4) * 128 + blk * 32 + (((p4 & 1) << 1) | (p4 >> 1)) * 8;
  LAS unsigned char* w2a = L + w2t + (q4 >> 1) * 64;
  LAS unsigned char* w2b = L + w2t + ((q4 >> 1) ^ 1) * 64;
  LAS unsigned char *w2sa = w2a + lsp, *w2sb = w2b + lsp;
  PIN(w2sa); PIN(w2sb);
#define PULL(step, pa, pb, off) do { w2f[step][0] = trd2((pa) + (off), (pa) + (off) + 1024); w2f[step][1] = trd2((pb) + (off), (pb) + (off) + 1024); } while (0)
  const int par = (0x1C >> (ct % 7)) & 1;
  const unsigned char* xg = setp + OFF_Z + rp * 131072;
  unsigned xl = L_XR + w * 8192;
  int xs2 = OFF_Z + rp * 131072 + w * 8192 + (2 ^ par) * 32768, xs3 = xs2 + 32768 - 65536 * par;
  asm volatile("" : "+s"(xl), "+s"(xs2), "+s"(xs3));
  int slo = OFF_Z + (rp * 4 + w) * 32768 + si * 2048;
  v4u hq[56], rc[4], res[2], sx[8];
  u64 nanv = 0;
  const int c0 = 7 * w + rot, c3 = c0 - 7 * (rot >> 2), c4 = c0 - 7 * ((rot + 1) >> 2);
  const int hb0 = OFF_HID + (rp * 4 + rg) * 114688, kb0 = OFF_CK + (rp * 4 + rg) * 4096 + w * 896, ckv = lane < 56 ? l16 : OOB;
#define CHUNK(n) (((n) % 28 / 4 < 3 ? c0 : (n) % 28 / 4 == 3 ? c3 : c4) + (n) % 28 / 4)
#define HFRAG(n) ldg(rs, l16 + ((n) & 3) * 1024, hb0 + CHUNK(n) * 4096 + ((n) / 28) * (2 * 114688))
#define HLOAD(n) hq[n] = HFRAG(n)
  const int hoff = OFF_HID + (rp * 4 * 112 + ct * 8 + 2 * w) * 1024;
  int hv0 = dt == 7 ? OOB : l16, hv1 = dt == 7 ? OOB : l16 + 1024;
  const int cko = OFF_CK + rp * 16384 + (ct * 4 + w) * 64;
  int ckw = (p4 || dt == 7) ? OOB : lane;
  PIN(hv0); PIN(hv1); PIN(ckw);
#define CKPUT(sum, u) __builtin_amdgcn_raw_buffer_store_b32(ckey - red_step<1>(red_step<0>(sum)), rs, ckw, cko + ((u) ^ par) * 4096, SC1)
  const int sxo = OFF_SX + (rp * 4 + w) * 8192;
  LAS unsigned char* xr[2];
#pragma unroll
  for (int i = 0; i < 2; ++i) { xr[i] = L + L_XR + tra + i * 65536; PIN(xr[i]); }
  LAS unsigned char *rr = L + L_XR + tra + (4 * dt + w) * 1024, *rr2 = rr + 65536;
  LAS unsigned char* str = L + L_STAT + (lane & 31) * 8;
  LAS unsigned char *pa = w2a + lrw + 8192, *pb = w2b + lrw + 8192;
  w2a += L_XR + w * 8192; w2b += L_XR + w * 8192;
  LAS unsigned char* stw = str + (w ^ par) * 256;
  PIN(str); PIN(stw); PIN(rr); PIN(rr2); PIN(w2a); PIN(w2b);
  LAS unsigned char* qr = L + (p4 ? L_TB : L_TS) + hf * 16;
  LAS unsigned char* uw = L + L_UVT + hf * 512 + (32 * w + (lane & 31)) * 4;
  LAS unsigned char* uvp = L + L_UVT + (32 * w + 4 * hf) * 4;
  LAS unsigned char* ivp = L + L_TS + (si * 32 + (lane >> 2)) * 2;
  int sxw = sel(lane & 8, OOB, (p4 * 8 + hf * 4 + blk * 2 + ((lane >> 2) & 1)) * 16);
  PIN(pa); PIN(pb); PIN(qr); PIN(uw); PIN(uvp); PIN(ivp); PIN(sxw);
  asm volatile("" : "+s"(slo));
  SB();
  asm volatile("s_waitcnt vmcnt(2)" : "+v"(mt0), "+v"(mt1) :: "memory");
  if (__builtin_expect(__any(tagbad(tagbad(0u, mt0, tagA, tagB), mt1, tagA, tagB) != 0u), 0)) {
    _Pragma("nounroll") for (unsigned spin = 0; spin < SPIN_CAP; ++spin) {
      FRESH();
      COUNT(21);
      if (tagbad(0u, mt0, tagA, tagB)) mt0 = ldg(rs, mto, OFF_MT);
      if (tagbad(0u, mt1, tagA, tagB)) mt1 = ldg(rs, mto, OFF_MT + 16384);
      if (!__any(tagbad(tagbad(0u, mt0, tagA, tagB), mt1, tagA, tagB) != 0u)) break;
      PAUSE();
    }
  }
  *(LAS unsigned*)(mtw) = mt0.x;
  *(LAS unsigned*)(mtw + 512) = mt1.x;
  *(LAS unsigned*)(mtw + 1024) = mt0.z;
  *(LAS unsigned*)(mtw + 1536) = mt1.z;
  {
    int late = (lane & 1) ? max((int)(mt0.z - tb), (int)(mt1.z - tb)) : 0;
    late = max(late, __builtin_amdgcn_update_dpp(late, late, 0xB1, 0xF, 0xF, false));
    late = max(late, __builtin_amdgcn_update_dpp(late, late, 0x4E, 0xF, 0xF, false));
    late = max(late, __builtin_amdgcn_update_dpp(late, late, 0x141, 0xF, 0xF, false));
    late = max(late, __builtin_amdgcn_update_dpp(late, late, 0x140, 0xF, 0xF, false));
    late = max(late, __builtin_amdgcn_update_dpp(late, late, 0x142, 0xA, 0xF, false));
    late = max(late, __builtin_amdgcn_update_dpp(late, late, 0x143, 0xC, 0xF, false));
    if (lane == 63) *(LAS int*)(L + L_ETA + w * 4) = late;
  }
  w1f[22] = trd2(w1x, w1x + 512);
  w1f[23] = trd2(w1y, w1y + 512);
  BAR();
  STAMP(9);
  f32x4 uu[4], vv[4];
  v4u sl0, sl1;
  unsigned ckey = lane ? 0u : tagA;
  {
    const v4u lt = lds128(L + L_ETA);
    const unsigned j0 = *(LAS u16*)ivp, j1 = *(LAS u16*)(ivp + 32);
    v4u qa[32];
#pragma unroll
    for (int i = 0; i < 4; ++i) qa[i] = lds128(qr + i * 32);
    SB();
    bool sent = false;
    auto send = [&]() __attribute__((always_inline)) {
      STAMPB(26);
      asm volatile("buffer_load_dwordx4 %0, %2, %3, %4 offen sc1\n\tbuffer_load_dwordx4 %1, %2, %3, %4 offen offset:1024 sc1"
                   :: "v"(sl0), "v"(sl1), "v"(l16), "s"(rs), "s"(slo) : "memory");
      static_for<0, 8>([&](auto Nc) __attribute__((always_inline)) {
        if constexpr ((Nc.v & 3) == 0) grp<Nc.v * 1024, Nc.v * 1024 - 65536>(so, xl, xs2);
        xfer<Nc.v & 3, true>(l16, rs, so);
      });
    };
    asm volatile("" : "=v"(sl0), "=v"(sl1));
    const int wait = min(__builtin_amdgcn_readfirstlane(max(max((int)lt.x, (int)lt.y), max((int)lt.z, (int)lt.w))), 300) + ZLAG;
    f32x4 c0_, c1_;
    static_for<0, 32>([&](auto Sc) __attribute__((always_inline)) {
      constexpr int s = Sc.v;
      if constexpr (s % 8 == 0) {
        if (!sent && (int)(now() - te) >= wait) { sent = true; send(); }
        SB();
      }
      mfma4_va<s == 0>(c0_, c1_, qa[s], w1f[s]);
      SB();
      if constexpr (s < 28) qa[s + 4] = lds128(qr + (s + 4) * 32);
      if constexpr (s < 8) W1PULL(24 + s, s);
      if constexpr (s >= 8 && s < 16) w2f[(s - 8) >> 1][s & 1] = trd2(((s & 1) ? pb : pa) + ((s - 8) >> 1) * 2048, ((s & 1) ? pb : pa) + ((s - 8) >> 1) * 2048 + 1024);
      if constexpr (s >= 16 && s < 22) w2f[4 + ((s - 16) >> 1)][s & 1] = trd2(((s & 1) ? w2sb : w2sa) + ((s - 16) >> 1) * 2048, ((s & 1) ? w2sb : w2sa) + ((s - 16) >> 1) * 2048 + 1024);
      SB();
    });
    float i0 = __builtin_amdgcn_rcpf(bf_lo(j0)), i1 = __builtin_amdgcn_rcpf(bf_lo(j1));
    PIN(i0); PIN(i1); PIN(ckey);
    asm volatile("s_nop 1" : "+v"(c0_), "+v"(c1_));
    float un_ = c0_[0] + c1_[0];
    swap_add32(un_, -LOG2E * (c0_[1] + c1_[1]));
    *(LAS float*)uw = un_;
#pragma unroll
    for (int i = 0; i < 4; ++i) { uu[i] = *(LAS f32x4*)(uvp + i * 32); vv[i] = *(LAS f32x4*)(uvp + 512 + i * 32); }
    SB();
    if (!sent) {
      while ((int)(now() - te) < wait) { }
      send();
    }
    static_for<0, 8>([&](auto Nc) __attribute__((always_inline)) {
      if constexpr ((Nc.v & 3) == 0) grp<32768 + Nc.v * 1024, Nc.v * 1024 - 65536>(so, xl, xs3);
      xfer<Nc.v & 3, true>(l16, rs, so);
    });
    STAMPB(27);
    asm volatile("s_waitcnt vmcnt(16)" : "+v"(sl0), "+v"(sl1) :: "memory");
    STAMPB(28);
    if (__builtin_expect(__any(max4(max4(0u, sl0), sl1) == POISON), 0)) {
      _Pragma("nounroll") for (unsigned spin = 0; spin < SPIN_CAP; ++spin) {
        FRESH();
        if (spin) PAUSE();
        COUNT(22);
        sl0 = ldg(rs, l16, slo);
        sl1 = ldg(rs, l16 + 1024, slo);
        if (!__any(max4(max4(0u, sl0), sl1) == POISON)) break;
      }
    }
    float sm[8], sq[8];
#pragma unroll
    for (int i = 0; i < 4; ++i) {
      const f32x2 a = f32x2{bf_lo(sl0[i]), bf_hi(sl0[i])} * f32x2{i0, i0}, c = f32x2{bf_lo(sl1[i]), bf_hi(sl1[i])} * f32x2{i1, i1};
      const f32x2 s2 = a + c, q2 = __builtin_elementwise_fma(a, a, c * c);
      sm[2 * i] = s2.x; sm[2 * i + 1] = s2.y; sq[2 * i] = q2.x; sq[2 * i + 1] = q2.y;
    }
#pragma unroll
    for (int i = 0; i < 4; ++i) { swap_add32(sm[i], sm[i + 4]); swap_add32(sq[i], sq[i + 4]); }
#pragma unroll
    for (int i = 0; i < 2; ++i) { swap_add16(sm[i], sm[i + 2]); swap_add16(sq[i], sq[i + 2]); }
#pragma unroll
    for (int i = 0; i < 2; ++i) { sm[i] = dpp_add<0x124>(dpp_add<0x128>(sm[i])); sq[i] = dpp_add<0x124>(dpp_add<0x128>(sq[i])); }
    const bool od = lane & 4;
    stg(rs, sxw, OFF_SX + ((rp * 4 + w) * 16 + si) * 512,
        v4u{f2u(od ? sm[1] : sm[0]), tagA, f2u(od ? sq[1] : sq[0]), tagB});
    SB();
  }
  STAMPB(29);
  W2P(14, 0, lsp, 0); W2P(15, 1, lsp, 0); W2P(16, 2, lsp, 0); W2P(17, 3, lsp, 0); W2P(18, 0, lsp, 4096); W2P(19, 1, lsp, 0);
  WAITV(15);
  STAMP(10);
  BAR();
  STAMP(11);

#define XA(t) (xr[(t) >> 6] + ((t) & 63) * 1024)
#define XF(t) trd2(XA(t), XA(t) + 256)
#define XEN
#define REDO(p) \
  if (XEN __builtin_expect(nanv != 0, 0)) { \
    unsigned m0s; \
    asm volatile("s_mov_b32 %0, m0" : "=s"(m0s)); \
    for (unsigned spin = 0; spin < SPIN_CAP / 16; ++spin) { \
      COUNT(22); \
      _Pragma("nounroll") for (int i = 0; i < 8; ++i) dma_x4(xg + ((p) ^ par) * 32768 + i * 4096, l16, L_XR + (p) * 32768 + i * 4096); \
      WAITV(0); \
      _Pragma("unroll") for (int s = 0; s < 32; ++s) { \
        const v4u t = XF(32 * (p) + s); \
        if (s == 0) mfma_av<true>(acc[(p) % 3], w1f[s], t); else mfma_av<false>(acc[(p) % 3], w1f[s], t); \
        SB(); \
      } \
      mfma_done(acc[(p) % 3]); \
      if (!__any(!(acc[(p) % 3][0] == acc[(p) % 3][0]))) break; \
    } \
    set_m0(m0s); \
  }
  f32x16 acc[3];
  f32x2 ec[4];
  float ep[48], ee[48], ey[48];
  unsigned cs = 0, sbad = 0;
  float ssm = 0.f, ssq = 0.f;
  v4u x[128], eo;
#pragma unroll
  for (int t = 0; t < 4; ++t) x[t] = XF(t);
  SB();
  auto eslot = [&](auto Sc) __attribute__((always_inline)) {
    static_for<0, 48>([&](auto Vc) __attribute__((always_inline)) {
      constexpr int V = Vc.v, k = V & 15, st = Sc.v - epi_slot(V);
      if constexpr (st == 6 && (k & 1)) {
        unsigned o = pack2(ey[V - 1], ey[V]);
        PIN(o);
        eo[(k >> 1) & 3] = o;
        if constexpr (k == 7) { stg(rs, hv0, hoff + ((V >> 4) ^ par) * 114688, eo); cs = sum4(0u, eo); PIN(cs); }
        if constexpr (k == 15) { stg(rs, hv1, hoff + ((V >> 4) ^ par) * 114688, eo); CKPUT(sum4(cs, eo), V >> 4); }
      }
      if constexpr (st == 5) { ey[V] = ep[V] * ee[V]; PIN(ey[V]); }
      if constexpr (st == 4) { ee[V] = __builtin_amdgcn_rcpf(ee[V]); PIN(ee[V]); }
      if constexpr (st == 3) { ee[V] = 1.0f + ee[V]; PIN(ee[V]); }
      if constexpr (st == 2) { ee[V] = __builtin_amdgcn_exp2f(ep[V]); PIN(ee[V]); }
      if constexpr (st == 1) { ep[V] = __builtin_fmaf(ec[V >> 4].x, acc[(V >> 4) % 3][k], ep[V]); PIN(ep[V]); }
      if constexpr (st == 0) { ep[V] = __builtin_fmaf(ec[V >> 4].y, uu[k >> 2][k & 3], vv[k >> 2][k & 3]); PIN(ep[V]); }
    });
    SB();
  };
  static_for<0, 64>([&](auto Jc) __attribute__((always_inline)) {
    constexpr int J = Jc.v, p = J >> 4, j = J & 15;
    asm volatile("" :: "v"(x[2 * J + 1]));
    mfma_av<j == 0>(acc[p % 3], w1f[2 * j], x[2 * J]);
    if constexpr (J < 10 || (J >= 14 && J < 20)) {
      constexpr int n = J < 10 ? J : J - 4;
      if constexpr ((n & 3) == 0) grp<65536 + (n >> 3) * 32768 + (n & 4) * 1024, (n & 4) * 1024>(so, xl, n < 8 ? xs2 : xs3);
      xfer<n & 3, true>(l16, rs, so);
    }
    if constexpr (J >= 10 && J < 14)
      asm volatile("buffer_load_dwordx4 %0, %2, %3, %4 offen offset:%5 sc1\n\tbuffer_load_dwordx4 %1, %2, %3, %4 offen offset:%6 sc1"
                   : "=&v"(sx[2 * (J - 10)]), "=&v"(sx[2 * (J - 10) + 1])
                   : "v"(l16), "s"(rs), "s"(sxo + ((J - 10) & 2) * 2048), "n"((J & 1) * 2048), "n"((J & 1) * 2048 + 1024) : "memory");
    if constexpr (J == 43) asm volatile("" :: "a"(w2f[13][1]));
    if constexpr (J >= 20 && J < 28) W2P(J, J & 3, xl, (J - 20) * 1024);
    if constexpr (J >= 28 && J < 34) W2P(J, J & 3, lsp, (J - 28) * 1024);
    if constexpr (J >= 35 && J < 43) W2P(J - 1, (J - 35) & 3, xl, 32768 + (J - 35) * 1024);
    if constexpr (J >= 43 && J < 51) W2P(J - 1, (J - 43) & 3, xl, (J - 43) * 1024);
    static_for<hc_upto(J), hc_upto(J + 1)>([&](auto Nc) __attribute__((always_inline)) { HLOAD(Nc.v); });
    SB();
    constexpr bool sym = epi_pair(J) || J < 19 || J > 60;
    if constexpr (J < 62) { x[2 * J + 4] = XF(2 * J + 4); if constexpr (!sym) x[2 * J + 5] = XF(2 * J + 5); }
    if constexpr (J >= 38 && J < 42) w2f[10 + J - 38][0] = trd2(w2a + (J - 38) * 2048, w2a + (J - 38) * 2048 + 1024);
    SB();
    if constexpr (epi_pair(J)) eslot(IC<2 * J>{});
    mfma_av<false>(acc[p % 3], w1f[2 * j + 1], x[2 * J + 1]);
    if constexpr (sym && J < 62) x[2 * J + 5] = XF(2 * J + 5);
    if constexpr (J >= 38 && J < 42) w2f[10 + J - 38][1] = trd2(w2b + (J - 38) * 2048, w2b + (J - 38) * 2048 + 1024);
    if constexpr (J >= 19 && J < 25) w2f[7 + ((J - 19) >> 1)][(J - 19) & 1] = trd2((((J - 19) & 1) ? w2sb : w2sa) + ((J - 19) >> 1) * 2048, (((J - 19) & 1) ? w2sb : w2sa) + ((J - 19) >> 1) * 2048 + 1024);
    SB();
    if constexpr (J == 10) { waitv<c_between(-1, 10)>(); __builtin_amdgcn_s_barrier(); FRESH(); }
    if constexpr (J == 34) waitv<c_between(19, 34)>();
    if constexpr (J == 37) waitv<c_between(27, 37)>();
    if constexpr (J == EO - 4 || J == EO - 3) {
      constexpr int i = 4 * (J - EO + 4);
      asm volatile("s_waitcnt vmcnt(%4)" : "+v"(sx[i]), "+v"(sx[i + 1]), "+v"(sx[i + 2]), "+v"(sx[i + 3]) : "n"(c_between(11 + i / 2, J)) : "memory");
      unsigned b0 = tagbad(0u, sx[i], tagA, tagB), b1 = tagbad(0u, sx[i + 1], tagA, tagB);
      float m0 = u2f(sx[i].x) + u2f(sx[i + 1].x), m1 = u2f(sx[i + 2].x) + u2f(sx[i + 3].x);
      PIN(m0); PIN(m1);
      float q0 = u2f(sx[i].z) + u2f(sx[i + 1].z), q1 = u2f(sx[i + 2].z) + u2f(sx[i + 3].z);
      PIN(q0); PIN(q1);
      sbad |= tagbad(b0, sx[i + 2], tagA, tagB) | tagbad(b1, sx[i + 3], tagA, tagB);
      ssm += m0 + m1; PIN(ssm);
      ssq += q0 + q1; PIN(ssq);
      PIN(sbad);
      SB();
    }
    if constexpr (J == EO - 2) {
      if (__builtin_expect(__any(sbad != 0u), 0)) {
        _Pragma("nounroll") for (unsigned spin = 0; spin < SPIN_CAP; ++spin) {
          FRESH();
          if (spin) PAUSE();
          COUNT(23);
          sbad = 0; ssm = 0.f; ssq = 0.f;
#pragma unroll
          for (int i = 0; i < 8; ++i) sx[i] = ldg(rs, l16 + (i & 3) * 1024, sxo + (i >> 2) * 4096);
#pragma unroll
          for (int i = 0; i < 8; i += 4) {
            sbad = tagbad(tagbad(tagbad(tagbad(sbad, sx[i], tagA, tagB), sx[i + 1], tagA, tagB), sx[i + 2], tagA, tagB), sx[i + 3], tagA, tagB);
            ssm += (u2f(sx[i].x) + u2f(sx[i + 1].x)) + (u2f(sx[i + 2].x) + u2f(sx[i + 3].x));
            ssq += (u2f(sx[i].z) + u2f(sx[i + 1].z)) + (u2f(sx[i + 2].z) + u2f(sx[i + 3].z));
          }
          if (!__any(sbad != 0u)) break;
        }
      }
      const float mean = swap32_add(ssm) * (1.0f / 512.0f);
      const float ms = __builtin_fmaf(swap32_add(ssq), 1.0f / 512.0f, 1e-5f);
      const float a = -LOG2E * __builtin_amdgcn_rsqf(fmaxf(__builtin_fmaf(-mean, mean, ms), 1e-5f));
      *(LAS f32x2*)(stw) = f32x2{a, -a * mean};
      SB();
    }
    if constexpr (J == EO - 1) {
      asm volatile("s_waitcnt lgkmcnt(4)" ::: "memory");
      __builtin_amdgcn_s_barrier();
      FRESH();
#pragma unroll
      for (int i = 0; i < 4; ++i) ec[i] = *(LAS f32x2*)(str + i * 256);
      SB();
    }
    if constexpr (!epi_pair(J)) eslot(IC<2 * J>{});
    eslot(IC<2 * J + 1>{});
    if constexpr (J == 16) asm volatile("s_nop 4");
    if constexpr (j == 0 && p >= 1) { PIN(acc[(p - 1) % 3]); nanv = __builtin_amdgcn_ballot_w64(!(acc[(p - 1) % 3][0] == acc[(p - 1) % 3][0])); asm volatile("" : "+s"(nanv)); }
    if constexpr (j == 1 && p >= 1) {
      if constexpr (p == 1) REDO(0)
      if constexpr (p == 2) REDO(1)
      if constexpr (p == 3) REDO(2)
      rc[p - 1] = trd2((p == 3 ? rr2 : rr) + ((p - 1) & 1) * 32768, (p == 3 ? rr2 : rr) + ((p - 1) & 1) * 32768 + 256);
    }
    if constexpr (j == 2 && p >= 1) {
      asm volatile("s_waitcnt lgkmcnt(4)" ::: "memory");
      __builtin_amdgcn_s_barrier();
      FRESH();
    }
    SB();
    if constexpr (J == 15) STAMP(16);
    if constexpr (J == 31) STAMP(17);
    if constexpr (J == 47) STAMP(18);
  });
  STAMP(12);
  static_assert(EO <= 30 && epi_slot(31) < epi_slot(32) && epi_slot(16) + 1 >= 2 * 34 && epi_slot(32) + 1 >= 2 * 50 && epi_slot(47) + 6 < 128, "schedule");
  static_for<18, 24>([&](auto Ic) __attribute__((always_inline)) { HLOAD(Ic.v); });
  SB();
  asm volatile("s_nop 3" : "+v"(acc[0]));
  nanv = __builtin_amdgcn_ballot_w64(!(acc[0][0] == acc[0][0]));
  REDO(3)
  rc[3] = trd2(rr2 + 32768, rr2 + 32768 + 256);
#pragma unroll
  for (int h = 0; h < 2; ++h) {
    v4u o;
#pragma unroll
    for (int i = 0; i < 4; ++i) {
      const int k = 8 * h + 2 * i;
      const f32x2 t = __builtin_elementwise_fma(f32x2{ec[3].y, ec[3].y}, f32x2{uu[k >> 2][k & 3], uu[k >> 2][(k & 3) + 1]}, f32x2{vv[k >> 2][k & 3], vv[k >> 2][(k & 3) + 1]});
      const f32x2 pp = __builtin_elementwise_fma(f32x2{ec[3].x, ec[3].x}, f32x2{acc[0][k], acc[0][k + 1]}, t);
      const f32x2 d = f32x2{__builtin_amdgcn_exp2f(pp.x), __builtin_amdgcn_exp2f(pp.y)} + f32x2{1.0f, 1.0f};
      const f32x2 y = pp * f32x2{__builtin_amdgcn_rcpf(d.x), __builtin_amdgcn_rcpf(d.y)};
      o[i] = pack2(y.x, y.y);
    }
    stg(rs, h ? hv1 : hv0, hoff + (3 ^ par) * 114688, o);
    if (h) CKPUT(sum4(cs, o), 3); else cs = sum4(0u, o);
    SB();
  }
  static_for<24, 28>([&](auto Ic) __attribute__((always_inline)) { HLOAD(Ic.v); });
  SB();
  STAMP(13);

  f32x16 d[2][2];
  LAS unsigned char* redw = L + L_XR + w * 8192 + l16;
  LAS unsigned char* redr = L + L_XR + w * 2048 + l16;
#define REDW(un, q) *(LAS f32x4*)(redw + (un) * 32768 + (q) * 1024) = \
    f32x4{d[un][(q) >> 2][4 * ((q) & 3)], d[un][(q) >> 2][4 * ((q) & 3) + 1], d[un][(q) >> 2][4 * ((q) & 3) + 2], d[un][(q) >> 2][4 * ((q) & 3) + 3]}
  LAS unsigned char *w2ua = w2a + 65536, *w2ub = w2b + 65536;
  PIN(w2ua); PIN(w2ub);
#define CHECK(un) \
  if (__builtin_expect((unsigned)__builtin_amdgcn_readlane((int)m[un], 63) != 14u * tagA, 0)) { \
    _Pragma("nounroll") for (unsigned spin = 0; spin < SPIN_CAP; ++spin) { \
      FRESH(); \
      COUNT(24 + (un)); \
      unsigned t = sum4(0u, ldg(rs, ckv, kb0 + (un) * 8192)); \
      static_for<0, 7>([&](auto Cc) __attribute__((always_inline)) { \
        constexpr int c = Cc.v; \
        v4u q[4]; \
        _Pragma("unroll") for (int i = 0; i < 4; ++i) q[i] = ldg(rs, l16 + i * 1024, hb0 + CHUNK(4 * c) * 4096 + (un) * (2 * 114688)); \
        t = sum8(sum8(t, q[0], q[1]), q[2], q[3]); \
        mfma_av<c == 0>(d[un][0], w2f[4 * c][0], q[0]); mfma_av<c == 0>(d[un][1], w2f[4 * c][1], q[0]); \
        _Pragma("unroll") for (int i = 1; i < 4; ++i) { mfma_av<false>(d[un][0], w2f[4 * c + i][0], q[i]); mfma_av<false>(d[un][1], w2f[4 * c + i][1], q[i]); } \
        SB(); \
      }); \
      mfma_done(d[un][1]); \
      PIN(d[un][0]); \
      if ((unsigned)__builtin_amdgcn_readlane((int)red_all(t), 63) == 14u * tagA) break; \
      PAUSE(); \
    } \
    if (un) { _Pragma("unroll") for (int q = 0; q < 4; ++q) REDW(1, q); } \
  }
  unsigned m[2] = {0u, 0u};
  v4u gbv, scv, ck[2];
  f32x4 pr[8];
  float gb[8], rx[2][8];
  unsigned orow = (rp * 128 + ((lane & 31) >> 3) * 16 + rg * 8 + (lane & 7)) * 1024 + dt * 128 + w * 32 + hf * 16;
  static_for<0, 112>([&](auto Xc) __attribute__((always_inline)) {
    constexpr int X = Xc.v, I = X < 100 ? X >> 1 : 50 + (X - 100) % 6, e = X < 100 ? X & 1 : (X - 100) / 6, un = I / 28;
    mfma_av<I % 28 == 0>(d[un][e], w2f[I % 28][e], hq[I]);
    SB();
    if constexpr (X < 100 && e == 0) {
      if constexpr (I < 3) PULL(14 + I, w2sa, w2sb, I * 2048);
      if constexpr (I >= 3 && I < 7) PULL(14 + I, w2a, w2b, 32768 + (I - 3) * 2048);
      if constexpr (I >= 7 && I < 11) PULL(14 + I, w2a, w2b, (I - 7) * 2048);
      if constexpr (I == 21) waitv<hl_upto(21) - 28 + 1>();
      if constexpr (I >= 21 && I < 24) PULL(4 + I, w2ua, w2ub, (I - 21) * 2048);
      if constexpr (I == 28) { m[0] = sum4(m[0], ck[0]); PIN(m[0]); }
      if constexpr (I >= 29 && I < 35) { m[0] = red_step<I - 29>(m[0]); PIN(m[0]); }
      if constexpr (I == 35) CHECK(0)
      if constexpr (I >= 35 && I < 43) REDW(0, I - 35);
    }
    if constexpr (X < 100 && e == 1) {
      if constexpr (I < 6) W2P(50 + I, I & 3, xl, 65536 + I * 1024);
      static_for<hl_upto(I), hl_upto(I + 1)>([&](auto Nc) __attribute__((always_inline)) { HLOAD(Nc.v); });
      if constexpr (I == 6) ck[0] = ldg(rs, ckv, kb0);
      if constexpr (I == 37) ck[1] = ldg(rs, ckv, kb0 + 8192);
    }
    if constexpr (X < 100 ? e == 1 : e == 0) { m[un] = sum4(m[un], hq[I]); PIN(m[un]); }
    if constexpr (X == 105) { m[1] = sum4(m[1], ck[1]); PIN(m[1]); }
    if constexpr (X >= 106) { m[1] = red_step<X - 106>(m[1]); PIN(m[1]); }
    if constexpr (X == 23 || X == 24) { const v4u ev = rc[2 * (X - 23)], od = rc[2 * (X - 23) + 1]; res[X - 23] = (rg ^ par) ? od : ev; PIN(res[X - 23]); }
    if constexpr (X == 22) { gbv = lds128(L + L_TG + (64 * dt + 16 * w + 8 * hf) * 2); scv = lds128(L + L_TS + (64 * dt + 16 * w + 8 * hf) * 2); PIN(orow); }
    if constexpr (X >= 26 && X < 42 && (X & 1) == 0) {
      constexpr int q = (X - 26) >> 1;
      const float inv = __builtin_amdgcn_rcpf((q & 1) ? bf_hi(scv[q >> 1]) : bf_lo(scv[q >> 1]));
      gb[q] = -LN2 * ((q & 1) ? bf_hi(gbv[q >> 1]) : bf_lo(gbv[q >> 1]));
      rx[0][q] = inv * ((q & 1) ? bf_hi(res[0][q >> 1]) : bf_lo(res[0][q >> 1]));
      rx[1][q] = inv * ((q & 1) ? bf_hi(res[1][q >> 1]) : bf_lo(res[1][q >> 1]));
      PIN(gb[q]); PIN(rx[0][q]); PIN(rx[1][q]);
    }
    if constexpr (X == 86) BAR();
    if constexpr (X == 86 || X == 88) {
#pragma unroll
      for (int k = X - 86; k < X - 84; ++k) { pr[2 * k] = *(LAS f32x4*)(redr + k * 8192); pr[2 * k + 1] = *(LAS f32x4*)(redr + k * 8192 + 1024); }
    }
    if constexpr (X >= 91 && X < 99) {
      constexpr int q = X - 91;
      float t = (pr[q >> 2][q & 3] + pr[2 + (q >> 2)][q & 3]);
      PIN(t);
      float t2_ = (pr[4 + (q >> 2)][q & 3] + pr[6 + (q >> 2)][q & 3]);
      PIN(t2_);
      rx[0][q] = __builtin_fmaf(gb[q], t + t2_, rx[0][q]);
      PIN(rx[0][q]);
    }
    if constexpr (X == 99)
      *(GAS v4u*)((GAS unsigned char*)out + orow) = v4u{pack2(rx[0][0], rx[0][1]), pack2(rx[0][2], rx[0][3]), pack2(rx[0][4], rx[0][5]), pack2(rx[0][6], rx[0][7])};
    if constexpr (X == 108) asm volatile("s_nop 3" : "+v"(d[1][0]));
    if constexpr (X >= 108) REDW(1, X - 108);
    SB();
    if constexpr (X == 55) STAMP(14);
  });
  STAMP(19);
  asm volatile("s_nop 7" : "+v"(d[1][1]));
  CHECK(1)
#pragma unroll
  for (int q = 4; q < 8; ++q) REDW(1, q);
  BAR();
  {
    f32x4 s0 = {}, s1 = {};
#pragma unroll
    for (int k = 0; k < 4; ++k) { s0 += *(LAS f32x4*)(redr + k * 8192 + 32768); s1 += *(LAS f32x4*)(redr + k * 8192 + 32768 + 1024); }
    float y[8];
#pragma unroll
    for (int q = 0; q < 8; ++q) y[q] = __builtin_fmaf(gb[q], q < 4 ? s0[q & 3] : s1[q & 3], rx[1][q]);
    *(GAS v4u*)((GAS unsigned char*)out + orow + 65536) = v4u{pack2(y[0], y[1]), pack2(y[2], y[3]), pack2(y[4], y[5]), pack2(y[6], y[7])};
  }
  if (b == 0 && tid == 0) __hip_atomic_store((GAS unsigned*)ws, epoch + 1u, __ATOMIC_RELAXED, __HIP_MEMORY_SCOPE_AGENT);
  STAMP(15);
}

extern "C" void kernel_launch(void* const* d_in, const int* in_sizes, int n_in, void* d_out, int out_size, void* d_ws, size_t ws_size,
                              hipStream_t stream) {
  (void)in_sizes; (void)n_in; (void)out_size; (void)ws_size;
  (void)hipFuncSetAttribute((const void*)s4_block_kernel, hipFuncAttributeMaxDynamicSharedMemorySize, LDS_BYTES);
  s4_block_kernel<<<256, 256, LDS_BYTES, stream>>>((cptr)d_in[0], (cptr)d_in[1], (cptr)d_in[2], (cptr)d_in[3], (cptr)d_in[4], (cptr)d_in[5], (cptr)d_in[6],
                                                   (cptr)d_in[7], (cptr)d_in[8], (cptr)d_in[10], (cptr)d_in[11], (cptr)d_in[12], (cptr)d_in[13],
                                                   (unsigned char*)d_out, (unsigned char*)d_ws, (cptr)d_in[9]);
}
```

```cpp
#include <hip/hip_runtime.h>

#define GAS __attribute__((address_space(1)))
#define LAS __attribute__((address_space(3)))
#define DI __device__ __forceinline__
typedef unsigned v4u __attribute__((ext_vector_type(4)));
typedef unsigned v2u __attribute__((ext_vector_type(2)));
typedef float f32x2 __attribute__((ext_vector_type(2)));
typedef float f32x4 __attribute__((ext_vector_type(4)));
typedef float f32x16 __attribute__((ext_vector_type(16)));
typedef __bf16 bf16x2 __attribute__((ext_vector_type(2)));
typedef __bf16 bf16x8 __attribute__((ext_vector_type(8)));
typedef short s16x4 __attribute__((ext_vector_type(4)));
typedef unsigned short u16;
typedef unsigned long long u64;

constexpr int SET_BASE = 4096;
constexpr int OFF_MT = 0;
constexpr int OFF_SG = 32768;
constexpr int OFF_MG = 65536;
constexpr int OFF_SX = 524288;
constexpr int OFF_XC = 458752;
constexpr int OFF_XG = 1 << 20;
constexpr int OFF_Z = OFF_XG + (2 << 20);
constexpr int OFF_HID = OFF_Z + 2097152;
constexpr int OFF_CK = OFF_HID + 7340032;
constexpr int SET_BYTES = OFF_CK + 262144;
static_assert(OFF_MG + 393216 <= OFF_XC && OFF_XC + 65536 <= OFF_SX, "set size");

constexpr int L_TS = 0, L_TB = 1024, L_TG = 2048;
constexpr int L_UVT = 3072;
constexpr int L_M = 4096;
constexpr int L_UVS = L_M;
constexpr int UVS_ROW = 544, UVS_CH = 57 * UVS_ROW;
constexpr int L_KC = L_M;
constexpr int KC_STRIDE = 4160;
constexpr int L_H = L_KC + 8 * KC_STRIDE;
constexpr int H_STRIDE = 4576;
constexpr int L_XB = L_H + 2 * H_STRIDE;
constexpr int L_RX = L_XB + 8192;
constexpr int L_R = L_M + 65536;
constexpr int L_TOP = L_M + 131072 + 24576;
constexpr int L_KT = L_TOP + 16;
constexpr int L_STAT = L_TOP;
constexpr int L_ETA = L_RX + 12288;
constexpr int L_XR = L_M;
constexpr int L_SP = L_M + 131072;
constexpr int LDS_BYTES = 163840;
static_assert(L_TOP + 4096 <= LDS_BYTES && L_ETA + 16 <= L_R && L_UVS + 2 * UVS_CH <= L_R && L_SP + 24576 <= L_TOP, "LDS map");
static_assert(L_H % 16 == 0 && L_XB % 16 == 0 && L_RX % 16 == 0 && L_KT % 16 == 0, "alignment");

#define ZLAG 30
#define MTK 124
#define SXJ 14
#define EOV 30
#define HC0 52
#define HC1 64
#define HL0 9
#define HT0 4
#define HT1 6
constexpr int EO = EOV;
constexpr int CODE_BYTES = 36096;
constexpr int SC1 = 16;
constexpr int OOB = 0x7FFFFF00;
constexpr unsigned SPIN_CAP = 1u << 15;
constexpr unsigned POISON = 0xFFFFFFFFu;
constexpr float LOG2E = 1.4426950408889634f, LN2 = 0.6931471805599453f, INV2PI = 0.15915494309189535f;

#define SB() __builtin_amdgcn_sched_barrier(0)
#define WAITV(n) asm volatile("s_waitcnt vmcnt(" #n ")" ::: "memory")
#define PIN(x) asm volatile("" : "+v"(x))
#define FRESH() asm volatile("" ::: "memory")
#define PAUSE() asm volatile("s_sleep 4" ::: "memory")
#define BAR() do { asm volatile("s_waitcnt lgkmcnt(0)" ::: "memory"); __builtin_amdgcn_s_barrier(); asm volatile("" ::: "memory"); } while (0)
#define STAMP(i)
#define SARG
#define SPASS
#define COUNT(i)

#define STAMPC(i)
#define STAMPA(i)
#define STAMPB(i) STAMP(i)
DI float u2f(unsigned u) { return __builtin_bit_cast(float, u); }
DI unsigned f2u(float f) { return __builtin_bit_cast(unsigned, f); }
DI float dot2(unsigned u, float c) { const bf16x2 v = __builtin_bit_cast(bf16x2, u); return __builtin_amdgcn_fdot2_f32_bf16(v, v, c, false); }
DI float bf_lo(unsigned u) { return __builtin_bit_cast(float, u << 16); }
DI float bf_hi(unsigned u) { return __builtin_bit_cast(float, u & 0xFFFF0000u); }
DI unsigned pack2(float a, float b) { f32x2 v = {a, b}; return __builtin_bit_cast(unsigned, __builtin_convertvector(v, bf16x2)); }
DI float rbf(float a) { return bf_lo(pack2(a, a)); }
DI unsigned sel(bool c, unsigned a, unsigned b) { asm("" : "+v"(a), "+v"(b)); return c ? a : b; }
DI unsigned guard(unsigned u) { return u < 0xFFFFFFFEu ? u : 0xFFFFFFFEu; }
DI unsigned max4(unsigned m, v4u v) {
  asm("v_max3_u32 %0, %0, %1, %2\n\tv_max3_u32 %0, %0, %3, %4" : "+v"(m) : "v"(v.x), "v"(v.y), "v"(v.z), "v"(v.w));
  return m;
}
DI f32x4 mfma16(v4u a, v4u b, f32x4 c) { return __builtin_amdgcn_mfma_f32_16x16x32_bf16(__builtin_bit_cast(bf16x8, a), __builtin_bit_cast(bf16x8, b), c, 0, 0, 0); }
DI f32x16 mfma32(v4u a, v4u b, f32x16 c) { return __builtin_amdgcn_mfma_f32_32x32x16_bf16(__builtin_bit_cast(bf16x8, a), __builtin_bit_cast(bf16x8, b), c, 0, 0, 0); }
template <bool FIRST> DI void mfma_av(f32x16& acc, const v4u& a, const v4u& b) {
  if (FIRST) asm volatile("v_mfma_f32_32x32x16_bf16 %0, %1, %2, 0" : "=&v"(acc) : "a"(a), "v"(b));
  else asm volatile("v_mfma_f32_32x32x16_bf16 %0, %1, %2, %0" : "+v"(acc) : "a"(a), "v"(b));
}
template <bool FIRST> DI void mfma4_va(f32x4& c0, f32x4& c1, const v4u& a, const v4u& b) {
  const v2u al = {a.x, a.y}, ah = {a.z, a.w}, bl = {b.x, b.y}, bh = {b.z, b.w};
  if (FIRST) asm volatile("v_mfma_f32_4x4x4_16b_bf16 %0, %2, %4, 0\n\tv_mfma_f32_4x4x4_16b_bf16 %1, %3, %5, 0" : "=&v"(c0), "=&v"(c1) : "v"(al), "v"(ah), "a"(bl), "a"(bh));
  else asm volatile("s_nop 0\n\tv_mfma_f32_4x4x4_16b_bf16 %0, %2, %4, %0\n\tv_mfma_f32_4x4x4_16b_bf16 %1, %3, %5, %1" : "+v"(c0), "+v"(c1) : "v"(al), "v"(ah), "a"(bl), "a"(bh));
}
template <bool FIRST> DI void mfma16_v(f32x4& acc, const v4u& a, const v4u& b) {
  if (FIRST) asm volatile("v_mfma_f32_16x16x32_bf16 %0, %1, %2, 0" : "=&v"(acc) : "v"(a), "v"(b));
  else asm volatile("v_mfma_f32_16x16x32_bf16 %0, %1, %2, %0" : "+v"(acc) : "v"(a), "v"(b));
}
DI void mfma_done(f32x16& acc) { asm volatile("s_nop 15" : "+v"(acc)); }
DI v2u trd(LAS unsigned char* p) { return __builtin_bit_cast(v2u, __builtin_amdgcn_ds_read_tr16_b64_v4i16((LAS s16x4*)p)); }
DI v4u trd2(LAS unsigned char* p0, LAS unsigned char* p1) { const v2u a = trd(p0), b = trd(p1); return v4u{a.x, a.y, b.x, b.y}; }
DI v4u lds128(LAS unsigned char* p) { return *(LAS v4u*)p; }
DI v4u ldg(__amdgpu_buffer_rsrc_t r, int voff, int soff) { return __builtin_amdgcn_raw_buffer_load_b128(r, voff, soff, SC1); }
DI void stg(__amdgpu_buffer_rsrc_t r, int voff, int soff, v4u v) {
  __builtin_amdgcn_raw_buffer_store_b128(v, r, voff, soff, SC1);
  asm volatile("s_nop 1" :: "v"(v) : "memory");
}
template <int CTRL> DI float dpp_add(float v) {
  return v + __builtin_bit_cast(float, __builtin_amdgcn_update_dpp(0, __builtin_bit_cast(int, v), CTRL, 0xF, 0xF, true));
}
DI float swap16_add(float v) { const unsigned u = __builtin_bit_cast(unsigned, v); const auto r = __builtin_amdgcn_permlane16_swap(u, u, false, false);
  return u2f(r[0]) + u2f(r[1]); }
DI float swap32_add(float v) { const unsigned u = __builtin_bit_cast(unsigned, v); const auto r = __builtin_amdgcn_permlane32_swap(u, u, false, false);
  return u2f(r[0]) + u2f(r[1]); }
DI float quad_sum(float v) { v = dpp_add<0xB1>(v); return dpp_add<0x4E>(v); }
DI float row_sum(float v) { return dpp_add<0x140>(dpp_add<0x141>(quad_sum(v))); }
DI float wave_sum(float v) { return swap32_add(swap16_add(row_sum(v))); }
DI f32x2 cmul(f32x2 a, f32x2 e, f32x2 ep) { const f32x2 t = f32x2{a.x, a.x} * e; return __builtin_elementwise_fma(f32x2{a.y, a.y}, ep, t); }
DI f32x2 cexp(float re2, float rev) {
  const float m = __builtin_amdgcn_exp2f(re2), f = __builtin_amdgcn_fractf(rev);
  return f32x2{m * __builtin_amdgcn_cosf(f), m * __builtin_amdgcn_sinf(f)};
}
template <int LI, int SI> DI void grp(int& so, unsigned lbase, int sbase) {
  asm volatile("s_add_i32 m0, %1, %3\n\ts_add_i32 %0, %2, %4" : "=s"(so) : "s"(lbase), "s"(sbase), "n"(LI), "n"(SI) : "scc", "memory");
}
template <int K, bool COH> DI void xfer(unsigned v, __amdgpu_buffer_rsrc_t r, int so) {
  if (COH) asm volatile("buffer_load_dwordx4 %0, %1, %2 offen offset:%3 sc1 lds" :: "v"(v), "s"(r), "s"(so), "n"(K * 1024) : "memory");
  else asm volatile("buffer_load_dwordx4 %0, %1, %2 offen offset:%3 lds" :: "v"(v), "s"(r), "s"(so), "n"(K * 1024) : "memory");
}
DI void set_m0(unsigned lds_dst) { asm volatile("s_mov_b32 m0, %0\n\ts_nop 0" :: "s"(lds_dst) : "memory"); }
DI void dma_x4(const unsigned char* sbase, unsigned v, unsigned lds_dst) {
  asm volatile("s_mov_b32 m0, %2\n\ts_nop 0\n\tglobal_load_lds_dwordx4 %0, %1 sc1\n\tglobal_load_lds_dwordx4 %0, %1 offset:1024 sc1\n\t"
               "global_load_lds_dwordx4 %0, %1 offset:2048 sc1\n\tglobal_load_lds_dwordx4 %0, %1 offset:3072 sc1"
               :: "v"(v), "s"(sbase), "s"(lds_dst) : "memory");
}
template <int I> struct IC { static constexpr int v = I; };
template <int I, int N, class F> DI void static_for(F&& f) { if constexpr (I < N) { f(IC<I>{}); static_for<I + 1, N>(f); } }
DI unsigned now() { u64 t; asm volatile("s_memrealtime %0\n\ts_waitcnt lgkmcnt(0)" : "=s"(t) :: "memory"); return (unsigned)t; }
DI void reload(v4u& d, __amdgpu_buffer_rsrc_t r, int voff, int soff) { d = ldg(r, voff, soff); FRESH(); }
DI void reload1(unsigned& d, __amdgpu_buffer_rsrc_t r, int voff, int soff) { d = __builtin_amdgcn_raw_buffer_load_b32(r, voff, soff, SC1); FRESH(); }
template <int N> DI void waitv() { asm volatile("s_waitcnt vmcnt(%0)" :: "n"(N) : "memory"); }
struct ConvOrder { int t[36], u[36]; };
constexpr ConvOrder conv_order() {
  ConvOrder o = {};
  int done[8] = {}, last = -1;
  for (int k = 0; k < 36; ++k) {
    int best = -1;
    for (int t = 0; t < 8; ++t)
      if (t != last && done[t] <= t && (best < 0 || t - done[t] > best - done[best])) best = t;
    o.t[k] = best; o.u[k] = done[best]++; last = best;
  }
  return o;
}
constexpr ConvOrder conv_order_last() {
  ConvOrder o = {};
  int k = 0;
  for (int p = 0; p < 4; ++p)
    for (int u = 0; u <= 2 * p + 1; ++u) {
      o.t[k] = 2 * p + 1; o.u[k] = u; ++k;
      if (u <= 2 * p) { o.t[k] = 2 * p; o.u[k] = u; ++k; }
    }
  return o;
}
constexpr int epi_slot(int v) { return v < 32 ? 2 * EO + v : 2 * 50 - 1 + (v - 32); }
constexpr bool epi_pair(int j) { return (j >= EO && j < EO + 19) || (j >= 50 && j < 61); }
constexpr int hc_upto(int j) { return (j < HC0 ? 0 : j - HC0) + (j < HC1 ? 0 : j - HC1); }
constexpr int hl_upto(int i) { return i < HL0 ? 28 : i < HL0 + 28 ? 28 + (i - HL0) : 56; }
constexpr int c_ops(int j) {
  return (j >= SXJ && j < SXJ + 4 ? 2 : j < 34 || (j >= 35 && j < 51) ? 1 : 0) + hc_upto(j + 1) - hc_upto(j) +
         (j == EO + 6 || j == EO + 14 || j == 56 ? 1 : j == EO + 10 || j == EO + 18 || j == 60 ? 2 : 0);
}
constexpr int c_between(int a, int b) { int n = 0; for (int j = a + 1; j < b; ++j) n += c_ops(j); return n; }
DI unsigned tagbad(unsigned bad, v4u g, unsigned tagA, unsigned tagB) {
  return __builtin_amdgcn_bitop3_b32(__builtin_amdgcn_bitop3_b32(bad, g.y, tagA, 0xF6), g.w, tagB, 0xF6);
}
template <int R0, int R1, int R2, int R3> DI void put4(unsigned base, f32x2 a, f32x2 b, f32x2 c, f32x2 d) {
  asm volatile("s_mov_b32 m0, %4\n\ts_nop 0\n\tds_write_addtid_b32 %0 offset:%5\n\tds_write_addtid_b32 %1 offset:%6\n\t"
               "ds_write_addtid_b32 %2 offset:%7\n\tds_write_addtid_b32 %3 offset:%8"
               :: "v"(pack2(a.x, a.y)), "v"(pack2(b.x, b.y)), "v"(pack2(c.x, c.y)), "v"(pack2(d.x, d.y)), "s"(base),
                  "n"(R0 * UVS_ROW), "n"(R1 * UVS_ROW), "n"(R2 * UVS_ROW), "n"(R3 * UVS_ROW) : "memory");
}
DI unsigned sum4(unsigned m, v4u v) {
  asm("v_add3_u32 %0, %0, %1, %2\n\tv_add3_u32 %0, %0, %3, %4" : "+v"(m) : "v"(v.x), "v"(v.y), "v"(v.z), "v"(v.w));
  return m;
}
DI unsigned sum8(unsigned m, v4u a, v4u b) {
  asm("v_add3_u32 %0, %0, %1, %2\n\tv_add3_u32 %0, %0, %3, %4\n\tv_add3_u32 %0, %0, %5, %6\n\tv_add3_u32 %0, %0, %7, %8"
      : "+v"(m) : "v"(a.x), "v"(a.y), "v"(a.z), "v"(a.w), "v"(b.x), "v"(b.y), "v"(b.z), "v"(b.w));
  return m;
}
template <int I> DI unsigned red_step(unsigned v) {
  constexpr int ctrl[6] = {0xB1, 0x4E, 0x141, 0x140, 0x142, 0x143}, rows[6] = {0xF, 0xF, 0xF, 0xF, 0xA, 0xC};
  return v + (unsigned)__builtin_amdgcn_update_dpp(0, (int)v, ctrl[I], rows[I], 0xF, true);
}
DI unsigned red_all(unsigned v) { return red_step<5>(red_step<4>(red_step<3>(red_step<2>(red_step<1>(red_step<0>(v)))))); }
DI void swap_add32(float& a, float b) { const auto r = __builtin_amdgcn_permlane32_swap(f2u(a), f2u(b), false, false); a = u2f(r[0]) + u2f(r[1]); }
DI void swap_add16(float& a, float b) { const auto r = __builtin_amdgcn_permlane16_swap(f2u(a), f2u(b), false, false); a = u2f(r[0]) + u2f(r[1]); }

typedef const unsigned char* cptr;
typedef unsigned v16u __attribute__((ext_vector_type(16)));
DI cptr mkptr(unsigned lo, unsigned hi) { return (cptr)(((u64)hi << 32) | lo); }

__global__ void __launch_bounds__(256, 1)
s4_block_kernel(cptr, cptr, cptr, cptr, cptr, cptr, cptr, cptr, cptr, cptr, cptr, cptr, cptr, unsigned char*, unsigned char*, cptr) {
  STAMP(0);
  v16u ka0, ka1;
  asm volatile("s_load_dwordx16 %0, %2, 0x0\n\ts_load_dwordx16 %1, %2, 0x40" : "=&s"(ka0), "=&s"(ka1) : "s"(__builtin_amdgcn_kernarg_segment_ptr()));
  SB();
  LAS unsigned char* const L = (LAS unsigned char*)16u - 16;
  const int tid = threadIdx.x, lane = tid & 63, w = __builtin_amdgcn_readfirstlane(tid >> 6);
  const int b = blockIdx.x, xcd = b & 7, slot = b >> 3, id = xcd * 32 + slot;
  const int ks = (id >> 6) * 4 + w, cc = id & 63;
  const int rp = 4 * (xcd >> 1) + (slot >> 3), rg = xcd & 1, dt = slot & 7, si = 8 * rg + dt;
  const int ct = 7 * rg + min(dt, 6);
  const int rot = (0x0340 >> (4 * (2 * (w & 1) + rg))) & 7;

  const unsigned char* pcode;
  asm volatile("s_getpc_b64 %0" : "=s"(pcode));
  const unsigned warm = *(const GAS unsigned*)((const GAS unsigned char*)pcode + (unsigned)min((((slot & 7) * 4 + w) * 9 + min(lane, 8)) * 128, CODE_BYTES));
  const int g16 = lane >> 4, q4 = (lane & 15) >> 2, p4 = lane & 3, hf = lane >> 5, blk = (lane >> 4) & 1, l16 = lane * 16;
  const unsigned lrw = L_R + w * 16384;
  unsigned lsp = L_SP + w * 6144;
  unsigned wmo[3], co = (ks * 32 + (lane & 31)) * 2, xo = (id >> 2) * 32768 + (id & 3) * 256 + (tid & 7) * 4096 + (tid >> 3) * 8, ro = id * 8192 + w * 2048 + hf * 1024 + (lane & 31) * 16, po = id * 512 + tid * 2;
#pragma unroll
  for (int i = 0; i < 3; ++i) { const unsigned idx = lane + 64 * i; wmo[i] = (ks * 32 * 6144 + cc * 96 - i * 1024) + idx / 6 * (6144 - 96) + idx * 16; PIN(wmo[i]); }
  PIN(co); PIN(xo); PIN(ro); PIN(po);
  set_m0(lrw);
  SB();
  asm volatile("s_waitcnt lgkmcnt(0)" : "+s"(ka0), "+s"(ka1));
  const cptr xin = mkptr(ka0[0], ka0[1]), pAre = mkptr(ka0[2], ka0[3]), pAim = mkptr(ka0[4], ka0[5]), pBre = mkptr(ka0[6], ka0[7]);
  const cptr pBim = mkptr(ka0[8], ka0[9]), pCre = mkptr(ka0[10], ka0[11]), pCim = mkptr(ka0[12], ka0[13]), pldt = mkptr(ka0[14], ka0[15]);
  const cptr pD = mkptr(ka1[0], ka1[1]), w1 = mkptr(ka1[2], ka1[3]), w2 = mkptr(ka1[4], ka1[5]), pc = mkptr(ka1[6], ka1[7]);
  const cptr wmod = mkptr(ka1[8], ka1[9]), out = mkptr(ka1[10], ka1[11]), ws = mkptr(ka1[12], ka1[13]);
  const unsigned epoch_v = __hip_atomic_load((GAS unsigned*)ws, __ATOMIC_RELAXED, __HIP_MEMORY_SCOPE_AGENT);
  asm volatile("global_load_lds_dwordx4 %0, %3\n\tglobal_load_lds_dwordx4 %1, %3 offset:1024\n\tglobal_load_lds_dwordx4 %2, %3 offset:2048"
               :: "v"(wmo[0]), "v"(wmo[1]), "v"(wmo[2]), "s"(wmod) : "memory");
  const unsigned cval = *(const GAS u16*)((const GAS unsigned char*)pc + co);
  SB();
  v2u xq[4];
#pragma unroll
  for (int i = 0; i < 4; ++i) xq[i] = *(const GAS v2u*)((const GAS unsigned char*)xin + 1024 * i + xo);
  const v4u ra = *(const GAS v4u*)((const GAS unsigned char*)xin + ro), rb = *(const GAS v4u*)((const GAS unsigned char*)xin + 512 + ro);
  SB();
#define PARAM(p) (unsigned)*(const GAS u16*)((const GAS unsigned char*)(p) + po)
  const unsigned uAre = PARAM(pAre), uAim = PARAM(pAim), uBre = PARAM(pBre), uBim = PARAM(pBim), uCre = PARAM(pCre), uCim = PARAM(pCim);
#undef PARAM
  SB();
  const unsigned char* setp = ws + SET_BASE;
  const __amdgpu_buffer_rsrc_t rs = __builtin_amdgcn_make_buffer_rsrc((void*)setp, 0, SET_BYTES, 0x00020000);
  unsigned uldt, uD;
  asm volatile("s_load_dword %0, %2, %4\n\ts_load_dword %1, %3, %4" : "=&s"(uldt), "=&s"(uD) : "s"(pldt), "s"(pD), "s"(id * 4));
  SB();
  STAMPA(26);
  unsigned wto = id * 7168 + (w >> 1) * 3584 + min(lane, 27) * 128;
  PIN(wto);
  unsigned w1v[4], w2v[4];
#pragma unroll
  for (int i = 0; i < 4; ++i) {
    w1v[i] = (lane >> 3) * 3584 + (((lane & 7) ^ (blk << 2)) * 16) + i * (57344 - 1024);
    w2v[i] = (lane >> 3) * 1024 + (((lane & 7) ^ (blk << 2)) * 16) + i * (8192 - 1024);
    PIN(w1v[i]); PIN(w2v[i]);
  }
  int w1b = ct * 256 + (w >> 1) * 128 + (w & 1) * (8 * 3584), w2lo = (448 * w + 64 * rot) * 1024 + dt * 128;
  int w2c3 = w2lo - (rot >> 2) * (448 * 1024), w2hi = w2lo - ((rot + 1) >> 2) * (448 * 1024), so = 0;
  unsigned lrx = L_RX + w * 3072, lto = L_TOP + w * 1024;
  int zs = OFF_Z + (2 * id + (w >> 1)) * 64;
  int za = (((w & 1) + 2 * (g16 & 1)) * 8 + q4 * 2 + (g16 >> 1)) * 32768 + p4 * 16, zb = za + 32 * 32768;
  int tra = (hf * 8 + q4) * 64 + blk * 32 + p4 * 8;
  const int wsrc = (w & 2) + hf;
  int w1t = q4 * 128 + (((w & 1) ^ (q4 >> 1)) * 64) + blk * 32 + p4 * 8;
  LAS unsigned char *w1a = L + L_R + wsrc * 16384 + w1t, *w1s = L + L_SP + wsrc * 6144 + w1t;
  LAS unsigned char *w1x = L + L_RX + wsrc * 3072 + w1t, *w1y = L + L_TOP + wsrc * 1024 + w1t;
#define W1PULL(step, n) w1f[step] = trd2(((n) < 16 ? w1a : w1s) + ((n) & 15) * 1024, ((n) < 16 ? w1a : w1s) + ((n) & 15) * 1024 + 512)
  LAS unsigned char* ka = L + L_KC + ((w >> 1) * 4 + q4) * KC_STRIDE + 2 * (32 - 8 * g16 + 4 * p4) - 8 + 256 * (w & 1);
  LAS unsigned char* hb = L + L_H + (w >> 1) * H_STRIDE + (lane & 15) * 32 + g16 * 16 + 256 - 256 * (w & 1);
  LAS unsigned char* zp = L + sel(tid < 64, L_KC + ((lane >> 1) & 7) * KC_STRIDE + (lane & 1) * 16, L_H + hf * H_STRIDE + min(lane & 31, 29) * 16);
  LAS unsigned char* ub = L + L_UVS + (lane & 15) * UVS_ROW + g16 * 16;
  int gmo = OFF_MG + (g16 * 256 + id) * 256 + (lane & 15) * 16, gmo2 = OFF_MG + ((4 + blk) * 256 + id) * 256 + (lane & 15) * 16;
  int mgo = sel(lane < 48, (lane >> 1) * 256 + (lane & 1) * 8, OOB), sgo = sel(lane & 31, OOB, hf * 8);
  LAS unsigned char* xbw = L + L_XB + ((w & 1) * 4 + (w >> 1)) * 512 + lane * 8;
  int xcw = sel(lane & 7, OOB, tid), xcr = sel(lane & 7, OOB, (tid >> 3) * 1024), xgw = (tid >> 3) * 16384 + (tid & 7) * 16;
  PIN(xcw); PIN(xcr); PIN(xgw);
  LAS unsigned char* xbr = L + L_XB + w * 2048 + (lane & 15) * 32 + g16 * 8;
  LAS unsigned char *exa = L + L_XB + w * 2048 + l16, *exb = L + L_RX + w * 3072 + l16, *eya = L + L_XB + (w ^ 1) * 2048 + l16, *eyb = L + L_RX + (w ^ 1) * 3072 + l16;
  PIN(xbw); PIN(xbr); PIN(exa); PIN(exb); PIN(eya); PIN(eyb);
  int mto = (tid >> 1) * 128 + (tid & 1) * 16;
  LAS unsigned char* mtw = L + (tid & 1) * L_TG + (tid >> 1) * 4;
  PIN(mto); PIN(mtw);
  PIN(za); PIN(zb); PIN(tra); PIN(w1a); PIN(w1s); PIN(w1x); PIN(w1y); PIN(ka); PIN(hb); PIN(zp); PIN(ub); PIN(gmo); PIN(gmo2); PIN(mgo); PIN(sgo);
  asm volatile("" : "+s"(w1b), "+s"(w2lo), "+s"(w2c3), "+s"(w2hi), "+s"(lsp), "+s"(lrx), "+s"(lto), "+s"(zs));
#define W1P(n, k, lbase, li) do { if constexpr ((k) == 0) grp<(li), (n) * 57344>(so, lbase, w1b); xfer<(k), false>(w1v[k], rw1, so); } while (0)
#define W2P(n, k, lbase, li) do { if constexpr ((k) == 0) grp<(li), (n) * 8192>(so, lbase, (n) < 24 ? w2lo : (n) < 32 ? w2c3 : w2hi); xfer<(k), false>(w2v[k], rw2, so); } while (0)
  SB();
  LAS unsigned char* koff[12];
  {
    float fj = (float)(8 * tid - 3);
    PIN(fj);
    const unsigned lin = (unsigned)(8 * tid - 3 + 0x4300 - 128) * 0x10001u + 0x10000u;
#pragma unroll
    for (int i = 0; i < 12; i += 2) {
      const unsigned r = sel(tid < 32, lin + i * 0x10001u, pack2(fj + (float)i, fj + (float)(i + 1)));
      koff[i] = L + ((r & 0xFFFFu) * 4 + (L_KT + (128 - 0x4300) * 4));
      koff[i + 1] = L + ((r >> 16) * 4 + (L_KT + (128 - 0x4300) * 4));
      PIN(koff[i]);
      if (i < 10) PIN(koff[i + 1]);
    }
  }
  *(LAS v4u*)(L + L_KT - 16) = v4u{0u, 0u, 0u, 0u};
  LAS unsigned char* ktw[4];
  {
    const int row = w * 16 + (lane & 15), sg = max((row >> 3) - 1, 0), rr_ = row - 8 * sg;
    const int idx0 = 128 * sg + ((rr_ >> (3 - sg)) << (7 - sg)) + (rr_ & ((8 >> sg) - 1)) + 1, st = 8 >> sg;
#pragma unroll
    for (int r = 0; r < 4; ++r) {
      const int idx = sel(row < 40, idx0 + st * (4 * g16 + r), tid == 136 && r == 0 ? 0 : 641 + lane);
      ktw[r] = L + L_KT + idx * 4;
      PIN(ktw[r]);
    }
  }
  const __amdgpu_buffer_rsrc_t rw1 = __builtin_amdgcn_make_buffer_rsrc((void*)w1, 0, 1835008, 0x00020000);
  const __amdgpu_buffer_rsrc_t rw2 = __builtin_amdgcn_make_buffer_rsrc((void*)w2, 0, 1835008, 0x00020000);
  SB();
  STAMPA(27);
  asm volatile("s_waitcnt lgkmcnt(0)" : "+s"(uldt), "+s"(uD));
  const unsigned epoch = __builtin_amdgcn_readfirstlane(epoch_v);
  asm volatile("" :: "s"(epoch));
  STAMPA(28);
  const unsigned tagA = epoch * 0x9E3779B1u + 0x7F4A7C15u, tagB = ~tagA;
  {
    const float c = bf_lo(cval);
    const float s = c * __builtin_amdgcn_rcpf(1.0f + __builtin_amdgcn_exp2f(-LOG2E * c));
    *(LAS u16*)(L + lrw + 3072 + (lane & 31) * 2) = (u16)pack2(s, s);
    SB();
    asm volatile("" :: "v"(xq[3]));
    STAMPA(29);
    const v4u xc0 = {xq[0].x, xq[1].x, xq[2].x, xq[3].x}, xc1 = {xq[0].y, xq[1].y, xq[2].y, xq[3].y};
    stg(rs, xgw, OFF_XG + (id & 3) * 524288 + (id >> 2) * 128, xc0);
    stg(rs, xgw, OFF_XG + (id & 3) * 524288 + (id >> 2) * 128 + 8192, xc1);
    SB();
    STAMP(1);
    LAS unsigned char* a = L + lrw + (8 * g16 + q4) * 96 + p4 * 8;
    const v4u sc = lds128(L + lrw + 3072 + g16 * 16);
    const v4u m0 = trd2(a, a + 384), m1 = trd2(a + 32, a + 416), m2 = trd2(a + 64, a + 448);
    SB();
    f32x2 fa[8];
#pragma unroll
    for (int i = 0; i < 4; ++i) { fa[i] = f32x2{bf_lo(ra[i]), bf_hi(ra[i])}; fa[4 + i] = f32x2{bf_lo(rb[i]), bf_hi(rb[i])}; }
    const f32x2 s2 = ((fa[0] + fa[1]) + (fa[2] + fa[3])) + ((fa[4] + fa[5]) + (fa[6] + fa[7]));
    float mean = s2.x + s2.y;
    PIN(mean);
    SB();
    const f32x4 c0 = mfma16(sc, m0, f32x4{}), c1 = mfma16(sc, m1, f32x4{}), c2 = mfma16(sc, m2, f32x4{});
    SB();
    mean = swap16_add(row_sum(mean)) * (1.0f / 512.0f);
    const unsigned k0 = red_step<2>(red_step<1>(red_step<0>(sum4(0u, xc0)))), k1 = red_step<2>(red_step<1>(red_step<0>(sum4(0u, xc1))));
    __builtin_amdgcn_raw_buffer_store_b64(v2u{tagA - k0, tagA - k1}, rs, xcw, OFF_XC + id * 256, SC1);
    PIN(mean);
    SB();
    __builtin_amdgcn_raw_buffer_store_b64(v2u{sel(lane < 16, f2u(c0[0]), sel(lane < 32, f2u(c1[0]), f2u(c2[0]))), (lane & 1) ? tagB : tagA}, rs, mgo,
                                          OFF_MG + cc * 24 * 256 + ks * 16, SC1);
    SB();
    f32x2 q2 = {0.f, 0.f}, q3 = {0.f, 0.f};
#pragma unroll
    for (int i = 0; i < 8; i += 2) {
      const f32x2 d0 = fa[i] - f32x2{mean, mean}, d1 = fa[i + 1] - f32x2{mean, mean};
      q2 = __builtin_elementwise_fma(d0, d0, q2);
      q3 = __builtin_elementwise_fma(d1, d1, q3);
    }
    q2 += q3;
    const float var = rbf(rbf(swap16_add(row_sum(q2.x + q2.y)) * (1.0f / 512.0f)) + 1e-5f);
    __builtin_amdgcn_raw_buffer_store_b64(v2u{pack2(mean, __builtin_amdgcn_sqrtf(var)), hf ? tagB : tagA}, rs, sgo, OFF_SG + ((w & 1) * 512 + id * 2 + (w >> 1)) * 16, SC1);
    SB();
  }
  unsigned wt;
  SB();
  STAMP(2);
  unsigned dsk = tid == 136 ? uD : 0u;
  PIN(dsk);

  {
    const float dtv = rbf(__builtin_amdgcn_exp2f(LOG2E * (w >> 1 ? bf_hi(uldt) : bf_lo(uldt))));
    const float are = dtv * bf_lo(uAre) * LOG2E, aim = dtv * bf_lo(uAim) * INV2PI;
    const float br = dtv * bf_lo(uBre), bi = dtv * bf_lo(uBim), cr = bf_lo(uCre), ci = bf_lo(uCim);
    const f32x2 wv = {cr * br - ci * bi, cr * bi + ci * br};
    const unsigned row = L_UVS + (w >> 1) * UVS_CH + (w & 1) * 256;
    const f32x2 e1 = cexp(are, aim), e8 = cexp(8.f * are, -8.f * aim), e128 = cexp(128.f * are, 128.f * aim);
    const f32x2 e1p = {-e1.y, e1.x}, e8p = {-e8.y, e8.x}, e128p = {-e128.y, e128.x};
    const v4u pois = {POISON, POISON, POISON, POISON};
    f32x2 z[8], v, v2, y, y2;
#define M1(a) cmul(a, e1, e1p)
#define M8(a) cmul(a, e8, e8p)
#define MK(a) cmul(a, e128, e128p)
    z[0] = M1(wv);
    put4<40, 41, 42, 0>(row, wv, f32x2{1.f, 0.f}, e8, z[0]);
    asm volatile("global_load_dword %0, %1, %2" : "=v"(wt) : "v"(wto), "s"(w1 + (w & 1) * (w2 - w1)), "v"(warm) : "memory");
    v = M8(e8); z[1] = M1(z[0]); v2 = M8(v); z[2] = M1(z[1]);
    put4<43, 1, 44, 2>(row, v, z[1], v2, z[2]);
    W1P(0, 0, lrw, 0); W1P(1, 1, lrw, 0); W1P(2, 2, lrw, 0); W1P(3, 3, lrw, 0);
    v = M8(v2); z[3] = M1(z[2]); v2 = M8(v); z[4] = M1(z[3]);
    put4<45, 3, 46, 4>(row, v, z[3], v2, z[4]);
    v = M8(v2); z[5] = M1(z[4]); v2 = M8(v); z[6] = M1(z[5]);
    put4<47, 5, 48, 6>(row, v, z[5], v2, z[6]);
    z[7] = M1(z[6]); z[0] = MK(z[0]); z[1] = MK(z[1]); z[2] = MK(z[2]);
    put4<7, 8, 9, 10>(row, z[7], z[0], z[1], z[2]);
    STAMPC(26);
    stg(rs, (tid >> 3) * 32768 + (tid & 7) * 16, OFF_Z + id * 128, pois);
    STAMPC(27);
    z[3] = MK(z[3]); z[4] = MK(z[4]); z[5] = MK(z[5]); z[6] = MK(z[6]);
    put4<11, 12, 13, 14>(row, z[3], z[4], z[5], z[6]);
    z[7] = MK(z[7]); z[1] = MK(z[1]); z[3] = MK(z[3]); z[5] = MK(z[5]);
    put4<15, 16, 17, 18>(row, z[7], z[1], z[3], z[5]);
    z[7] = MK(z[7]); z[1] = MK(z[1]); z[3] = MK(z[3]); z[5] = MK(z[5]);
    put4<19, 20, 21, 22>(row, z[7], z[1], z[3], z[5]);
    stg(rs, (tid >> 3) * 32768 + (tid & 7) * 16, OFF_Z + id * 128 + 32 * 32768, pois);
    STAMPC(28);
    y = MK(z[7]); z[3] = MK(z[3]); y2 = MK(y); z[4] = MK(z[3]);
    put4<23, 24, 25, 26>(row, y, z[3], y2, z[4]);
    y = MK(y2); z[3] = MK(z[4]); y2 = MK(y); z[4] = MK(z[3]);
    put4<27, 28, 29, 30>(row, y, z[3], y2, z[4]);
    y = MK(y2); v = M8(v2); y2 = MK(y); v2 = M8(v);
    put4<31, 49, 32, 50>(row, y, v, y2, v2);
    y = MK(y2); v = M8(v2); y2 = MK(y); v2 = M8(v);
    put4<33, 51, 34, 52>(row, y, v, y2, v2);
    y = MK(y2); v = M8(v2); y2 = MK(y); v2 = M8(v);
    put4<35, 53, 36, 54>(row, y, v, y2, v2);
    y = MK(y2); v = M8(v2); y2 = MK(y); v2 = M8(v);
    put4<37, 55, 38, 56>(row, y, v, y2, v2);
    y = MK(y2);
    *(LAS unsigned*)(L + row + 39 * UVS_ROW + lane * 4) = pack2(y.x, y.y);
    STAMPC(29);
    W1P(4, 0, lrw, 4096); W1P(5, 1, lrw, 0); W1P(6, 2, lrw, 0); W1P(7, 3, lrw, 0);
#undef M1
#undef M8
#undef MK
  }
  BAR();
  STAMP(3);
  v4u gx0, gx1, gs0, gs1, gs2, gs3, gm, gn;
  unsigned gc0, gc1;
  const int xgo = OFF_XG + id * 8192, xco = OFF_XC + id * 4;
  v4u fv[2][8], fu[2][8];
  if (w < 3) {
    LAS unsigned char* ut = ub + w * 16 * UVS_ROW;
#pragma unroll
    for (int s = 0; s < 8; ++s)
#pragma unroll
      for (int c = 0; c < 2; ++c) { fv[c][s] = lds128(ub + c * UVS_CH + 41 * UVS_ROW + s * 64); fu[c][s] = lds128(ut + c * UVS_CH + s * 64); SB(); }
    f32x4 a0 = {}, a1 = {};
#pragma unroll
    for (int s = 0; s < 8; ++s) { a0 = mfma16(fv[0][s], fu[0][s], a0); SB(); a1 = mfma16(fv[1][s], fu[1][s], a1); SB(); }
    *(LAS unsigned*)ktw[0] = pack2(a0[0] + bf_lo(dsk), a1[0] + bf_hi(dsk));
#pragma unroll
    for (int r = 1; r < 4; ++r) *(LAS unsigned*)ktw[r] = pack2(a0[r], a1[r]);
  }
  SB();
#define LD1(i, so) "buffer_load_dwordx4 %" #i ", %10, %14, " so " offen sc1\n\t"
  asm volatile("buffer_load_dwordx4 %6, %11, %14, 0 offen sc1\n\tbuffer_load_dwordx4 %7, %12, %14, 0 offen sc1\n\t"
               LD1(0, "%15") LD1(1, "%16") "buffer_load_dword %8, %13, %14, %21 offen sc1\n\tbuffer_load_dword %9, %13, %14, %22 offen sc1\n\t"
               LD1(2, "%17") LD1(3, "%18") LD1(4, "%19") LD1(5, "%20")
               : "=&v"(gx0), "=&v"(gx1), "=&v"(gs0), "=&v"(gs1), "=&v"(gs2), "=&v"(gs3), "=&v"(gm), "=&v"(gn), "=&v"(gc0), "=&v"(gc1)
               : "v"(tid * 16), "v"(gmo), "v"(gmo2), "v"(xcr), "s"(rs), "s"(xgo), "s"(xgo + 4096),
                 "s"(OFF_SG), "s"(OFF_SG + 8192), "s"(OFF_SG + 4096), "s"(OFF_SG + 12288), "s"(xco), "s"(xco + 32768) : "memory");
#undef LD1
  SB();
  BAR();
  {
    unsigned k[11];
#pragma unroll
    for (int i = 0; i < 11; ++i) k[i] = *(LAS unsigned*)koff[i];
    SB();
    if (w < 2) *(LAS v4u*)zp = v4u{0u, 0u, 0u, 0u};
#pragma unroll
    for (int c = 0; c < 2; ++c) {
      unsigned e[5], o[5];
#pragma unroll
      for (int i = 0; i < 5; ++i) {
        o[i] = __builtin_amdgcn_perm(k[2 * i + 1], k[2 * i], c ? 0x07060302u : 0x05040100u);
        e[i] = __builtin_amdgcn_perm(k[2 * i + 2], k[2 * i + 1], c ? 0x07060302u : 0x05040100u);
      }
      LAS unsigned char* d = L + L_KC + c * 4 * KC_STRIDE + 32 + tid * 16;
      *(LAS v4u*)(d) = v4u{e[1], e[2], e[3], e[4]};
      *(LAS v4u*)(d + KC_STRIDE) = v4u{o[1], o[2], o[3], o[4]};
      *(LAS v4u*)(d + 2 * KC_STRIDE) = v4u{e[0], e[1], e[2], e[3]};
      *(LAS v4u*)(d + 3 * KC_STRIDE) = v4u{o[0], o[1], o[2], o[3]};
    }
  }
  SB();
  STAMP(4);
  {
    asm volatile("s_waitcnt vmcnt(0)" : "+v"(gx0), "+v"(gx1), "+v"(gs0), "+v"(gs1), "+v"(gs2), "+v"(gs3), "+v"(gm), "+v"(gn), "+v"(gc0), "+v"(gc1), "+v"(wt) :: "memory");
#define XBAD() (red_step<2>(red_step<1>(red_step<0>(sum8(gc0 + gc1, gx0, gx1)))) ^ (2u * tagA))
#define TB(a, c) tagbad(tagbad(0u, a, tagA, tagB), c, tagA, tagB)
    if (__builtin_expect(__any((XBAD() | TB(gm, gn) | TB(gs0, gs1) | TB(gs2, gs3)) != 0u), 0)) {
      _Pragma("nounroll") for (unsigned spin = 0; spin < SPIN_CAP; ++spin) {
        if (spin) PAUSE();
        COUNT(20);
        if (XBAD()) { reload(gx0, rs, tid * 16, xgo); reload(gx1, rs, tid * 16, xgo + 4096); reload1(gc0, rs, xcr, xco); reload1(gc1, rs, xcr, xco + 32768); }
        if (TB(gs0, gs1)) { reload(gs0, rs, tid * 16, OFF_SG); reload(gs1, rs, tid * 16, OFF_SG + 8192); }
        if (TB(gs2, gs3)) { reload(gs2, rs, tid * 16, OFF_SG + 4096); reload(gs3, rs, tid * 16, OFF_SG + 12288); }
        if (TB(gm, gn)) { reload(gm, rs, gmo, 0); reload(gn, rs, gmo2, 0); }
        if (!__any((XBAD() | TB(gm, gn) | TB(gs0, gs1) | TB(gs2, gs3)) != 0u)) break;
      }
    }
#undef XBAD
#undef TB
  }
  STAMP(5);
  W1P(8, 0, lrw, 8192); W1P(9, 1, lrw, 0);
  float zsc;
  v4u mtp;
  {
    float r0 = u2f(gm.x), r1 = u2f(gm.z), r2 = u2f(gn.x), r3 = u2f(gn.z);
#define RSTEP(c) r0 = dpp_add<c>(r0); PIN(r0); r1 = dpp_add<c>(r1); PIN(r1); r2 = dpp_add<c>(r2); PIN(r2); r3 = dpp_add<c>(r3); PIN(r3)
    RSTEP(0xB1); RSTEP(0x4E); RSTEP(0x141); RSTEP(0x140);
#undef RSTEP
    const int i0 = (int)pack2(r0, r1), i1 = (int)pack2(r2, r3);
    unsigned md[6];
#pragma unroll
    for (int k = 0; k < 6; ++k) md[k] = (unsigned)__builtin_amdgcn_readlane(k < 4 ? i0 : i1, 16 * (k & 3));
#define MOD(k) f32x2{bf_lo(md[k]), bf_hi(md[k])}
    const f32x2 one = {1.0f, 1.0f}, sr = one + MOD(4), cr = one + MOD(1);
    unsigned sp = pack2(sr.x, sr.y);
    const unsigned cp = pack2(cr.x, cr.y);
    sp |= ((sp & 0x7FFFu) ? 0u : 0x3080u) | ((sp & 0x7FFF0000u) ? 0u : 0x30800000u);
    const f32x2 S = {bf_lo(sp), bf_hi(sp)}, G = MOD(2) * S, A = f32x2{bf_lo(cp), bf_hi(cp)} * G, B = MOD(0) * G;
#undef MOD
    zsc = w >> 1 ? S.y : S.x;
    mtp = v4u{sel(lane, md[5], sp), tagA, md[3], tagB};
    auto rows4 = [&](const v4u& gx, const v4u& ga, const v4u& gb_, int i) __attribute__((always_inline)) {
      const f32x2 ma = {bf_lo(ga.x), bf_lo(ga.z)}, mb = {bf_lo(gb_.x), bf_lo(gb_.z)};
      const f32x2 ra_ = {__builtin_amdgcn_rcpf(bf_hi(ga.x)), __builtin_amdgcn_rcpf(bf_hi(ga.z))}, rb_ = {__builtin_amdgcn_rcpf(bf_hi(gb_.x)), __builtin_amdgcn_rcpf(bf_hi(gb_.z))};
      const f32x2 h0 = __builtin_elementwise_fma((f32x2{bf_lo(gx.x), bf_lo(gx.y)} - ma) * ra_, f32x2{A.x, A.x}, f32x2{B.x, B.x});
      const f32x2 h1 = __builtin_elementwise_fma((f32x2{bf_lo(gx.z), bf_lo(gx.w)} - mb) * rb_, f32x2{A.x, A.x}, f32x2{B.x, B.x});
      const f32x2 h2 = __builtin_elementwise_fma((f32x2{bf_hi(gx.x), bf_hi(gx.y)} - ma) * ra_, f32x2{A.y, A.y}, f32x2{B.y, B.y});
      const f32x2 h3 = __builtin_elementwise_fma((f32x2{bf_hi(gx.z), bf_hi(gx.w)} - mb) * rb_, f32x2{A.y, A.y}, f32x2{B.y, B.y});
      *(LAS v2u*)(L + L_H + 480 + tid * 8 + i * 2048) = v2u{pack2(h0.x, h0.y), pack2(h1.x, h1.y)};
      *(LAS v2u*)(L + L_H + H_STRIDE + 480 + tid * 8 + i * 2048) = v2u{pack2(h2.x, h2.y), pack2(h3.x, h3.y)};
      *(LAS v2u*)(xbw + i * 1024) = v2u{__builtin_amdgcn_perm(gx.y, gx.x, 0x05040100u), __builtin_amdgcn_perm(gx.w, gx.z, 0x05040100u)};
      *(LAS v2u*)(xbw + 4096 + i * 1024) = v2u{__builtin_amdgcn_perm(gx.y, gx.x, 0x07060302u), __builtin_amdgcn_perm(gx.w, gx.z, 0x07060302u)};
    };
    W1P(10, 2, lrw, 0); W1P(11, 3, lrw, 0);
    rows4(gx0, gs0, gs1, 0);
    W1P(12, 0, lrw, 12288); W1P(13, 1, lrw, 0); W1P(14, 2, lrw, 0); W1P(15, 3, lrw, 0);
    rows4(gx1, gs2, gs3, 1);
    W1P(16, 0, lsp, 0); W1P(17, 1, lsp, 0);
  }
  BAR();
  const unsigned tb = now();
  STAMP(6);

  v4u w1f[32], w2f[28][2], mt0, mt1;
  {
    const bool rh = w & 1;
    constexpr ConvOrder O = conv_order(), OL = conv_order_last();
    v2u A[4][8][2], xk[4];
    v4u B[4][8];
    f32x4 acc[8], keep[4];
#pragma unroll
    for (int u = 0; u < 8; ++u) {
      A[0][u][0] = trd(ka + 512 * u + 8);
      A[0][u][1] = trd(ka + 512 * u);
      B[0][7 - u] = lds128(hb + 64 * (8 * (7 - u) + 3));
    }
    SB();
    if (lane) mtp.z = tb;
    stg(rs, tid < 2 ? tid * 16 : OOB, OFF_MT + id * 128, mtp);
    SB();
    auto hand_over = [&](auto Pc) __attribute__((always_inline)) {
      constexpr int i = Pc.v;
      PIN(acc[2 * i]); PIN(acc[2 * i + 1]);
      const f32x4 ev = acc[2 * i], od = acc[2 * i + 1];
      const f32x4 snd = rh ? ev : od, own = rh ? od : ev;
      *(LAS f32x4*)(i < 2 ? exa + i * 1024 : exb + (i - 1) * 1024) = snd;
#pragma unroll
      for (int c = 0; c < 4; ++c) { float t = keep[i][c] + own[c]; PIN(t); keep[i][c] = t; }
    };
    static_for<0, 144>([&](auto Kc) __attribute__((always_inline)) {
      constexpr int K = Kc.v, r = K / 36, k = K % 36, t = r < 3 ? O.t[k] : OL.t[k], u = r < 3 ? O.u[k] : OL.u[k];
      if constexpr (k == 0 && r > 0) asm volatile("" :: "v"(B[r][7]));
      mfma16_v<r == 0 && u == 0>(acc[t], v4u{A[r][u][0].x, A[r][u][0].y, A[r][u][1].x, A[r][u][1].y}, B[r][t - u]);
      SB();
      if constexpr (K == 24) { WAITV(13); __builtin_amdgcn_s_barrier(); FRESH(); }
      if constexpr (K >= 24 && K < 36) W1PULL(K - 24, K - 24);
      if constexpr (K == 40) { asm volatile("" :: "a"(w1f[11])); __builtin_amdgcn_s_barrier(); FRESH(); }
      if constexpr (K == 60) { WAITV(9); __builtin_amdgcn_s_barrier(); FRESH(); }
      if constexpr (K >= 60 && K < 70) W1PULL(K - 48, K - 48);
      if constexpr (K == 76) { asm volatile("" :: "a"(w1f[21])); __builtin_amdgcn_s_barrier(); FRESH(); }
      if constexpr (K == 142) WAITV(16);
      if constexpr (r == 2 && k < 4) xk[k] = *(LAS v2u*)(xbr + k * 512);
      constexpr int n = k - (r < 2 ? 0 : 4);
      if constexpr (r < 3 && n >= 0 && n < 24) {
        if constexpr (n % 3 == 2) B[r + 1][n / 3] = lds128(hb + 64 * (8 * (n / 3) - r + 2));
        else A[r + 1][n / 3][n % 3] = trd(ka + 512 * (n / 3) + 64 * (r + 1) + 8 - 8 * (n % 3));
      }
      if constexpr (K >= 100 && K < 116) {
        constexpr int e = K - 100;
        const unsigned xw = (e & 2) ? xk[e >> 2].y : xk[e >> 2].x;
        float t2_ = zsc * ((e & 1) ? bf_hi(xw) : bf_lo(xw));
        PIN(t2_);
        keep[e >> 2][e & 3] = t2_;
      }
      if constexpr (K == 108 + 8) hand_over(IC<0>{});
      if constexpr (K == 108 + 17) hand_over(IC<1>{});
      if constexpr (K == 108 + 30) hand_over(IC<2>{});
      if constexpr (K < 14 && K % 4 == 1) W1P(18 + K / 4, (2 + K / 4) & 3, lsp, 4096);
      if constexpr (K == 17) W1P(22, 0, lrx, 0);
      if constexpr (K == 21) W1P(23, 0, lto, 0);
      if constexpr (K >= 41 && K < 65 && K % 3 == 2) W1P(24 + (K - 41) / 3, ((K - 41) / 3) & 3, lrw, (K - 41) / 3 * 1024);
      if constexpr (K >= 66 && K < 98 && K % 4 == 2) W2P((K - 66) / 4, ((K - 66) / 4) & 3, lrw, 8192 + (K - 66) / 4 * 1024);
      if constexpr (K >= 98 && K < 122 && K % 4 == 2) W2P((K - 66) / 4, ((K - 98) / 4) & 3, lsp, (K - 98) / 4 * 1024);
      if constexpr (K == MTK)
        asm volatile("buffer_load_dwordx4 %0, %2, %3, 0 offen sc1\n\tbuffer_load_dwordx4 %1, %2, %3, %4 offen sc1"
                     : "=&v"(mt0), "=&v"(mt1) : "v"(mto), "s"(rs), "s"(16384) : "memory");
      SB();
    });
    STAMP(7);
    asm volatile("s_nop 7" : "+v"(acc[6]), "+v"(acc[7]));
    hand_over(IC<3>{});
    BAR();
#pragma unroll
    for (int i = 0; i < 4; ++i) keep[i] += *(LAS f32x4*)(i < 2 ? eya + i * 1024 : eyb + (i - 1) * 1024);
#pragma unroll
    for (int i = 0; i < 2; ++i) {
      const auto s0 = __builtin_amdgcn_permlane16_swap(pack2(keep[2 * i][0], keep[2 * i][1]), pack2(keep[2 * i + 1][0], keep[2 * i + 1][1]), false, false);
      const auto s1 = __builtin_amdgcn_permlane16_swap(pack2(keep[2 * i][2], keep[2 * i][3]), pack2(keep[2 * i + 1][2], keep[2 * i + 1][3]), false, false);
      stg(rs, i ? zb : za, zs, v4u{guard(s0[0]), guard(s1[0]), guard(s0[1]), guard(s1[1])});
    }
    SB();
  }
  STAMP(8);

  static_assert(OFF_MT == 0, "table offset");
  const unsigned te = now();
  const int w2t = (4 * hf + q4) * 128 + blk * 32 + (((p4 & 1) << 1) | (p4 >> 1)) * 8;
  LAS unsigned char* w2a = L + w2t + (q4 >> 1) * 64;
  LAS unsigned char* w2b = L + w2t + ((q4 >> 1) ^ 1) * 64;
  LAS unsigned char *w2sa = w2a + lsp, *w2sb = w2b + lsp;
  PIN(w2sa); PIN(w2sb);
#define PULL(step, pa, pb, off) do { w2f[step][0] = trd2((pa) + (off), (pa) + (off) + 1024); w2f[step][1] = trd2((pb) + (off), (pb) + (off) + 1024); } while (0)
  const int par = (0x1C >> (ct % 7)) & 1;
  const unsigned char* xg = setp + OFF_Z + rp * 131072;
  unsigned xl = L_XR + w * 8192;
  int xs2 = OFF_Z + rp * 131072 + w * 8192 + (2 ^ par) * 32768, xs3 = xs2 + 32768 - 65536 * par;
  asm volatile("" : "+s"(xl), "+s"(xs2), "+s"(xs3));
  int slo = OFF_Z + (rp * 4 + w) * 32768 + si * 2048;
  v4u hq[56], rc[4], res[2], sx[8];
  u64 nanv = 0;
  const int c0 = 7 * w + rot, c3 = c0 - 7 * (rot >> 2), c4 = c0 - 7 * ((rot + 1) >> 2);
  const int hb0 = OFF_HID + (rp * 4 + rg) * 114688, kb0 = OFF_CK + (rp * 4 + rg) * 4096 + w * 896, ckv = lane < 56 ? l16 : OOB;
#define CHUNK(n) (((n) % 28 / 4 < 3 ? c0 : (n) % 28 / 4 == 3 ? c3 : c4) + (n) % 28 / 4)
#define HFRAG(n) ldg(rs, l16 + ((n) & 3) * 1024, hb0 + CHUNK(n) * 4096 + ((n) / 28) * (2 * 114688))
#define HLOAD(n) hq[n] = HFRAG(n)
  const int hoff = OFF_HID + (rp * 4 * 112 + ct * 8 + 2 * w) * 1024;
  int hv0 = dt == 7 ? OOB : l16, hv1 = dt == 7 ? OOB : l16 + 1024;
  const int cko = OFF_CK + rp * 16384 + (ct * 4 + w) * 64;
  int ckw = (p4 || dt == 7) ? OOB : lane;
  PIN(hv0); PIN(hv1); PIN(ckw);
#define CKPUT(sum, u) __builtin_amdgcn_raw_buffer_store_b32(ckey - red_step<1>(red_step<0>(sum)), rs, ckw, cko + ((u) ^ par) * 4096, SC1)
  const int sxo = OFF_SX + (rp * 4 + w) * 8192;
  LAS unsigned char* xr[2];
#pragma unroll
  for (int i = 0; i < 2; ++i) { xr[i] = L + L_XR + tra + i * 65536; PIN(xr[i]); }
  LAS unsigned char *rr = L + L_XR + tra + (4 * dt + w) * 1024, *rr2 = rr + 65536;
  LAS unsigned char* str = L + L_STAT + (lane & 31) * 8;
  LAS unsigned char *pa = w2a + lrw + 8192, *pb = w2b + lrw + 8192;
  w2a += L_XR + w * 8192; w2b += L_XR + w * 8192;
  LAS unsigned char* stw = str + (w ^ par) * 256;
  PIN(str); PIN(stw); PIN(rr); PIN(rr2); PIN(w2a); PIN(w2b);
  LAS unsigned char* qr = L + (p4 ? L_TB : L_TS) + hf * 16;
  LAS unsigned char* uw = L + L_UVT + hf * 512 + (32 * w + (lane & 31)) * 4;
  LAS unsigned char* uvp = L + L_UVT + (32 * w + 4 * hf) * 4;
  LAS unsigned char* ivp = L + L_TS + (si * 32 + (lane >> 2)) * 2;
  int sxw = sel(lane & 8, OOB, (p4 * 8 + hf * 4 + blk * 2 + ((lane >> 2) & 1)) * 16);
  PIN(pa); PIN(pb); PIN(qr); PIN(uw); PIN(uvp); PIN(ivp); PIN(sxw);
  asm volatile("" : "+s"(slo));
  SB();
  asm volatile("s_waitcnt vmcnt(2)" : "+v"(mt0), "+v"(mt1) :: "memory");
  if (__builtin_expect(__any(tagbad(tagbad(0u, mt0, tagA, tagB), mt1, tagA, tagB) != 0u), 0)) {
    _Pragma("nounroll") for (unsigned spin = 0; spin < SPIN_CAP; ++spin) {
      FRESH();
      COUNT(21);
      if (tagbad(0u, mt0, tagA, tagB)) mt0 = ldg(rs, mto, OFF_MT);
      if (tagbad(0u, mt1, tagA, tagB)) mt1 = ldg(rs, mto, OFF_MT + 16384);
      if (!__any(tagbad(tagbad(0u, mt0, tagA, tagB), mt1, tagA, tagB) != 0u)) break;
      PAUSE();
    }
  }
  *(LAS unsigned*)(mtw) = mt0.x;
  *(LAS unsigned*)(mtw + 512) = mt1.x;
  *(LAS unsigned*)(mtw + 1024) = mt0.z;
  *(LAS unsigned*)(mtw + 1536) = mt1.z;
  {
    int late = (lane & 1) ? max((int)(mt0.z - tb), (int)(mt1.z - tb)) : 0;
    late = max(late, __builtin_amdgcn_update_dpp(late, late, 0xB1, 0xF, 0xF, false));
    late = max(late, __builtin_amdgcn_update_dpp(late, late, 0x4E, 0xF, 0xF, false));
    late = max(late, __builtin_amdgcn_update_dpp(late, late, 0x141, 0xF, 0xF, false));
    late = max(late, __builtin_amdgcn_update_dpp(late, late, 0x140, 0xF, 0xF, false));
    late = max(late, __builtin_amdgcn_update_dpp(late, late, 0x142, 0xA, 0xF, false));
    late = max(late, __builtin_amdgcn_update_dpp(late, late, 0x143, 0xC, 0xF, false));
    if (lane == 63) *(LAS int*)(L + L_ETA + w * 4) = late;
  }
  w1f[22] = trd2(w1x, w1x + 512);
  w1f[23] = trd2(w1y, w1y + 512);
  BAR();
  STAMP(9);
  f32x4 uu[4], vv[4];
  v4u sl0, sl1;
  unsigned ckey = lane ? 0u : tagA;
  {
    const v4u lt = lds128(L + L_ETA);
    const unsigned j0 = *(LAS u16*)ivp, j1 = *(LAS u16*)(ivp + 32);
    v4u qa[32];
#pragma unroll
    for (int i = 0; i < 4; ++i) qa[i] = lds128(qr + i * 32);
    SB();
    bool sent = false;
    auto send = [&]() __attribute__((always_inline)) {
      STAMPB(26);
      asm volatile("buffer_load_dwordx4 %0, %2, %3, %4 offen sc1\n\tbuffer_load_dwordx4 %1, %2, %3, %4 offen offset:1024 sc1"
                   :: "v"(sl0), "v"(sl1), "v"(l16), "s"(rs), "s"(slo) : "memory");
      static_for<0, 8>([&](auto Nc) __attribute__((always_inline)) {
        if constexpr ((Nc.v & 3) == 0) grp<Nc.v * 1024, Nc.v * 1024 - 65536>(so, xl, xs2);
        xfer<Nc.v & 3, true>(l16, rs, so);
      });
    };
    asm volatile("" : "=v"(sl0), "=v"(sl1));
    const int wait = min(__builtin_amdgcn_readfirstlane(max(max((int)lt.x, (int)lt.y), max((int)lt.z, (int)lt.w))), 300) + ZLAG;
    f32x4 c0_, c1_;
    static_for<0, 32>([&](auto Sc) __attribute__((always_inline)) {
      constexpr int s = Sc.v;
      if constexpr (s % 8 == 0) {
        if (!sent && (int)(now() - te) >= wait) { sent = true; send(); }
        SB();
      }
      mfma4_va<s == 0>(c0_, c1_, qa[s], w1f[s]);
      SB();
      if constexpr (s < 28) qa[s + 4] = lds128(qr + (s + 4) * 32);
      if constexpr (s < 8) W1PULL(24 + s, s);
      if constexpr (s >= 8 && s < 16) w2f[(s - 8) >> 1][s & 1] = trd2(((s & 1) ? pb : pa) + ((s - 8) >> 1) * 2048, ((s & 1) ? pb : pa) + ((s - 8) >> 1) * 2048 + 1024);
      if constexpr (s >= 16 && s < 22) w2f[4 + ((s - 16) >> 1)][s & 1] = trd2(((s & 1) ? w2sb : w2sa) + ((s - 16) >> 1) * 2048, ((s & 1) ? w2sb : w2sa) + ((s - 16) >> 1) * 2048 + 1024);
      SB();
    });
    float i0 = __builtin_amdgcn_rcpf(bf_lo(j0)), i1 = __builtin_amdgcn_rcpf(bf_lo(j1));
    PIN(i0); PIN(i1); PIN(ckey);
    asm volatile("s_nop 1" : "+v"(c0_), "+v"(c1_));
    float un_ = c0_[0] + c1_[0];
    swap_add32(un_, -LOG2E * (c0_[1] + c1_[1]));
    *(LAS float*)uw = un_;
#pragma unroll
    for (int i = 0; i < 4; ++i) { uu[i] = *(LAS f32x4*)(uvp + i * 32); vv[i] = *(LAS f32x4*)(uvp + 512 + i * 32); }
    SB();
    if (!sent) {
      while ((int)(now() - te) < wait) { }
      send();
    }
    static_for<0, 8>([&](auto Nc) __attribute__((always_inline)) {
      if constexpr ((Nc.v & 3) == 0) grp<32768 + Nc.v * 1024, Nc.v * 1024 - 65536>(so, xl, xs3);
      xfer<Nc.v & 3, true>(l16, rs, so);
    });
    STAMPB(27);
    asm volatile("s_waitcnt vmcnt(16)" : "+v"(sl0), "+v"(sl1) :: "memory");
    STAMPB(28);
    if (__builtin_expect(__any(max4(max4(0u, sl0), sl1) == POISON), 0)) {
      _Pragma("nounroll") for (unsigned spin = 0; spin < SPIN_CAP; ++spin) {
        FRESH();
        if (spin) PAUSE();
        COUNT(22);
        sl0 = ldg(rs, l16, slo);
        sl1 = ldg(rs, l16 + 1024, slo);
        if (!__any(max4(max4(0u, sl0), sl1) == POISON)) break;
      }
    }
    float sm[8], sq[8];
#pragma unroll
    for (int i = 0; i < 4; ++i) {
      const f32x2 a = f32x2{bf_lo(sl0[i]), bf_hi(sl0[i])} * f32x2{i0, i0}, c = f32x2{bf_lo(sl1[i]), bf_hi(sl1[i])} * f32x2{i1, i1};
      const f32x2 s2 = a + c, q2 = __builtin_elementwise_fma(a, a, c * c);
      sm[2 * i] = s2.x; sm[2 * i + 1] = s2.y; sq[2 * i] = q2.x; sq[2 * i + 1] = q2.y;
    }
#pragma unroll
    for (int i = 0; i < 4; ++i) { swap_add32(sm[i], sm[i + 4]); swap_add32(sq[i], sq[i + 4]); }
#pragma unroll
    for (int i = 0; i < 2; ++i) { swap_add16(sm[i], sm[i + 2]); swap_add16(sq[i], sq[i + 2]); }
#pragma unroll
    for (int i = 0; i < 2; ++i) { sm[i] = dpp_add<0x124>(dpp_add<0x128>(sm[i])); sq[i] = dpp_add<0x124>(dpp_add<0x128>(sq[i])); }
    const bool od = lane & 4;
    stg(rs, sxw, OFF_SX + ((rp * 4 + w) * 16 + si) * 512,
        v4u{f2u(od ? sm[1] : sm[0]), tagA, f2u(od ? sq[1] : sq[0]), tagB});
    SB();
  }
  STAMPB(29);
  W2P(14, 0, lsp, 0); W2P(15, 1, lsp, 0); W2P(16, 2, lsp, 0); W2P(17, 3, lsp, 0); W2P(18, 0, lsp, 4096); W2P(19, 1, lsp, 0);
  WAITV(15);
  STAMP(10);
  BAR();
  STAMP(11);

#define XA(t) (xr[(t) >> 6] + ((t) & 63) * 1024)
#define XF(t) trd2(XA(t), XA(t) + 256)
#define XEN
#define REDO(p) \
  if (XEN __builtin_expect(nanv != 0, 0)) { \
    unsigned m0s; \
    asm volatile("s_mov_b32 %0, m0" : "=s"(m0s)); \
    for (unsigned spin = 0; spin < SPIN_CAP / 16; ++spin) { \
      COUNT(22); \
      _Pragma("nounroll") for (int i = 0; i < 8; ++i) dma_x4(xg + ((p) ^ par) * 32768 + i * 4096, l16, L_XR + (p) * 32768 + i * 4096); \
      WAITV(0); \
      _Pragma("unroll") for (int s = 0; s < 32; ++s) { \
        const v4u t = XF(32 * (p) + s); \
        if (s == 0) mfma_av<true>(acc[(p) % 3], w1f[s], t); else mfma_av<false>(acc[(p) % 3], w1f[s], t); \
        SB(); \
      } \
      mfma_done(acc[(p) % 3]); \
      if (!__any(!(acc[(p) % 3][0] == acc[(p) % 3][0]))) break; \
    } \
    set_m0(m0s); \
  }
  f32x16 acc[3];
  f32x2 ec[4];
  float ep[48], ee[48], ey[48];
  unsigned cs = 0, sbad = 0;
  float ssm = 0.f, ssq = 0.f;
  v4u x[128], eo;
#pragma unroll
  for (int t = 0; t < 4; ++t) x[t] = XF(t);
  SB();
  auto eslot = [&](auto Sc) __attribute__((always_inline)) {
    static_for<0, 48>([&](auto Vc) __attribute__((always_inline)) {
      constexpr int V = Vc.v, k = V & 15, st = Sc.v - epi_slot(V);
      if constexpr (st == 6 && (k & 1)) {
        unsigned o = pack2(ey[V - 1], ey[V]);
        PIN(o);
        eo[(k >> 1) & 3] = o;
        if constexpr (k == 7) { stg(rs, hv0, hoff + ((V >> 4) ^ par) * 114688, eo); cs = sum4(0u, eo); PIN(cs); }
        if constexpr (k == 15) { stg(rs, hv1, hoff + ((V >> 4) ^ par) * 114688, eo); CKPUT(sum4(cs, eo), V >> 4); }
      }
      if constexpr (st == 5) { ey[V] = ep[V] * ee[V]; PIN(ey[V]); }
      if constexpr (st == 4) { ee[V] = __builtin_amdgcn_rcpf(ee[V]); PIN(ee[V]); }
      if constexpr (st == 3) { ee[V] = 1.0f + ee[V]; PIN(ee[V]); }
      if constexpr (st == 2) { ee[V] = __builtin_amdgcn_exp2f(ep[V]); PIN(ee[V]); }
      if constexpr (st == 1) { ep[V] = __builtin_fmaf(ec[V >> 4].x, acc[(V >> 4) % 3][k], ep[V]); PIN(ep[V]); }
      if constexpr (st == 0) { ep[V] = __builtin_fmaf(ec[V >> 4].y, uu[k >> 2][k & 3], vv[k >> 2][k & 3]); PIN(ep[V]); }
    });
    SB();
  };
  static_for<0, 64>([&](auto Jc) __attribute__((always_inline)) {
    constexpr int J = Jc.v, p = J >> 4, j = J & 15;
    asm volatile("" :: "v"(x[2 * J + 1]));
    mfma_av<j == 0>(acc[p % 3], w1f[2 * j], x[2 * J]);
    if constexpr (J < SXJ || (J >= SXJ + 4 && J < 20)) {
      constexpr int n = J < SXJ ? J : J - 4;
      if constexpr ((n & 3) == 0) grp<65536 + (n >> 3) * 32768 + (n & 4) * 1024, (n & 4) * 1024>(so, xl, n < 8 ? xs2 : xs3);
      xfer<n & 3, true>(l16, rs, so);
    }
    if constexpr (J >= SXJ && J < SXJ + 4)
      asm volatile("buffer_load_dwordx4 %0, %2, %3, %4 offen offset:%5 sc1\n\tbuffer_load_dwordx4 %1, %2, %3, %4 offen offset:%6 sc1"
                   : "=&v"(sx[2 * (J - SXJ)]), "=&v"(sx[2 * (J - SXJ) + 1])
                   : "v"(l16), "s"(rs), "s"(sxo + ((J - SXJ) & 2) * 2048), "n"(((J - SXJ) & 1) * 2048), "n"(((J - SXJ) & 1) * 2048 + 1024) : "memory");
    if constexpr (J == 43) asm volatile("" :: "a"(w2f[13][1]));
    if constexpr (J >= 20 && J < 28) W2P(J, J & 3, xl, (J - 20) * 1024);
    if constexpr (J >= 28 && J < 34) W2P(J, J & 3, lsp, (J - 28) * 1024);
    if constexpr (J >= 35 && J < 43) W2P(J - 1, (J - 35) & 3, xl, 32768 + (J - 35) * 1024);
    if constexpr (J >= 43 && J < 51) W2P(J - 1, (J - 43) & 3, xl, (J - 43) * 1024);
    static_for<hc_upto(J), hc_upto(J + 1)>([&](auto Nc) __attribute__((always_inline)) { HLOAD(Nc.v); });
    SB();
    constexpr bool sym = epi_pair(J) || J < 19 || J > 60;
    if constexpr (J < 62) { x[2 * J + 4] = XF(2 * J + 4); if constexpr (!sym) x[2 * J + 5] = XF(2 * J + 5); }
    if constexpr (J >= 38 && J < 42) w2f[10 + J - 38][0] = trd2(w2a + (J - 38) * 2048, w2a + (J - 38) * 2048 + 1024);
    SB();
    if constexpr (epi_pair(J)) eslot(IC<2 * J>{});
    mfma_av<false>(acc[p % 3], w1f[2 * j + 1], x[2 * J + 1]);
    if constexpr (sym && J < 62) x[2 * J + 5] = XF(2 * J + 5);
    if constexpr (J >= 38 && J < 42) w2f[10 + J - 38][1] = trd2(w2b + (J - 38) * 2048, w2b + (J - 38) * 2048 + 1024);
    if constexpr (J >= 19 && J < 25) w2f[7 + ((J - 19) >> 1)][(J - 19) & 1] = trd2((((J - 19) & 1) ? w2sb : w2sa) + ((J - 19) >> 1) * 2048, (((J - 19) & 1) ? w2sb : w2sa) + ((J - 19) >> 1) * 2048 + 1024);
    SB();
    if constexpr (J == 10) { waitv<c_between(-1, 10)>(); __builtin_amdgcn_s_barrier(); FRESH(); }
    if constexpr (J == 18) waitv<c_between(7, 18)>();
    if constexpr (J == 34) waitv<c_between(19, 34)>();
    if constexpr (J == 37) waitv<c_between(27, 37)>();
    if constexpr (J == EO - 4 || J == EO - 3) {
      constexpr int i = 4 * (J - EO + 4);
      asm volatile("s_waitcnt vmcnt(%4)" : "+v"(sx[i]), "+v"(sx[i + 1]), "+v"(sx[i + 2]), "+v"(sx[i + 3]) : "n"(c_between(SXJ + 1 + i / 2, J)) : "memory");
      unsigned b0 = tagbad(0u, sx[i], tagA, tagB), b1 = tagbad(0u, sx[i + 1], tagA, tagB);
      float m0 = u2f(sx[i].x) + u2f(sx[i + 1].x), m1 = u2f(sx[i + 2].x) + u2f(sx[i + 3].x);
      PIN(m0); PIN(m1);
      float q0 = u2f(sx[i].z) + u2f(sx[i + 1].z), q1 = u2f(sx[i + 2].z) + u2f(sx[i + 3].z);
      PIN(q0); PIN(q1);
      sbad |= tagbad(b0, sx[i + 2], tagA, tagB) | tagbad(b1, sx[i + 3], tagA, tagB);
      ssm += m0 + m1; PIN(ssm);
      ssq += q0 + q1; PIN(ssq);
      PIN(sbad);
      SB();
    }
    if constexpr (J == EO - 2) {
      if (__builtin_expect(__any(sbad != 0u), 0)) {
        _Pragma("nounroll") for (unsigned spin = 0; spin < SPIN_CAP; ++spin) {
          FRESH();
          if (spin) PAUSE();
          COUNT(23);
          sbad = 0; ssm = 0.f; ssq = 0.f;
#pragma unroll
          for (int i = 0; i < 8; ++i) sx[i] = ldg(rs, l16 + (i & 3) * 1024, sxo + (i >> 2) * 4096);
#pragma unroll
          for (int i = 0; i < 8; i += 4) {
            sbad = tagbad(tagbad(tagbad(tagbad(sbad, sx[i], tagA, tagB), sx[i + 1], tagA, tagB), sx[i + 2], tagA, tagB), sx[i + 3], tagA, tagB);
            ssm += (u2f(sx[i].x) + u2f(sx[i + 1].x)) + (u2f(sx[i + 2].x) + u2f(sx[i + 3].x));
            ssq += (u2f(sx[i].z) + u2f(sx[i + 1].z)) + (u2f(sx[i + 2].z) + u2f(sx[i + 3].z));
          }
          if (!__any(sbad != 0u)) break;
        }
      }
      const float mean = swap32_add(ssm) * (1.0f / 512.0f);
      const float ms = __builtin_fmaf(swap32_add(ssq), 1.0f / 512.0f, 1e-5f);
      const float a = -LOG2E * __builtin_amdgcn_rsqf(fmaxf(__builtin_fmaf(-mean, mean, ms), 1e-5f));
      *(LAS f32x2*)(stw) = f32x2{a, -a * mean};
      SB();
    }
    if constexpr (J == EO - 1) {
      asm volatile("s_waitcnt lgkmcnt(4)" ::: "memory");
      __builtin_amdgcn_s_barrier();
      FRESH();
#pragma unroll
      for (int i = 0; i < 4; ++i) ec[i] = *(LAS f32x2*)(str + i * 256);
      SB();
    }
    if constexpr (!epi_pair(J)) eslot(IC<2 * J>{});
    eslot(IC<2 * J + 1>{});
    if constexpr (J == 16) asm volatile("s_nop 4");
    if constexpr (j == 0 && p >= 1) { PIN(acc[(p - 1) % 3]); nanv = __builtin_amdgcn_ballot_w64(!(acc[(p - 1) % 3][0] == acc[(p - 1) % 3][0])); asm volatile("" : "+s"(nanv)); }
    if constexpr (j == 1 && p >= 1) {
      if constexpr (p == 1) REDO(0)
      if constexpr (p == 2) REDO(1)
      if constexpr (p == 3) REDO(2)
      rc[p - 1] = trd2((p == 3 ? rr2 : rr) + ((p - 1) & 1) * 32768, (p == 3 ? rr2 : rr) + ((p - 1) & 1) * 32768 + 256);
    }
    if constexpr (j == 2 && p >= 1) {
      asm volatile("s_waitcnt lgkmcnt(4)" ::: "memory");
      __builtin_amdgcn_s_barrier();
      FRESH();
    }
    SB();
    if constexpr (J == 15) STAMP(16);
    if constexpr (J == 31) STAMP(17);
    if constexpr (J == 47) STAMP(18);
  });
  STAMP(12);
  static_assert(EO <= 33 && SXJ >= 8 && SXJ <= 16 && hc_upto(64) <= 24 && epi_slot(31) < epi_slot(32) && epi_slot(16) + 1 >= 2 * 34 && epi_slot(32) + 1 >= 2 * 50 && epi_slot(47) + 6 < 128, "schedule");
  static_for<hc_upto(64), hc_upto(64) + HT0>([&](auto Ic) __attribute__((always_inline)) { HLOAD(Ic.v); });
  SB();
  asm volatile("s_nop 9" : "+v"(acc[0]));
  nanv = __builtin_amdgcn_ballot_w64(!(acc[0][0] == acc[0][0]));
  REDO(3)
  rc[3] = trd2(rr2 + 32768, rr2 + 32768 + 256);
#pragma unroll
  for (int h = 0; h < 2; ++h) {
    v4u o;
#pragma unroll
    for (int i = 0; i < 4; ++i) {
      const int k = 8 * h + 2 * i;
      const f32x2 t = __builtin_elementwise_fma(f32x2{ec[3].y, ec[3].y}, f32x2{uu[k >> 2][k & 3], uu[k >> 2][(k & 3) + 1]}, f32x2{vv[k >> 2][k & 3], vv[k >> 2][(k & 3) + 1]});
      const f32x2 pp = __builtin_elementwise_fma(f32x2{ec[3].x, ec[3].x}, f32x2{acc[0][k], acc[0][k + 1]}, t);
      const f32x2 d = f32x2{__builtin_amdgcn_exp2f(pp.x), __builtin_amdgcn_exp2f(pp.y)} + f32x2{1.0f, 1.0f};
      const f32x2 y = pp * f32x2{__builtin_amdgcn_rcpf(d.x), __builtin_amdgcn_rcpf(d.y)};
      o[i] = pack2(y.x, y.y);
    }
    PIN(o);
    if (h == 0) static_for<hc_upto(64) + HT0, hc_upto(64) + HT0 + HT1>([&](auto Ic) __attribute__((always_inline)) { HLOAD(Ic.v); });
    stg(rs, h ? hv1 : hv0, hoff + (3 ^ par) * 114688, o);
    if (h) CKPUT(sum4(cs, o), 3); else cs = sum4(0u, o);
    SB();
  }
  static_for<hc_upto(64) + HT0 + HT1, 28>([&](auto Ic) __attribute__((always_inline)) { HLOAD(Ic.v); });
  SB();
  STAMP(13);

  f32x16 d[2][2];
  LAS unsigned char* redw = L + L_XR + w * 8192 + l16;
  LAS unsigned char* redr = L + L_XR + w * 2048 + l16;
#define REDW(un, q) *(LAS f32x4*)(redw + (un) * 32768 + (q) * 1024) = \
    f32x4{d[un][(q) >> 2][4 * ((q) & 3)], d[un][(q) >> 2][4 * ((q) & 3) + 1], d[un][(q) >> 2][4 * ((q) & 3) + 2], d[un][(q) >> 2][4 * ((q) & 3) + 3]}
  LAS unsigned char *w2ua = w2a + 65536, *w2ub = w2b + 65536;
  PIN(w2ua); PIN(w2ub);
#define CHECK(un) \
  if (__builtin_expect((unsigned)__builtin_amdgcn_readlane((int)m[un], 63) != 14u * tagA, 0)) { \
    _Pragma("nounroll") for (unsigned spin = 0; spin < SPIN_CAP; ++spin) { \
      FRESH(); \
      COUNT(24 + (un)); \
      v4u qq[28]; \
      const v4u kk = ldg(rs, ckv, kb0 + (un) * 8192); \
      static_for<0, 12>([&](auto Nc) __attribute__((always_inline)) { qq[Nc.v] = HFRAG(28 * (un) + Nc.v); }); \
      SB(); \
      unsigned t = sum4(0u, kk); \
      static_for<0, 7>([&](auto Cc) __attribute__((always_inline)) { \
        constexpr int c = Cc.v; \
        const v4u* q = &qq[4 * c]; \
        if constexpr (c + 3 < 7) { _Pragma("unroll") for (int i = 0; i < 4; ++i) qq[4 * c + 12 + i] = ldg(rs, l16 + i * 1024, hb0 + CHUNK(4 * c + 12) * 4096 + (un) * (2 * 114688)); } \
        SB(); \
        t = sum8(sum8(t, q[0], q[1]), q[2], q[3]); \
        mfma_av<c == 0>(d[un][0], w2f[4 * c][0], q[0]); mfma_av<c == 0>(d[un][1], w2f[4 * c][1], q[0]); \
        _Pragma("unroll") for (int i = 1; i < 4; ++i) { mfma_av<false>(d[un][0], w2f[4 * c + i][0], q[i]); mfma_av<false>(d[un][1], w2f[4 * c + i][1], q[i]); } \
        SB(); \
      }); \
      mfma_done(d[un][1]); \
      PIN(d[un][0]); \
      if ((unsigned)__builtin_amdgcn_readlane((int)red_all(t), 63) == 14u * tagA) break; \
      PAUSE(); \
    } \
    if (un) { _Pragma("unroll") for (int q = 0; q < 4; ++q) REDW(1, q); } \
  }
  unsigned m[2] = {0u, 0u};
  v4u gbv, scv, ck[2];
  f32x4 pr[8];
  float gb[8], rx[2][8];
  unsigned orow = (rp * 128 + ((lane & 31) >> 3) * 16 + rg * 8 + (lane & 7)) * 1024 + dt * 128 + w * 32 + hf * 16;
  static_for<0, 112>([&](auto Xc) __attribute__((always_inline)) {
    constexpr int X = Xc.v, I = X < 100 ? X >> 1 : 50 + (X - 100) % 6, e = X < 100 ? X & 1 : (X - 100) / 6, un = I / 28;
    mfma_av<I % 28 == 0>(d[un][e], w2f[I % 28][e], hq[I]);
    SB();
    if constexpr (X < 100 && e == 0) {
      if constexpr (I < 3) PULL(14 + I, w2sa, w2sb, I * 2048);
      if constexpr (I >= 3 && I < 7) PULL(14 + I, w2a, w2b, 32768 + (I - 3) * 2048);
      if constexpr (I >= 7 && I < 11) PULL(14 + I, w2a, w2b, (I - 7) * 2048);
      if constexpr (I == 21) waitv<hl_upto(21) - 28 + 1>();
      if constexpr (I >= 21 && I < 24) PULL(4 + I, w2ua, w2ub, (I - 21) * 2048);
      if constexpr (I == 28) { m[0] = sum4(m[0], ck[0]); PIN(m[0]); }
      if constexpr (I >= 29 && I < 35) { m[0] = red_step<I - 29>(m[0]); PIN(m[0]); }
      if constexpr (I == 35) CHECK(0)
      if constexpr (I >= 35 && I < 43) REDW(0, I - 35);
    }
    if constexpr (X < 100 && e == 1) {
      if constexpr (I < 6) W2P(50 + I, I & 3, xl, 65536 + I * 1024);
      static_for<hl_upto(I), hl_upto(I + 1)>([&](auto Nc) __attribute__((always_inline)) { HLOAD(Nc.v); });
      if constexpr (I == 6) ck[0] = ldg(rs, ckv, kb0);
      if constexpr (I == HL0 + 28) ck[1] = ldg(rs, ckv, kb0 + 8192);
    }
    if constexpr (X < 100 ? e == 1 : e == 0) { m[un] = sum4(m[un], hq[I]); PIN(m[un]); }
    if constexpr (X == 105) { m[1] = sum4(m[1], ck[1]); PIN(m[1]); }
    if constexpr (X >= 106) { m[1] = red_step<X - 106>(m[1]); PIN(m[1]); }
    if constexpr (X == 23 || X == 24) { const v4u ev = rc[2 * (X - 23)], od = rc[2 * (X - 23) + 1]; res[X - 23] = (rg ^ par) ? od : ev; PIN(res[X - 23]); }
    if constexpr (X == 22) { gbv = lds128(L + L_TG + (64 * dt + 16 * w + 8 * hf) * 2); scv = lds128(L + L_TS + (64 * dt + 16 * w + 8 * hf) * 2); PIN(orow); }
    if constexpr (X >= 26 && X < 42 && (X & 1) == 0) {
      constexpr int q = (X - 26) >> 1;
      const float inv = __builtin_amdgcn_rcpf((q & 1) ? bf_hi(scv[q >> 1]) : bf_lo(scv[q >> 1]));
      gb[q] = -LN2 * ((q & 1) ? bf_hi(gbv[q >> 1]) : bf_lo(gbv[q >> 1]));
      rx[0][q] = inv * ((q & 1) ? bf_hi(res[0][q >> 1]) : bf_lo(res[0][q >> 1]));
      rx[1][q] = inv * ((q & 1) ? bf_hi(res[1][q >> 1]) : bf_lo(res[1][q >> 1]));
      PIN(gb[q]); PIN(rx[0][q]); PIN(rx[1][q]);
    }
    if constexpr (X == 86) BAR();
    if constexpr (X == 86 || X == 88) {
#pragma unroll
      for (int k = X - 86; k < X - 84; ++k) { pr[2 * k] = *(LAS f32x4*)(redr + k * 8192); pr[2 * k + 1] = *(LAS f32x4*)(redr + k * 8192 + 1024); }
    }
    if constexpr (X >= 91 && X < 99) {
      constexpr int q = X - 91;
      float t = (pr[q >> 2][q & 3] + pr[2 + (q >> 2)][q & 3]);
      PIN(t);
      float t2_ = (pr[4 + (q >> 2)][q & 3] + pr[6 + (q >> 2)][q & 3]);
      PIN(t2_);
      rx[0][q] = __builtin_fmaf(gb[q], t + t2_, rx[0][q]);
      PIN(rx[0][q]);
    }
    if constexpr (X == 99)
      *(GAS v4u*)((GAS unsigned char*)out + orow) = v4u{pack2(rx[0][0], rx[0][1]), pack2(rx[0][2], rx[0][3]), pack2(rx[0][4], rx[0][5]), pack2(rx[0][6], rx[0][7])};
    if constexpr (X == 108) asm volatile("s_nop 3" : "+v"(d[1][0]));
    if constexpr (X >= 108) REDW(1, X - 108);
    SB();
    if constexpr (X == 55) STAMP(14);
  });
  STAMP(19);
  asm volatile("s_nop 7" : "+v"(d[1][1]));
  CHECK(1)
#pragma unroll
  for (int q = 4; q < 8; ++q) REDW(1, q);
  BAR();
  {
    f32x4 s0 = {}, s1 = {};
#pragma unroll
    for (int k = 0; k < 4; ++k) { s0 += *(LAS f32x4*)(redr + k * 8192 + 32768); s1 += *(LAS f32x4*)(redr + k * 8192 + 32768 + 1024); }
    float y[8];
#pragma unroll
    for (int q = 0; q < 8; ++q) y[q] = __builtin_fmaf(gb[q], q < 4 ? s0[q & 3] : s1[q & 3], rx[1][q]);
    *(GAS v4u*)((GAS unsigned char*)out + orow + 65536) = v4u{pack2(y[0], y[1]), pack2(y[2], y[3]), pack2(y[4], y[5]), pack2(y[6], y[7])};
  }
  if (b == 0 && tid == 0) __hip_atomic_store((GAS unsigned*)ws, epoch + 1u, __ATOMIC_RELAXED, __HIP_MEMORY_SCOPE_AGENT);
  STAMP(15);
}

extern "C" void kernel_launch(void* const* d_in, const int* in_sizes, int n_in, void* d_out, int out_size, void* d_ws, size_t ws_size,
                              hipStream_t stream) {
  (void)in_sizes; (void)n_in; (void)out_size; (void)ws_size;
  (void)hipFuncSetAttribute((const void*)s4_block_kernel, hipFuncAttributeMaxDynamicSharedMemorySize, LDS_BYTES);
  s4_block_kernel<<<256, 256, LDS_BYTES, stream>>>((cptr)d_in[0], (cptr)d_in[1], (cptr)d_in[2], (cptr)d_in[3], (cptr)d_in[4], (cptr)d_in[5], (cptr)d_in[6],
                                                   (cptr)d_in[7], (cptr)d_in[8], (cptr)d_in[10], (cptr)d_in[11], (cptr)d_in[12], (cptr)d_in[13],
                                                   (unsigned char*)d_out, (unsigned char*)d_ws, (cptr)d_in[9]);
}
```

```cpp
#include <hip/hip_runtime.h>

#define GAS __attribute__((address_space(1)))
#define LAS __attribute__((address_space(3)))
#define DI __device__ __forceinline__
typedef unsigned v4u __attribute__((ext_vector_type(4)));
typedef unsigned v2u __attribute__((ext_vector_type(2)));
typedef float f32x2 __attribute__((ext_vector_type(2)));
typedef float f32x4 __attribute__((ext_vector_type(4)));
typedef float f32x16 __attribute__((ext_vector_type(16)));
typedef __bf16 bf16x2 __attribute__((ext_vector_type(2)));
typedef __bf16 bf16x8 __attribute__((ext_vector_type(8)));
typedef short s16x4 __attribute__((ext_vector_type(4)));
typedef unsigned short u16;
typedef unsigned long long u64;

constexpr int SET_BASE = 0;
constexpr int OFF_EP = 49152;
constexpr int OFF_MT = 0;
constexpr int OFF_SG = 32768;
constexpr int OFF_MG = 65536;
constexpr int OFF_SX = 524288;
constexpr int OFF_XC = 458752;
constexpr int OFF_XG = 1 << 20;
constexpr int OFF_Z = OFF_XG + (2 << 20);
constexpr int OFF_HID = OFF_Z + 2097152;
constexpr int OFF_CK = OFF_HID + 7340032;
constexpr int SET_BYTES = OFF_CK + 262144;
static_assert(OFF_MG + 393216 <= OFF_XC && OFF_XC + 65536 <= OFF_SX, "set size");

constexpr int L_TS = 0, L_TB = 1024, L_TG = 2048;
constexpr int L_UVT = 3072;
constexpr int L_M = 4096;
constexpr int L_UVS = L_M;
constexpr int UVS_ROW = 544, UVS_CH = 57 * UVS_ROW;
constexpr int L_KC = L_M;
constexpr int KC_STRIDE = 4160;
constexpr int L_H = L_KC + 8 * KC_STRIDE;
constexpr int H_STRIDE = 4576;
constexpr int L_XB = L_H + 2 * H_STRIDE;
constexpr int L_RX = L_XB + 16384;
constexpr int L_ETA = L_RX + 4096;
constexpr int L_R = L_M + 65536;
constexpr int L_TOP = L_M + 131072 + 24576;
constexpr int L_KT = L_TOP + 16;
constexpr int L_STAT = L_TOP;
constexpr int L_XR = L_M;
constexpr int L_SP = L_M + 131072;
constexpr int LDS_BYTES = 163840;
static_assert(L_TOP + 4096 <= LDS_BYTES && L_ETA + 16 <= L_R && L_UVS + 2 * UVS_CH <= L_R && L_SP + 24576 <= L_TOP, "LDS map");
static_assert(L_H % 16 == 0 && L_XB % 16 == 0 && L_RX % 16 == 0 && L_KT % 16 == 0, "alignment");

#define ZLAG 23
#define MTK 112
#define SXJ 16
#define EOV 30
#define HC0 55
#define HC1 61
#define HL0 9
#define HL1 21
#define HLR 1
#define HT0 2
#define HT1 6
constexpr int EO = EOV;
constexpr int CODE_BYTES = 35072;
constexpr int SC1 = 16;
constexpr int OOB = 0x7FFFFF00;
constexpr unsigned SPIN_CAP = 1u << 15;
constexpr unsigned POISON = 0xFFFFFFFFu;
constexpr float LOG2E = 1.4426950408889634f, LN2 = 0.6931471805599453f, INV2PI = 0.15915494309189535f;

#define SB() __builtin_amdgcn_sched_barrier(0)
#define WAITV(n) asm volatile("s_waitcnt vmcnt(" #n ")" ::: "memory")
#define PIN(x) asm volatile("" : "+v"(x))
#define FRESH() asm volatile("" ::: "memory")
#define PAUSE() asm volatile("s_sleep 4" ::: "memory")
#define BAR() do { asm volatile("s_waitcnt lgkmcnt(0)" ::: "memory"); __builtin_amdgcn_s_barrier(); asm volatile("" ::: "memory"); } while (0)
#define STAMP(i)
#define SARG
#define SPASS
#define COUNT(i)

#define STAMPC(i)
#define STAMPA(i)
#define STAMPB(i) STAMP(i)
DI float u2f(unsigned u) { return __builtin_bit_cast(float, u); }
DI unsigned f2u(float f) { return __builtin_bit_cast(unsigned, f); }
DI float dot2(unsigned u, float c) { const bf16x2 v = __builtin_bit_cast(bf16x2, u); return __builtin_amdgcn_fdot2_f32_bf16(v, v, c, false); }
DI float bf_lo(unsigned u) { return __builtin_bit_cast(float, u << 16); }
DI float bf_hi(unsigned u) { return __builtin_bit_cast(float, u & 0xFFFF0000u); }
DI unsigned pack2(float a, float b) { f32x2 v = {a, b}; return __builtin_bit_cast(unsigned, __builtin_convertvector(v, bf16x2)); }
DI float rbf(float a) { return bf_lo(pack2(a, a)); }
DI unsigned sel(bool c, unsigned a, unsigned b) { asm("" : "+v"(a), "+v"(b)); return c ? a : b; }
DI unsigned guard(unsigned u) { return u < 0xFFFFFFFEu ? u : 0xFFFFFFFEu; }
DI unsigned max4(unsigned m, v4u v) {
  asm("v_max3_u32 %0, %0, %1, %2\n\tv_max3_u32 %0, %0, %3, %4" : "+v"(m) : "v"(v.x), "v"(v.y), "v"(v.z), "v"(v.w));
  return m;
}
DI f32x4 mfma16(v4u a, v4u b, f32x4 c) { return __builtin_amdgcn_mfma_f32_16x16x32_bf16(__builtin_bit_cast(bf16x8, a), __builtin_bit_cast(bf16x8, b), c, 0, 0, 0); }
DI f32x16 mfma32(v4u a, v4u b, f32x16 c) { return __builtin_amdgcn_mfma_f32_32x32x16_bf16(__builtin_bit_cast(bf16x8, a), __builtin_bit_cast(bf16x8, b), c, 0, 0, 0); }
template <bool FIRST> DI void mfma_av(f32x16& acc, const v4u& a, const v4u& b) {
  if (FIRST) asm volatile("v_mfma_f32_32x32x16_bf16 %0, %1, %2, 0" : "=&v"(acc) : "a"(a), "v"(b));
  else asm volatile("v_mfma_f32_32x32x16_bf16 %0, %1, %2, %0" : "+v"(acc) : "a"(a), "v"(b));
}
template <bool FIRST, int BLK> DI void mfma4_va(f32x4& c0, f32x4& c1, const v4u& a, const v4u& b) {
  const v2u al = {a.x, a.y}, ah = {a.z, a.w}, bl = {b.x, b.y}, bh = {b.z, b.w};
  if (FIRST) asm volatile("v_mfma_f32_4x4x4_16b_bf16 %0, %2, %4, 0 cbsz:3 abid:%6\n\tv_mfma_f32_4x4x4_16b_bf16 %1, %3, %5, 0 cbsz:3 abid:%6"
                          : "=&v"(c0), "=&v"(c1) : "v"(al), "v"(ah), "a"(bl), "a"(bh), "n"(BLK));
  else asm volatile("v_mfma_f32_4x4x4_16b_bf16 %0, %2, %4, %0 cbsz:3 abid:%6\n\tv_mfma_f32_4x4x4_16b_bf16 %1, %3, %5, %1 cbsz:3 abid:%6"
                    : "+v"(c0), "+v"(c1) : "v"(al), "v"(ah), "a"(bl), "a"(bh), "n"(BLK));
}
template <bool FIRST> DI void mfma16_v(f32x4& acc, const v4u& a, const v4u& b) {
  if (FIRST) asm volatile("v_mfma_f32_16x16x32_bf16 %0, %1, %2, 0" : "=&v"(acc) : "v"(a), "v"(b));
  else asm volatile("v_mfma_f32_16x16x32_bf16 %0, %1, %2, %0" : "+v"(acc) : "v"(a), "v"(b));
}
DI void mfma_done(f32x16& acc) { asm volatile("s_nop 15" : "+v"(acc)); }
DI v2u trd(LAS unsigned char* p) { return __builtin_bit_cast(v2u, __builtin_amdgcn_ds_read_tr16_b64_v4i16((LAS s16x4*)p)); }
DI v4u trd2(LAS unsigned char* p0, LAS unsigned char* p1) { const v2u a = trd(p0), b = trd(p1); return v4u{a.x, a.y, b.x, b.y}; }
DI v4u lds128(LAS unsigned char* p) { return *(LAS v4u*)p; }
DI v4u ldg(__amdgpu_buffer_rsrc_t r, int voff, int soff) { return __builtin_amdgcn_raw_buffer_load_b128(r, voff, soff, SC1); }
DI void stg(__amdgpu_buffer_rsrc_t r, int voff, int soff, v4u v) {
  __builtin_amdgcn_raw_buffer_store_b128(v, r, voff, soff, SC1);
  asm volatile("s_nop 1" :: "v"(v) : "memory");
}
template <int CTRL> DI float dpp_add(float v) {
  return v + __builtin_bit_cast(float, __builtin_amdgcn_update_dpp(0, __builtin_bit_cast(int, v), CTRL, 0xF, 0xF, true));
}
DI float swap16_add(float v) { const unsigned u = __builtin_bit_cast(unsigned, v); const auto r = __builtin_amdgcn_permlane16_swap(u, u, false, false);
  return u2f(r[0]) + u2f(r[1]); }
DI float swap32_add(float v) { const unsigned u = __builtin_bit_cast(unsigned, v); const auto r = __builtin_amdgcn_permlane32_swap(u, u, false, false);
  return u2f(r[0]) + u2f(r[1]); }
DI float quad_sum(float v) { v = dpp_add<0xB1>(v); return dpp_add<0x4E>(v); }
DI float row_sum(float v) { return dpp_add<0x140>(dpp_add<0x141>(quad_sum(v))); }
DI float wave_sum(float v) { return swap32_add(swap16_add(row_sum(v))); }
DI f32x2 cmul(f32x2 a, f32x2 e, f32x2 ep) { const f32x2 t = f32x2{a.x, a.x} * e; return __builtin_elementwise_fma(f32x2{a.y, a.y}, ep, t); }
DI f32x2 cexp(float re2, float rev) {
  const float m = __builtin_amdgcn_exp2f(re2), f = __builtin_amdgcn_fractf(rev);
  return f32x2{m * __builtin_amdgcn_cosf(f), m * __builtin_amdgcn_sinf(f)};
}
template <int LI, int SI> DI void grp(int& so, unsigned lbase, int sbase) {
  asm volatile("s_add_i32 m0, %1, %3\n\ts_add_i32 %0, %2, %4" : "=s"(so) : "s"(lbase), "s"(sbase), "n"(LI), "n"(SI) : "scc", "memory");
}
template <int K, bool COH> DI void xfer(unsigned v, __amdgpu_buffer_rsrc_t r, int so) {
  if (COH) asm volatile("buffer_load_dwordx4 %0, %1, %2 offen offset:%3 sc1 lds" :: "v"(v), "s"(r), "s"(so), "n"(K * 1024) : "memory");
  else asm volatile("buffer_load_dwordx4 %0, %1, %2 offen offset:%3 lds" :: "v"(v), "s"(r), "s"(so), "n"(K * 1024) : "memory");
}
DI void set_m0(unsigned lds_dst) { asm volatile("s_mov_b32 m0, %0\n\ts_nop 0" :: "s"(lds_dst) : "memory"); }
DI void dma_x4(const unsigned char* sbase, unsigned v, unsigned lds_dst) {
  asm volatile("s_mov_b32 m0, %2\n\ts_nop 0\n\tglobal_load_lds_dwordx4 %0, %1 sc1\n\tglobal_load_lds_dwordx4 %0, %1 offset:1024 sc1\n\t"
               "global_load_lds_dwordx4 %0, %1 offset:2048 sc1\n\tglobal_load_lds_dwordx4 %0, %1 offset:3072 sc1"
               :: "v"(v), "s"(sbase), "s"(lds_dst) : "memory");
}
template <int I> struct IC { static constexpr int v = I; };
template <int I, int N, class F> DI void static_for(F&& f) { if constexpr (I < N) { f(IC<I>{}); static_for<I + 1, N>(f); } }
DI unsigned now() { u64 t; asm volatile("s_memrealtime %0\n\ts_waitcnt lgkmcnt(0)" : "=s"(t) :: "memory"); return (unsigned)t; }
DI void reload(v4u& d, __amdgpu_buffer_rsrc_t r, int voff, int soff) { d = ldg(r, voff, soff); FRESH(); }
DI void reload1(unsigned& d, __amdgpu_buffer_rsrc_t r, int voff, int soff) { d = __builtin_amdgcn_raw_buffer_load_b32(r, voff, soff, SC1); FRESH(); }
template <int N> DI void waitv() { asm volatile("s_waitcnt vmcnt(%0)" :: "n"(N) : "memory"); }
struct ConvOrder { int t[36], u[36]; };
constexpr ConvOrder conv_order() {
  ConvOrder o = {};
  int done[8] = {}, last = -1;
  for (int k = 0; k < 36; ++k) {
    int best = -1;
    for (int t = 0; t < 8; ++t)
      if (t != last && done[t] <= t && (best < 0 || t - done[t] > best - done[best])) best = t;
    o.t[k] = best; o.u[k] = done[best]++; last = best;
  }
  return o;
}
constexpr ConvOrder conv_order_last() {
  ConvOrder o = {};
  int k = 0;
  for (int p = 0; p < 4; ++p)
    for (int u = 0; u <= 2 * p + 1; ++u) {
      o.t[k] = 2 * p + 1; o.u[k] = u; ++k;
      if (u <= 2 * p) { o.t[k] = 2 * p; o.u[k] = u; ++k; }
    }
  return o;
}
constexpr int epi_slot(int v) { return v < 32 ? 2 * EO + v : 2 * 50 - 1 + (v - 32); }
constexpr bool epi_pair(int j) { return (j >= EO && j < EO + 19) || (j >= 50 && j < 61); }
constexpr int hc_upto(int j) { return (j < HC0 ? 0 : j - HC0) + (j < HC1 ? 0 : j - HC1); }
constexpr int hl_upto(int i) { return i < HL0 ? 28 : i < HL0 + 12 ? 28 + (i - HL0) : i < HL1 ? 40 : i < HL1 + 16 / HLR ? 40 + HLR * (i - HL1) : 56; }
constexpr int c_ops(int j) {
  return (j >= SXJ && j < SXJ + 4 ? 2 : j < 34 || (j >= 35 && j < 51) ? 1 : 0) + hc_upto(j + 1) - hc_upto(j) +
         (j == EO + 6 || j == EO + 14 || j == 56 ? 1 : j == EO + 10 || j == EO + 18 || j == 60 ? 2 : 0);
}
constexpr int c_between(int a, int b) { int n = 0; for (int j = a + 1; j < b; ++j) n += c_ops(j); return n; }
DI unsigned tagbad(unsigned bad, v4u g, unsigned tagA, unsigned tagB) {
  return __builtin_amdgcn_bitop3_b32(__builtin_amdgcn_bitop3_b32(bad, g.y, tagA, 0xF6), g.w, tagB, 0xF6);
}
template <int R0, int R1, int R2, int R3> DI void put4(unsigned base, f32x2 a, f32x2 b, f32x2 c, f32x2 d) {
  asm volatile("s_mov_b32 m0, %4\n\ts_nop 0\n\tds_write_addtid_b32 %0 offset:%5\n\tds_write_addtid_b32 %1 offset:%6\n\t"
               "ds_write_addtid_b32 %2 offset:%7\n\tds_write_addtid_b32 %3 offset:%8"
               :: "v"(pack2(a.x, a.y)), "v"(pack2(b.x, b.y)), "v"(pack2(c.x, c.y)), "v"(pack2(d.x, d.y)), "s"(base),
                  "n"(R0 * UVS_ROW), "n"(R1 * UVS_ROW), "n"(R2 * UVS_ROW), "n"(R3 * UVS_ROW) : "memory");
}
DI unsigned sum4(unsigned m, v4u v) {
  asm("v_add3_u32 %0, %0, %1, %2\n\tv_add3_u32 %0, %0, %3, %4" : "+v"(m) : "v"(v.x), "v"(v.y), "v"(v.z), "v"(v.w));
  return m;
}
DI void acc4(unsigned& m, v4u v) {
  asm volatile("v_add3_u32 %0, %0, %1, %2\n\tv_add3_u32 %0, %0, %3, %4" : "+v"(m) : "v"(v.x), "v"(v.y), "v"(v.z), "v"(v.w));
}
DI unsigned sum8(unsigned m, v4u a, v4u b) {
  asm("v_add3_u32 %0, %0, %1, %2\n\tv_add3_u32 %0, %0, %3, %4\n\tv_add3_u32 %0, %0, %5, %6\n\tv_add3_u32 %0, %0, %7, %8"
      : "+v"(m) : "v"(a.x), "v"(a.y), "v"(a.z), "v"(a.w), "v"(b.x), "v"(b.y), "v"(b.z), "v"(b.w));
  return m;
}
template <int I> DI unsigned red_step(unsigned v) {
  constexpr int ctrl[6] = {0xB1, 0x4E, 0x141, 0x140, 0x142, 0x143}, rows[6] = {0xF, 0xF, 0xF, 0xF, 0xA, 0xC};
  return v + (unsigned)__builtin_amdgcn_update_dpp(0, (int)v, ctrl[I], rows[I], 0xF, true);
}
DI unsigned red_all(unsigned v) { return red_step<5>(red_step<4>(red_step<3>(red_step<2>(red_step<1>(red_step<0>(v)))))); }
DI void swap_add32(float& a, float b) { const auto r = __builtin_amdgcn_permlane32_swap(f2u(a), f2u(b), false, false); a = u2f(r[0]) + u2f(r[1]); }
DI void swap_add16(float& a, float b) { const auto r = __builtin_amdgcn_permlane16_swap(f2u(a), f2u(b), false, false); a = u2f(r[0]) + u2f(r[1]); }

typedef const unsigned char* cptr;
typedef unsigned v16u __attribute__((ext_vector_type(16)));
DI cptr mkptr(unsigned lo, unsigned hi) { return (cptr)(((u64)hi << 32) | lo); }

__global__ void __launch_bounds__(256, 1)
s4_block_kernel(cptr, cptr, cptr, cptr, cptr, cptr, cptr, cptr, cptr, cptr, cptr, cptr, cptr, unsigned char*, unsigned char*, cptr) {
  STAMP(0);
  v16u ka0, ka1;
  asm volatile("s_load_dwordx16 %0, %2, 0x0\n\ts_load_dwordx16 %1, %2, 0x40" : "=&s"(ka0), "=&s"(ka1) : "s"(__builtin_amdgcn_kernarg_segment_ptr()));
  SB();
  LAS unsigned char* const L = (LAS unsigned char*)16u - 16;
  const int tid = threadIdx.x, lane = tid & 63, w = __builtin_amdgcn_readfirstlane(tid >> 6);
  const int b = blockIdx.x, xcd = b & 7, slot = b >> 3, id = xcd * 32 + slot;
  const int ks = (id >> 6) * 4 + w, cc = id & 63;
  const int rp = 4 * (xcd >> 1) + (slot >> 3), rg = xcd & 1, dt = slot & 7, si = 8 * rg + dt;
  const int ct = 7 * rg + min(dt, 6);
  const int rot = (0x0340 >> (4 * (2 * (w & 1) + rg))) & 7;

  const unsigned char* pcode;
  asm volatile("s_getpc_b64 %0" : "=s"(pcode));
  unsigned warm;
  asm volatile("global_load_dword %0, %1, %2" : "=v"(warm) : "v"(min((((slot & 7) * 4 + w) * 9 + min(lane, 8)) * 128, CODE_BYTES)), "s"(pcode) : "memory");
  const int g16 = lane >> 4, q4 = (lane & 15) >> 2, p4 = lane & 3, hf = lane >> 5, blk = (lane >> 4) & 1, l16 = lane * 16;
  const unsigned lrw = L_R + w * 16384;
  unsigned lsp = L_SP + w * 6144;
  unsigned co = (ks * 32 + (lane & 31)) * 2, xo = (id >> 2) * 32768 + (id & 3) * 256 + (tid & 7) * 4096 + (tid >> 3) * 8, ro = id * 8192 + w * 2048 + hf * 1024 + (lane & 31) * 16, po = id * 512 + tid * 2;
  PIN(co); PIN(xo); PIN(ro); PIN(po);
  set_m0(lrw);
  SB();
  asm volatile("s_waitcnt lgkmcnt(0)" : "+s"(ka0), "+s"(ka1));
  const cptr xin = mkptr(ka0[0], ka0[1]), pAre = mkptr(ka0[2], ka0[3]), pAim = mkptr(ka0[4], ka0[5]), pBre = mkptr(ka0[6], ka0[7]);
  const cptr pBim = mkptr(ka0[8], ka0[9]), pCre = mkptr(ka0[10], ka0[11]), pCim = mkptr(ka0[12], ka0[13]), pldt = mkptr(ka0[14], ka0[15]);
  const cptr pD = mkptr(ka1[0], ka1[1]), w1 = mkptr(ka1[2], ka1[3]), w2 = mkptr(ka1[4], ka1[5]), pc = mkptr(ka1[6], ka1[7]);
  const cptr wmod = mkptr(ka1[8], ka1[9]), out = mkptr(ka1[10], ka1[11]), ws = mkptr(ka1[12], ka1[13]);
  unsigned uldt, uD;
  asm volatile("s_load_dword %0, %2, %4\n\ts_load_dword %1, %3, %4" : "=&s"(uldt), "=&s"(uD) : "s"(pldt), "s"(pD), "s"(id * 4));
  unsigned uAre, uAim, uBre, uBim, uCre, uCim;
  asm volatile("global_load_ushort %0, %6, %7\n\tglobal_load_ushort %1, %6, %8\n\tglobal_load_ushort %2, %6, %9\n\t"
               "global_load_ushort %3, %6, %10\n\tglobal_load_ushort %4, %6, %11\n\tglobal_load_ushort %5, %6, %12"
               : "=&v"(uAre), "=&v"(uAim), "=&v"(uBre), "=&v"(uBim), "=&v"(uCre), "=&v"(uCim)
               : "v"(po), "s"(pAre), "s"(pAim), "s"(pBre), "s"(pBim), "s"(pCre), "s"(pCim) : "memory");
  unsigned epoch_v, cval;
  v2u xq[4];
  v4u ra, rb;
  asm volatile("global_load_dword %0, %8, %12 sc1\n\tglobal_load_dwordx2 %1, %9, %13\n\tglobal_load_dwordx2 %2, %9, %13 offset:1024\n\t"
               "global_load_dwordx2 %3, %9, %13 offset:2048\n\tglobal_load_dwordx2 %4, %9, %13 offset:3072\n\t"
               "global_load_dwordx4 %5, %10, %13\n\tglobal_load_dwordx4 %6, %10, %13 offset:512\n\tglobal_load_ushort %7, %11, %14"
               : "=&v"(epoch_v), "=&v"(xq[0]), "=&v"(xq[1]), "=&v"(xq[2]), "=&v"(xq[3]), "=&v"(ra), "=&v"(rb), "=&v"(cval)
               : "v"(OFF_EP), "v"(xo), "v"(ro), "v"(co), "s"(ws), "s"(xin), "s"(pc) : "memory");
  unsigned wmo[3];
#pragma unroll
  for (int i = 0; i < 3; ++i) { const unsigned idx = lane + 64 * i; wmo[i] = (ks * 32 * 6144 + cc * 96 - i * 1024) + idx / 6 * (6144 - 96) + idx * 16; }
  asm volatile("global_load_lds_dwordx4 %0, %3\n\tglobal_load_lds_dwordx4 %1, %3 offset:1024\n\tglobal_load_lds_dwordx4 %2, %3 offset:2048"
               :: "v"(wmo[0]), "v"(wmo[1]), "v"(wmo[2]), "s"(wmod) : "memory");
  const unsigned char* setp = ws + SET_BASE;
  const __amdgpu_buffer_rsrc_t rs = __builtin_amdgcn_make_buffer_rsrc((void*)setp, 0, SET_BYTES, 0x00020000);
  SB();
  STAMPA(26);
  unsigned wto = id * 7168 + (w >> 1) * 3584 + min(lane, 27) * 128;
  PIN(wto);
  unsigned w1v[4], w2v[4];
#pragma unroll
  for (int i = 0; i < 4; ++i) {
    w1v[i] = (lane >> 3) * 3584 + (((lane & 7) ^ (blk << 2)) * 16) + i * (57344 - 1024);
    w2v[i] = (lane >> 3) * 1024 + (((lane & 7) ^ (blk << 2)) * 16) + i * (8192 - 1024);
    PIN(w1v[i]); PIN(w2v[i]);
  }
  int w1b = ct * 256 + (w >> 1) * 128 + (w & 1) * (8 * 3584) + ((dt + 1) >> 3 << 24), w2lo = (448 * w + 64 * rot) * 1024 + dt * 128;
  int w2c3 = w2lo - (rot >> 2) * (448 * 1024), w2hi = w2lo - ((rot + 1) >> 2) * (448 * 1024), so = 0;
  unsigned lrx = L_RX + w * 1024, lto = L_TOP + w * 1024;
  int zs = OFF_Z + (2 * id + (w >> 1)) * 64;
  int za = ((((w & 1) + 2 * (g16 & 1)) * 4 + q4) * 4 + (g16 >> 1)) * 32768 + p4 * 16;
  int tra = (hf * 8 + q4) * 64 + blk * 32 + p4 * 8;
  const int wsrc = (w & 2) + hf;
  int w1t = q4 * 128 + (((w & 1) ^ (q4 >> 1)) * 64) + blk * 32 + p4 * 8;
  LAS unsigned char *w1a = L + L_R + wsrc * 16384 + w1t, *w1s = L + L_SP + wsrc * 6144 + w1t;
  LAS unsigned char *w1x = L + L_RX + wsrc * 1024 + w1t, *w1y = L + L_TOP + wsrc * 1024 + w1t;
#define W1PULL(step, n) w1f[step] = trd2(((n) < 16 ? w1a : w1s) + ((n) & 15) * 1024, ((n) < 16 ? w1a : w1s) + ((n) & 15) * 1024 + 512)
  LAS unsigned char* ka = L + L_KC + ((w >> 1) * 4 + q4) * KC_STRIDE + 2 * (32 - 8 * g16 + 4 * p4) - 8 + 256 * (w & 1);
  LAS unsigned char* hb = L + L_H + (w >> 1) * H_STRIDE + (lane & 15) * 32 + g16 * 16 + 256 - 256 * (w & 1);
  LAS unsigned char* zp = L + sel(tid < 64, L_KC + ((lane >> 1) & 7) * KC_STRIDE + (lane & 1) * 16, L_H + hf * H_STRIDE + min(lane & 31, 29) * 16);
  LAS unsigned char* ub = L + L_UVS + (lane & 15) * UVS_ROW + g16 * 16;
  int gmo = OFF_MG + (g16 * 256 + id) * 256 + (lane & 15) * 16, gmo2 = OFF_MG + ((4 + blk) * 256 + id) * 256 + (lane & 15) * 16;
  int mgo = sel(lane < 48, (lane >> 1) * 256 + (lane & 1) * 8, OOB), sgo = sel(lane & 31, OOB, hf * 8);
  LAS unsigned char* xbw = L + L_XB + ((w & 1) * 4 + (w >> 1)) * 1024 + l16;
  int xcw = sel(lane & 7, OOB, tid), xcr = sel(lane & 7, OOB, (tid >> 3) * 1024), xgw = (tid >> 3) * 16384 + (tid & 7) * 16;
  PIN(xcw); PIN(xcr); PIN(xgw);
  LAS unsigned char *exa = L + L_XB + w * 4096 + l16, *eya = L + L_XB + (w ^ 1) * 4096 + l16;
  LAS unsigned char* xbr = L + L_XB + w * 4096 + ((lane & 15) * 4 + g16) * 16;
  PIN(xbw); PIN(xbr); PIN(exa); PIN(eya);
  int mto = (w * 64 + (lane >> 1)) * 64 + (lane & 1) * 16;
  LAS unsigned char* mtw = L + (lane & 1) * L_TG + (w * 64 + (lane >> 1)) * 4;
  PIN(mto); PIN(mtw);
  PIN(za); PIN(tra); PIN(w1a); PIN(w1s); PIN(w1x); PIN(w1y); PIN(ka); PIN(hb); PIN(zp); PIN(ub); PIN(gmo); PIN(gmo2); PIN(mgo); PIN(sgo);
  asm volatile("" : "+s"(w1b), "+s"(w2lo), "+s"(w2c3), "+s"(w2hi), "+s"(lsp), "+s"(lrx), "+s"(lto), "+s"(zs));
#define W1P(n, k, lbase, li) do { if constexpr ((k) == 0) grp<(li), (n) * 57344>(so, lbase, w1b); xfer<(k), false>(w1v[k], rw1, so); } while (0)
#define W2P(n, k, lbase, li) do { if constexpr ((k) == 0) grp<(li), (n) * 8192>(so, lbase, (n) < 24 ? w2lo : (n) < 32 ? w2c3 : w2hi); xfer<(k), false>(w2v[k], rw2, so); } while (0)
  SB();
  LAS unsigned char* koff[12];
  {
    float fj = (float)(8 * tid - 3);
    PIN(fj);
    const unsigned lin = (unsigned)(8 * tid - 3 + 0x4300 - 128) * 0x10001u + 0x10000u;
#pragma unroll
    for (int i = 0; i < 12; i += 2) {
      const unsigned r = sel(tid < 32, lin + i * 0x10001u, pack2(fj + (float)i, fj + (float)(i + 1)));
      koff[i] = L + ((r & 0xFFFFu) * 4 + (L_KT + (128 - 0x4300) * 4));
      koff[i + 1] = L + ((r >> 16) * 4 + (L_KT + (128 - 0x4300) * 4));
      PIN(koff[i]);
      if (i < 10) PIN(koff[i + 1]);
    }
  }
  *(LAS v4u*)(L + L_KT - 16) = v4u{0u, 0u, 0u, 0u};
  LAS unsigned char* ktw[4];
  {
    const int row = w * 16 + (lane & 15), sg = max((row >> 3) - 1, 0), rr_ = row - 8 * sg;
    const int idx0 = 128 * sg + ((rr_ >> (3 - sg)) << (7 - sg)) + (rr_ & ((8 >> sg) - 1)) + 1, st = 8 >> sg;
#pragma unroll
    for (int r = 0; r < 4; ++r) {
      const int idx = sel(row < 40, idx0 + st * (4 * g16 + r), tid == 136 && r == 0 ? 0 : 641 + lane);
      ktw[r] = L + L_KT + idx * 4;
      PIN(ktw[r]);
    }
  }
  const int par = (0x1C >> (ct % 7)) & 1;
  const unsigned char* xg = setp + OFF_Z + rp * 131072;
  unsigned xl = L_XR + w * 8192;
  int xs2 = OFF_Z + rp * 131072 + w * 8192 + (2 ^ par) * 32768, xs3 = xs2 + 32768 - 65536 * par;
  asm volatile("" : "+s"(xl), "+s"(xs2), "+s"(xs3));
  int slo = OFF_Z + (rp * 4 + w) * 32768 + si * 2048;
  asm volatile("" : "+s"(slo));
  const int w2t = (4 * hf + q4) * 128 + blk * 32 + (((p4 & 1) << 1) | (p4 >> 1)) * 8;
  LAS unsigned char* const w2a0 = L + w2t + (q4 >> 1) * 64;
  LAS unsigned char* const w2b0 = L + w2t + ((q4 >> 1) ^ 1) * 64;
  LAS unsigned char *pa = w2a0 + lrw + 8192, *pb = w2b0 + lrw + 8192;
  LAS unsigned char* qr = L + (p4 ? L_TB : L_TS) + hf * 16 + ((lane >> 2) & 7) * 32;
  PIN(pa); PIN(pb); PIN(qr);
#define PULL(step, pa, pb, off) do { w2f[step][0] = trd2((pa) + (off), (pa) + (off) + 1024); w2f[step][1] = trd2((pb) + (off), (pb) + (off) + 1024); } while (0)
  const __amdgpu_buffer_rsrc_t rw1 = __builtin_amdgcn_make_buffer_rsrc((void*)w1, 0, 1835008, 0x00020000);
  const __amdgpu_buffer_rsrc_t rw2 = __builtin_amdgcn_make_buffer_rsrc((void*)w2, 0, 1835008, 0x00020000);
  SB();
  STAMPA(27);
  asm volatile("s_waitcnt lgkmcnt(0)" : "+s"(uldt), "+s"(uD));
  asm volatile("s_waitcnt vmcnt(11)" : "+v"(uAre), "+v"(uAim), "+v"(uBre), "+v"(uBim), "+v"(uCre), "+v"(uCim), "+v"(warm));
    const float dtv = rbf(__builtin_amdgcn_exp2f(LOG2E * (w >> 1 ? bf_hi(uldt) : bf_lo(uldt))));
    const float are = dtv * bf_lo(uAre) * LOG2E, aim = dtv * bf_lo(uAim) * INV2PI;
    const float br = dtv * bf_lo(uBre), bi = dtv * bf_lo(uBim), cr = bf_lo(uCre), ci = bf_lo(uCim);
    const f32x2 wv = {cr * br - ci * bi, cr * bi + ci * br};
    const unsigned row = L_UVS + (w >> 1) * UVS_CH + (w & 1) * 256;
    const f32x2 e1 = cexp(are, aim), e8 = cexp(8.f * are, -8.f * aim), e128 = cexp(128.f * are, 128.f * aim);
    const f32x2 e1p = {-e1.y, e1.x}, e8p = {-e8.y, e8.x}, e128p = {-e128.y, e128.x};
    f32x2 z[8], v, v2, y, y2;
#define M1(a) cmul(a, e1, e1p)
#define M8(a) cmul(a, e8, e8p)
#define MK(a) cmul(a, e128, e128p)
    z[0] = M1(wv);
    put4<40, 41, 42, 0>(row, wv, f32x2{1.f, 0.f}, e8, z[0]);
    v = M8(e8); z[1] = M1(z[0]); v2 = M8(v); z[2] = M1(z[1]);
    put4<43, 1, 44, 2>(row, v, z[1], v2, z[2]);
  SB();
  asm volatile("s_waitcnt vmcnt(6)" : "+v"(epoch_v), "+v"(xq[0]), "+v"(xq[1]), "+v"(xq[2]), "+v"(xq[3]));
  STAMPA(29);
  const unsigned epoch = __builtin_amdgcn_readfirstlane(epoch_v);
  const unsigned tagA = epoch * 0x9E3779B1u + 0x7F4A7C15u, tagB = ~tagA;
  {
    const v4u xc0 = {xq[0].x, xq[1].x, xq[2].x, xq[3].x}, xc1 = {xq[0].y, xq[1].y, xq[2].y, xq[3].y};
    stg(rs, xgw, OFF_XG + (id & 3) * 524288 + (id >> 2) * 128, xc0);
    stg(rs, xgw, OFF_XG + (id & 3) * 524288 + (id >> 2) * 128 + 8192, xc1);
    SB();
    STAMP(1);
    asm volatile("s_waitcnt vmcnt(5)" : "+v"(ra), "+v"(rb), "+v"(cval));
    const float c = bf_lo(cval);
    const float s = c * __builtin_amdgcn_rcpf(1.0f + __builtin_amdgcn_exp2f(-LOG2E * c));
    *(LAS u16*)(L + lrw + 3072 + (lane & 31) * 2) = (u16)pack2(s, s);
    f32x2 fa[8];
#pragma unroll
    for (int i = 0; i < 4; ++i) { fa[i] = f32x2{bf_lo(ra[i]), bf_hi(ra[i])}; fa[4 + i] = f32x2{bf_lo(rb[i]), bf_hi(rb[i])}; }
    const f32x2 s2 = ((fa[0] + fa[1]) + (fa[2] + fa[3])) + ((fa[4] + fa[5]) + (fa[6] + fa[7]));
    float mean = swap16_add(row_sum(s2.x + s2.y)) * (1.0f / 512.0f);
    const unsigned k0 = red_step<2>(red_step<1>(red_step<0>(sum4(0u, xc0)))), k1 = red_step<2>(red_step<1>(red_step<0>(sum4(0u, xc1))));
    __builtin_amdgcn_raw_buffer_store_b64(v2u{tagA - k0, tagA - k1}, rs, xcw, OFF_XC + id * 256, SC1);
    PIN(mean);
    SB();
    f32x2 q2 = {0.f, 0.f}, q3 = {0.f, 0.f};
#pragma unroll
    for (int i = 0; i < 8; i += 2) {
      const f32x2 d0 = fa[i] - f32x2{mean, mean}, d1 = fa[i + 1] - f32x2{mean, mean};
      q2 = __builtin_elementwise_fma(d0, d0, q2);
      q3 = __builtin_elementwise_fma(d1, d1, q3);
    }
    q2 += q3;
    float qs = q2.x + q2.y;
    PIN(qs);
    WAITV(3);
    LAS unsigned char* a = L + lrw + (8 * g16 + q4) * 96 + p4 * 8;
    const v4u sc = lds128(L + lrw + 3072 + g16 * 16);
    const v4u m0 = trd2(a, a + 384), m1 = trd2(a + 32, a + 416), m2 = trd2(a + 64, a + 448);
    SB();
    const float var = rbf(rbf(swap16_add(row_sum(qs)) * (1.0f / 512.0f)) + 1e-5f);
    unsigned st = pack2(mean, __builtin_amdgcn_sqrtf(var));
    PIN(st);
    SB();
    const f32x4 c0 = mfma16(sc, m0, f32x4{}), c1 = mfma16(sc, m1, f32x4{}), c2 = mfma16(sc, m2, f32x4{});
    SB();
    __builtin_amdgcn_raw_buffer_store_b64(v2u{st, hf ? tagB : tagA}, rs, sgo, OFF_SG + ((w & 1) * 512 + id * 2 + (w >> 1)) * 16, SC1);
    __builtin_amdgcn_raw_buffer_store_b64(v2u{sel(lane < 16, f2u(c0[0]), sel(lane < 32, f2u(c1[0]), f2u(c2[0]))), (lane & 1) ? tagB : tagA}, rs, mgo,
                                          OFF_MG + cc * 24 * 256 + ks * 16, SC1);
    SB();
  }
  unsigned wt;
  SB();
  STAMP(2);
  unsigned dsk = tid == 136 ? uD : 0u;
  PIN(dsk);

    asm volatile("global_load_dword %0, %1, %2" : "=v"(wt) : "v"(wto), "s"(w1 + (w & 1) * (w2 - w1)) : "memory");
    W1P(0, 0, lrw, 0); W1P(1, 1, lrw, 0); W1P(2, 2, lrw, 0); W1P(3, 3, lrw, 0);
    v = M8(v2); z[3] = M1(z[2]); v2 = M8(v); z[4] = M1(z[3]);
    put4<45, 3, 46, 4>(row, v, z[3], v2, z[4]);
    v = M8(v2); z[5] = M1(z[4]); v2 = M8(v); z[6] = M1(z[5]);
    put4<47, 5, 48, 6>(row, v, z[5], v2, z[6]);
    z[7] = M1(z[6]); z[0] = MK(z[0]); z[1] = MK(z[1]); z[2] = MK(z[2]);
    put4<7, 8, 9, 10>(row, z[7], z[0], z[1], z[2]);
    z[3] = MK(z[3]); z[4] = MK(z[4]); z[5] = MK(z[5]); z[6] = MK(z[6]);
    put4<11, 12, 13, 14>(row, z[3], z[4], z[5], z[6]);
    z[7] = MK(z[7]); z[1] = MK(z[1]); z[3] = MK(z[3]); z[5] = MK(z[5]);
    put4<15, 16, 17, 18>(row, z[7], z[1], z[3], z[5]);
    z[7] = MK(z[7]); z[1] = MK(z[1]); z[3] = MK(z[3]); z[5] = MK(z[5]);
    put4<19, 20, 21, 22>(row, z[7], z[1], z[3], z[5]);
    y = MK(z[7]); z[3] = MK(z[3]); y2 = MK(y); z[4] = MK(z[3]);
    put4<23, 24, 25, 26>(row, y, z[3], y2, z[4]);
    y = MK(y2); z[3] = MK(z[4]); y2 = MK(y); z[4] = MK(z[3]);
    put4<27, 28, 29, 30>(row, y, z[3], y2, z[4]);
    y = MK(y2); v = M8(v2); y2 = MK(y); v2 = M8(v);
    put4<31, 49, 32, 50>(row, y, v, y2, v2);
    stg(rs, (tid >> 3) * 32768 + (tid & 7) * 16, OFF_Z + id * 128, v4u{POISON, POISON, POISON, POISON});
    y = MK(y2); v = M8(v2); y2 = MK(y); v2 = M8(v);
    put4<33, 51, 34, 52>(row, y, v, y2, v2);
    y = MK(y2); v = M8(v2); y2 = MK(y); v2 = M8(v);
    put4<35, 53, 36, 54>(row, y, v, y2, v2);
    y = MK(y2); v = M8(v2); y2 = MK(y); v2 = M8(v);
    put4<37, 55, 38, 56>(row, y, v, y2, v2);
    stg(rs, (tid >> 3) * 32768 + (tid & 7) * 16, OFF_Z + id * 128 + 32 * 32768, v4u{POISON, POISON, POISON, POISON});
    y = MK(y2);
    *(LAS unsigned*)(L + row + 39 * UVS_ROW + lane * 4) = pack2(y.x, y.y);
    STAMPC(29);
    W1P(4, 0, lrw, 4096); W1P(5, 1, lrw, 0); W1P(6, 2, lrw, 0); W1P(7, 3, lrw, 0);
#undef M1
#undef M8
#undef MK
  BAR();
  STAMP(3);
  v4u gx0, gx1, gs0, gs1, gs2, gs3, gm, gn;
  unsigned gc0, gc1;
  const int xgo = OFF_XG + id * 8192, xco = OFF_XC + id * 4;
  v4u fv[2][8], fu[2][8];
  if (w < 3) {
    LAS unsigned char* ut = ub + w * 16 * UVS_ROW;
#pragma unroll
    for (int s = 0; s < 8; ++s)
#pragma unroll
      for (int c = 0; c < 2; ++c) { fv[c][s] = lds128(ub + c * UVS_CH + 41 * UVS_ROW + s * 64); fu[c][s] = lds128(ut + c * UVS_CH + s * 64); SB(); }
    f32x4 a0 = {}, a1 = {};
#pragma unroll
    for (int s = 0; s < 8; ++s) { a0 = mfma16(fv[0][s], fu[0][s], a0); SB(); a1 = mfma16(fv[1][s], fu[1][s], a1); SB(); }
    *(LAS unsigned*)ktw[0] = pack2(a0[0] + bf_lo(dsk), a1[0] + bf_hi(dsk));
#pragma unroll
    for (int r = 1; r < 4; ++r) *(LAS unsigned*)ktw[r] = pack2(a0[r], a1[r]);
  }
  SB();
#define LD1(i, so) "buffer_load_dwordx4 %" #i ", %10, %14, " so " offen sc1\n\t"
  asm volatile("buffer_load_dwordx4 %6, %11, %14, 0 offen sc1\n\tbuffer_load_dwordx4 %7, %12, %14, 0 offen sc1\n\t"
               LD1(0, "%15") LD1(1, "%16") "buffer_load_dword %8, %13, %14, %21 offen sc1\n\tbuffer_load_dword %9, %13, %14, %22 offen sc1\n\t"
               LD1(2, "%17") LD1(3, "%18") LD1(4, "%19") LD1(5, "%20")
               : "=&v"(gx0), "=&v"(gx1), "=&v"(gs0), "=&v"(gs1), "=&v"(gs2), "=&v"(gs3), "=&v"(gm), "=&v"(gn), "=&v"(gc0), "=&v"(gc1)
               : "v"(tid * 16), "v"(gmo), "v"(gmo2), "v"(xcr), "s"(rs), "s"(xgo), "s"(xgo + 4096),
                 "s"(OFF_SG), "s"(OFF_SG + 8192), "s"(OFF_SG + 4096), "s"(OFF_SG + 12288), "s"(xco), "s"(xco + 32768) : "memory");
#undef LD1
  SB();
  BAR();
  {
    unsigned k[11];
#pragma unroll
    for (int i = 0; i < 11; ++i) k[i] = *(LAS unsigned*)koff[i];
    SB();
    if (w < 2) *(LAS v4u*)zp = v4u{0u, 0u, 0u, 0u};
#pragma unroll
    for (int c = 0; c < 2; ++c) {
      unsigned e[5], o[5];
#pragma unroll
      for (int i = 0; i < 5; ++i) {
        o[i] = __builtin_amdgcn_perm(k[2 * i + 1], k[2 * i], c ? 0x07060302u : 0x05040100u);
        e[i] = __builtin_amdgcn_perm(k[2 * i + 2], k[2 * i + 1], c ? 0x07060302u : 0x05040100u);
      }
      LAS unsigned char* d = L + L_KC + c * 4 * KC_STRIDE + 32 + tid * 16;
      *(LAS v4u*)(d) = v4u{e[1], e[2], e[3], e[4]};
      *(LAS v4u*)(d + KC_STRIDE) = v4u{o[1], o[2], o[3], o[4]};
      *(LAS v4u*)(d + 2 * KC_STRIDE) = v4u{e[0], e[1], e[2], e[3]};
      *(LAS v4u*)(d + 3 * KC_STRIDE) = v4u{o[0], o[1], o[2], o[3]};
    }
  }
  SB();
  STAMP(4);
  {
    asm volatile("s_waitcnt vmcnt(0)" : "+v"(gx0), "+v"(gx1), "+v"(gs0), "+v"(gs1), "+v"(gs2), "+v"(gs3), "+v"(gm), "+v"(gn), "+v"(gc0), "+v"(gc1), "+v"(wt) :: "memory");
#define XBAD() (red_step<2>(red_step<1>(red_step<0>(sum8(gc0 + gc1, gx0, gx1)))) ^ (2u * tagA))
#define TB(a, c) tagbad(tagbad(0u, a, tagA, tagB), c, tagA, tagB)
    if (__builtin_expect(__any((XBAD() | TB(gm, gn) | TB(gs0, gs1) | TB(gs2, gs3)) != 0u), 0)) {
      _Pragma("nounroll") for (unsigned spin = 0; spin < SPIN_CAP; ++spin) {
        if (spin) PAUSE();
        COUNT(20);
        if (XBAD()) { reload(gx0, rs, tid * 16, xgo); reload(gx1, rs, tid * 16, xgo + 4096); reload1(gc0, rs, xcr, xco); reload1(gc1, rs, xcr, xco + 32768); }
        if (TB(gs0, gs1)) { reload(gs0, rs, tid * 16, OFF_SG); reload(gs1, rs, tid * 16, OFF_SG + 8192); }
        if (TB(gs2, gs3)) { reload(gs2, rs, tid * 16, OFF_SG + 4096); reload(gs3, rs, tid * 16, OFF_SG + 12288); }
        if (TB(gm, gn)) { reload(gm, rs, gmo, 0); reload(gn, rs, gmo2, 0); }
        if (!__any((XBAD() | TB(gm, gn) | TB(gs0, gs1) | TB(gs2, gs3)) != 0u)) break;
      }
    }
#undef XBAD
#undef TB
  }
  STAMP(5);
  W1P(8, 0, lrw, 8192); W1P(9, 1, lrw, 0);
  v4u mtp;
  {
    float r0 = u2f(gm.x), r1 = u2f(gm.z), r2 = u2f(gn.x), r3 = u2f(gn.z);
#define RSTEP(c) r0 = dpp_add<c>(r0); PIN(r0); r1 = dpp_add<c>(r1); PIN(r1); r2 = dpp_add<c>(r2); PIN(r2); r3 = dpp_add<c>(r3); PIN(r3)
    RSTEP(0xB1); RSTEP(0x4E); RSTEP(0x141); RSTEP(0x140);
#undef RSTEP
    const int i0 = (int)pack2(r0, r1), i1 = (int)pack2(r2, r3);
    unsigned md[6];
#pragma unroll
    for (int k = 0; k < 6; ++k) md[k] = (unsigned)__builtin_amdgcn_readlane(k < 4 ? i0 : i1, 16 * (k & 3));
#define MOD(k) f32x2{bf_lo(md[k]), bf_hi(md[k])}
    const f32x2 one = {1.0f, 1.0f}, sr = one + MOD(4), cr = one + MOD(1);
    unsigned sp = pack2(sr.x, sr.y);
    const unsigned cp = pack2(cr.x, cr.y);
    sp |= ((sp & 0x7FFFu) ? 0u : 0x3080u) | ((sp & 0x7FFF0000u) ? 0u : 0x30800000u);
    const f32x2 S = {bf_lo(sp), bf_hi(sp)}, G = MOD(2) * S, A = f32x2{bf_lo(cp), bf_hi(cp)} * G, B = MOD(0) * G;
#undef MOD
    mtp = v4u{sel(lane, md[5], sp), tagA, md[3], tagB};
    auto rows4 = [&](const v4u& gx, const v4u& ga, const v4u& gb_, int i) __attribute__((always_inline)) {
      const f32x2 ma = {bf_lo(ga.x), bf_lo(ga.z)}, mb = {bf_lo(gb_.x), bf_lo(gb_.z)};
      const f32x2 ra_ = {__builtin_amdgcn_rcpf(bf_hi(ga.x)), __builtin_amdgcn_rcpf(bf_hi(ga.z))}, rb_ = {__builtin_amdgcn_rcpf(bf_hi(gb_.x)), __builtin_amdgcn_rcpf(bf_hi(gb_.z))};
      const f32x2 x0 = {bf_lo(gx.x), bf_lo(gx.y)}, x1 = {bf_lo(gx.z), bf_lo(gx.w)}, x2 = {bf_hi(gx.x), bf_hi(gx.y)}, x3 = {bf_hi(gx.z), bf_hi(gx.w)};
      const f32x2 h0 = __builtin_elementwise_fma((x0 - ma) * ra_, f32x2{A.x, A.x}, f32x2{B.x, B.x});
      const f32x2 h1 = __builtin_elementwise_fma((x1 - mb) * rb_, f32x2{A.x, A.x}, f32x2{B.x, B.x});
      const f32x2 h2 = __builtin_elementwise_fma((x2 - ma) * ra_, f32x2{A.y, A.y}, f32x2{B.y, B.y});
      const f32x2 h3 = __builtin_elementwise_fma((x3 - mb) * rb_, f32x2{A.y, A.y}, f32x2{B.y, B.y});
      *(LAS v2u*)(L + L_H + 480 + tid * 8 + i * 2048) = v2u{pack2(h0.x, h0.y), pack2(h1.x, h1.y)};
      *(LAS v2u*)(L + L_H + H_STRIDE + 480 + tid * 8 + i * 2048) = v2u{pack2(h2.x, h2.y), pack2(h3.x, h3.y)};
      const f32x2 y0 = x0 * f32x2{S.x, S.x}, y1 = x1 * f32x2{S.x, S.x}, y2 = x2 * f32x2{S.y, S.y}, y3 = x3 * f32x2{S.y, S.y};
      *(LAS f32x4*)(xbw + i * 2048) = f32x4{y0.x, y0.y, y1.x, y1.y};
      *(LAS f32x4*)(xbw + 8192 + i * 2048) = f32x4{y2.x, y2.y, y3.x, y3.y};
    };
    W1P(10, 2, lrw, 0); W1P(11, 3, lrw, 0);
    rows4(gx0, gs0, gs1, 0);
    W1P(12, 0, lrw, 12288); W1P(13, 1, lrw, 0); W1P(14, 2, lrw, 0); W1P(15, 3, lrw, 0);
    rows4(gx1, gs2, gs3, 1);
    W1P(16, 0, lsp, 0); W1P(17, 1, lsp, 0);
  }
  BAR();
  const unsigned tb = now();
  STAMP(6);

  v4u w1f[32], w2f[28][2], mt0, mt1, sl0, sl1;
  auto wave_late = [&]() __attribute__((always_inline)) {
    constexpr int ctl[5] = {0x4E, 0x124, 0x128, 0x142, 0x143}, rm[5] = {0xF, 0xF, 0xF, 0xA, 0xC};
    int v = max((int)(mt0.z - tb), (int)(mt1.z - tb));
    static_for<0, 5>([&](auto Ic) __attribute__((always_inline)) { v = max(v, __builtin_amdgcn_update_dpp(v, v, ctl[Ic.v], rm[Ic.v], 0xF, false)); });
    return min(__builtin_amdgcn_readlane(v, 63), 300);
  };
  auto send = [&]() __attribute__((always_inline)) {
    STAMPB(26);
    asm volatile("buffer_load_dwordx4 %0, %2, %3, %4 offen sc1\n\tbuffer_load_dwordx4 %1, %2, %3, %4 offen offset:1024 sc1"
                 :: "v"(sl0), "v"(sl1), "v"(l16), "s"(rs), "s"(slo) : "memory");
    static_for<0, 8>([&](auto Nc) __attribute__((always_inline)) {
      if constexpr ((Nc.v & 3) == 0) grp<Nc.v * 1024, Nc.v * 1024 - 65536>(so, xl, xs2);
      xfer<Nc.v & 3, true>(l16, rs, so);
    });
  };
  {
    const bool rh = w & 1;
    constexpr ConvOrder O = conv_order(), OL = conv_order_last();
    v2u A[4][8][2];
    v4u B[4][8];
    f32x4 acc[8], keep[4];
#pragma unroll
    for (int u = 0; u < 8; ++u) {
      A[0][u][0] = trd(ka + 512 * u + 8);
      A[0][u][1] = trd(ka + 512 * u);
      B[0][7 - u] = lds128(hb + 64 * (8 * (7 - u) + 3));
    }
    SB();
    if (lane) mtp.z = tb;
    stg(rs, tid < 2 ? tid * 16 : OOB, OFF_MT + id * 64, mtp);
    SB();
    auto hand_over = [&](auto Pc) __attribute__((always_inline)) {
      constexpr int i = Pc.v;
      PIN(acc[2 * i]); PIN(acc[2 * i + 1]);
      const f32x4 ev = acc[2 * i], od = acc[2 * i + 1];
      const f32x4 snd = rh ? ev : od, own = rh ? od : ev;
      *(LAS f32x4*)(exa + i * 1024) = snd;
#pragma unroll
      for (int c = 0; c < 4; ++c) { float t = keep[i][c] + own[c]; PIN(t); keep[i][c] = t; }
    };
    auto put_z = [&](auto Pc) __attribute__((always_inline)) {
      constexpr int i = Pc.v;
      const auto s0 = __builtin_amdgcn_permlane16_swap(pack2(keep[2 * i][0], keep[2 * i][1]), pack2(keep[2 * i + 1][0], keep[2 * i + 1][1]), false, false);
      const auto s1 = __builtin_amdgcn_permlane16_swap(pack2(keep[2 * i][2], keep[2 * i][3]), pack2(keep[2 * i + 1][2], keep[2 * i + 1][3]), false, false);
      stg(rs, za, zs + i * 65536, v4u{guard(s0[0]), guard(s1[0]), guard(s0[1]), guard(s1[1])});
    };
    static_for<0, 144>([&](auto Kc) __attribute__((always_inline)) {
      constexpr int K = Kc.v, r = K / 36, k = K % 36, t = r < 3 ? O.t[k] : OL.t[k], u = r < 3 ? O.u[k] : OL.u[k];
      if constexpr (k == 0 && r > 0) asm volatile("" :: "v"(B[r][7]));
      mfma16_v<r == 0 && u == 0>(acc[t], v4u{A[r][u][0].x, A[r][u][0].y, A[r][u][1].x, A[r][u][1].y}, B[r][t - u]);
      SB();
      if constexpr (K == 24) { WAITV(13); __builtin_amdgcn_s_barrier(); FRESH(); }
      if constexpr (K >= 24 && K < 36) W1PULL(K - 24, K - 24);
      if constexpr (K == 40) { asm volatile("" :: "a"(w1f[11])); __builtin_amdgcn_s_barrier(); FRESH(); }
      if constexpr (K == 60) { WAITV(9); __builtin_amdgcn_s_barrier(); FRESH(); }
      if constexpr (K >= 60 && K < 70) W1PULL(K - 48, K - 48);
      if constexpr (K == 76) { asm volatile("" :: "a"(w1f[21])); __builtin_amdgcn_s_barrier(); FRESH(); }
      if constexpr (K == 101) { WAITV(9); __builtin_amdgcn_s_barrier(); FRESH(); }
      if constexpr (K >= 101 && K < 109) W1PULL(K - 77, K - 101);
      if constexpr (K == 109) w1f[22] = trd2(w1x, w1x + 512);
      if constexpr (K == 110) w1f[23] = trd2(w1y, w1y + 512);
      if constexpr (r == 2 && k < 4) keep[k] = *(LAS f32x4*)(xbr + k * 1024);
      constexpr int n = k - (r < 2 ? 0 : 4);
      if constexpr (r < 3 && n >= 0 && n < 24) {
        if constexpr (n % 3 == 2) B[r + 1][n / 3] = lds128(hb + 64 * (8 * (n / 3) - r + 2));
        else A[r + 1][n / 3][n % 3] = trd(ka + 512 * (n / 3) + 64 * (r + 1) + 8 - 8 * (n % 3));
      }
      if constexpr (K == 108 + 10) hand_over(IC<0>{});
      if constexpr (K == 108 + 19) hand_over(IC<1>{});
      if constexpr (K == 108 + 30) hand_over(IC<2>{});
      if constexpr (K < 14 && K % 4 == 1) W1P(18 + K / 4, (2 + K / 4) & 3, lsp, 4096);
      if constexpr (K == 17) W1P(22, 0, lrx, 0);
      if constexpr (K == 21) W1P(23, 0, lto, 0);
      if constexpr (K >= 41 && K < 65 && K % 3 == 2) W1P(24 + (K - 41) / 3, ((K - 41) / 3) & 3, lrw, (K - 41) / 3 * 1024);
      if constexpr (K >= 66 && K < 98 && K % 4 == 2) W2P((K - 66) / 4, ((K - 66) / 4) & 3, lrw, 8192 + (K - 66) / 4 * 1024);
      if constexpr (K >= 98 && K < 122 && K % 4 == 2) W2P((K - 66) / 4, ((K - 98) / 4) & 3, lsp, (K - 98) / 4 * 1024);
      if constexpr (K == MTK)
        asm volatile("buffer_load_dwordx4 %0, %2, %3, 0 offen sc1\n\tbuffer_load_dwordx4 %1, %2, %3, 0 offen offset:2048 sc1"
                     : "=&v"(mt0), "=&v"(mt1) : "v"(mto), "s"(rs) : "memory");
      SB();
    });
    STAMP(7);
    asm volatile("s_nop 7" : "+v"(acc[6]), "+v"(acc[7]));
    hand_over(IC<3>{});
    BAR();
#pragma unroll
    for (int i = 0; i < 4; ++i) keep[i] += *(LAS f32x4*)(eya + i * 1024);
    put_z(IC<0>{});
    put_z(IC<1>{});
    SB();
  }
  STAMP(8);

  static_assert(OFF_MT == 0, "table offset");
  const unsigned tc = now();
  asm volatile("s_waitcnt vmcnt(%2)" : "+v"(mt0), "+v"(mt1) : "n"(2 + (MTK < 118 ? (121 - MTK) / 4 : 0)) : "memory");
  if (__builtin_expect(__any(tagbad(tagbad(0u, mt0, tagA, tagB), mt1, tagA, tagB) != 0u), 0)) {
    _Pragma("nounroll") for (unsigned spin = 0; spin < SPIN_CAP; ++spin) {
      FRESH();
      COUNT(21);
      if (tagbad(0u, mt0, tagA, tagB)) mt0 = ldg(rs, mto, OFF_MT);
      if (tagbad(0u, mt1, tagA, tagB)) mt1 = ldg(rs, mto, OFF_MT + 2048);
      if (!__any(tagbad(tagbad(0u, mt0, tagA, tagB), mt1, tagA, tagB) != 0u)) break;
      PAUSE();
    }
  }
  *(LAS unsigned*)(mtw) = mt0.x; *(LAS unsigned*)(mtw + 128) = mt1.x; *(LAS unsigned*)(mtw + 1024) = mt0.z; *(LAS unsigned*)(mtw + 1152) = mt1.z;
  int wait = wave_late() + ZLAG;
  *(LAS int*)(L + L_ETA + w * 4) = wait;
  v4u hq[56], rc[4], res[2], sx[8];
  u64 nanv = 0;
  const int c0 = 7 * w + rot, c3 = c0 - 7 * (rot >> 2), c4 = c0 - 7 * ((rot + 1) >> 2);
  const int hb0 = OFF_HID + (rp * 4 + rg) * 114688, kb0 = OFF_CK + (rp * 4 + rg) * 4096 + w * 896, ckv = lane < 56 ? l16 : OOB;
#define CHUNK(n) (((n) % 28 / 4 < 3 ? c0 : (n) % 28 / 4 == 3 ? c3 : c4) + (n) % 28 / 4)
#define HFRAG(n) ldg(rs, l16 + ((n) & 3) * 1024, hb0 + CHUNK(n) * 4096 + ((n) / 28) * (2 * 114688))
#define HLOAD(n) hq[n] = HFRAG(n)
  const int hoff = OFF_HID + (rp * 4 * 112 + ct * 8 + 2 * w) * 1024;
  const int cko = OFF_CK + rp * 16384 + (ct * 4 + w) * 64;
#define CKPUT(sum, u) __builtin_amdgcn_raw_buffer_store_b32(ckey - red_step<1>(red_step<0>(sum)), rs, ckw, cko + ((u) ^ par) * 4096, SC1)
  const int sxo = OFF_SX + (rp * 4 + w) * 8192;
  int hv0, hv1, ckw;
  unsigned ckey;
  LAS unsigned char *xr[2], *rr, *rr2, *str, *stw, *w2a, *w2b;
  LAS unsigned char *w2sa = pa + (lsp - lrw - 8192), *w2sb = pb + (lsp - lrw - 8192);
  LAS unsigned char* uw = L + L_UVT + hf * 512 + (32 * w + (lane & 31)) * 4;
  LAS unsigned char* uvp = L + L_UVT + (32 * w + 4 * hf) * 4;
  int sxw = sel(lane & 8, OOB, (p4 * 8 + hf * 4 + blk * 2 + ((lane >> 2) & 1)) * 16);
  xr[0] = L + L_XR + tra;
  PIN(w2sa); PIN(w2sb); PIN(uw); PIN(uvp); PIN(sxw); PIN(xr[0]);
  BAR();
  STAMP(9);
  f32x4 uu[4], vv[4];
  {
    const v4u lt = lds128(L + L_ETA);
    const unsigned j0 = *(LAS u16*)(L + L_TS + (si * 32 + (lane >> 2)) * 2), j1 = *(LAS u16*)(L + L_TS + (si * 32 + 16 + (lane >> 2)) * 2);
    v4u qa[4];
#pragma unroll
    for (int i = 0; i < 4; ++i) qa[i] = lds128(qr + i * 256);
    SB();
    WAITV(2);
    asm volatile("" : "=v"(sl0), "=v"(sl1));
    wait = __builtin_amdgcn_readfirstlane(max(max((int)lt.x, (int)lt.y), max((int)lt.z, (int)lt.w)));
    bool sent = false;
    f32x4 cu[4];
    static_for<0, 32>([&](auto Sc) __attribute__((always_inline)) {
      constexpr int s = Sc.v;
      if constexpr (s == 0 || s == 16) {
        if (!sent && (int)(now() - tc) >= wait) { sent = true; send(); }
        SB();
      }
      mfma4_va<(s < 2), s & 7>(cu[2 * (s & 1)], cu[2 * (s & 1) + 1], qa[s >> 3], w1f[s]);
      if constexpr (s < 28 && (s & 1)) {
        constexpr int n = s >> 1;
        LAS unsigned char* src = (n < 8 ? ((n & 1) ? pb : pa) : ((n & 1) ? w2sb : w2sa)) + ((n >> 1) & 3) * 2048;
        w2f[n >> 1][n & 1] = trd2(src, src + 1024);
      }
      SB();
    });
    float i0 = __builtin_amdgcn_rcpf(bf_lo(j0)), i1 = __builtin_amdgcn_rcpf(bf_lo(j1));
    PIN(i0); PIN(i1);
    asm volatile("s_nop 3" : "+v"(cu[0]), "+v"(cu[1]), "+v"(cu[2]), "+v"(cu[3]));
    float un_ = (cu[0][0] + cu[1][0]) + (cu[2][0] + cu[3][0]);
    swap_add32(un_, -LOG2E * ((cu[0][1] + cu[1][1]) + (cu[2][1] + cu[3][1])));
    *(LAS float*)uw = un_;
    SB();
    if (!sent) {
      while ((int)(now() - tc) < wait) { }
      send();
    }
    static_for<0, 8>([&](auto Nc) __attribute__((always_inline)) {
      if constexpr ((Nc.v & 3) == 0) grp<32768 + Nc.v * 1024, Nc.v * 1024 - 65536>(so, xl, xs3);
      xfer<Nc.v & 3, true>(l16, rs, so);
    });
    STAMPB(27);
    asm volatile("s_waitcnt vmcnt(16)" : "+v"(sl0), "+v"(sl1) :: "memory");
    STAMPB(28);
    if (__builtin_expect(__any(max4(max4(0u, sl0), sl1) == POISON), 0)) {
      _Pragma("nounroll") for (unsigned spin = 0; spin < SPIN_CAP; ++spin) {
        FRESH();
        if (spin) PAUSE();
        COUNT(22);
        sl0 = ldg(rs, l16, slo);
        sl1 = ldg(rs, l16 + 1024, slo);
        if (!__any(max4(max4(0u, sl0), sl1) == POISON)) break;
      }
    }
    float sm[8], sq[8];
#pragma unroll
    for (int i = 0; i < 4; ++i) {
      const f32x2 a = f32x2{bf_lo(sl0[i]), bf_hi(sl0[i])} * f32x2{i0, i0}, c = f32x2{bf_lo(sl1[i]), bf_hi(sl1[i])} * f32x2{i1, i1};
      const f32x2 s2 = a + c, q2 = __builtin_elementwise_fma(a, a, c * c);
      sm[2 * i] = s2.x; sm[2 * i + 1] = s2.y; sq[2 * i] = q2.x; sq[2 * i + 1] = q2.y;
    }
#pragma unroll
    for (int i = 0; i < 4; ++i) { swap_add32(sm[i], sm[i + 4]); swap_add32(sq[i], sq[i + 4]); }
#pragma unroll
    for (int i = 0; i < 2; ++i) { swap_add16(sm[i], sm[i + 2]); swap_add16(sq[i], sq[i + 2]); }
#pragma unroll
    for (int i = 0; i < 2; ++i) { sm[i] = dpp_add<0x124>(dpp_add<0x128>(sm[i])); sq[i] = dpp_add<0x124>(dpp_add<0x128>(sq[i])); }
    const bool od = lane & 4;
    stg(rs, sxw, OFF_SX + ((rp * 4 + w) * 16 + si) * 512,
        v4u{f2u(od ? sm[1] : sm[0]), tagA, f2u(od ? sq[1] : sq[0]), tagB});
    SB();
  }
  STAMPB(29);
  asm volatile("" :: "a"(w2f[6][1]));
  W2P(14, 0, lsp, 0); W2P(15, 1, lsp, 0); W2P(16, 2, lsp, 0); W2P(17, 3, lsp, 0); W2P(18, 0, lsp, 4096); W2P(19, 1, lsp, 0);
  WAITV(15);
  STAMP(10);
  BAR();
  STAMP(11);

#define XA(t) (xr[(t) >> 6] + ((t) & 63) * 1024)
#define XF(t) trd2(XA(t), XA(t) + 256)
#define XEN
#define REDO(p) \
  if (XEN __builtin_expect(nanv != 0, 0)) { \
    unsigned m0s; \
    asm volatile("s_mov_b32 %0, m0" : "=s"(m0s)); \
    for (unsigned spin = 0; spin < SPIN_CAP / 16; ++spin) { \
      COUNT(22); \
      _Pragma("nounroll") for (int i = 0; i < 8; ++i) dma_x4(xg + ((p) ^ par) * 32768 + i * 4096, l16, L_XR + (p) * 32768 + i * 4096); \
      WAITV(0); \
      _Pragma("unroll") for (int s = 0; s < 32; ++s) { \
        const v4u t = XF(32 * (p) + s); \
        if (s == 0) mfma_av<true>(acc[(p) % 3], w1f[s], t); else mfma_av<false>(acc[(p) % 3], w1f[s], t); \
        SB(); \
      } \
      mfma_done(acc[(p) % 3]); \
      if (!__any(!(acc[(p) % 3][0] == acc[(p) % 3][0]))) break; \
    } \
    set_m0(m0s); \
  }
  f32x16 acc[3];
  f32x2 ec[4];
  float ep[48], ee[48], ey[48], tl[16];
  unsigned cs = 0, sbad = 0;
  float ssm = 0.f, ssq = 0.f;
  v4u x[128], eo;
#pragma unroll
  for (int t = 0; t < 4; ++t) x[t] = XF(t);
  SB();
  auto eslot = [&](auto Sc) __attribute__((always_inline)) {
    static_for<0, 48>([&](auto Vc) __attribute__((always_inline)) {
      constexpr int V = Vc.v, k = V & 15, st = Sc.v - epi_slot(V);
      if constexpr (st == 6 && (k & 1)) {
        unsigned o = pack2(ey[V - 1], ey[V]);
        PIN(o);
        eo[(k >> 1) & 3] = o;
        if constexpr (k == 7) { stg(rs, hv0, hoff + ((V >> 4) ^ par) * 114688, eo); cs = sum4(0u, eo); PIN(cs); }
        if constexpr (k == 15) { stg(rs, hv1, hoff + ((V >> 4) ^ par) * 114688, eo); CKPUT(sum4(cs, eo), V >> 4); }
      }
      if constexpr (st == 5) { ey[V] = ep[V] * ee[V]; PIN(ey[V]); }
      if constexpr (st == 4) { ee[V] = __builtin_amdgcn_rcpf(ee[V]); PIN(ee[V]); }
      if constexpr (st == 3) { ee[V] = 1.0f + ee[V]; PIN(ee[V]); }
      if constexpr (st == 2) { ee[V] = __builtin_amdgcn_exp2f(ep[V]); PIN(ee[V]); }
      if constexpr (st == 1) { ep[V] = __builtin_fmaf(ec[V >> 4].x, acc[(V >> 4) % 3][k], ep[V]); PIN(ep[V]); }
      if constexpr (st == 0) { ep[V] = __builtin_fmaf(ec[V >> 4].y, uu[k >> 2][k & 3], vv[k >> 2][k & 3]); PIN(ep[V]); }
    });
    SB();
  };
  static_for<0, 64>([&](auto Jc) __attribute__((always_inline)) {
    constexpr int J = Jc.v, p = J >> 4, j = J & 15;
    asm volatile("" :: "v"(x[2 * J + 1]));
    mfma_av<j == 0>(acc[p % 3], w1f[2 * j], x[2 * J]);
    if constexpr (J < SXJ || (J >= SXJ + 4 && J < 20)) {
      constexpr int n = J < SXJ ? J : J - 4;
      if constexpr ((n & 3) == 0) grp<65536 + (n >> 3) * 32768 + (n & 4) * 1024, (n & 4) * 1024>(so, xl, n < 8 ? xs2 : xs3);
      xfer<n & 3, true>(l16, rs, so);
    }
    if constexpr (J >= SXJ && J < SXJ + 4)
      asm volatile("buffer_load_dwordx4 %0, %2, %3, %4 offen offset:%5 sc1\n\tbuffer_load_dwordx4 %1, %2, %3, %4 offen offset:%6 sc1"
                   : "=&v"(sx[2 * (J - SXJ)]), "=&v"(sx[2 * (J - SXJ) + 1])
                   : "v"(l16), "s"(rs), "s"(sxo + ((J - SXJ) & 2) * 2048), "n"(((J - SXJ) & 1) * 2048), "n"(((J - SXJ) & 1) * 2048 + 1024) : "memory");
    if constexpr (J == 43) asm volatile("" :: "a"(w2f[13][1]));
    if constexpr (J >= 20 && J < 28) W2P(J, J & 3, xl, (J - 20) * 1024);
    if constexpr (J >= 28 && J < 34) W2P(J, J & 3, lsp, (J - 28) * 1024);
    if constexpr (J >= 35 && J < 43) W2P(J - 1, (J - 35) & 3, xl, 32768 + (J - 35) * 1024);
    if constexpr (J >= 43 && J < 51) W2P(J - 1, (J - 43) & 3, xl, (J - 43) * 1024);
    static_for<hc_upto(J), hc_upto(J + 1)>([&](auto Nc) __attribute__((always_inline)) { HLOAD(Nc.v); });
    SB();
    constexpr bool sym = epi_pair(J) || J < 19 || J > 60;
    if constexpr (J < 62) { x[2 * J + 4] = XF(2 * J + 4); if constexpr (!sym) x[2 * J + 5] = XF(2 * J + 5); }
    if constexpr (J >= 38 && J < 42) w2f[10 + J - 38][0] = trd2(w2a + (J - 38) * 2048, w2a + (J - 38) * 2048 + 1024);
    SB();
    if constexpr (epi_pair(J)) eslot(IC<2 * J>{});
    mfma_av<false>(acc[p % 3], w1f[2 * j + 1], x[2 * J + 1]);
    if constexpr (sym && J < 62) x[2 * J + 5] = XF(2 * J + 5);
    if constexpr (J >= 38 && J < 42) w2f[10 + J - 38][1] = trd2(w2b + (J - 38) * 2048, w2b + (J - 38) * 2048 + 1024);
    if constexpr (J >= 19 && J < 25) w2f[7 + ((J - 19) >> 1)][(J - 19) & 1] = trd2((((J - 19) & 1) ? w2sb : w2sa) + ((J - 19) >> 1) * 2048, (((J - 19) & 1) ? w2sb : w2sa) + ((J - 19) >> 1) * 2048 + 1024);
    SB();
    if constexpr (J == 10) { waitv<c_between(-1, 10)>(); __builtin_amdgcn_s_barrier(); FRESH(); }
    if constexpr (J == 18) waitv<c_between(7, 18)>();
    if constexpr (J == 34) waitv<c_between(19, 34)>();
    if constexpr (J == 37) waitv<c_between(27, 37)>();
    if constexpr (J == 1) { xr[1] = xr[0] + 65536; rr = xr[0] + (4 * dt + w) * 1024; rr2 = rr + 65536; PIN(xr[1]); PIN(rr); PIN(rr2); }
    if constexpr (J == 2) { hv0 = dt == 7 ? OOB : l16; hv1 = dt == 7 ? OOB : l16 + 1024; PIN(hv0); PIN(hv1); }
    if constexpr (J == 3) { ckw = (p4 || dt == 7) ? OOB : lane; ckey = lane ? 0u : tagA; PIN(ckw); PIN(ckey); }
    if constexpr (J == 4) { str = L + L_STAT + (lane & 31) * 8; stw = str + (w ^ par) * 256; PIN(str); PIN(stw); }
    if constexpr (J == 5) { w2a = pa + (xl - lrw - 8192); w2b = pb + (xl - lrw - 8192); PIN(w2a); PIN(w2b); }
    if constexpr (J >= 6 && J < 10) { uu[J - 6] = *(LAS f32x4*)(uvp + (J - 6) * 32); vv[J - 6] = *(LAS f32x4*)(uvp + 512 + (J - 6) * 32); }
    if constexpr (J == EO - 4 || J == EO - 3) {
      constexpr int i = 4 * (J - EO + 4);
      asm volatile("s_waitcnt vmcnt(%4)" : "+v"(sx[i]), "+v"(sx[i + 1]), "+v"(sx[i + 2]), "+v"(sx[i + 3]) : "n"(c_between(SXJ + 1 + i / 2, J)) : "memory");
      unsigned b0 = tagbad(0u, sx[i], tagA, tagB), b1 = tagbad(0u, sx[i + 1], tagA, tagB);
      float m0 = u2f(sx[i].x) + u2f(sx[i + 1].x), m1 = u2f(sx[i + 2].x) + u2f(sx[i + 3].x);
      PIN(m0); PIN(m1);
      float q0 = u2f(sx[i].z) + u2f(sx[i + 1].z), q1 = u2f(sx[i + 2].z) + u2f(sx[i + 3].z);
      PIN(q0); PIN(q1);
      sbad |= tagbad(b0, sx[i + 2], tagA, tagB) | tagbad(b1, sx[i + 3], tagA, tagB);
      ssm += m0 + m1; PIN(ssm);
      ssq += q0 + q1; PIN(ssq);
      PIN(sbad);
      SB();
    }
    if constexpr (J == EO - 2) {
      if (__builtin_expect(__any(sbad != 0u), 0)) {
        _Pragma("nounroll") for (unsigned spin = 0; spin < SPIN_CAP; ++spin) {
          FRESH();
          if (spin) PAUSE();
          COUNT(23);
          sbad = 0; ssm = 0.f; ssq = 0.f;
#pragma unroll
          for (int i = 0; i < 8; ++i) sx[i] = ldg(rs, l16 + (i & 3) * 1024, sxo + (i >> 2) * 4096);
#pragma unroll
          for (int i = 0; i < 8; i += 4) {
            sbad = tagbad(tagbad(tagbad(tagbad(sbad, sx[i], tagA, tagB), sx[i + 1], tagA, tagB), sx[i + 2], tagA, tagB), sx[i + 3], tagA, tagB);
            ssm += (u2f(sx[i].x) + u2f(sx[i + 1].x)) + (u2f(sx[i + 2].x) + u2f(sx[i + 3].x));
            ssq += (u2f(sx[i].z) + u2f(sx[i + 1].z)) + (u2f(sx[i + 2].z) + u2f(sx[i + 3].z));
          }
          if (!__any(sbad != 0u)) break;
        }
      }
      const float mean = swap32_add(ssm) * (1.0f / 512.0f);
      const float ms = __builtin_fmaf(swap32_add(ssq), 1.0f / 512.0f, 1e-5f);
      const float a = -LOG2E * __builtin_amdgcn_rsqf(fmaxf(__builtin_fmaf(-mean, mean, ms), 1e-5f));
      *(LAS f32x2*)(stw) = f32x2{a, -a * mean};
      SB();
    }
    if constexpr (J == EO - 1) {
      asm volatile("s_waitcnt lgkmcnt(4)" ::: "memory");
      __builtin_amdgcn_s_barrier();
      FRESH();
#pragma unroll
      for (int i = 0; i < 4; ++i) ec[i] = *(LAS f32x2*)(str + i * 256);
      SB();
    }
    if constexpr (!epi_pair(J)) eslot(IC<2 * J>{});
    eslot(IC<2 * J + 1>{});
    if constexpr (J == 16) asm volatile("s_nop 4");
    if constexpr (J >= 61) {
#pragma unroll
      for (int k = (J - 61) * 6; k < (J == 63 ? 16 : (J - 60) * 6); ++k) { tl[k] = __builtin_fmaf(ec[3].y, uu[k >> 2][k & 3], vv[k >> 2][k & 3]); PIN(tl[k]); }
      SB();
    }
    if constexpr (j == 0 && p >= 1) { PIN(acc[(p - 1) % 3]); nanv = __builtin_amdgcn_ballot_w64(!(acc[(p - 1) % 3][0] == acc[(p - 1) % 3][0])); asm volatile("" : "+s"(nanv)); }
    if constexpr (j == 1 && p >= 1) {
      if constexpr (p == 1) REDO(0)
      if constexpr (p == 2) REDO(1)
      if constexpr (p == 3) REDO(2)
      rc[p - 1] = trd2((p == 3 ? rr2 : rr) + ((p - 1) & 1) * 32768, (p == 3 ? rr2 : rr) + ((p - 1) & 1) * 32768 + 256);
    }
    if constexpr (j == 2 && p >= 1) {
      asm volatile("s_waitcnt lgkmcnt(4)" ::: "memory");
      __builtin_amdgcn_s_barrier();
      FRESH();
    }
    SB();
    if constexpr (J == 15) STAMP(16);
    if constexpr (J == 31) STAMP(17);
    if constexpr (J == 47) STAMP(18);
  });
  STAMP(12);
  static_assert(EO <= 33 && SXJ >= 8 && SXJ <= 16 && MTK > 101 && hc_upto(64) <= 24 && hc_upto(57) <= 20 && epi_slot(31) < epi_slot(32) && epi_slot(16) + 1 >= 2 * 34 && epi_slot(32) + 1 >= 2 * 50 && epi_slot(47) + 6 < 128, "schedule");
  static_for<hc_upto(64), hc_upto(64) + HT0>([&](auto Ic) __attribute__((always_inline)) { HLOAD(Ic.v); });
  SB();
  asm volatile("s_nop 7" : "+v"(acc[0]));
  nanv = __builtin_amdgcn_ballot_w64(!(acc[0][0] == acc[0][0]));
  REDO(3)
#pragma unroll
  for (int h = 0; h < 2; ++h) {
    v4u o;
#pragma unroll
    for (int i = 0; i < 4; ++i) {
      const int k = 8 * h + 2 * i;
      const f32x2 t = {tl[k], tl[k + 1]};
      const f32x2 pp = __builtin_elementwise_fma(f32x2{ec[3].x, ec[3].x}, f32x2{acc[0][k], acc[0][k + 1]}, t);
      const f32x2 d = f32x2{__builtin_amdgcn_exp2f(pp.x), __builtin_amdgcn_exp2f(pp.y)} + f32x2{1.0f, 1.0f};
      const f32x2 y = pp * f32x2{__builtin_amdgcn_rcpf(d.x), __builtin_amdgcn_rcpf(d.y)};
      o[i] = pack2(y.x, y.y);
    }
    PIN(o);
    if (h == 0) static_for<hc_upto(64) + HT0, hc_upto(64) + HT0 + HT1>([&](auto Ic) __attribute__((always_inline)) { HLOAD(Ic.v); });
    stg(rs, h ? hv1 : hv0, hoff + (3 ^ par) * 114688, o);
    if (h) CKPUT(sum4(cs, o), 3); else cs = sum4(0u, o);
    SB();
  }
  static_for<hc_upto(64) + HT0 + HT1, 28>([&](auto Ic) __attribute__((always_inline)) { HLOAD(Ic.v); });
  rc[3] = trd2(rr2 + 32768, rr2 + 32768 + 256);
  SB();
  STAMP(13);

  f32x16 d[2][2];
  LAS unsigned char* redw = L + L_XR + w * 8192 + l16;
  LAS unsigned char* redr = L + L_XR + w * 2048 + l16;
#define REDW(un, q) *(LAS f32x4*)(redw + (un) * 32768 + (q) * 1024) = \
    f32x4{d[un][(q) >> 2][4 * ((q) & 3)], d[un][(q) >> 2][4 * ((q) & 3) + 1], d[un][(q) >> 2][4 * ((q) & 3) + 2], d[un][(q) >> 2][4 * ((q) & 3) + 3]}
  LAS unsigned char *w2ua = w2a + 65536, *w2ub = w2b + 65536;
  PIN(w2ua); PIN(w2ub);
#define CHECK(un) \
  if (__builtin_expect((unsigned)__builtin_amdgcn_readlane((int)m[un], 63) != 14u * tagA, 0)) { \
    _Pragma("nounroll") for (unsigned spin = 0; spin < SPIN_CAP; ++spin) { \
      FRESH(); \
      COUNT(24 + (un)); \
      v4u qq[28]; \
      const v4u kk = ldg(rs, ckv, kb0 + (un) * 8192); \
      static_for<0, 12>([&](auto Nc) __attribute__((always_inline)) { qq[Nc.v] = HFRAG(28 * (un) + Nc.v); }); \
      SB(); \
      unsigned t = sum4(0u, kk); \
      static_for<0, 7>([&](auto Cc) __attribute__((always_inline)) { \
        constexpr int c = Cc.v; \
        const v4u* q = &qq[4 * c]; \
        if constexpr (c + 3 < 7) { _Pragma("unroll") for (int i = 0; i < 4; ++i) qq[4 * c + 12 + i] = ldg(rs, l16 + i * 1024, hb0 + CHUNK(4 * c + 12) * 4096 + (un) * (2 * 114688)); } \
        SB(); \
        t = sum8(sum8(t, q[0], q[1]), q[2], q[3]); \
        mfma_av<c == 0>(d[un][0], w2f[4 * c][0], q[0]); mfma_av<c == 0>(d[un][1], w2f[4 * c][1], q[0]); \
        _Pragma("unroll") for (int i = 1; i < 4; ++i) { mfma_av<false>(d[un][0], w2f[4 * c + i][0], q[i]); mfma_av<false>(d[un][1], w2f[4 * c + i][1], q[i]); } \
        SB(); \
      }); \
      mfma_done(d[un][1]); \
      PIN(d[un][0]); \
      if ((unsigned)__builtin_amdgcn_readlane((int)red_all(t), 63) == 14u * tagA) break; \
      PAUSE(); \
    } \
    if (un) { _Pragma("unroll") for (int q = 0; q < 4; ++q) REDW(1, q); } \
  }
  unsigned m[2] = {0u, 0u};
  v4u gbv, scv, ck[2];
  f32x4 pr[8];
  float gb[8], rx[2][8];
  unsigned orow = (rp * 64 + ((lane & 31) >> 3) * 16 + rg * 8 + (lane & 7)) * 1024 + dt * 128 + w * 32 + hf * 16;
  static_for<0, 112>([&](auto Xc) __attribute__((always_inline)) {
    constexpr int X = Xc.v, I = X < 100 ? X >> 1 : 50 + (X - 100) % 6, e = X < 100 ? X & 1 : (X - 100) / 6, un = I / 28;
    mfma_av<I % 28 == 0>(d[un][e], w2f[I % 28][e], hq[I]);
    SB();
    if constexpr (X < 100 && e == 0) {
      if constexpr (I < 3) PULL(14 + I, w2sa, w2sb, I * 2048);
      if constexpr (I >= 3 && I < 7) PULL(14 + I, w2a, w2b, 32768 + (I - 3) * 2048);
      if constexpr (I >= 7 && I < 11) PULL(14 + I, w2a, w2b, (I - 7) * 2048);
      if constexpr (I == 21) waitv<hl_upto(21) - 28 + 1>();
      if constexpr (I >= 21 && I < 24) PULL(4 + I, w2ua, w2ub, (I - 21) * 2048);
      if constexpr (I == 28) acc4(m[0], ck[0]);
      if constexpr (I >= 29 && I < 35) { m[0] = red_step<I - 29>(m[0]); PIN(m[0]); }
      if constexpr (I == 35) CHECK(0)
      if constexpr (I >= 35 && I < 43) REDW(0, I - 35);
    }
    if constexpr (X < 100 && e == 1) {
      if constexpr (I < 6) W2P(50 + I, I & 3, xl, 65536 + I * 1024);
      static_for<hl_upto(I), hl_upto(I + 1)>([&](auto Nc) __attribute__((always_inline)) { HLOAD(Nc.v); });
      if constexpr (I == 6) ck[0] = ldg(rs, ckv, kb0);
      if constexpr (I == HL1 + 16 / HLR) ck[1] = ldg(rs, ckv, kb0 + 8192);
    }
    if constexpr (X < 100 ? e == 1 : e == 0) acc4(m[un], hq[I]);
    if constexpr (X == 105) acc4(m[1], ck[1]);
    if constexpr (X >= 106) { m[1] = red_step<X - 106>(m[1]); PIN(m[1]); }
    if constexpr (X == 23 || X == 24) { const v4u ev = rc[2 * (X - 23)], od = rc[2 * (X - 23) + 1]; res[X - 23] = (rg ^ par) ? od : ev; PIN(res[X - 23]); }
    if constexpr (X == 22) { gbv = lds128(L + L_TG + (64 * dt + 16 * w + 8 * hf) * 2); scv = lds128(L + L_TS + (64 * dt + 16 * w + 8 * hf) * 2); PIN(orow); }
    if constexpr (X >= 26 && X < 42 && (X & 1) == 0) {
      constexpr int q = (X - 26) >> 1;
      const float inv = __builtin_amdgcn_rcpf((q & 1) ? bf_hi(scv[q >> 1]) : bf_lo(scv[q >> 1]));
      gb[q] = -LN2 * ((q & 1) ? bf_hi(gbv[q >> 1]) : bf_lo(gbv[q >> 1]));
      rx[0][q] = inv * ((q & 1) ? bf_hi(res[0][q >> 1]) : bf_lo(res[0][q >> 1]));
      rx[1][q] = inv * ((q & 1) ? bf_hi(res[1][q >> 1]) : bf_lo(res[1][q >> 1]));
      PIN(gb[q]); PIN(rx[0][q]); PIN(rx[1][q]);
    }
    if constexpr (X == 86) BAR();
    if constexpr (X == 86 || X == 88) {
#pragma unroll
      for (int k = X - 86; k < X - 84; ++k) { pr[2 * k] = *(LAS f32x4*)(redr + k * 8192); pr[2 * k + 1] = *(LAS f32x4*)(redr + k * 8192 + 1024); }
    }
    if constexpr (X >= 91 && X < 99) {
      constexpr int q = X - 91;
      float t = (pr[q >> 2][q & 3] + pr[2 + (q >> 2)][q & 3]);
      PIN(t);
      float t2_ = (pr[4 + (q >> 2)][q & 3] + pr[6 + (q >> 2)][q & 3]);
      PIN(t2_);
      rx[0][q] = __builtin_fmaf(gb[q], t + t2_, rx[0][q]);
      PIN(rx[0][q]);
    }
    if constexpr (X == 99)
      { const v4u o_ = v4u{pack2(rx[0][0], rx[0][1]), pack2(rx[0][2], rx[0][3]), pack2(rx[0][4], rx[0][5]), pack2(rx[0][6], rx[0][7])}; __builtin_amdgcn_raw_buffer_store_b128(o_, __builtin_amdgcn_make_buffer_rsrc((void*)out, 0, 2097152, 0x00020000), orow, 0, 16); asm volatile("s_nop 1" :: "v"(o_)); }
    if constexpr (X == 84) __builtin_amdgcn_raw_buffer_store_b32(epoch + 1u, rs, (b | tid) ? OOB : 0, OFF_EP, SC1);
    if constexpr (X == 108) asm volatile("s_nop 3" : "+v"(d[1][0]));
    if constexpr (X >= 108) REDW(1, X - 108);
    SB();
    if constexpr (X == 55) STAMP(14);
  });
  STAMP(19);
  asm volatile("s_nop 7" : "+v"(d[1][1]));
  CHECK(1)
#pragma unroll
  for (int q = 4; q < 8; ++q) REDW(1, q);
  BAR();
  {
#pragma unroll
    for (int k = 0; k < 8; ++k) pr[k] = *(LAS f32x4*)(redr + (k >> 1) * 8192 + 32768 + (k & 1) * 1024);
    SB();
    const f32x4 s0 = (pr[0] + pr[2]) + (pr[4] + pr[6]), s1 = (pr[1] + pr[3]) + (pr[5] + pr[7]);
    float y[8];
#pragma unroll
    for (int q = 0; q < 8; ++q) y[q] = __builtin_fmaf(gb[q], q < 4 ? s0[q & 3] : s1[q & 3], rx[1][q]);
    { const v4u o_ = v4u{pack2(y[0], y[1]), pack2(y[2], y[3]), pack2(y[4], y[5]), pack2(y[6], y[7])}; __builtin_amdgcn_raw_buffer_store_b128(o_, __builtin_amdgcn_make_buffer_rsrc((void*)out, 0, 2097152, 0x00020000), orow, 1048576, 0); asm volatile("s_nop 1" :: "v"(o_)); }
  }
  STAMP(15);
}

extern "C" void kernel_launch(void* const* d_in, const int* in_sizes, int n_in, void* d_out, int out_size, void* d_ws, size_t ws_size,
                              hipStream_t stream) {
  (void)in_sizes; (void)n_in; (void)out_size; (void)ws_size;
  (void)hipFuncSetAttribute((const void*)s4_block_kernel, hipFuncAttributeMaxDynamicSharedMemorySize, LDS_BYTES);
  s4_block_kernel<<<256, 256, LDS_BYTES, stream>>>((cptr)d_in[0], (cptr)d_in[1], (cptr)d_in[2], (cptr)d_in[3], (cptr)d_in[4], (cptr)d_in[5], (cptr)d_in[6],
                                                   (cptr)d_in[7], (cptr)d_in[8], (cptr)d_in[10], (cptr)d_in[11], (cptr)d_in[12], (cptr)d_in[13],
                                                   (unsigned char*)d_out, (unsigned char*)d_ws, (cptr)d_in[9]);
}
```

```cpp
#define ECHO 11
#include <hip/hip_runtime.h>

#define GAS __attribute__((address_space(1)))
#define LAS __attribute__((address_space(3)))
#define DI __device__ __forceinline__
typedef unsigned v4u __attribute__((ext_vector_type(4)));
typedef unsigned v2u __attribute__((ext_vector_type(2)));
typedef float f32x2 __attribute__((ext_vector_type(2)));
typedef float f32x4 __attribute__((ext_vector_type(4)));
typedef float f32x16 __attribute__((ext_vector_type(16)));
typedef __bf16 bf16x2 __attribute__((ext_vector_type(2)));
typedef __bf16 bf16x8 __attribute__((ext_vector_type(8)));
typedef short s16x4 __attribute__((ext_vector_type(4)));
typedef unsigned short u16;
typedef unsigned long long u64;

constexpr int SET_BASE = 0;
constexpr int OFF_EP = 49152;
constexpr int OFF_MT = 0;
constexpr int OFF_SG = 32768;
constexpr int OFF_MG = 65536;
constexpr int OFF_SX = 524288;
constexpr int OFF_XC = 458752;
constexpr int OFF_XG = 1 << 20;
constexpr int OFF_Z = OFF_XG + (2 << 20);
constexpr int OFF_HID = OFF_Z + 2097152;
constexpr int OFF_CK = OFF_HID + 7340032;
constexpr int SET_BYTES = OFF_CK + 262144;
static_assert(OFF_MG + 393216 <= OFF_XC && OFF_XC + 65536 <= OFF_SX, "set size");

constexpr int L_TS = 0, L_TB = 1024, L_TG = 2048;
constexpr int L_UVT = 3072;
constexpr int L_M = 4096;
constexpr int L_UVS = L_M;
constexpr int UVS_ROW = 544, UVS_CH = 57 * UVS_ROW;
constexpr int L_KC = L_M;
constexpr int KC_STRIDE = 4160;
constexpr int L_H = L_KC + 8 * KC_STRIDE;
constexpr int H_STRIDE = 4576;
constexpr int L_XB = L_H + 2 * H_STRIDE;
constexpr int L_RX = L_XB + 16384;
constexpr int L_ETA = L_RX + 4096;
constexpr int L_R = L_M + 65536;
constexpr int L_TOP = L_M + 131072 + 24576;
constexpr int L_KT = L_TOP + 16;
constexpr int L_STAT = L_TOP;
constexpr int L_XR = L_M;
constexpr int L_SP = L_M + 131072;
constexpr int LDS_BYTES = 163840;
static_assert(L_TOP + 4096 <= LDS_BYTES && L_ETA + 16 <= L_R && L_UVS + 2 * UVS_CH <= L_R && L_SP + 24576 <= L_TOP, "LDS map");
static_assert(L_H % 16 == 0 && L_XB % 16 == 0 && L_RX % 16 == 0 && L_KT % 16 == 0, "alignment");

#define ZLAG 23
#define MTK 112
#define SXJ 16
#define EOV 30
#define HC0 55
#define HC1 61
#define HL0 9
#define HL1 21
#define HLR 1
#define HT0 2
#define HT1 6
constexpr int EO = EOV;
constexpr int CODE_BYTES = 35072;
constexpr int SC1 = 16;
constexpr int OOB = 0x7FFFFF00;
constexpr unsigned SPIN_CAP = 1u << 15;
constexpr unsigned POISON = 0xFFFFFFFFu;
constexpr float LOG2E = 1.4426950408889634f, LN2 = 0.6931471805599453f, INV2PI = 0.15915494309189535f;

#define SB() __builtin_amdgcn_sched_barrier(0)
#define WAITV(n) asm volatile("s_waitcnt vmcnt(" #n ")" ::: "memory")
#define PIN(x) asm volatile("" : "+v"(x))
#define FRESH() asm volatile("" ::: "memory")
#define PAUSE() asm volatile("s_sleep 4" ::: "memory")
#define BAR() do { asm volatile("s_waitcnt lgkmcnt(0)" ::: "memory"); __builtin_amdgcn_s_barrier(); asm volatile("" ::: "memory"); } while (0)
#define STAMP(i) do { if ((i) == 0 || (i) == ECHO || (i) == 15) { SB(); u64 t_; asm volatile("s_memrealtime %0\n\ts_waitcnt lgkmcnt(0)" : "=s"(t_) :: "memory"); if ((i) == 0) echo0 = (unsigned)t_; if ((i) == ECHO) echo1 = (unsigned)t_; if ((i) == 15 && (blockIdx.x & 7) == 4) { while ((unsigned)__builtin_amdgcn_s_memrealtime() - (unsigned)t_ < 4 * (echo1 - echo0)) __builtin_amdgcn_s_sleep(1); } SB(); } } while (0)
#define SARG
#define SPASS
#define COUNT(i)

#define STAMPC(i)
#define STAMPA(i)
#define STAMPB(i) STAMP(i)
DI float u2f(unsigned u) { return __builtin_bit_cast(float, u); }
DI unsigned f2u(float f) { return __builtin_bit_cast(unsigned, f); }
DI float dot2(unsigned u, float c) { const bf16x2 v = __builtin_bit_cast(bf16x2, u); return __builtin_amdgcn_fdot2_f32_bf16(v, v, c, false); }
DI float bf_lo(unsigned u) { return __builtin_bit_cast(float, u << 16); }
DI float bf_hi(unsigned u) { return __builtin_bit_cast(float, u & 0xFFFF0000u); }
DI unsigned pack2(float a, float b) { f32x2 v = {a, b}; return __builtin_bit_cast(unsigned, __builtin_convertvector(v, bf16x2)); }
DI float rbf(float a) { return bf_lo(pack2(a, a)); }
DI unsigned sel(bool c, unsigned a, unsigned b) { asm("" : "+v"(a), "+v"(b)); return c ? a : b; }
DI unsigned guard(unsigned u) { return u < 0xFFFFFFFEu ? u : 0xFFFFFFFEu; }
DI unsigned max4(unsigned m, v4u v) {
  asm("v_max3_u32 %0, %0, %1, %2\n\tv_max3_u32 %0, %0, %3, %4" : "+v"(m) : "v"(v.x), "v"(v.y), "v"(v.z), "v"(v.w));
  return m;
}
DI f32x4 mfma16(v4u a, v4u b, f32x4 c) { return __builtin_amdgcn_mfma_f32_16x16x32_bf16(__builtin_bit_cast(bf16x8, a), __builtin_bit_cast(bf16x8, b), c, 0, 0, 0); }
DI f32x16 mfma32(v4u a, v4u b, f32x16 c) { return __builtin_amdgcn_mfma_f32_32x32x16_bf16(__builtin_bit_cast(bf16x8, a), __builtin_bit_cast(bf16x8, b), c, 0, 0, 0); }
template <bool FIRST> DI void mfma_av(f32x16& acc, const v4u& a, const v4u& b) {
  if (FIRST) asm volatile("v_mfma_f32_32x32x16_bf16 %0, %1, %2, 0" : "=&v"(acc) : "a"(a), "v"(b));
  else asm volatile("v_mfma_f32_32x32x16_bf16 %0, %1, %2, %0" : "+v"(acc) : "a"(a), "v"(b));
}
template <bool FIRST, int BLK> DI void mfma4_va(f32x4& c0, f32x4& c1, const v4u& a, const v4u& b) {
  const v2u al = {a.x, a.y}, ah = {a.z, a.w}, bl = {b.x, b.y}, bh = {b.z, b.w};
  if (FIRST) asm volatile("v_mfma_f32_4x4x4_16b_bf16 %0, %2, %4, 0 cbsz:3 abid:%6\n\tv_mfma_f32_4x4x4_16b_bf16 %1, %3, %5, 0 cbsz:3 abid:%6"
                          : "=&v"(c0), "=&v"(c1) : "v"(al), "v"(ah), "a"(bl), "a"(bh), "n"(BLK));
  else asm volatile("v_mfma_f32_4x4x4_16b_bf16 %0, %2, %4, %0 cbsz:3 abid:%6\n\tv_mfma_f32_4x4x4_16b_bf16 %1, %3, %5, %1 cbsz:3 abid:%6"
                    : "+v"(c0), "+v"(c1) : "v"(al), "v"(ah), "a"(bl), "a"(bh), "n"(BLK));
}
template <bool FIRST> DI void mfma16_v(f32x4& acc, const v4u& a, const v4u& b) {
  if (FIRST) asm volatile("v_mfma_f32_16x16x32_bf16 %0, %1, %2, 0" : "=&v"(acc) : "v"(a), "v"(b));
  else asm volatile("v_mfma_f32_16x16x32_bf16 %0, %1, %2, %0" : "+v"(acc) : "v"(a), "v"(b));
}
DI void mfma_done(f32x16& acc) { asm volatile("s_nop 15" : "+v"(acc)); }
DI v2u trd(LAS unsigned char* p) { return __builtin_bit_cast(v2u, __builtin_amdgcn_ds_read_tr16_b64_v4i16((LAS s16x4*)p)); }
DI v4u trd2(LAS unsigned char* p0, LAS unsigned char* p1) { const v2u a = trd(p0), b = trd(p1); return v4u{a.x, a.y, b.x, b.y}; }
DI v4u lds128(LAS unsigned char* p) { return *(LAS v4u*)p; }
DI v4u ldg(__amdgpu_buffer_rsrc_t r, int voff, int soff) { return __builtin_amdgcn_raw_buffer_load_b128(r, voff, soff, SC1); }
DI void stg(__amdgpu_buffer_rsrc_t r, int voff, int soff, v4u v) {
  __builtin_amdgcn_raw_buffer_store_b128(v, r, voff, soff, SC1);
  asm volatile("s_nop 1" :: "v"(v) : "memory");
}
template <int CTRL> DI float dpp_add(float v) {
  return v + __builtin_bit_cast(float, __builtin_amdgcn_update_dpp(0, __builtin_bit_cast(int, v), CTRL, 0xF, 0xF, true));
}
DI float swap16_add(float v) { const unsigned u = __builtin_bit_cast(unsigned, v); const auto r = __builtin_amdgcn_permlane16_swap(u, u, false, false);
  return u2f(r[0]) + u2f(r[1]); }
DI float swap32_add(float v) { const unsigned u = __builtin_bit_cast(unsigned, v); const auto r = __builtin_amdgcn_permlane32_swap(u, u, false, false);
  return u2f(r[0]) + u2f(r[1]); }
DI float quad_sum(float v) { v = dpp_add<0xB1>(v); return dpp_add<0x4E>(v); }
DI float row_sum(float v) { return dpp_add<0x140>(dpp_add<0x141>(quad_sum(v))); }
DI float wave_sum(float v) { return swap32_add(swap16_add(row_sum(v))); }
DI f32x2 cmul(f32x2 a, f32x2 e, f32x2 ep) { const f32x2 t = f32x2{a.x, a.x} * e; return __builtin_elementwise_fma(f32x2{a.y, a.y}, ep, t); }
DI f32x2 cexp(float re2, float rev) {
  const float m = __builtin_amdgcn_exp2f(re2), f = __builtin_amdgcn_fractf(rev);
  return f32x2{m * __builtin_amdgcn_cosf(f), m * __builtin_amdgcn_sinf(f)};
}
template <int LI, int SI> DI void grp(int& so, unsigned lbase, int sbase) {
  asm volatile("s_add_i32 m0, %1, %3\n\ts_add_i32 %0, %2, %4" : "=s"(so) : "s"(lbase), "s"(sbase), "n"(LI), "n"(SI) : "scc", "memory");
}
template <int K, bool COH> DI void xfer(unsigned v, __amdgpu_buffer_rsrc_t r, int so) {
  if (COH) asm volatile("buffer_load_dwordx4 %0, %1, %2 offen offset:%3 sc1 lds" :: "v"(v), "s"(r), "s"(so), "n"(K * 1024) : "memory");
  else asm volatile("buffer_load_dwordx4 %0, %1, %2 offen offset:%3 lds" :: "v"(v), "s"(r), "s"(so), "n"(K * 1024) : "memory");
}
DI void set_m0(unsigned lds_dst) { asm volatile("s_mov_b32 m0, %0\n\ts_nop 0" :: "s"(lds_dst) : "memory"); }
DI void dma_x4(const unsigned char* sbase, unsigned v, unsigned lds_dst) {
  asm volatile("s_mov_b32 m0, %2\n\ts_nop 0\n\tglobal_load_lds_dwordx4 %0, %1 sc1\n\tglobal_load_lds_dwordx4 %0, %1 offset:1024 sc1\n\t"
               "global_load_lds_dwordx4 %0, %1 offset:2048 sc1\n\tglobal_load_lds_dwordx4 %0, %1 offset:3072 sc1"
               :: "v"(v), "s"(sbase), "s"(lds_dst) : "memory");
}
template <int I> struct IC { static constexpr int v = I; };
template <int I, int N, class F> DI void static_for(F&& f) { if constexpr (I < N) { f(IC<I>{}); static_for<I + 1, N>(f); } }
DI unsigned now() { u64 t; asm volatile("s_memrealtime %0\n\ts_waitcnt lgkmcnt(0)" : "=s"(t) :: "memory"); return (unsigned)t; }
DI void reload(v4u& d, __amdgpu_buffer_rsrc_t r, int voff, int soff) { d = ldg(r, voff, soff); FRESH(); }
DI void reload1(unsigned& d, __amdgpu_buffer_rsrc_t r, int voff, int soff) { d = __builtin_amdgcn_raw_buffer_load_b32(r, voff, soff, SC1); FRESH(); }
template <int N> DI void waitv() { asm volatile("s_waitcnt vmcnt(%0)" :: "n"(N) : "memory"); }
struct ConvOrder { int t[36], u[36]; };
constexpr ConvOrder conv_order() {
  ConvOrder o = {};
  int done[8] = {}, last = -1;
  for (int k = 0; k < 36; ++k) {
    int best = -1;
    for (int t = 0; t < 8; ++t)
      if (t != last && done[t] <= t && (best < 0 || t - done[t] > best - done[best])) best = t;
    o.t[k] = best; o.u[k] = done[best]++; last = best;
  }
  return o;
}
constexpr ConvOrder conv_order_last() {
  ConvOrder o = {};
  int k = 0;
  for (int p = 0; p < 4; ++p)
    for (int u = 0; u <= 2 * p + 1; ++u) {
      o.t[k] = 2 * p + 1; o.u[k] = u; ++k;
      if (u <= 2 * p) { o.t[k] = 2 * p; o.u[k] = u; ++k; }
    }
  return o;
}
constexpr int epi_slot(int v) { return v < 32 ? 2 * EO + v : 2 * 50 - 1 + (v - 32); }
constexpr bool epi_pair(int j) { return (j >= EO && j < EO + 19) || (j >= 50 && j < 61); }
constexpr int hc_upto(int j) { return (j < HC0 ? 0 : j - HC0) + (j < HC1 ? 0 : j - HC1); }
constexpr int hl_upto(int i) { return i < HL0 ? 28 : i < HL0 + 12 ? 28 + (i - HL0) : i < HL1 ? 40 : i < HL1 + 16 / HLR ? 40 + HLR * (i - HL1) : 56; }
constexpr int c_ops(int j) {
  return (j >= SXJ && j < SXJ + 4 ? 2 : j < 34 || (j >= 35 && j < 51) ? 1 : 0) + hc_upto(j + 1) - hc_upto(j) +
         (j == EO + 6 || j == EO + 14 || j == 56 ? 1 : j == EO + 10 || j == EO + 18 || j == 60 ? 2 : 0);
}
constexpr int c_between(int a, int b) { int n = 0; for (int j = a + 1; j < b; ++j) n += c_ops(j); return n; }
DI unsigned tagbad(unsigned bad, v4u g, unsigned tagA, unsigned tagB) {
  return __builtin_amdgcn_bitop3_b32(__builtin_amdgcn_bitop3_b32(bad, g.y, tagA, 0xF6), g.w, tagB, 0xF6);
}
template <int R0, int R1, int R2, int R3> DI void put4(unsigned base, f32x2 a, f32x2 b, f32x2 c, f32x2 d) {
  asm volatile("s_mov_b32 m0, %4\n\ts_nop 0\n\tds_write_addtid_b32 %0 offset:%5\n\tds_write_addtid_b32 %1 offset:%6\n\t"
               "ds_write_addtid_b32 %2 offset:%7\n\tds_write_addtid_b32 %3 offset:%8"
               :: "v"(pack2(a.x, a.y)), "v"(pack2(b.x, b.y)), "v"(pack2(c.x, c.y)), "v"(pack2(d.x, d.y)), "s"(base),
                  "n"(R0 * UVS_ROW), "n"(R1 * UVS_ROW), "n"(R2 * UVS_ROW), "n"(R3 * UVS_ROW) : "memory");
}
DI unsigned sum4(unsigned m, v4u v) {
  asm("v_add3_u32 %0, %0, %1, %2\n\tv_add3_u32 %0, %0, %3, %4" : "+v"(m) : "v"(v.x), "v"(v.y), "v"(v.z), "v"(v.w));
  return m;
}
DI void acc4(unsigned& m, v4u v) {
  asm volatile("v_add3_u32 %0, %0, %1, %2\n\tv_add3_u32 %0, %0, %3, %4" : "+v"(m) : "v"(v.x), "v"(v.y), "v"(v.z), "v"(v.w));
}
DI unsigned sum8(unsigned m, v4u a, v4u b) {
  asm("v_add3_u32 %0, %0, %1, %2\n\tv_add3_u32 %0, %0, %3, %4\n\tv_add3_u32 %0, %0, %5, %6\n\tv_add3_u32 %0, %0, %7, %8"
      : "+v"(m) : "v"(a.x), "v"(a.y), "v"(a.z), "v"(a.w), "v"(b.x), "v"(b.y), "v"(b.z), "v"(b.w));
  return m;
}
template <int I> DI unsigned red_step(unsigned v) {
  constexpr int ctrl[6] = {0xB1, 0x4E, 0x141, 0x140, 0x142, 0x143}, rows[6] = {0xF, 0xF, 0xF, 0xF, 0xA, 0xC};
  return v + (unsigned)__builtin_amdgcn_update_dpp(0, (int)v, ctrl[I], rows[I], 0xF, true);
}
DI unsigned red_all(unsigned v) { return red_step<5>(red_step<4>(red_step<3>(red_step<2>(red_step<1>(red_step<0>(v)))))); }
DI void swap_add32(float& a, float b) { const auto r = __builtin_amdgcn_permlane32_swap(f2u(a), f2u(b), false, false); a = u2f(r[0]) + u2f(r[1]); }
DI void swap_add16(float& a, float b) { const auto r = __builtin_amdgcn_permlane16_swap(f2u(a), f2u(b), false, false); a = u2f(r[0]) + u2f(r[1]); }

typedef const unsigned char* cptr;
typedef unsigned v16u __attribute__((ext_vector_type(16)));
DI cptr mkptr(unsigned lo, unsigned hi) { return (cptr)(((u64)hi << 32) | lo); }

__global__ void __launch_bounds__(256, 1)
s4_block_kernel(cptr, cptr, cptr, cptr, cptr, cptr, cptr, cptr, cptr, cptr, cptr, cptr, cptr, unsigned char*, unsigned char*, cptr) {
  unsigned echo0 = 0, echo1 = 0;
  STAMP(0);
  v16u ka0, ka1;
  asm volatile("s_load_dwordx16 %0, %2, 0x0\n\ts_load_dwordx16 %1, %2, 0x40" : "=&s"(ka0), "=&s"(ka1) : "s"(__builtin_amdgcn_kernarg_segment_ptr()));
  SB();
  LAS unsigned char* const L = (LAS unsigned char*)16u - 16;
  const int tid = threadIdx.x, lane = tid & 63, w = __builtin_amdgcn_readfirstlane(tid >> 6);
  const int b = blockIdx.x, xcd = b & 7, slot = b >> 3, id = xcd * 32 + slot;
  const int ks = (id >> 6) * 4 + w, cc = id & 63;
  const int rp = 4 * (xcd >> 1) + (slot >> 3), rg = xcd & 1, dt = slot & 7, si = 8 * rg + dt;
  const int ct = 7 * rg + min(dt, 6);
  const int rot = (0x0340 >> (4 * (2 * (w & 1) + rg))) & 7;

  const unsigned char* pcode;
  asm volatile("s_getpc_b64 %0" : "=s"(pcode));
  unsigned warm;
  asm volatile("global_load_dword %0, %1, %2" : "=v"(warm) : "v"(min((((slot & 7) * 4 + w) * 9 + min(lane, 8)) * 128, CODE_BYTES)), "s"(pcode) : "memory");
  const int g16 = lane >> 4, q4 = (lane & 15) >> 2, p4 = lane & 3, hf = lane >> 5, blk = (lane >> 4) & 1, l16 = lane * 16;
  const unsigned lrw = L_R + w * 16384;
  unsigned lsp = L_SP + w * 6144;
  unsigned co = (ks * 32 + (lane & 31)) * 2, xo = (id >> 2) * 32768 + (id & 3) * 256 + (tid & 7) * 4096 + (tid >> 3) * 8, ro = id * 8192 + w * 2048 + hf * 1024 + (lane & 31) * 16, po = id * 512 + tid * 2;
  PIN(co); PIN(xo); PIN(ro); PIN(po);
  set_m0(lrw);
  SB();
  asm volatile("s_waitcnt lgkmcnt(0)" : "+s"(ka0), "+s"(ka1));
  const cptr xin = mkptr(ka0[0], ka0[1]), pAre = mkptr(ka0[2], ka0[3]), pAim = mkptr(ka0[4], ka0[5]), pBre = mkptr(ka0[6], ka0[7]);
  const cptr pBim = mkptr(ka0[8], ka0[9]), pCre = mkptr(ka0[10], ka0[11]), pCim = mkptr(ka0[12], ka0[13]), pldt = mkptr(ka0[14], ka0[15]);
  const cptr pD = mkptr(ka1[0], ka1[1]), w1 = mkptr(ka1[2], ka1[3]), w2 = mkptr(ka1[4], ka1[5]), pc = mkptr(ka1[6], ka1[7]);
  const cptr wmod = mkptr(ka1[8], ka1[9]), out = mkptr(ka1[10], ka1[11]), ws = mkptr(ka1[12], ka1[13]);
  unsigned uldt, uD;
  asm volatile("s_load_dword %0, %2, %4\n\ts_load_dword %1, %3, %4" : "=&s"(uldt), "=&s"(uD) : "s"(pldt), "s"(pD), "s"(id * 4));
  unsigned uAre, uAim, uBre, uBim, uCre, uCim;
  asm volatile("global_load_ushort %0, %6, %7\n\tglobal_load_ushort %1, %6, %8\n\tglobal_load_ushort %2, %6, %9\n\t"
               "global_load_ushort %3, %6, %10\n\tglobal_load_ushort %4, %6, %11\n\tglobal_load_ushort %5, %6, %12"
               : "=&v"(uAre), "=&v"(uAim), "=&v"(uBre), "=&v"(uBim), "=&v"(uCre), "=&v"(uCim)
               : "v"(po), "s"(pAre), "s"(pAim), "s"(pBre), "s"(pBim), "s"(pCre), "s"(pCim) : "memory");
  unsigned epoch_v, cval;
  v2u xq[4];
  v4u ra, rb;
  asm volatile("global_load_dword %0, %8, %12 sc1\n\tglobal_load_dwordx2 %1, %9, %13\n\tglobal_load_dwordx2 %2, %9, %13 offset:1024\n\t"
               "global_load_dwordx2 %3, %9, %13 offset:2048\n\tglobal_load_dwordx2 %4, %9, %13 offset:3072\n\t"
               "global_load_dwordx4 %5, %10, %13\n\tglobal_load_dwordx4 %6, %10, %13 offset:512\n\tglobal_load_ushort %7, %11, %14"
               : "=&v"(epoch_v), "=&v"(xq[0]), "=&v"(xq[1]), "=&v"(xq[2]), "=&v"(xq[3]), "=&v"(ra), "=&v"(rb), "=&v"(cval)
               : "v"(OFF_EP), "v"(xo), "v"(ro), "v"(co), "s"(ws), "s"(xin), "s"(pc) : "memory");
  unsigned wmo[3];
#pragma unroll
  for (int i = 0; i < 3; ++i) { const unsigned idx = lane + 64 * i; wmo[i] = (ks * 32 * 6144 + cc * 96 - i * 1024) + idx / 6 * (6144 - 96) + idx * 16; }
  asm volatile("global_load_lds_dwordx4 %0, %3\n\tglobal_load_lds_dwordx4 %1, %3 offset:1024\n\tglobal_load_lds_dwordx4 %2, %3 offset:2048"
               :: "v"(wmo[0]), "v"(wmo[1]), "v"(wmo[2]), "s"(wmod) : "memory");
  const unsigned char* setp = ws + SET_BASE;
  const __amdgpu_buffer_rsrc_t rs = __builtin_amdgcn_make_buffer_rsrc((void*)setp, 0, SET_BYTES, 0x00020000);
  SB();
  STAMPA(26);
  unsigned wto = id * 7168 + (w >> 1) * 3584 + min(lane, 27) * 128;
  PIN(wto);
  unsigned w1v[4], w2v[4];
#pragma unroll
  for (int i = 0; i < 4; ++i) {
    w1v[i] = (lane >> 3) * 3584 + (((lane & 7) ^ (blk << 2)) * 16) + i * (57344 - 1024);
    w2v[i] = (lane >> 3) * 1024 + (((lane & 7) ^ (blk << 2)) * 16) + i * (8192 - 1024);
    PIN(w1v[i]); PIN(w2v[i]);
  }
  int w1b = ct * 256 + (w >> 1) * 128 + (w & 1) * (8 * 3584) + ((dt + 1) >> 3 << 24), w2lo = (448 * w + 64 * rot) * 1024 + dt * 128;
  int w2c3 = w2lo - (rot >> 2) * (448 * 1024), w2hi = w2lo - ((rot + 1) >> 2) * (448 * 1024), so = 0;
  unsigned lrx = L_RX + w * 1024, lto = L_TOP + w * 1024;
  int zs = OFF_Z + (2 * id + (w >> 1)) * 64;
  int za = ((((w & 1) + 2 * (g16 & 1)) * 4 + q4) * 4 + (g16 >> 1)) * 32768 + p4 * 16;
  int tra = (hf * 8 + q4) * 64 + blk * 32 + p4 * 8;
  const int wsrc = (w & 2) + hf;
  int w1t = q4 * 128 + (((w & 1) ^ (q4 >> 1)) * 64) + blk * 32 + p4 * 8;
  LAS unsigned char *w1a = L + L_R + wsrc * 16384 + w1t, *w1s = L + L_SP + wsrc * 6144 + w1t;
  LAS unsigned char *w1x = L + L_RX + wsrc * 1024 + w1t, *w1y = L + L_TOP + wsrc * 1024 + w1t;
#define W1PULL(step, n) w1f[step] = trd2(((n) < 16 ? w1a : w1s) + ((n) & 15) * 1024, ((n) < 16 ? w1a : w1s) + ((n) & 15) * 1024 + 512)
  LAS unsigned char* ka = L + L_KC + ((w >> 1) * 4 + q4) * KC_STRIDE + 2 * (32 - 8 * g16 + 4 * p4) - 8 + 256 * (w & 1);
  LAS unsigned char* hb = L + L_H + (w >> 1) * H_STRIDE + (lane & 15) * 32 + g16 * 16 + 256 - 256 * (w & 1);
  LAS unsigned char* zp = L + sel(tid < 64, L_KC + ((lane >> 1) & 7) * KC_STRIDE + (lane & 1) * 16, L_H + hf * H_STRIDE + min(lane & 31, 29) * 16);
  LAS unsigned char* ub = L + L_UVS + (lane & 15) * UVS_ROW + g16 * 16;
  int gmo = OFF_MG + (g16 * 256 + id) * 256 + (lane & 15) * 16, gmo2 = OFF_MG + ((4 + blk) * 256 + id) * 256 + (lane & 15) * 16;
  int mgo = sel(lane < 48, (lane >> 1) * 256 + (lane & 1) * 8, OOB), sgo = sel(lane & 31, OOB, hf * 8);
  LAS unsigned char* xbw = L + L_XB + ((w & 1) * 4 + (w >> 1)) * 1024 + l16;
  int xcw = sel(lane & 7, OOB, tid), xcr = sel(lane & 7, OOB, (tid >> 3) * 1024), xgw = (tid >> 3) * 16384 + (tid & 7) * 16;
  PIN(xcw); PIN(xcr); PIN(xgw);
  LAS unsigned char *exa = L + L_XB + w * 4096 + l16, *eya = L + L_XB + (w ^ 1) * 4096 + l16;
  LAS unsigned char* xbr = L + L_XB + w * 4096 + ((lane & 15) * 4 + g16) * 16;
  PIN(xbw); PIN(xbr); PIN(exa); PIN(eya);
  int mto = (w * 64 + (lane >> 1)) * 64 + (lane & 1) * 16;
  LAS unsigned char* mtw = L + (lane & 1) * L_TG + (w * 64 + (lane >> 1)) * 4;
  PIN(mto); PIN(mtw);
  PIN(za); PIN(tra); PIN(w1a); PIN(w1s); PIN(w1x); PIN(w1y); PIN(ka); PIN(hb); PIN(zp); PIN(ub); PIN(gmo); PIN(gmo2); PIN(mgo); PIN(sgo);
  asm volatile("" : "+s"(w1b), "+s"(w2lo), "+s"(w2c3), "+s"(w2hi), "+s"(lsp), "+s"(lrx), "+s"(lto), "+s"(zs));
#define W1P(n, k, lbase, li) do { if constexpr ((k) == 0) grp<(li), (n) * 57344>(so, lbase, w1b); xfer<(k), false>(w1v[k], rw1, so); } while (0)
#define W2P(n, k, lbase, li) do { if constexpr ((k) == 0) grp<(li), (n) * 8192>(so, lbase, (n) < 24 ? w2lo : (n) < 32 ? w2c3 : w2hi); xfer<(k), false>(w2v[k], rw2, so); } while (0)
  SB();
  LAS unsigned char* koff[12];
  {
    float fj = (float)(8 * tid - 3);
    PIN(fj);
    const unsigned lin = (unsigned)(8 * tid - 3 + 0x4300 - 128) * 0x10001u + 0x10000u;
#pragma unroll
    for (int i = 0; i < 12; i += 2) {
      const unsigned r = sel(tid < 32, lin + i * 0x10001u, pack2(fj + (float)i, fj + (float)(i + 1)));
      koff[i] = L + ((r & 0xFFFFu) * 4 + (L_KT + (128 - 0x4300) * 4));
      koff[i + 1] = L + ((r >> 16) * 4 + (L_KT + (128 - 0x4300) * 4));
      PIN(koff[i]);
      if (i < 10) PIN(koff[i + 1]);
    }
  }
  *(LAS v4u*)(L + L_KT - 16) = v4u{0u, 0u, 0u, 0u};
  LAS unsigned char* ktw[4];
  {
    const int row = w * 16 + (lane & 15), sg = max((row >> 3) - 1, 0), rr_ = row - 8 * sg;
    const int idx0 = 128 * sg + ((rr_ >> (3 - sg)) << (7 - sg)) + (rr_ & ((8 >> sg) - 1)) + 1, st = 8 >> sg;
#pragma unroll
    for (int r = 0; r < 4; ++r) {
      const int idx = sel(row < 40, idx0 + st * (4 * g16 + r), tid == 136 && r == 0 ? 0 : 641 + lane);
      ktw[r] = L + L_KT + idx * 4;
      PIN(ktw[r]);
    }
  }
  const int par = (0x1C >> (ct % 7)) & 1;
  const unsigned char* xg = setp + OFF_Z + rp * 131072;
  unsigned xl = L_XR + w * 8192;
  int xs2 = OFF_Z + rp * 131072 + w * 8192 + (2 ^ par) * 32768, xs3 = xs2 + 32768 - 65536 * par;
  asm volatile("" : "+s"(xl), "+s"(xs2), "+s"(xs3));
  int slo = OFF_Z + (rp * 4 + w) * 32768 + si * 2048;
  asm volatile("" : "+s"(slo));
  const int w2t = (4 * hf + q4) * 128 + blk * 32 + (((p4 & 1) << 1) | (p4 >> 1)) * 8;
  LAS unsigned char* const w2a0 = L + w2t + (q4 >> 1) * 64;
  LAS unsigned char* const w2b0 = L + w2t + ((q4 >> 1) ^ 1) * 64;
  LAS unsigned char *pa = w2a0 + lrw + 8192, *pb = w2b0 + lrw + 8192;
  LAS unsigned char* qr = L + (p4 ? L_TB : L_TS) + hf * 16 + ((lane >> 2) & 7) * 32;
  PIN(pa); PIN(pb); PIN(qr);
#define PULL(step, pa, pb, off) do { w2f[step][0] = trd2((pa) + (off), (pa) + (off) + 1024); w2f[step][1] = trd2((pb) + (off), (pb) + (off) + 1024); } while (0)
  const __amdgpu_buffer_rsrc_t rw1 = __builtin_amdgcn_make_buffer_rsrc((void*)w1, 0, 1835008, 0x00020000);
  const __amdgpu_buffer_rsrc_t rw2 = __builtin_amdgcn_make_buffer_rsrc((void*)w2, 0, 1835008, 0x00020000);
  SB();
  STAMPA(27);
  asm volatile("s_waitcnt lgkmcnt(0)" : "+s"(uldt), "+s"(uD));
  asm volatile("s_waitcnt vmcnt(11)" : "+v"(uAre), "+v"(uAim), "+v"(uBre), "+v"(uBim), "+v"(uCre), "+v"(uCim), "+v"(warm));
    const float dtv = rbf(__builtin_amdgcn_exp2f(LOG2E * (w >> 1 ? bf_hi(uldt) : bf_lo(uldt))));
    const float are = dtv * bf_lo(uAre) * LOG2E, aim = dtv * bf_lo(uAim) * INV2PI;
    const float br = dtv * bf_lo(uBre), bi = dtv * bf_lo(uBim), cr = bf_lo(uCre), ci = bf_lo(uCim);
    const f32x2 wv = {cr * br - ci * bi, cr * bi + ci * br};
    const unsigned row = L_UVS + (w >> 1) * UVS_CH + (w & 1) * 256;
    const f32x2 e1 = cexp(are, aim), e8 = cexp(8.f * are, -8.f * aim), e128 = cexp(128.f * are, 128.f * aim);
    const f32x2 e1p = {-e1.y, e1.x}, e8p = {-e8.y, e8.x}, e128p = {-e128.y, e128.x};
    f32x2 z[8], v, v2, y, y2;
#define M1(a) cmul(a, e1, e1p)
#define M8(a) cmul(a, e8, e8p)
#define MK(a) cmul(a, e128, e128p)
    z[0] = M1(wv);
    put4<40, 41, 42, 0>(row, wv, f32x2{1.f, 0.f}, e8, z[0]);
    v = M8(e8); z[1] = M1(z[0]); v2 = M8(v); z[2] = M1(z[1]);
    put4<43, 1, 44, 2>(row, v, z[1], v2, z[2]);
  SB();
  asm volatile("s_waitcnt vmcnt(6)" : "+v"(epoch_v), "+v"(xq[0]), "+v"(xq[1]), "+v"(xq[2]), "+v"(xq[3]));
  STAMPA(29);
  const unsigned epoch = __builtin_amdgcn_readfirstlane(epoch_v);
  const unsigned tagA = epoch * 0x9E3779B1u + 0x7F4A7C15u, tagB = ~tagA;
  {
    const v4u xc0 = {xq[0].x, xq[1].x, xq[2].x, xq[3].x}, xc1 = {xq[0].y, xq[1].y, xq[2].y, xq[3].y};
    stg(rs, xgw, OFF_XG + (id & 3) * 524288 + (id >> 2) * 128, xc0);
    stg(rs, xgw, OFF_XG + (id & 3) * 524288 + (id >> 2) * 128 + 8192, xc1);
    SB();
    STAMP(1);
    asm volatile("s_waitcnt vmcnt(5)" : "+v"(ra), "+v"(rb), "+v"(cval));
    const float c = bf_lo(cval);
    const float s = c * __builtin_amdgcn_rcpf(1.0f + __builtin_amdgcn_exp2f(-LOG2E * c));
    *(LAS u16*)(L + lrw + 3072 + (lane & 31) * 2) = (u16)pack2(s, s);
    f32x2 fa[8];
#pragma unroll
    for (int i = 0; i < 4; ++i) { fa[i] = f32x2{bf_lo(ra[i]), bf_hi(ra[i])}; fa[4 + i] = f32x2{bf_lo(rb[i]), bf_hi(rb[i])}; }
    const f32x2 s2 = ((fa[0] + fa[1]) + (fa[2] + fa[3])) + ((fa[4] + fa[5]) + (fa[6] + fa[7]));
    float mean = swap16_add(row_sum(s2.x + s2.y)) * (1.0f / 512.0f);
    const unsigned k0 = red_step<2>(red_step<1>(red_step<0>(sum4(0u, xc0)))), k1 = red_step<2>(red_step<1>(red_step<0>(sum4(0u, xc1))));
    __builtin_amdgcn_raw_buffer_store_b64(v2u{tagA - k0, tagA - k1}, rs, xcw, OFF_XC + id * 256, SC1);
    PIN(mean);
    SB();
    f32x2 q2 = {0.f, 0.f}, q3 = {0.f, 0.f};
#pragma unroll
    for (int i = 0; i < 8; i += 2) {
      const f32x2 d0 = fa[i] - f32x2{mean, mean}, d1 = fa[i + 1] - f32x2{mean, mean};
      q2 = __builtin_elementwise_fma(d0, d0, q2);
      q3 = __builtin_elementwise_fma(d1, d1, q3);
    }
    q2 += q3;
    float qs = q2.x + q2.y;
    PIN(qs);
    WAITV(3);
    LAS unsigned char* a = L + lrw + (8 * g16 + q4) * 96 + p4 * 8;
    const v4u sc = lds128(L + lrw + 3072 + g16 * 16);
    const v4u m0 = trd2(a, a + 384), m1 = trd2(a + 32, a + 416), m2 = trd2(a + 64, a + 448);
    SB();
    const float var = rbf(rbf(swap16_add(row_sum(qs)) * (1.0f / 512.0f)) + 1e-5f);
    unsigned st = pack2(mean, __builtin_amdgcn_sqrtf(var));
    PIN(st);
    SB();
    const f32x4 c0 = mfma16(sc, m0, f32x4{}), c1 = mfma16(sc, m1, f32x4{}), c2 = mfma16(sc, m2, f32x4{});
    SB();
    __builtin_amdgcn_raw_buffer_store_b64(v2u{st, hf ? tagB : tagA}, rs, sgo, OFF_SG + ((w & 1) * 512 + id * 2 + (w >> 1)) * 16, SC1);
    __builtin_amdgcn_raw_buffer_store_b64(v2u{sel(lane < 16, f2u(c0[0]), sel(lane < 32, f2u(c1[0]), f2u(c2[0]))), (lane & 1) ? tagB : tagA}, rs, mgo,
                                          OFF_MG + cc * 24 * 256 + ks * 16, SC1);
    SB();
  }
  unsigned wt;
  SB();
  STAMP(2);
  unsigned dsk = tid == 136 ? uD : 0u;
  PIN(dsk);

    asm volatile("global_load_dword %0, %1, %2" : "=v"(wt) : "v"(wto), "s"(w1 + (w & 1) * (w2 - w1)) : "memory");
    v = M8(v2); z[3] = M1(z[2]); v2 = M8(v); z[4] = M1(z[3]);
    put4<45, 3, 46, 4>(row, v, z[3], v2, z[4]);
    v = M8(v2); z[5] = M1(z[4]); v2 = M8(v); z[6] = M1(z[5]);
    put4<47, 5, 48, 6>(row, v, z[5], v2, z[6]);
    z[7] = M1(z[6]); z[0] = MK(z[0]); z[1] = MK(z[1]); z[2] = MK(z[2]);
    put4<7, 8, 9, 10>(row, z[7], z[0], z[1], z[2]);
    z[3] = MK(z[3]); z[4] = MK(z[4]); z[5] = MK(z[5]); z[6] = MK(z[6]);
    put4<11, 12, 13, 14>(row, z[3], z[4], z[5], z[6]);
    z[7] = MK(z[7]); z[1] = MK(z[1]); z[3] = MK(z[3]); z[5] = MK(z[5]);
    put4<15, 16, 17, 18>(row, z[7], z[1], z[3], z[5]);
    z[7] = MK(z[7]); z[1] = MK(z[1]); z[3] = MK(z[3]); z[5] = MK(z[5]);
    put4<19, 20, 21, 22>(row, z[7], z[1], z[3], z[5]);
    y = MK(z[7]); z[3] = MK(z[3]); y2 = MK(y); z[4] = MK(z[3]);
    put4<23, 24, 25, 26>(row, y, z[3], y2, z[4]);
    y = MK(y2); z[3] = MK(z[4]); y2 = MK(y); z[4] = MK(z[3]);
    put4<27, 28, 29, 30>(row, y, z[3], y2, z[4]);
    y = MK(y2); v = M8(v2); y2 = MK(y); v2 = M8(v);
    put4<31, 49, 32, 50>(row, y, v, y2, v2);
    y = MK(y2); v = M8(v2); y2 = MK(y); v2 = M8(v);
    put4<33, 51, 34, 52>(row, y, v, y2, v2);
    y = MK(y2); v = M8(v2); y2 = MK(y); v2 = M8(v);
    put4<35, 53, 36, 54>(row, y, v, y2, v2);
    y = MK(y2); v = M8(v2); y2 = MK(y); v2 = M8(v);
    put4<37, 55, 38, 56>(row, y, v, y2, v2);
    y = MK(y2);
    *(LAS unsigned*)(L + row + 39 * UVS_ROW + lane * 4) = pack2(y.x, y.y);
    STAMPC(29);
#undef M1
#undef M8
#undef MK
  BAR();
  STAMP(3);
  v4u gx0, gx1, gs0, gs1, gs2, gs3, gm, gn;
  unsigned gc0, gc1;
  const int xgo = OFF_XG + id * 8192, xco = OFF_XC + id * 4;
  v4u fv[2][8], fu[2][8];
  if (w < 3) {
    LAS unsigned char* ut = ub + w * 16 * UVS_ROW;
#pragma unroll
    for (int s = 0; s < 8; ++s)
#pragma unroll
      for (int c = 0; c < 2; ++c) { fv[c][s] = lds128(ub + c * UVS_CH + 41 * UVS_ROW + s * 64); fu[c][s] = lds128(ut + c * UVS_CH + s * 64); SB(); }
    f32x4 a0 = {}, a1 = {};
#pragma unroll
    for (int s = 0; s < 8; ++s) { a0 = mfma16(fv[0][s], fu[0][s], a0); SB(); a1 = mfma16(fv[1][s], fu[1][s], a1); SB(); }
    *(LAS unsigned*)ktw[0] = pack2(a0[0] + bf_lo(dsk), a1[0] + bf_hi(dsk));
#pragma unroll
    for (int r = 1; r < 4; ++r) *(LAS unsigned*)ktw[r] = pack2(a0[r], a1[r]);
  }
  SB();
#define LD1(i, so) "buffer_load_dwordx4 %" #i ", %10, %14, " so " offen sc1\n\t"
  asm volatile("buffer_load_dwordx4 %6, %11, %14, 0 offen sc1\n\tbuffer_load_dwordx4 %7, %12, %14, 0 offen sc1\n\t"
               LD1(0, "%15") LD1(1, "%16") "buffer_load_dword %8, %13, %14, %21 offen sc1\n\tbuffer_load_dword %9, %13, %14, %22 offen sc1\n\t"
               LD1(2, "%17") LD1(3, "%18") LD1(4, "%19") LD1(5, "%20")
               : "=&v"(gx0), "=&v"(gx1), "=&v"(gs0), "=&v"(gs1), "=&v"(gs2), "=&v"(gs3), "=&v"(gm), "=&v"(gn), "=&v"(gc0), "=&v"(gc1)
               : "v"(tid * 16), "v"(gmo), "v"(gmo2), "v"(xcr), "s"(rs), "s"(xgo), "s"(xgo + 4096),
                 "s"(OFF_SG), "s"(OFF_SG + 8192), "s"(OFF_SG + 4096), "s"(OFF_SG + 12288), "s"(xco), "s"(xco + 32768) : "memory");
#undef LD1
  SB();
  BAR();
  {
    unsigned k[11];
#pragma unroll
    for (int i = 0; i < 11; ++i) k[i] = *(LAS unsigned*)koff[i];
    SB();
    stg(rs, (tid >> 3) * 32768 + (tid & 7) * 16, OFF_Z + id * 128, v4u{POISON, POISON, POISON, POISON});
    stg(rs, (tid >> 3) * 32768 + (tid & 7) * 16, OFF_Z + id * 128 + 32 * 32768, v4u{POISON, POISON, POISON, POISON});
    SB();
    if (w < 2) *(LAS v4u*)zp = v4u{0u, 0u, 0u, 0u};
#pragma unroll
    for (int c = 0; c < 2; ++c) {
      unsigned e[5], o[5];
#pragma unroll
      for (int i = 0; i < 5; ++i) {
        o[i] = __builtin_amdgcn_perm(k[2 * i + 1], k[2 * i], c ? 0x07060302u : 0x05040100u);
        e[i] = __builtin_amdgcn_perm(k[2 * i + 2], k[2 * i + 1], c ? 0x07060302u : 0x05040100u);
      }
      LAS unsigned char* d = L + L_KC + c * 4 * KC_STRIDE + 32 + tid * 16;
      *(LAS v4u*)(d) = v4u{e[1], e[2], e[3], e[4]};
      *(LAS v4u*)(d + KC_STRIDE) = v4u{o[1], o[2], o[3], o[4]};
      *(LAS v4u*)(d + 2 * KC_STRIDE) = v4u{e[0], e[1], e[2], e[3]};
      *(LAS v4u*)(d + 3 * KC_STRIDE) = v4u{o[0], o[1], o[2], o[3]};
    }
  }
    W1P(0, 0, lrw, 0); W1P(1, 1, lrw, 0); W1P(2, 2, lrw, 0); W1P(3, 3, lrw, 0);
    W1P(4, 0, lrw, 4096); W1P(5, 1, lrw, 0); W1P(6, 2, lrw, 0); W1P(7, 3, lrw, 0);
  SB();
  STAMP(4);
  {
    asm volatile("s_waitcnt vmcnt(10)" : "+v"(gx0), "+v"(gx1), "+v"(gs0), "+v"(gs1), "+v"(gs2), "+v"(gs3), "+v"(gm), "+v"(gn), "+v"(gc0), "+v"(gc1) :: "memory");
#define XBAD() (red_step<2>(red_step<1>(red_step<0>(sum8(gc0 + gc1, gx0, gx1)))) ^ (2u * tagA))
#define TB(a, c) tagbad(tagbad(0u, a, tagA, tagB), c, tagA, tagB)
    if (__builtin_expect(__any((XBAD() | TB(gm, gn) | TB(gs0, gs1) | TB(gs2, gs3)) != 0u), 0)) {
      _Pragma("nounroll") for (unsigned spin = 0; spin < SPIN_CAP; ++spin) {
        if (spin) PAUSE();
        COUNT(20);
        if (XBAD()) { reload(gx0, rs, tid * 16, xgo); reload(gx1, rs, tid * 16, xgo + 4096); reload1(gc0, rs, xcr, xco); reload1(gc1, rs, xcr, xco + 32768); }
        if (TB(gs0, gs1)) { reload(gs0, rs, tid * 16, OFF_SG); reload(gs1, rs, tid * 16, OFF_SG + 8192); }
        if (TB(gs2, gs3)) { reload(gs2, rs, tid * 16, OFF_SG + 4096); reload(gs3, rs, tid * 16, OFF_SG + 12288); }
        if (TB(gm, gn)) { reload(gm, rs, gmo, 0); reload(gn, rs, gmo2, 0); }
        if (!__any((XBAD() | TB(gm, gn) | TB(gs0, gs1) | TB(gs2, gs3)) != 0u)) break;
      }
    }
#undef XBAD
#undef TB
  }
  STAMP(5);
  W1P(8, 0, lrw, 8192); W1P(9, 1, lrw, 0);
  v4u mtp;
  {
    float r0 = u2f(gm.x), r1 = u2f(gm.z), r2 = u2f(gn.x), r3 = u2f(gn.z);
#define RSTEP(c) r0 = dpp_add<c>(r0); PIN(r0); r1 = dpp_add<c>(r1); PIN(r1); r2 = dpp_add<c>(r2); PIN(r2); r3 = dpp_add<c>(r3); PIN(r3)
    RSTEP(0xB1); RSTEP(0x4E); RSTEP(0x141); RSTEP(0x140);
#undef RSTEP
    const int i0 = (int)pack2(r0, r1), i1 = (int)pack2(r2, r3);
    unsigned md[6];
#pragma unroll
    for (int k = 0; k < 6; ++k) md[k] = (unsigned)__builtin_amdgcn_readlane(k < 4 ? i0 : i1, 16 * (k & 3));
#define MOD(k) f32x2{bf_lo(md[k]), bf_hi(md[k])}
    const f32x2 one = {1.0f, 1.0f}, sr = one + MOD(4), cr = one + MOD(1);
    unsigned sp = pack2(sr.x, sr.y);
    const unsigned cp = pack2(cr.x, cr.y);
    sp |= ((sp & 0x7FFFu) ? 0u : 0x3080u) | ((sp & 0x7FFF0000u) ? 0u : 0x30800000u);
    const f32x2 S = {bf_lo(sp), bf_hi(sp)}, G = MOD(2) * S, A = f32x2{bf_lo(cp), bf_hi(cp)} * G, B = MOD(0) * G;
#undef MOD
    mtp = v4u{sel(lane, md[5], sp), tagA, md[3], tagB};
    auto rows4 = [&](const v4u& gx, const v4u& ga, const v4u& gb_, int i) __attribute__((always_inline)) {
      const f32x2 ma = {bf_lo(ga.x), bf_lo(ga.z)}, mb = {bf_lo(gb_.x), bf_lo(gb_.z)};
      const f32x2 ra_ = {__builtin_amdgcn_rcpf(bf_hi(ga.x)), __builtin_amdgcn_rcpf(bf_hi(ga.z))}, rb_ = {__builtin_amdgcn_rcpf(bf_hi(gb_.x)), __builtin_amdgcn_rcpf(bf_hi(gb_.z))};
      const f32x2 x0 = {bf_lo(gx.x), bf_lo(gx.y)}, x1 = {bf_lo(gx.z), bf_lo(gx.w)}, x2 = {bf_hi(gx.x), bf_hi(gx.y)}, x3 = {bf_hi(gx.z), bf_hi(gx.w)};
      const f32x2 h0 = __builtin_elementwise_fma((x0 - ma) * ra_, f32x2{A.x, A.x}, f32x2{B.x, B.x});
      const f32x2 h1 = __builtin_elementwise_fma((x1 - mb) * rb_, f32x2{A.x, A.x}, f32x2{B.x, B.x});
      const f32x2 h2 = __builtin_elementwise_fma((x2 - ma) * ra_, f32x2{A.y, A.y}, f32x2{B.y, B.y});
      const f32x2 h3 = __builtin_elementwise_fma((x3 - mb) * rb_, f32x2{A.y, A.y}, f32x2{B.y, B.y});
      *(LAS v2u*)(L + L_H + 480 + tid * 8 + i * 2048) = v2u{pack2(h0.x, h0.y), pack2(h1.x, h1.y)};
      *(LAS v2u*)(L + L_H + H_STRIDE + 480 + tid * 8 + i * 2048) = v2u{pack2(h2.x, h2.y), pack2(h3.x, h3.y)};
      const f32x2 y0 = x0 * f32x2{S.x, S.x}, y1 = x1 * f32x2{S.x, S.x}, y2 = x2 * f32x2{S.y, S.y}, y3 = x3 * f32x2{S.y, S.y};
      *(LAS f32x4*)(xbw + i * 2048) = f32x4{y0.x, y0.y, y1.x, y1.y};
      *(LAS f32x4*)(xbw + 8192 + i * 2048) = f32x4{y2.x, y2.y, y3.x, y3.y};
    };
    W1P(10, 2, lrw, 0); W1P(11, 3, lrw, 0);
    rows4(gx0, gs0, gs1, 0);
    W1P(12, 0, lrw, 12288); W1P(13, 1, lrw, 0); W1P(14, 2, lrw, 0); W1P(15, 3, lrw, 0);
    rows4(gx1, gs2, gs3, 1);
    W1P(16, 0, lsp, 0); W1P(17, 1, lsp, 0);
  }
  asm volatile("s_waitcnt vmcnt(18)" : "+v"(wt) :: "memory");
  BAR();
  const unsigned tb = now();
  STAMP(6);

  v4u w1f[32], w2f[28][2], mt0, mt1, sl0, sl1;
  auto wave_late = [&]() __attribute__((always_inline)) {
    constexpr int ctl[5] = {0x4E, 0x124, 0x128, 0x142, 0x143}, rm[5] = {0xF, 0xF, 0xF, 0xA, 0xC};
    int v = max((int)(mt0.z - tb), (int)(mt1.z - tb));
    static_for<0, 5>([&](auto Ic) __attribute__((always_inline)) { v = max(v, __builtin_amdgcn_update_dpp(v, v, ctl[Ic.v], rm[Ic.v], 0xF, false)); });
    return min(__builtin_amdgcn_readlane(v, 63), 300);
  };
  auto send = [&]() __attribute__((always_inline)) {
    STAMPB(26);
    asm volatile("buffer_load_dwordx4 %0, %2, %3, %4 offen sc1\n\tbuffer_load_dwordx4 %1, %2, %3, %4 offen offset:1024 sc1"
                 :: "v"(sl0), "v"(sl1), "v"(l16), "s"(rs), "s"(slo) : "memory");
    static_for<0, 8>([&](auto Nc) __attribute__((always_inline)) {
      if constexpr ((Nc.v & 3) == 0) grp<Nc.v * 1024, Nc.v * 1024 - 65536>(so, xl, xs2);
      xfer<Nc.v & 3, true>(l16, rs, so);
    });
  };
  {
    const bool rh = w & 1;
    constexpr ConvOrder O = conv_order(), OL = conv_order_last();
    v2u A[4][8][2];
    v4u B[4][8];
    f32x4 acc[8], keep[4];
#pragma unroll
    for (int u = 0; u < 8; ++u) {
      A[0][u][0] = trd(ka + 512 * u + 8);
      A[0][u][1] = trd(ka + 512 * u);
      B[0][7 - u] = lds128(hb + 64 * (8 * (7 - u) + 3));
    }
    SB();
    if (lane) mtp.z = tb;
    stg(rs, tid < 2 ? tid * 16 : OOB, OFF_MT + id * 64, mtp);
    SB();
    auto hand_over = [&](auto Pc) __attribute__((always_inline)) {
      constexpr int i = Pc.v;
      PIN(acc[2 * i]); PIN(acc[2 * i + 1]);
      const f32x4 ev = acc[2 * i], od = acc[2 * i + 1];
      const f32x4 snd = rh ? ev : od, own = rh ? od : ev;
      *(LAS f32x4*)(exa + i * 1024) = snd;
#pragma unroll
      for (int c = 0; c < 4; ++c) { float t = keep[i][c] + own[c]; PIN(t); keep[i][c] = t; }
    };
    auto put_z = [&](auto Pc) __attribute__((always_inline)) {
      constexpr int i = Pc.v;
      const auto s0 = __builtin_amdgcn_permlane16_swap(pack2(keep[2 * i][0], keep[2 * i][1]), pack2(keep[2 * i + 1][0], keep[2 * i + 1][1]), false, false);
      const auto s1 = __builtin_amdgcn_permlane16_swap(pack2(keep[2 * i][2], keep[2 * i][3]), pack2(keep[2 * i + 1][2], keep[2 * i + 1][3]), false, false);
      stg(rs, za, zs + i * 65536, v4u{guard(s0[0]), guard(s1[0]), guard(s0[1]), guard(s1[1])});
    };
    static_for<0, 144>([&](auto Kc) __attribute__((always_inline)) {
      constexpr int K = Kc.v, r = K / 36, k = K % 36, t = r < 3 ? O.t[k] : OL.t[k], u = r < 3 ? O.u[k] : OL.u[k];
      if constexpr (k == 0 && r > 0) asm volatile("" :: "v"(B[r][7]));
      mfma16_v<r == 0 && u == 0>(acc[t], v4u{A[r][u][0].x, A[r][u][0].y, A[r][u][1].x, A[r][u][1].y}, B[r][t - u]);
      SB();
      if constexpr (K == 24) { WAITV(13); __builtin_amdgcn_s_barrier(); FRESH(); }
      if constexpr (K >= 24 && K < 36) W1PULL(K - 24, K - 24);
      if constexpr (K == 40) { asm volatile("" :: "a"(w1f[11])); __builtin_amdgcn_s_barrier(); FRESH(); }
      if constexpr (K == 60) { WAITV(9); __builtin_amdgcn_s_barrier(); FRESH(); }
      if constexpr (K >= 60 && K < 70) W1PULL(K - 48, K - 48);
      if constexpr (K == 76) { asm volatile("" :: "a"(w1f[21])); __builtin_amdgcn_s_barrier(); FRESH(); }
      if constexpr (K == 101) { WAITV(9); __builtin_amdgcn_s_barrier(); FRESH(); }
      if constexpr (K >= 101 && K < 109) W1PULL(K - 77, K - 101);
      if constexpr (K == 109) w1f[22] = trd2(w1x, w1x + 512);
      if constexpr (K == 110) w1f[23] = trd2(w1y, w1y + 512);
      if constexpr (r == 2 && k < 4) keep[k] = *(LAS f32x4*)(xbr + k * 1024);
      constexpr int n = k - (r < 2 ? 0 : 4);
      if constexpr (r < 3 && n >= 0 && n < 24) {
        if constexpr (n % 3 == 2) B[r + 1][n / 3] = lds128(hb + 64 * (8 * (n / 3) - r + 2));
        else A[r + 1][n / 3][n % 3] = trd(ka + 512 * (n / 3) + 64 * (r + 1) + 8 - 8 * (n % 3));
      }
      if constexpr (K == 108 + 10) hand_over(IC<0>{});
      if constexpr (K == 108 + 19) hand_over(IC<1>{});
      if constexpr (K == 108 + 30) hand_over(IC<2>{});
      if constexpr (K < 14 && K % 4 == 1) W1P(18 + K / 4, (2 + K / 4) & 3, lsp, 4096);
      if constexpr (K == 17) W1P(22, 0, lrx, 0);
      if constexpr (K == 21) W1P(23, 0, lto, 0);
      if constexpr (K >= 41 && K < 65 && K % 3 == 2) W1P(24 + (K - 41) / 3, ((K - 41) / 3) & 3, lrw, (K - 41) / 3 * 1024);
      if constexpr (K >= 66 && K < 98 && K % 4 == 2) W2P((K - 66) / 4, ((K - 66) / 4) & 3, lrw, 8192 + (K - 66) / 4 * 1024);
      if constexpr (K >= 98 && K < 122 && K % 4 == 2) W2P((K - 66) / 4, ((K - 98) / 4) & 3, lsp, (K - 98) / 4 * 1024);
      if constexpr (K == MTK)
        asm volatile("buffer_load_dwordx4 %0, %2, %3, 0 offen sc1\n\tbuffer_load_dwordx4 %1, %2, %3, 0 offen offset:2048 sc1"
                     : "=&v"(mt0), "=&v"(mt1) : "v"(mto), "s"(rs) : "memory");
      SB();
    });
    STAMP(7);
    asm volatile("s_nop 7" : "+v"(acc[6]), "+v"(acc[7]));
    hand_over(IC<3>{});
    BAR();
#pragma unroll
    for (int i = 0; i < 4; ++i) keep[i] += *(LAS f32x4*)(eya + i * 1024);
    put_z(IC<0>{});
    put_z(IC<1>{});
    SB();
  }
  STAMP(8);

  static_assert(OFF_MT == 0, "table offset");
  const unsigned tc = now();
  asm volatile("s_waitcnt vmcnt(%2)" : "+v"(mt0), "+v"(mt1) : "n"(2 + (MTK < 118 ? (121 - MTK) / 4 : 0)) : "memory");
  if (__builtin_expect(__any(tagbad(tagbad(0u, mt0, tagA, tagB), mt1, tagA, tagB) != 0u), 0)) {
    _Pragma("nounroll") for (unsigned spin = 0; spin < SPIN_CAP; ++spin) {
      FRESH();
      COUNT(21);
      if (tagbad(0u, mt0, tagA, tagB)) mt0 = ldg(rs, mto, OFF_MT);
      if (tagbad(0u, mt1, tagA, tagB)) mt1 = ldg(rs, mto, OFF_MT + 2048);
      if (!__any(tagbad(tagbad(0u, mt0, tagA, tagB), mt1, tagA, tagB) != 0u)) break;
      PAUSE();
    }
  }
  *(LAS unsigned*)(mtw) = mt0.x; *(LAS unsigned*)(mtw + 128) = mt1.x; *(LAS unsigned*)(mtw + 1024) = mt0.z; *(LAS unsigned*)(mtw + 1152) = mt1.z;
  int wait = wave_late() + ZLAG;
  *(LAS int*)(L + L_ETA + w * 4) = wait;
  v4u hq[56], rc[4], res[2], sx[8];
  u64 nanv = 0;
  const int c0 = 7 * w + rot, c3 = c0 - 7 * (rot >> 2), c4 = c0 - 7 * ((rot + 1) >> 2);
  const int hb0 = OFF_HID + (rp * 4 + rg) * 114688, kb0 = OFF_CK + (rp * 4 + rg) * 4096 + w * 896, ckv = lane < 56 ? l16 : OOB;
#define CHUNK(n) (((n) % 28 / 4 < 3 ? c0 : (n) % 28 / 4 == 3 ? c3 : c4) + (n) % 28 / 4)
#define HFRAG(n) ldg(rs, l16 + ((n) & 3) * 1024, hb0 + CHUNK(n) * 4096 + ((n) / 28) * (2 * 114688))
#define HLOAD(n) hq[n] = HFRAG(n)
  const int hoff = OFF_HID + (rp * 4 * 112 + ct * 8 + 2 * w) * 1024;
  const int cko = OFF_CK + rp * 16384 + (ct * 4 + w) * 64;
#define CKPUT(sum, u) __builtin_amdgcn_raw_buffer_store_b32(ckey - red_step<1>(red_step<0>(sum)), rs, ckw, cko + ((u) ^ par) * 4096, SC1)
  const int sxo = OFF_SX + (rp * 4 + w) * 8192;
  int hv0, hv1, ckw;
  unsigned ckey;
  LAS unsigned char *xr[2], *rr, *rr2, *str, *stw, *w2a, *w2b;
  LAS unsigned char *w2sa = pa + (lsp - lrw - 8192), *w2sb = pb + (lsp - lrw - 8192);
  LAS unsigned char* uw = L + L_UVT + hf * 512 + (32 * w + (lane & 31)) * 4;
  LAS unsigned char* uvp = L + L_UVT + (32 * w + 4 * hf) * 4;
  int sxw = sel(lane & 8, OOB, (p4 * 8 + hf * 4 + blk * 2 + ((lane >> 2) & 1)) * 16);
  xr[0] = L + L_XR + tra;
  PIN(w2sa); PIN(w2sb); PIN(uw); PIN(uvp); PIN(sxw); PIN(xr[0]);
  BAR();
  STAMP(9);
  f32x4 uu[4], vv[4];
  {
    const v4u lt = lds128(L + L_ETA);
    const unsigned j0 = *(LAS u16*)(L + L_TS + (si * 32 + (lane >> 2)) * 2), j1 = *(LAS u16*)(L + L_TS + (si * 32 + 16 + (lane >> 2)) * 2);
    v4u qa[4];
#pragma unroll
    for (int i = 0; i < 4; ++i) qa[i] = lds128(qr + i * 256);
    SB();
    WAITV(2);
    asm volatile("" : "=v"(sl0), "=v"(sl1));
    wait = __builtin_amdgcn_readfirstlane(max(max((int)lt.x, (int)lt.y), max((int)lt.z, (int)lt.w)));
    bool sent = false;
    f32x4 cu[4];
    static_for<0, 32>([&](auto Sc) __attribute__((always_inline)) {
      constexpr int s = Sc.v;
      if constexpr (s == 0 || s == 16) {
        if (!sent && (int)(now() - tc) >= wait) { sent = true; send(); }
        SB();
      }
      mfma4_va<(s < 2), s & 7>(cu[2 * (s & 1)], cu[2 * (s & 1) + 1], qa[s >> 3], w1f[s]);
      if constexpr (s < 28 && (s & 1)) {
        constexpr int n = s >> 1;
        LAS unsigned char* src = (n < 8 ? ((n & 1) ? pb : pa) : ((n & 1) ? w2sb : w2sa)) + ((n >> 1) & 3) * 2048;
        w2f[n >> 1][n & 1] = trd2(src, src + 1024);
      }
      SB();
    });
    float i0 = __builtin_amdgcn_rcpf(bf_lo(j0)), i1 = __builtin_amdgcn_rcpf(bf_lo(j1));
    PIN(i0); PIN(i1);
    asm volatile("s_nop 3" : "+v"(cu[0]), "+v"(cu[1]), "+v"(cu[2]), "+v"(cu[3]));
    float un_ = (cu[0][0] + cu[1][0]) + (cu[2][0] + cu[3][0]);
    swap_add32(un_, -LOG2E * ((cu[0][1] + cu[1][1]) + (cu[2][1] + cu[3][1])));
    *(LAS float*)uw = un_;
    SB();
    if (!sent) {
      while ((int)(now() - tc) < wait) { }
      send();
    }
    static_for<0, 8>([&](auto Nc) __attribute__((always_inline)) {
      if constexpr ((Nc.v & 3) == 0) grp<32768 + Nc.v * 1024, Nc.v * 1024 - 65536>(so, xl, xs3);
      xfer<Nc.v & 3, true>(l16, rs, so);
    });
    STAMPB(27);
    asm volatile("s_waitcnt vmcnt(16)" : "+v"(sl0), "+v"(sl1) :: "memory");
    STAMPB(28);
    if (__builtin_expect(__any(max4(max4(0u, sl0), sl1) == POISON), 0)) {
      _Pragma("nounroll") for (unsigned spin = 0; spin < SPIN_CAP; ++spin) {
        FRESH();
        if (spin) PAUSE();
        COUNT(22);
        sl0 = ldg(rs, l16, slo);
        sl1 = ldg(rs, l16 + 1024, slo);
        if (!__any(max4(max4(0u, sl0), sl1) == POISON)) break;
      }
    }
    float sm[8], sq[8];
#pragma unroll
    for (int i = 0; i < 4; ++i) {
      const f32x2 a = f32x2{bf_lo(sl0[i]), bf_hi(sl0[i])} * f32x2{i0, i0}, c = f32x2{bf_lo(sl1[i]), bf_hi(sl1[i])} * f32x2{i1, i1};
      const f32x2 s2 = a + c, q2 = __builtin_elementwise_fma(a, a, c * c);
      sm[2 * i] = s2.x; sm[2 * i + 1] = s2.y; sq[2 * i] = q2.x; sq[2 * i + 1] = q2.y;
    }
#pragma unroll
    for (int i = 0; i < 4; ++i) { swap_add32(sm[i], sm[i + 4]); swap_add32(sq[i], sq[i + 4]); }
#pragma unroll
    for (int i = 0; i < 2; ++i) { swap_add16(sm[i], sm[i + 2]); swap_add16(sq[i], sq[i + 2]); }
#pragma unroll
    for (int i = 0; i < 2; ++i) { sm[i] = dpp_add<0x124>(dpp_add<0x128>(sm[i])); sq[i] = dpp_add<0x124>(dpp_add<0x128>(sq[i])); }
    const bool od = lane & 4;
    stg(rs, sxw, OFF_SX + ((rp * 4 + w) * 16 + si) * 512,
        v4u{f2u(od ? sm[1] : sm[0]), tagA, f2u(od ? sq[1] : sq[0]), tagB});
    SB();
  }
  STAMPB(29);
  asm volatile("" :: "a"(w2f[6][1]));
  W2P(14, 0, lsp, 0); W2P(15, 1, lsp, 0); W2P(16, 2, lsp, 0); W2P(17, 3, lsp, 0); W2P(18, 0, lsp, 4096); W2P(19, 1, lsp, 0);
  WAITV(15);
  STAMP(10);
  BAR();
  STAMP(11);

#define XA(t) (xr[(t) >> 6] + ((t) & 63) * 1024)
#define XF(t) trd2(XA(t), XA(t) + 256)
#define XEN
#define REDO(p) \
  if (XEN __builtin_expect(nanv != 0, 0)) { \
    unsigned m0s; \
    asm volatile("s_mov_b32 %0, m0" : "=s"(m0s)); \
    for (unsigned spin = 0; spin < SPIN_CAP / 16; ++spin) { \
      COUNT(22); \
      _Pragma("nounroll") for (int i = 0; i < 8; ++i) dma_x4(xg + ((p) ^ par) * 32768 + i * 4096, l16, L_XR + (p) * 32768 + i * 4096); \
      WAITV(0); \
      _Pragma("unroll") for (int s = 0; s < 32; ++s) { \
        const v4u t = XF(32 * (p) + s); \
        if (s == 0) mfma_av<true>(acc[(p) % 3], w1f[s], t); else mfma_av<false>(acc[(p) % 3], w1f[s], t); \
        SB(); \
      } \
      mfma_done(acc[(p) % 3]); \
      if (!__any(!(acc[(p) % 3][0] == acc[(p) % 3][0]))) break; \
    } \
    set_m0(m0s); \
  }
  f32x16 acc[3];
  f32x2 ec[4];
  float ep[48], ee[48], ey[48], tl[16];
  unsigned cs = 0, sbad = 0;
  float ssm = 0.f, ssq = 0.f;
  v4u x[128], eo;
#pragma unroll
  for (int t = 0; t < 4; ++t) x[t] = XF(t);
  SB();
  auto eslot = [&](auto Sc) __attribute__((always_inline)) {
    static_for<0, 48>([&](auto Vc) __attribute__((always_inline)) {
      constexpr int V = Vc.v, k = V & 15, st = Sc.v - epi_slot(V);
      if constexpr (st == 6 && (k & 1)) {
        unsigned o = pack2(ey[V - 1], ey[V]);
        PIN(o);
        eo[(k >> 1) & 3] = o;
        if constexpr (k == 7) { stg(rs, hv0, hoff + ((V >> 4) ^ par) * 114688, eo); cs = sum4(0u, eo); PIN(cs); }
        if constexpr (k == 15) { stg(rs, hv1, hoff + ((V >> 4) ^ par) * 114688, eo); CKPUT(sum4(cs, eo), V >> 4); }
      }
      if constexpr (st == 5) { ey[V] = ep[V] * ee[V]; PIN(ey[V]); }
      if constexpr (st == 4) { ee[V] = __builtin_amdgcn_rcpf(ee[V]); PIN(ee[V]); }
      if constexpr (st == 3) { ee[V] = 1.0f + ee[V]; PIN(ee[V]); }
      if constexpr (st == 2) { ee[V] = __builtin_amdgcn_exp2f(ep[V]); PIN(ee[V]); }
      if constexpr (st == 1) { ep[V] = __builtin_fmaf(ec[V >> 4].x, acc[(V >> 4) % 3][k], ep[V]); PIN(ep[V]); }
      if constexpr (st == 0) { ep[V] = __builtin_fmaf(ec[V >> 4].y, uu[k >> 2][k & 3], vv[k >> 2][k & 3]); PIN(ep[V]); }
    });
    SB();
  };
  static_for<0, 64>([&](auto Jc) __attribute__((always_inline)) {
    constexpr int J = Jc.v, p = J >> 4, j = J & 15;
    asm volatile("" :: "v"(x[2 * J + 1]));
    mfma_av<j == 0>(acc[p % 3], w1f[2 * j], x[2 * J]);
    if constexpr (J < SXJ || (J >= SXJ + 4 && J < 20)) {
      constexpr int n = J < SXJ ? J : J - 4;
      if constexpr ((n & 3) == 0) grp<65536 + (n >> 3) * 32768 + (n & 4) * 1024, (n & 4) * 1024>(so, xl, n < 8 ? xs2 : xs3);
      xfer<n & 3, true>(l16, rs, so);
    }
    if constexpr (J >= SXJ && J < SXJ + 4)
      asm volatile("buffer_load_dwordx4 %0, %2, %3, %4 offen offset:%5 sc1\n\tbuffer_load_dwordx4 %1, %2, %3, %4 offen offset:%6 sc1"
                   : "=&v"(sx[2 * (J - SXJ)]), "=&v"(sx[2 * (J - SXJ) + 1])
                   : "v"(l16), "s"(rs), "s"(sxo + ((J - SXJ) & 2) * 2048), "n"(((J - SXJ) & 1) * 2048), "n"(((J - SXJ) & 1) * 2048 + 1024) : "memory");
    if constexpr (J == 43) asm volatile("" :: "a"(w2f[13][1]));
    if constexpr (J >= 20 && J < 28) W2P(J, J & 3, xl, (J - 20) * 1024);
    if constexpr (J >= 28 && J < 34) W2P(J, J & 3, lsp, (J - 28) * 1024);
    if constexpr (J >= 35 && J < 43) W2P(J - 1, (J - 35) & 3, xl, 32768 + (J - 35) * 1024);
    if constexpr (J >= 43 && J < 51) W2P(J - 1, (J - 43) & 3, xl, (J - 43) * 1024);
    static_for<hc_upto(J), hc_upto(J + 1)>([&](auto Nc) __attribute__((always_inline)) { HLOAD(Nc.v); });
    SB();
    constexpr bool sym = epi_pair(J) || J < 19 || J > 60;
    if constexpr (J < 62) { x[2 * J + 4] = XF(2 * J + 4); if constexpr (!sym) x[2 * J + 5] = XF(2 * J + 5); }
    if constexpr (J >= 38 && J < 42) w2f[10 + J - 38][0] = trd2(w2a + (J - 38) * 2048, w2a + (J - 38) * 2048 + 1024);
    SB();
    if constexpr (epi_pair(J)) eslot(IC<2 * J>{});
    mfma_av<false>(acc[p % 3], w1f[2 * j + 1], x[2 * J + 1]);
    if constexpr (sym && J < 62) x[2 * J + 5] = XF(2 * J + 5);
    if constexpr (J >= 38 && J < 42) w2f[10 + J - 38][1] = trd2(w2b + (J - 38) * 2048, w2b + (J - 38) * 2048 + 1024);
    if constexpr (J >= 19 && J < 25) w2f[7 + ((J - 19) >> 1)][(J - 19) & 1] = trd2((((J - 19) & 1) ? w2sb : w2sa) + ((J - 19) >> 1) * 2048, (((J - 19) & 1) ? w2sb : w2sa) + ((J - 19) >> 1) * 2048 + 1024);
    SB();
    if constexpr (J == 10) { waitv<c_between(-1, 10)>(); __builtin_amdgcn_s_barrier(); FRESH(); }
    if constexpr (J == 18) waitv<c_between(7, 18)>();
    if constexpr (J == 34) waitv<c_between(19, 34)>();
    if constexpr (J == 37) waitv<c_between(27, 37)>();
    if constexpr (J == 1) { xr[1] = xr[0] + 65536; rr = xr[0] + (4 * dt + w) * 1024; rr2 = rr + 65536; PIN(xr[1]); PIN(rr); PIN(rr2); }
    if constexpr (J == 2) { hv0 = dt == 7 ? OOB : l16; hv1 = dt == 7 ? OOB : l16 + 1024; PIN(hv0); PIN(hv1); }
    if constexpr (J == 3) { ckw = (p4 || dt == 7) ? OOB : lane; ckey = lane ? 0u : tagA; PIN(ckw); PIN(ckey); }
    if constexpr (J == 4) { str = L + L_STAT + (lane & 31) * 8; stw = str + (w ^ par) * 256; PIN(str); PIN(stw); }
    if constexpr (J == 5) { w2a = pa + (xl - lrw - 8192); w2b = pb + (xl - lrw - 8192); PIN(w2a); PIN(w2b); }
    if constexpr (J >= 6 && J < 10) { uu[J - 6] = *(LAS f32x4*)(uvp + (J - 6) * 32); vv[J - 6] = *(LAS f32x4*)(uvp + 512 + (J - 6) * 32); }
    if constexpr (J == EO - 4 || J == EO - 3) {
      constexpr int i = 4 * (J - EO + 4);
      asm volatile("s_waitcnt vmcnt(%4)" : "+v"(sx[i]), "+v"(sx[i + 1]), "+v"(sx[i + 2]), "+v"(sx[i + 3]) : "n"(c_between(SXJ + 1 + i / 2, J)) : "memory");
      unsigned b0 = tagbad(0u, sx[i], tagA, tagB), b1 = tagbad(0u, sx[i + 1], tagA, tagB);
      float m0 = u2f(sx[i].x) + u2f(sx[i + 1].x), m1 = u2f(sx[i + 2].x) + u2f(sx[i + 3].x);
      PIN(m0); PIN(m1);
      float q0 = u2f(sx[i].z) + u2f(sx[i + 1].z), q1 = u2f(sx[i + 2].z) + u2f(sx[i + 3].z);
      PIN(q0); PIN(q1);
      sbad |= tagbad(b0, sx[i + 2], tagA, tagB) | tagbad(b1, sx[i + 3], tagA, tagB);
      ssm += m0 + m1; PIN(ssm);
      ssq += q0 + q1; PIN(ssq);
      PIN(sbad);
      SB();
    }
    if constexpr (J == EO - 2) {
      if (__builtin_expect(__any(sbad != 0u), 0)) {
        _Pragma("nounroll") for (unsigned spin = 0; spin < SPIN_CAP; ++spin) {
          FRESH();
          if (spin) PAUSE();
          COUNT(23);
          sbad = 0; ssm = 0.f; ssq = 0.f;
#pragma unroll
          for (int i = 0; i < 8; ++i) sx[i] = ldg(rs, l16 + (i & 3) * 1024, sxo + (i >> 2) * 4096);
#pragma unroll
          for (int i = 0; i < 8; i += 4) {
            sbad = tagbad(tagbad(tagbad(tagbad(sbad, sx[i], tagA, tagB), sx[i + 1], tagA, tagB), sx[i + 2], tagA, tagB), sx[i + 3], tagA, tagB);
            ssm += (u2f(sx[i].x) + u2f(sx[i + 1].x)) + (u2f(sx[i + 2].x) + u2f(sx[i + 3].x));
            ssq += (u2f(sx[i].z) + u2f(sx[i + 1].z)) + (u2f(sx[i + 2].z) + u2f(sx[i + 3].z));
          }
          if (!__any(sbad != 0u)) break;
        }
      }
      const float mean = swap32_add(ssm) * (1.0f / 512.0f);
      const float ms = __builtin_fmaf(swap32_add(ssq), 1.0f / 512.0f, 1e-5f);
      const float a = -LOG2E * __builtin_amdgcn_rsqf(fmaxf(__builtin_fmaf(-mean, mean, ms), 1e-5f));
      *(LAS f32x2*)(stw) = f32x2{a, -a * mean};
      SB();
    }
    if constexpr (J == EO - 1) {
      asm volatile("s_waitcnt lgkmcnt(4)" ::: "memory");
      __builtin_amdgcn_s_barrier();
      FRESH();
#pragma unroll
      for (int i = 0; i < 4; ++i) ec[i] = *(LAS f32x2*)(str + i * 256);
      SB();
    }
    if constexpr (!epi_pair(J)) eslot(IC<2 * J>{});
    eslot(IC<2 * J + 1>{});
    if constexpr (J == 16) asm volatile("s_nop 4");
    if constexpr (J >= 61) {
#pragma unroll
      for (int k = (J - 61) * 6; k < (J == 63 ? 16 : (J - 60) * 6); ++k) { tl[k] = __builtin_fmaf(ec[3].y, uu[k >> 2][k & 3], vv[k >> 2][k & 3]); PIN(tl[k]); }
      SB();
    }
    if constexpr (j == 0 && p >= 1) { PIN(acc[(p - 1) % 3]); nanv = __builtin_amdgcn_ballot_w64(!(acc[(p - 1) % 3][0] == acc[(p - 1) % 3][0])); asm volatile("" : "+s"(nanv)); }
    if constexpr (j == 1 && p >= 1) {
      if constexpr (p == 1) REDO(0)
      if constexpr (p == 2) REDO(1)
      if constexpr (p == 3) REDO(2)
      rc[p - 1] = trd2((p == 3 ? rr2 : rr) + ((p - 1) & 1) * 32768, (p == 3 ? rr2 : rr) + ((p - 1) & 1) * 32768 + 256);
    }
    if constexpr (j == 2 && p >= 1) {
      asm volatile("s_waitcnt lgkmcnt(4)" ::: "memory");
      __builtin_amdgcn_s_barrier();
      FRESH();
    }
    SB();
    if constexpr (J == 15) STAMP(16);
    if constexpr (J == 31) STAMP(17);
    if constexpr (J == 47) STAMP(18);
  });
  STAMP(12);
  static_assert(EO <= 33 && SXJ >= 8 && SXJ <= 16 && MTK > 101 && hc_upto(64) <= 24 && hc_upto(57) <= 20 && epi_slot(31) < epi_slot(32) && epi_slot(16) + 1 >= 2 * 34 && epi_slot(32) + 1 >= 2 * 50 && epi_slot(47) + 6 < 128, "schedule");
  static_for<hc_upto(64), hc_upto(64) + HT0>([&](auto Ic) __attribute__((always_inline)) { HLOAD(Ic.v); });
  SB();
  asm volatile("s_nop 7" : "+v"(acc[0]));
  nanv = __builtin_amdgcn_ballot_w64(!(acc[0][0] == acc[0][0]));
  REDO(3)
#pragma unroll
  for (int h = 0; h < 2; ++h) {
    v4u o;
#pragma unroll
    for (int i = 0; i < 4; ++i) {
      const int k = 8 * h + 2 * i;
      const f32x2 t = {tl[k], tl[k + 1]};
      const f32x2 pp = __builtin_elementwise_fma(f32x2{ec[3].x, ec[3].x}, f32x2{acc[0][k], acc[0][k + 1]}, t);
      const f32x2 d = f32x2{__builtin_amdgcn_exp2f(pp.x), __builtin_amdgcn_exp2f(pp.y)} + f32x2{1.0f, 1.0f};
      const f32x2 y = pp * f32x2{__builtin_amdgcn_rcpf(d.x), __builtin_amdgcn_rcpf(d.y)};
      o[i] = pack2(y.x, y.y);
    }
    PIN(o);
    if (h == 0) static_for<hc_upto(64) + HT0, hc_upto(64) + HT0 + HT1>([&](auto Ic) __attribute__((always_inline)) { HLOAD(Ic.v); });
    stg(rs, h ? hv1 : hv0, hoff + (3 ^ par) * 114688, o);
    if (h) CKPUT(sum4(cs, o), 3); else cs = sum4(0u, o);
    SB();
  }
  static_for<hc_upto(64) + HT0 + HT1, 28>([&](auto Ic) __attribute__((always_inline)) { HLOAD(Ic.v); });
  rc[3] = trd2(rr2 + 32768, rr2 + 32768 + 256);
  SB();
  STAMP(13);

  f32x16 d[2][2];
  LAS unsigned char* redw = L + L_XR + w * 8192 + l16;
  LAS unsigned char* redr = L + L_XR + w * 2048 + l16;
#define REDW(un, q) *(LAS f32x4*)(redw + (un) * 32768 + (q) * 1024) = \
    f32x4{d[un][(q) >> 2][4 * ((q) & 3)], d[un][(q) >> 2][4 * ((q) & 3) + 1], d[un][(q) >> 2][4 * ((q) & 3) + 2], d[un][(q) >> 2][4 * ((q) & 3) + 3]}
  LAS unsigned char *w2ua = w2a + 65536, *w2ub = w2b + 65536;
  PIN(w2ua); PIN(w2ub);
#define CHECK(un) \
  if (__builtin_expect((unsigned)__builtin_amdgcn_readlane((int)m[un], 63) != 14u * tagA, 0)) { \
    _Pragma("nounroll") for (unsigned spin = 0; spin < SPIN_CAP; ++spin) { \
      FRESH(); \
      COUNT(24 + (un)); \
      v4u qq[28]; \
      const v4u kk = ldg(rs, ckv, kb0 + (un) * 8192); \
      static_for<0, 12>([&](auto Nc) __attribute__((always_inline)) { qq[Nc.v] = HFRAG(28 * (un) + Nc.v); }); \
      SB(); \
      unsigned t = sum4(0u, kk); \
      static_for<0, 7>([&](auto Cc) __attribute__((always_inline)) { \
        constexpr int c = Cc.v; \
        const v4u* q = &qq[4 * c]; \
        if constexpr (c + 3 < 7) { _Pragma("unroll") for (int i = 0; i < 4; ++i) qq[4 * c + 12 + i] = ldg(rs, l16 + i * 1024, hb0 + CHUNK(4 * c + 12) * 4096 + (un) * (2 * 114688)); } \
        SB(); \
        t = sum8(sum8(t, q[0], q[1]), q[2], q[3]); \
        mfma_av<c == 0>(d[un][0], w2f[4 * c][0], q[0]); mfma_av<c == 0>(d[un][1], w2f[4 * c][1], q[0]); \
        _Pragma("unroll") for (int i = 1; i < 4; ++i) { mfma_av<false>(d[un][0], w2f[4 * c + i][0], q[i]); mfma_av<false>(d[un][1], w2f[4 * c + i][1], q[i]); } \
        SB(); \
      }); \
      mfma_done(d[un][1]); \
      PIN(d[un][0]); \
      if ((unsigned)__builtin_amdgcn_readlane((int)red_all(t), 63) == 14u * tagA) break; \
      PAUSE(); \
    } \
    if (un) { _Pragma("unroll") for (int q = 0; q < 4; ++q) REDW(1, q); } \
  }
  unsigned m[2] = {0u, 0u};
  v4u gbv, scv, ck[2];
  f32x4 pr[8];
  float gb[8], rx[2][8];
  unsigned orow = (rp * 64 + ((lane & 31) >> 3) * 16 + rg * 8 + (lane & 7)) * 1024 + dt * 128 + w * 32 + hf * 16;
  static_for<0, 112>([&](auto Xc) __attribute__((always_inline)) {
    constexpr int X = Xc.v, I = X < 100 ? X >> 1 : 50 + (X - 100) % 6, e = X < 100 ? X & 1 : (X - 100) / 6, un = I / 28;
    mfma_av<I % 28 == 0>(d[un][e], w2f[I % 28][e], hq[I]);
    SB();
    if constexpr (X < 100 && e == 0) {
      if constexpr (I < 3) PULL(14 + I, w2sa, w2sb, I * 2048);
      if constexpr (I >= 3 && I < 7) PULL(14 + I, w2a, w2b, 32768 + (I - 3) * 2048);
      if constexpr (I >= 7 && I < 11) PULL(14 + I, w2a, w2b, (I - 7) * 2048);
      if constexpr (I == 21) waitv<hl_upto(21) - 28 + 1>();
      if constexpr (I >= 21 && I < 24) PULL(4 + I, w2ua, w2ub, (I - 21) * 2048);
      if constexpr (I == 28) acc4(m[0], ck[0]);
      if constexpr (I >= 29 && I < 35) { m[0] = red_step<I - 29>(m[0]); PIN(m[0]); }
      if constexpr (I == 35) CHECK(0)
      if constexpr (I >= 35 && I < 43) REDW(0, I - 35);
    }
    if constexpr (X < 100 && e == 1) {
      if constexpr (I < 6) W2P(50 + I, I & 3, xl, 65536 + I * 1024);
      static_for<hl_upto(I), hl_upto(I + 1)>([&](auto Nc) __attribute__((always_inline)) { HLOAD(Nc.v); });
      if constexpr (I == 6) ck[0] = ldg(rs, ckv, kb0);
      if constexpr (I == HL1 + 16 / HLR) ck[1] = ldg(rs, ckv, kb0 + 8192);
    }
    if constexpr (X < 100 ? e == 1 : e == 0) acc4(m[un], hq[I]);
    if constexpr (X == 105) acc4(m[1], ck[1]);
    if constexpr (X >= 106) { m[1] = red_step<X - 106>(m[1]); PIN(m[1]); }
    if constexpr (X == 23 || X == 24) { const v4u ev = rc[2 * (X - 23)], od = rc[2 * (X - 23) + 1]; res[X - 23] = (rg ^ par) ? od : ev; PIN(res[X - 23]); }
    if constexpr (X == 22) { gbv = lds128(L + L_TG + (64 * dt + 16 * w + 8 * hf) * 2); scv = lds128(L + L_TS + (64 * dt + 16 * w + 8 * hf) * 2); PIN(orow); }
    if constexpr (X >= 26 && X < 42 && (X & 1) == 0) {
      constexpr int q = (X - 26) >> 1;
      const float inv = __builtin_amdgcn_rcpf((q & 1) ? bf_hi(scv[q >> 1]) : bf_lo(scv[q >> 1]));
      gb[q] = -LN2 * ((q & 1) ? bf_hi(gbv[q >> 1]) : bf_lo(gbv[q >> 1]));
      rx[0][q] = inv * ((q & 1) ? bf_hi(res[0][q >> 1]) : bf_lo(res[0][q >> 1]));
      rx[1][q] = inv * ((q & 1) ? bf_hi(res[1][q >> 1]) : bf_lo(res[1][q >> 1]));
      PIN(gb[q]); PIN(rx[0][q]); PIN(rx[1][q]);
    }
    if constexpr (X == 86) BAR();
    if constexpr (X == 86 || X == 88) {
#pragma unroll
      for (int k = X - 86; k < X - 84; ++k) { pr[2 * k] = *(LAS f32x4*)(redr + k * 8192); pr[2 * k + 1] = *(LAS f32x4*)(redr + k * 8192 + 1024); }
    }
    if constexpr (X >= 91 && X < 99) {
      constexpr int q = X - 91;
      float t = (pr[q >> 2][q & 3] + pr[2 + (q >> 2)][q & 3]);
      PIN(t);
      float t2_ = (pr[4 + (q >> 2)][q & 3] + pr[6 + (q >> 2)][q & 3]);
      PIN(t2_);
      rx[0][q] = __builtin_fmaf(gb[q], t + t2_, rx[0][q]);
      PIN(rx[0][q]);
    }
    if constexpr (X == 99)
      *(GAS v4u*)((GAS unsigned char*)out + orow) = v4u{pack2(rx[0][0], rx[0][1]), pack2(rx[0][2], rx[0][3]), pack2(rx[0][4], rx[0][5]), pack2(rx[0][6], rx[0][7])};
    if constexpr (X == 84) __builtin_amdgcn_raw_buffer_store_b32(epoch + 1u, rs, (b | tid) ? OOB : 0, OFF_EP, SC1);
    if constexpr (X == 108) asm volatile("s_nop 3" : "+v"(d[1][0]));
    if constexpr (X >= 108) REDW(1, X - 108);
    SB();
    if constexpr (X == 55) STAMP(14);
  });
  STAMP(19);
  asm volatile("s_nop 7" : "+v"(d[1][1]));
  CHECK(1)
#pragma unroll
  for (int q = 4; q < 8; ++q) REDW(1, q);
  BAR();
  {
#pragma unroll
    for (int k = 0; k < 8; ++k) pr[k] = *(LAS f32x4*)(redr + (k >> 1) * 8192 + 32768 + (k & 1) * 1024);
    SB();
    const f32x4 s0 = (pr[0] + pr[2]) + (pr[4] + pr[6]), s1 = (pr[1] + pr[3]) + (pr[5] + pr[7]);
    float y[8];
#pragma unroll
    for (int q = 0; q < 8; ++q) y[q] = __builtin_fmaf(gb[q], q < 4 ? s0[q & 3] : s1[q & 3], rx[1][q]);
    *(GAS v4u*)((GAS unsigned char*)(out + 1048576) + orow) = v4u{pack2(y[0], y[1]), pack2(y[2], y[3]), pack2(y[4], y[5]), pack2(y[6], y[7])};
  }
  STAMP(15);
}

extern "C" void kernel_launch(void* const* d_in, const int* in_sizes, int n_in, void* d_out, int out_size, void* d_ws, size_t ws_size,
                              hipStream_t stream) {
  (void)in_sizes; (void)n_in; (void)out_size; (void)ws_size;
  (void)hipFuncSetAttribute((const void*)s4_block_kernel, hipFuncAttributeMaxDynamicSharedMemorySize, LDS_BYTES);
  s4_block_kernel<<<256, 256, LDS_BYTES, stream>>>((cptr)d_in[0], (cptr)d_in[1], (cptr)d_in[2], (cptr)d_in[3], (cptr)d_in[4], (cptr)d_in[5], (cptr)d_in[6],
                                                   (cptr)d_in[7], (cptr)d_in[8], (cptr)d_in[10], (cptr)d_in[11], (cptr)d_in[12], (cptr)d_in[13],
                                                   (unsigned char*)d_out, (unsigned char*)d_ws, (cptr)d_in[9]);
}
```

```cpp
#include <hip/hip_runtime.h>

#define GAS __attribute__((address_space(1)))
#define LAS __attribute__((address_space(3)))
#define DI __device__ __forceinline__
typedef unsigned v4u __attribute__((ext_vector_type(4)));
typedef unsigned v2u __attribute__((ext_vector_type(2)));
typedef float f32x2 __attribute__((ext_vector_type(2)));
typedef float f32x4 __attribute__((ext_vector_type(4)));
typedef float f32x16 __attribute__((ext_vector_type(16)));
typedef __bf16 bf16x2 __attribute__((ext_vector_type(2)));
typedef __bf16 bf16x8 __attribute__((ext_vector_type(8)));
typedef short s16x4 __attribute__((ext_vector_type(4)));
typedef unsigned short u16;
typedef unsigned long long u64;

constexpr int SET_BASE = 0;
constexpr int OFF_EP = 49152;
constexpr int OFF_MT = 0;
constexpr int OFF_SG = 32768;
constexpr int OFF_MG = 65536;
constexpr int OFF_SX = 524288;
constexpr int OFF_XC = 458752;
constexpr int OFF_XG = 1 << 20;
constexpr int OFF_Z = OFF_XG + (2 << 20);
constexpr int OFF_HID = OFF_Z + 2097152;
constexpr int OFF_CK = OFF_HID + 7340032;
constexpr int SET_BYTES = OFF_CK + 262144;
static_assert(OFF_MG + 393216 <= OFF_XC && OFF_XC + 65536 <= OFF_SX, "set size");

constexpr int L_TS = 0, L_TB = 1024, L_TG = 2048;
constexpr int L_UVT = 3072;
constexpr int L_M = 4096;
constexpr int L_UVS = L_M;
constexpr int UVS_ROW = 544, UVS_CH = 57 * UVS_ROW;
constexpr int L_KC = L_M;
constexpr int KC_STRIDE = 4160;
constexpr int L_H = L_KC + 8 * KC_STRIDE;
constexpr int H_STRIDE = 4576;
constexpr int L_XB = L_H + 2 * H_STRIDE;
constexpr int L_RX = L_XB + 16384;
constexpr int L_ETA = L_RX + 4096;
constexpr int L_R = L_M + 65536;
constexpr int L_TOP = L_M + 131072 + 24576;
constexpr int L_KT = L_TOP + 16;
constexpr int L_STAT = L_TOP;
constexpr int L_XR = L_M;
constexpr int L_SP = L_M + 131072;
constexpr int LDS_BYTES = 163840;
static_assert(L_TOP + 4096 <= LDS_BYTES && L_ETA + 16 <= L_R && L_UVS + 2 * UVS_CH <= L_R && L_SP + 24576 <= L_TOP, "LDS map");
static_assert(L_H % 16 == 0 && L_XB % 16 == 0 && L_RX % 16 == 0 && L_KT % 16 == 0, "alignment");

#define ZLAG 23
#define MTK 119
#define SXJ 16
#define EOV 30
#define HC0 55
#define HC1 61
#define HL0 9
#define HL1 21
#define HLR 1
#define HT0 2
#define HT1 6
constexpr int EO = EOV;
constexpr int CODE_BYTES = 35072;
constexpr int SC1 = 16;
constexpr int OOB = 0x7FFFFF00;
constexpr unsigned SPIN_CAP = 1u << 15;
constexpr unsigned POISON = 0xFFFFFFFFu;
constexpr float LOG2E = 1.4426950408889634f, LN2 = 0.6931471805599453f, INV2PI = 0.15915494309189535f;

#define SB() __builtin_amdgcn_sched_barrier(0)
#define WAITV(n) asm volatile("s_waitcnt vmcnt(" #n ")" ::: "memory")
#define PIN(x) asm volatile("" : "+v"(x))
#define FRESH() asm volatile("" ::: "memory")
#define PAUSE() asm volatile("s_sleep 4" ::: "memory")
#define BAR() do { asm volatile("s_waitcnt lgkmcnt(0)" ::: "memory"); __builtin_amdgcn_s_barrier(); asm volatile("" ::: "memory"); } while (0)
#define STAMP(i)
#define SARG
#define SPASS
#define COUNT(i)

#define STAMPC(i)
#define STAMPA(i)
#define STAMPB(i) STAMP(i)
DI float u2f(unsigned u) { return __builtin_bit_cast(float, u); }
DI unsigned f2u(float f) { return __builtin_bit_cast(unsigned, f); }
DI float dot2(unsigned u, float c) { const bf16x2 v = __builtin_bit_cast(bf16x2, u); return __builtin_amdgcn_fdot2_f32_bf16(v, v, c, false); }
DI float bf_lo(unsigned u) { return __builtin_bit_cast(float, u << 16); }
DI float bf_hi(unsigned u) { return __builtin_bit_cast(float, u & 0xFFFF0000u); }
DI unsigned pack2(float a, float b) { f32x2 v = {a, b}; return __builtin_bit_cast(unsigned, __builtin_convertvector(v, bf16x2)); }
DI float rbf(float a) { return bf_lo(pack2(a, a)); }
DI unsigned sel(bool c, unsigned a, unsigned b) { asm("" : "+v"(a), "+v"(b)); return c ? a : b; }
DI unsigned guard(unsigned u) { return u < 0xFFFFFFFEu ? u : 0xFFFFFFFEu; }
DI unsigned max4(unsigned m, v4u v) {
  asm("v_max3_u32 %0, %0, %1, %2\n\tv_max3_u32 %0, %0, %3, %4" : "+v"(m) : "v"(v.x), "v"(v.y), "v"(v.z), "v"(v.w));
  return m;
}
DI f32x4 mfma16(v4u a, v4u b, f32x4 c) { return __builtin_amdgcn_mfma_f32_16x16x32_bf16(__builtin_bit_cast(bf16x8, a), __builtin_bit_cast(bf16x8, b), c, 0, 0, 0); }
DI f32x16 mfma32(v4u a, v4u b, f32x16 c) { return __builtin_amdgcn_mfma_f32_32x32x16_bf16(__builtin_bit_cast(bf16x8, a), __builtin_bit_cast(bf16x8, b), c, 0, 0, 0); }
template <bool FIRST> DI void mfma_av(f32x16& acc, const v4u& a, const v4u& b) {
  if (FIRST) asm volatile("v_mfma_f32_32x32x16_bf16 %0, %1, %2, 0" : "=&v"(acc) : "a"(a), "v"(b));
  else asm volatile("v_mfma_f32_32x32x16_bf16 %0, %1, %2, %0" : "+v"(acc) : "a"(a), "v"(b));
}
template <bool FIRST, int BLK> DI void mfma4_va(f32x4& c0, f32x4& c1, const v4u& a, const v4u& b) {
  const v2u al = {a.x, a.y}, ah = {a.z, a.w}, bl = {b.x, b.y}, bh = {b.z, b.w};
  if (FIRST) asm volatile("v_mfma_f32_4x4x4_16b_bf16 %0, %2, %4, 0 cbsz:3 abid:%6\n\tv_mfma_f32_4x4x4_16b_bf16 %1, %3, %5, 0 cbsz:3 abid:%6"
                          : "=&v"(c0), "=&v"(c1) : "v"(al), "v"(ah), "a"(bl), "a"(bh), "n"(BLK));
  else asm volatile("v_mfma_f32_4x4x4_16b_bf16 %0, %2, %4, %0 cbsz:3 abid:%6\n\tv_mfma_f32_4x4x4_16b_bf16 %1, %3, %5, %1 cbsz:3 abid:%6"
                    : "+v"(c0), "+v"(c1) : "v"(al), "v"(ah), "a"(bl), "a"(bh), "n"(BLK));
}
template <bool FIRST> DI void mfma16_v(f32x4& acc, const v4u& a, const v4u& b) {
  if (FIRST) asm volatile("v_mfma_f32_16x16x32_bf16 %0, %1, %2, 0" : "=&v"(acc) : "v"(a), "v"(b));
  else asm volatile("v_mfma_f32_16x16x32_bf16 %0, %1, %2, %0" : "+v"(acc) : "v"(a), "v"(b));
}
DI void mfma_done(f32x16& acc) { asm volatile("s_nop 15" : "+v"(acc)); }
DI v2u trd(LAS unsigned char* p) { return __builtin_bit_cast(v2u, __builtin_amdgcn_ds_read_tr16_b64_v4i16((LAS s16x4*)p)); }
DI v4u trd2(LAS unsigned char* p0, LAS unsigned char* p1) { const v2u a = trd(p0), b = trd(p1); return v4u{a.x, a.y, b.x, b.y}; }
DI v4u lds128(LAS unsigned char* p) { return *(LAS v4u*)p; }
DI v4u ldg(__amdgpu_buffer_rsrc_t r, int voff, int soff) { return __builtin_amdgcn_raw_buffer_load_b128(r, voff, soff, SC1); }
DI void stg(__amdgpu_buffer_rsrc_t r, int voff, int soff, v4u v) {
  __builtin_amdgcn_raw_buffer_store_b128(v, r, voff, soff, SC1);
  asm volatile("s_nop 1" :: "v"(v) : "memory");
}
template <int CTRL> DI float dpp_add(float v) {
  return v + __builtin_bit_cast(float, __builtin_amdgcn_update_dpp(0, __builtin_bit_cast(int, v), CTRL, 0xF, 0xF, true));
}
DI float swap16_add(float v) { const unsigned u = __builtin_bit_cast(unsigned, v); const auto r = __builtin_amdgcn_permlane16_swap(u, u, false, false);
  return u2f(r[0]) + u2f(r[1]); }
DI float swap32_add(float v) { const unsigned u = __builtin_bit_cast(unsigned, v); const auto r = __builtin_amdgcn_permlane32_swap(u, u, false, false);
  return u2f(r[0]) + u2f(r[1]); }
DI float quad_sum(float v) { v = dpp_add<0xB1>(v); return dpp_add<0x4E>(v); }
DI float row_sum(float v) { return dpp_add<0x140>(dpp_add<0x141>(quad_sum(v))); }
DI float wave_sum(float v) { return swap32_add(swap16_add(row_sum(v))); }
DI f32x2 cmul(f32x2 a, f32x2 e, f32x2 ep) { const f32x2 t = f32x2{a.x, a.x} * e; return __builtin_elementwise_fma(f32x2{a.y, a.y}, ep, t); }
DI f32x2 cexp(float re2, float rev) {
  const float m = __builtin_amdgcn_exp2f(re2), f = __builtin_amdgcn_fractf(rev);
  return f32x2{m * __builtin_amdgcn_cosf(f), m * __builtin_amdgcn_sinf(f)};
}
template <int LI, int SI> DI void grp(int& so, unsigned lbase, int sbase) {
  asm volatile("s_add_i32 m0, %1, %3\n\ts_add_i32 %0, %2, %4" : "=s"(so) : "s"(lbase), "s"(sbase), "n"(LI), "n"(SI) : "scc", "memory");
}
template <int K, bool COH> DI void xfer(unsigned v, __amdgpu_buffer_rsrc_t r, int so) {
  if (COH) asm volatile("buffer_load_dwordx4 %0, %1, %2 offen offset:%3 sc1 lds" :: "v"(v), "s"(r), "s"(so), "n"(K * 1024) : "memory");
  else asm volatile("buffer_load_dwordx4 %0, %1, %2 offen offset:%3 lds" :: "v"(v), "s"(r), "s"(so), "n"(K * 1024) : "memory");
}
DI void set_m0(unsigned lds_dst) { asm volatile("s_mov_b32 m0, %0\n\ts_nop 0" :: "s"(lds_dst) : "memory"); }
DI void dma_x4(const unsigned char* sbase, unsigned v, unsigned lds_dst) {
  asm volatile("s_mov_b32 m0, %2\n\ts_nop 0\n\tglobal_load_lds_dwordx4 %0, %1 sc1\n\tglobal_load_lds_dwordx4 %0, %1 offset:1024 sc1\n\t"
               "global_load_lds_dwordx4 %0, %1 offset:2048 sc1\n\tglobal_load_lds_dwordx4 %0, %1 offset:3072 sc1"
               :: "v"(v), "s"(sbase), "s"(lds_dst) : "memory");
}
template <int I> struct IC { static constexpr int v = I; };
template <int I, int N, class F> DI void static_for(F&& f) { if constexpr (I < N) { f(IC<I>{}); static_for<I + 1, N>(f); } }
DI unsigned now() { u64 t; asm volatile("s_memrealtime %0\n\ts_waitcnt lgkmcnt(0)" : "=s"(t) :: "memory"); return (unsigned)t; }
DI void reload(v4u& d, __amdgpu_buffer_rsrc_t r, int voff, int soff) { d = ldg(r, voff, soff); FRESH(); }
DI void reload1(unsigned& d, __amdgpu_buffer_rsrc_t r, int voff, int soff) { d = __builtin_amdgcn_raw_buffer_load_b32(r, voff, soff, SC1); FRESH(); }
template <int N> DI void waitv() { asm volatile("s_waitcnt vmcnt(%0)" :: "n"(N) : "memory"); }
struct ConvOrder { int t[36], u[36]; };
constexpr ConvOrder conv_order() {
  ConvOrder o = {};
  int done[8] = {}, last = -1;
  for (int k = 0; k < 36; ++k) {
    int best = -1;
    for (int t = 0; t < 8; ++t)
      if (t != last && done[t] <= t && (best < 0 || t - done[t] > best - done[best])) best = t;
    o.t[k] = best; o.u[k] = done[best]++; last = best;
  }
  return o;
}
constexpr ConvOrder conv_order_last() {
  ConvOrder o = {};
  int k = 0;
  for (int p = 0; p < 4; ++p)
    for (int u = 0; u <= 2 * p + 1; ++u) {
      o.t[k] = 2 * p + 1; o.u[k] = u; ++k;
      if (u <= 2 * p) { o.t[k] = 2 * p; o.u[k] = u; ++k; }
    }
  return o;
}
constexpr int epi_slot(int v) { return v < 32 ? 2 * EO + v : 2 * 50 - 1 + (v - 32); }
constexpr bool epi_pair(int j) { return (j >= EO && j < EO + 19) || (j >= 50 && j < 61); }
constexpr int hc_upto(int j) { return (j < HC0 ? 0 : j - HC0) + (j < HC1 ? 0 : j - HC1); }
constexpr int hl_upto(int i) { return i < HL0 ? 28 : i < HL0 + 12 ? 28 + (i - HL0) : i < HL1 ? 40 : i < HL1 + 16 / HLR ? 40 + HLR * (i - HL1) : 56; }
constexpr int c_ops(int j) {
  return (j >= SXJ && j < SXJ + 4 ? 2 : j < 34 || (j >= 35 && j < 51) ? 1 : 0) + hc_upto(j + 1) - hc_upto(j) +
         (j == EO + 6 || j == EO + 14 || j == 56 ? 1 : j == EO + 10 || j == EO + 18 || j == 60 ? 2 : 0);
}
constexpr int c_between(int a, int b) { int n = 0; for (int j = a + 1; j < b; ++j) n += c_ops(j); return n; }
DI unsigned tagbad(unsigned bad, v4u g, unsigned tagA, unsigned tagB) {
  return __builtin_amdgcn_bitop3_b32(__builtin_amdgcn_bitop3_b32(bad, g.y, tagA, 0xF6), g.w, tagB, 0xF6);
}
template <int R0, int R1, int R2, int R3> DI void put4(unsigned base, f32x2 a, f32x2 b, f32x2 c, f32x2 d) {
  asm volatile("s_mov_b32 m0, %4\n\ts_nop 0\n\tds_write_addtid_b32 %0 offset:%5\n\tds_write_addtid_b32 %1 offset:%6\n\t"
               "ds_write_addtid_b32 %2 offset:%7\n\tds_write_addtid_b32 %3 offset:%8"
               :: "v"(pack2(a.x, a.y)), "v"(pack2(b.x, b.y)), "v"(pack2(c.x, c.y)), "v"(pack2(d.x, d.y)), "s"(base),
                  "n"(R0 * UVS_ROW), "n"(R1 * UVS_ROW), "n"(R2 * UVS_ROW), "n"(R3 * UVS_ROW) : "memory");
}
DI unsigned sum4(unsigned m, v4u v) {
  asm("v_add3_u32 %0, %0, %1, %2\n\tv_add3_u32 %0, %0, %3, %4" : "+v"(m) : "v"(v.x), "v"(v.y), "v"(v.z), "v"(v.w));
  return m;
}
DI void acc4(unsigned& m, v4u v) {
  asm volatile("v_add3_u32 %0, %0, %1, %2\n\tv_add3_u32 %0, %0, %3, %4" : "+v"(m) : "v"(v.x), "v"(v.y), "v"(v.z), "v"(v.w));
}
DI unsigned sum8(unsigned m, v4u a, v4u b) {
  asm("v_add3_u32 %0, %0, %1, %2\n\tv_add3_u32 %0, %0, %3, %4\n\tv_add3_u32 %0, %0, %5, %6\n\tv_add3_u32 %0, %0, %7, %8"
      : "+v"(m) : "v"(a.x), "v"(a.y), "v"(a.z), "v"(a.w), "v"(b.x), "v"(b.y), "v"(b.z), "v"(b.w));
  return m;
}
template <int I> DI unsigned red_step(unsigned v) {
  constexpr int ctrl[6] = {0xB1, 0x4E, 0x141, 0x140, 0x142, 0x143}, rows[6] = {0xF, 0xF, 0xF, 0xF, 0xA, 0xC};
  return v + (unsigned)__builtin_amdgcn_update_dpp(0, (int)v, ctrl[I], rows[I], 0xF, true);
}
DI unsigned red_all(unsigned v) { return red_step<5>(red_step<4>(red_step<3>(red_step<2>(red_step<1>(red_step<0>(v)))))); }
DI void swap_add32(float& a, float b) { const auto r = __builtin_amdgcn_permlane32_swap(f2u(a), f2u(b), false, false); a = u2f(r[0]) + u2f(r[1]); }
DI void swap_add16(float& a, float b) { const auto r = __builtin_amdgcn_permlane16_swap(f2u(a), f2u(b), false, false); a = u2f(r[0]) + u2f(r[1]); }

typedef const unsigned char* cptr;
typedef unsigned v16u __attribute__((ext_vector_type(16)));
DI cptr mkptr(unsigned lo, unsigned hi) { return (cptr)(((u64)hi << 32) | lo); }

__global__ void __launch_bounds__(256, 1)
s4_block_kernel(cptr, cptr, cptr, cptr, cptr, cptr, cptr, cptr, cptr, cptr, cptr, cptr, cptr, unsigned char*, unsigned char*, cptr) {
  STAMP(0);
  v16u ka0, ka1;
  asm volatile("s_load_dwordx16 %0, %2, 0x0\n\ts_load_dwordx16 %1, %2, 0x40" : "=&s"(ka0), "=&s"(ka1) : "s"(__builtin_amdgcn_kernarg_segment_ptr()));
  SB();
  LAS unsigned char* const L = (LAS unsigned char*)16u - 16;
  const int tid = threadIdx.x, lane = tid & 63, w = __builtin_amdgcn_readfirstlane(tid >> 6);
  const int b = blockIdx.x, xcd = b & 7, slot = b >> 3, id = xcd * 32 + slot;
  const int ks = (id >> 6) * 4 + w, cc = id & 63;
  const int rp = 4 * (xcd >> 1) + (slot >> 3), rg = xcd & 1, dt = slot & 7, si = 8 * rg + dt;
  const int ct = 7 * rg + min(dt, 6);
  const int rot = (0x0340 >> (4 * (2 * (w & 1) + rg))) & 7;

  const unsigned char* pcode;
  asm volatile("s_getpc_b64 %0" : "=s"(pcode));
  unsigned warm;
  asm volatile("global_load_dword %0, %1, %2" : "=v"(warm) : "v"(min((((slot & 7) * 4 + w) * 9 + min(lane, 8)) * 128, CODE_BYTES)), "s"(pcode) : "memory");
  const int g16 = lane >> 4, q4 = (lane & 15) >> 2, p4 = lane & 3, hf = lane >> 5, blk = (lane >> 4) & 1, l16 = lane * 16;
  const unsigned lrw = L_R + w * 16384;
  unsigned lsp = L_SP + w * 6144;
  unsigned co = (ks * 32 + (lane & 31)) * 2, xo = (id >> 2) * 32768 + (id & 3) * 256 + (tid & 7) * 4096 + (tid >> 3) * 8, ro = id * 8192 + w * 2048 + hf * 1024 + (lane & 31) * 16, po = id * 512 + tid * 2;
  PIN(co); PIN(xo); PIN(ro); PIN(po);
  set_m0(lrw);
  SB();
  asm volatile("s_waitcnt lgkmcnt(0)" : "+s"(ka0), "+s"(ka1));
  const cptr xin = mkptr(ka0[0], ka0[1]), pAre = mkptr(ka0[2], ka0[3]), pAim = mkptr(ka0[4], ka0[5]), pBre = mkptr(ka0[6], ka0[7]);
  const cptr pBim = mkptr(ka0[8], ka0[9]), pCre = mkptr(ka0[10], ka0[11]), pCim = mkptr(ka0[12], ka0[13]), pldt = mkptr(ka0[14], ka0[15]);
  const cptr pD = mkptr(ka1[0], ka1[1]), w1 = mkptr(ka1[2], ka1[3]), w2 = mkptr(ka1[4], ka1[5]), pc = mkptr(ka1[6], ka1[7]);
  const cptr wmod = mkptr(ka1[8], ka1[9]), out = mkptr(ka1[10], ka1[11]), ws = mkptr(ka1[12], ka1[13]);
  unsigned uldt, uD;
  asm volatile("s_load_dword %0, %2, %4\n\ts_load_dword %1, %3, %4" : "=&s"(uldt), "=&s"(uD) : "s"(pldt), "s"(pD), "s"(id * 4));
  unsigned uAre, uAim, uBre, uBim, uCre, uCim;
  asm volatile("global_load_ushort %0, %6, %7\n\tglobal_load_ushort %1, %6, %8\n\tglobal_load_ushort %2, %6, %9\n\t"
               "global_load_ushort %3, %6, %10\n\tglobal_load_ushort %4, %6, %11\n\tglobal_load_ushort %5, %6, %12"
               : "=&v"(uAre), "=&v"(uAim), "=&v"(uBre), "=&v"(uBim), "=&v"(uCre), "=&v"(uCim)
               : "v"(po), "s"(pAre), "s"(pAim), "s"(pBre), "s"(pBim), "s"(pCre), "s"(pCim) : "memory");
  unsigned epoch_v, cval;
  v2u xq[4];
  v4u ra, rb;
  asm volatile("global_load_dword %0, %8, %12 sc1\n\tglobal_load_dwordx2 %1, %9, %13\n\tglobal_load_dwordx2 %2, %9, %13 offset:1024\n\t"
               "global_load_dwordx2 %3, %9, %13 offset:2048\n\tglobal_load_dwordx2 %4, %9, %13 offset:3072\n\t"
               "global_load_dwordx4 %5, %10, %13\n\tglobal_load_dwordx4 %6, %10, %13 offset:512\n\tglobal_load_ushort %7, %11, %14"
               : "=&v"(epoch_v), "=&v"(xq[0]), "=&v"(xq[1]), "=&v"(xq[2]), "=&v"(xq[3]), "=&v"(ra), "=&v"(rb), "=&v"(cval)
               : "v"(OFF_EP), "v"(xo), "v"(ro), "v"(co), "s"(ws), "s"(xin), "s"(pc) : "memory");
  unsigned wmo[3];
#pragma unroll
  for (int i = 0; i < 3; ++i) { const unsigned idx = lane + 64 * i; wmo[i] = (ks * 32 * 6144 + cc * 96 - i * 1024) + idx / 6 * (6144 - 96) + idx * 16; }
  asm volatile("global_load_lds_dwordx4 %0, %3\n\tglobal_load_lds_dwordx4 %1, %3 offset:1024\n\tglobal_load_lds_dwordx4 %2, %3 offset:2048"
               :: "v"(wmo[0]), "v"(wmo[1]), "v"(wmo[2]), "s"(wmod) : "memory");
  const unsigned char* setp = ws + SET_BASE;
  const __amdgpu_buffer_rsrc_t rs = __builtin_amdgcn_make_buffer_rsrc((void*)setp, 0, SET_BYTES, 0x00020000);
  SB();
  STAMPA(26);
  unsigned wto = id * 7168 + (w >> 1) * 3584 + min(lane, 27) * 128;
  PIN(wto);
  unsigned w1v[4], w2v[4];
#pragma unroll
  for (int i = 0; i < 4; ++i) {
    w1v[i] = (lane >> 3) * 3584 + (((lane & 7) ^ (blk << 2)) * 16) + i * (57344 - 1024);
    w2v[i] = (lane >> 3) * 1024 + (((lane & 7) ^ (blk << 2)) * 16) + i * (8192 - 1024);
    PIN(w1v[i]); PIN(w2v[i]);
  }
  int w1b = ct * 256 + (w >> 1) * 128 + (w & 1) * (8 * 3584) + ((dt + 1) >> 3 << 24), w2lo = (448 * w + 64 * rot) * 1024 + dt * 128;
  int w2c3 = w2lo - (rot >> 2) * (448 * 1024), w2hi = w2lo - ((rot + 1) >> 2) * (448 * 1024), so = 0;
  unsigned lrx = L_RX + w * 1024, lto = L_TOP + w * 1024;
  int zs = OFF_Z + (2 * id + (w >> 1)) * 64;
  int za = ((((w & 1) + 2 * (g16 & 1)) * 4 + q4) * 4 + (g16 >> 1)) * 32768 + p4 * 16;
  int tra = (hf * 8 + q4) * 64 + blk * 32 + p4 * 8;
  const int wsrc = (w & 2) + hf;
  int w1t = q4 * 128 + (((w & 1) ^ (q4 >> 1)) * 64) + blk * 32 + p4 * 8;
  LAS unsigned char *w1a = L + L_R + wsrc * 16384 + w1t, *w1s = L + L_SP + wsrc * 6144 + w1t;
  LAS unsigned char *w1x = L + L_RX + wsrc * 1024 + w1t, *w1y = L + L_TOP + wsrc * 1024 + w1t;
#define W1PULL(step, n) w1f[step] = trd2(((n) < 16 ? w1a : w1s) + ((n) & 15) * 1024, ((n) < 16 ? w1a : w1s) + ((n) & 15) * 1024 + 512)
  LAS unsigned char* ka = L + L_KC + ((w >> 1) * 4 + q4) * KC_STRIDE + 2 * (32 - 8 * g16 + 4 * p4) - 8 + 256 * (w & 1);
  LAS unsigned char* hb = L + L_H + (w >> 1) * H_STRIDE + (lane & 15) * 32 + g16 * 16 + 256 - 256 * (w & 1);
  LAS unsigned char* zp = L + sel(tid < 64, L_KC + ((lane >> 1) & 7) * KC_STRIDE + (lane & 1) * 16, L_H + hf * H_STRIDE + min(lane & 31, 29) * 16);
  LAS unsigned char* ub = L + L_UVS + (lane & 15) * UVS_ROW + g16 * 16;
  int gmo = OFF_MG + (g16 * 256 + id) * 256 + (lane & 15) * 16, gmo2 = OFF_MG + ((4 + blk) * 256 + id) * 256 + (lane & 15) * 16;
  int mgo = sel(lane < 48, (lane >> 1) * 256 + (lane & 1) * 8, OOB), sgo = sel(lane & 31, OOB, hf * 8);
  LAS unsigned char* xbw = L + L_XB + ((w & 1) * 4 + (w >> 1)) * 1024 + l16;
  int xcw = sel(lane & 7, OOB, tid), xcr = sel(lane & 7, OOB, (tid >> 3) * 1024), xgw = (tid >> 3) * 16384 + (tid & 7) * 16;
  PIN(xcw); PIN(xcr); PIN(xgw);
  LAS unsigned char *exa = L + L_XB + w * 4096 + l16, *eya = L + L_XB + (w ^ 1) * 4096 + l16;
  LAS unsigned char* xbr = L + L_XB + w * 4096 + ((lane & 15) * 4 + g16) * 16;
  PIN(xbw); PIN(xbr); PIN(exa); PIN(eya);
  int mto = (w * 64 + (lane >> 1)) * 64 + (lane & 1) * 16;
  LAS unsigned char* mtw = L + (lane & 1) * L_TG + (w * 64 + (lane >> 1)) * 4;
  PIN(mto); PIN(mtw);
  PIN(za); PIN(tra); PIN(w1a); PIN(w1s); PIN(w1x); PIN(w1y); PIN(ka); PIN(hb); PIN(zp); PIN(ub); PIN(gmo); PIN(gmo2); PIN(mgo); PIN(sgo);
  asm volatile("" : "+s"(w1b), "+s"(w2lo), "+s"(w2c3), "+s"(w2hi), "+s"(lsp), "+s"(lrx), "+s"(lto), "+s"(zs));
#define W1P(n, k, lbase, li) do { if constexpr ((k) == 0) grp<(li), (n) * 57344>(so, lbase, w1b); xfer<(k), false>(w1v[k], rw1, so); } while (0)
#define W2P(n, k, lbase, li) do { if constexpr ((k) == 0) grp<(li), (n) * 8192>(so, lbase, (n) < 24 ? w2lo : (n) < 32 ? w2c3 : w2hi); xfer<(k), false>(w2v[k], rw2, so); } while (0)
  SB();
  LAS unsigned char* koff[12];
  {
    float fj = (float)(8 * tid - 3);
    PIN(fj);
    const unsigned lin = (unsigned)(8 * tid - 3 + 0x4300 - 128) * 0x10001u + 0x10000u;
#pragma unroll
    for (int i = 0; i < 12; i += 2) {
      const unsigned r = sel(tid < 32, lin + i * 0x10001u, pack2(fj + (float)i, fj + (float)(i + 1)));
      koff[i] = L + ((r & 0xFFFFu) * 4 + (L_KT + (128 - 0x4300) * 4));
      koff[i + 1] = L + ((r >> 16) * 4 + (L_KT + (128 - 0x4300) * 4));
      PIN(koff[i]);
      if (i < 10) PIN(koff[i + 1]);
    }
  }
  *(LAS v4u*)(L + L_KT - 16) = v4u{0u, 0u, 0u, 0u};
  LAS unsigned char* ktw[4];
  {
    const int row = w * 16 + (lane & 15), sg = max((row >> 3) - 1, 0), rr_ = row - 8 * sg;
    const int idx0 = 128 * sg + ((rr_ >> (3 - sg)) << (7 - sg)) + (rr_ & ((8 >> sg) - 1)) + 1, st = 8 >> sg;
#pragma unroll
    for (int r = 0; r < 4; ++r) {
      const int idx = sel(row < 40, idx0 + st * (4 * g16 + r), tid == 136 && r == 0 ? 0 : 641 + lane);
      ktw[r] = L + L_KT + idx * 4;
      PIN(ktw[r]);
    }
  }
  const int par = (0x1C >> (ct % 7)) & 1;
  const unsigned char* xg = setp + OFF_Z + rp * 131072;
  unsigned xl = L_XR + w * 8192;
  int xs2 = OFF_Z + rp * 131072 + w * 8192 + (2 ^ par) * 32768, xs3 = xs2 + 32768 - 65536 * par;
  asm volatile("" : "+s"(xl), "+s"(xs2), "+s"(xs3));
  int slo = OFF_Z + (rp * 4 + w) * 32768 + si * 2048;
  asm volatile("" : "+s"(slo));
  const int w2t = (4 * hf + q4) * 128 + blk * 32 + (((p4 & 1) << 1) | (p4 >> 1)) * 8;
  LAS unsigned char* const w2a0 = L + w2t + (q4 >> 1) * 64;
  LAS unsigned char* const w2b0 = L + w2t + ((q4 >> 1) ^ 1) * 64;
  LAS unsigned char *pa = w2a0 + lrw + 8192, *pb = w2b0 + lrw + 8192;
  LAS unsigned char* qr = L + (p4 ? L_TB : L_TS) + hf * 16 + ((lane >> 2) & 7) * 32;
  PIN(pa); PIN(pb); PIN(qr);
#define PULL(step, pa, pb, off) do { w2f[step][0] = trd2((pa) + (off), (pa) + (off) + 1024); w2f[step][1] = trd2((pb) + (off), (pb) + (off) + 1024); } while (0)
  const __amdgpu_buffer_rsrc_t rw1 = __builtin_amdgcn_make_buffer_rsrc((void*)w1, 0, 1835008, 0x00020000);
  const __amdgpu_buffer_rsrc_t rw2 = __builtin_amdgcn_make_buffer_rsrc((void*)w2, 0, 1835008, 0x00020000);
  SB();
  STAMPA(27);
  asm volatile("s_waitcnt lgkmcnt(0)" : "+s"(uldt), "+s"(uD));
  asm volatile("s_waitcnt vmcnt(11)" : "+v"(uAre), "+v"(uAim), "+v"(uBre), "+v"(uBim), "+v"(uCre), "+v"(uCim), "+v"(warm));
    const float dtv = rbf(__builtin_amdgcn_exp2f(LOG2E * (w >> 1 ? bf_hi(uldt) : bf_lo(uldt))));
    const float are = dtv * bf_lo(uAre) * LOG2E, aim = dtv * bf_lo(uAim) * INV2PI;
    const float br = dtv * bf_lo(uBre), bi = dtv * bf_lo(uBim), cr = bf_lo(uCre), ci = bf_lo(uCim);
    const f32x2 wv = {cr * br - ci * bi, cr * bi + ci * br};
    const unsigned row = L_UVS + (w >> 1) * UVS_CH + (w & 1) * 256;
    const f32x2 e1 = cexp(are, aim), e8 = cexp(8.f * are, -8.f * aim), e128 = cexp(128.f * are, 128.f * aim);
    const f32x2 e1p = {-e1.y, e1.x}, e8p = {-e8.y, e8.x}, e128p = {-e128.y, e128.x};
    f32x2 z[8], v, v2, y, y2;
#define M1(a) cmul(a, e1, e1p)
#define M8(a) cmul(a, e8, e8p)
#define MK(a) cmul(a, e128, e128p)
    z[0] = M1(wv);
    put4<40, 41, 42, 0>(row, wv, f32x2{1.f, 0.f}, e8, z[0]);
    v = M8(e8); z[1] = M1(z[0]); v2 = M8(v); z[2] = M1(z[1]);
    put4<43, 1, 44, 2>(row, v, z[1], v2, z[2]);
  SB();
  asm volatile("s_waitcnt vmcnt(6)" : "+v"(epoch_v), "+v"(xq[0]), "+v"(xq[1]), "+v"(xq[2]), "+v"(xq[3]));
  STAMPA(29);
  const unsigned epoch = __builtin_amdgcn_readfirstlane(epoch_v);
  const unsigned tagA = epoch * 0x9E3779B1u + 0x7F4A7C15u, tagB = ~tagA;
  {
    const v4u xc0 = {xq[0].x, xq[1].x, xq[2].x, xq[3].x}, xc1 = {xq[0].y, xq[1].y, xq[2].y, xq[3].y};
    stg(rs, xgw, OFF_XG + (id & 3) * 524288 + (id >> 2) * 128, xc0);
    stg(rs, xgw, OFF_XG + (id & 3) * 524288 + (id >> 2) * 128 + 8192, xc1);
    SB();
    STAMP(1);
    asm volatile("s_waitcnt vmcnt(5)" : "+v"(ra), "+v"(rb), "+v"(cval));
    const float c = bf_lo(cval);
    const float s = c * __builtin_amdgcn_rcpf(1.0f + __builtin_amdgcn_exp2f(-LOG2E * c));
    *(LAS u16*)(L + lrw + 3072 + (lane & 31) * 2) = (u16)pack2(s, s);
    f32x2 fa[8];
#pragma unroll
    for (int i = 0; i < 4; ++i) { fa[i] = f32x2{bf_lo(ra[i]), bf_hi(ra[i])}; fa[4 + i] = f32x2{bf_lo(rb[i]), bf_hi(rb[i])}; }
    const f32x2 s2 = ((fa[0] + fa[1]) + (fa[2] + fa[3])) + ((fa[4] + fa[5]) + (fa[6] + fa[7]));
    float mean = swap16_add(row_sum(s2.x + s2.y)) * (1.0f / 512.0f);
    const unsigned k0 = red_step<2>(red_step<1>(red_step<0>(sum4(0u, xc0)))), k1 = red_step<2>(red_step<1>(red_step<0>(sum4(0u, xc1))));
    __builtin_amdgcn_raw_buffer_store_b64(v2u{tagA - k0, tagA - k1}, rs, xcw, OFF_XC + id * 256, SC1);
    PIN(mean);
    SB();
    f32x2 q2 = {0.f, 0.f}, q3 = {0.f, 0.f};
#pragma unroll
    for (int i = 0; i < 8; i += 2) {
      const f32x2 d0 = fa[i] - f32x2{mean, mean}, d1 = fa[i + 1] - f32x2{mean, mean};
      q2 = __builtin_elementwise_fma(d0, d0, q2);
      q3 = __builtin_elementwise_fma(d1, d1, q3);
    }
    q2 += q3;
    float qs = q2.x + q2.y;
    PIN(qs);
    WAITV(3);
    LAS unsigned char* a = L + lrw + (8 * g16 + q4) * 96 + p4 * 8;
    const v4u sc = lds128(L + lrw + 3072 + g16 * 16);
    const v4u m0 = trd2(a, a + 384), m1 = trd2(a + 32, a + 416), m2 = trd2(a + 64, a + 448);
    SB();
    const float var = rbf(rbf(swap16_add(row_sum(qs)) * (1.0f / 512.0f)) + 1e-5f);
    unsigned st = pack2(mean, __builtin_amdgcn_sqrtf(var));
    PIN(st);
    SB();
    const f32x4 c0 = mfma16(sc, m0, f32x4{}), c1 = mfma16(sc, m1, f32x4{}), c2 = mfma16(sc, m2, f32x4{});
    SB();
    __builtin_amdgcn_raw_buffer_store_b64(v2u{st, hf ? tagB : tagA}, rs, sgo, OFF_SG + ((w & 1) * 512 + id * 2 + (w >> 1)) * 16, SC1);
    __builtin_amdgcn_raw_buffer_store_b64(v2u{sel(lane < 16, f2u(c0[0]), sel(lane < 32, f2u(c1[0]), f2u(c2[0]))), (lane & 1) ? tagB : tagA}, rs, mgo,
                                          OFF_MG + cc * 24 * 256 + ks * 16, SC1);
    SB();
  }
  unsigned wt;
  SB();
  STAMP(2);
  unsigned dsk = tid == 136 ? uD : 0u;
  PIN(dsk);

    asm volatile("global_load_dword %0, %1, %2" : "=v"(wt) : "v"(wto), "s"(w1 + (w & 1) * (w2 - w1)) : "memory");
    v = M8(v2); z[3] = M1(z[2]); v2 = M8(v); z[4] = M1(z[3]);
    put4<45, 3, 46, 4>(row, v, z[3], v2, z[4]);
    v = M8(v2); z[5] = M1(z[4]); v2 = M8(v); z[6] = M1(z[5]);
    put4<47, 5, 48, 6>(row, v, z[5], v2, z[6]);
    z[7] = M1(z[6]); z[0] = MK(z[0]); z[1] = MK(z[1]); z[2] = MK(z[2]);
    put4<7, 8, 9, 10>(row, z[7], z[0], z[1], z[2]);
    z[3] = MK(z[3]); z[4] = MK(z[4]); z[5] = MK(z[5]); z[6] = MK(z[6]);
    put4<11, 12, 13, 14>(row, z[3], z[4], z[5], z[6]);
    z[7] = MK(z[7]); z[1] = MK(z[1]); z[3] = MK(z[3]); z[5] = MK(z[5]);
    put4<15, 16, 17, 18>(row, z[7], z[1], z[3], z[5]);
    z[7] = MK(z[7]); z[1] = MK(z[1]); z[3] = MK(z[3]); z[5] = MK(z[5]);
    put4<19, 20, 21, 22>(row, z[7], z[1], z[3], z[5]);
    y = MK(z[7]); z[3] = MK(z[3]); y2 = MK(y); z[4] = MK(z[3]);
    put4<23, 24, 25, 26>(row, y, z[3], y2, z[4]);
    y = MK(y2); z[3] = MK(z[4]); y2 = MK(y); z[4] = MK(z[3]);
    put4<27, 28, 29, 30>(row, y, z[3], y2, z[4]);
    y = MK(y2); v = M8(v2); y2 = MK(y); v2 = M8(v);
    put4<31, 49, 32, 50>(row, y, v, y2, v2);
    y = MK(y2); v = M8(v2); y2 = MK(y); v2 = M8(v);
    put4<33, 51, 34, 52>(row, y, v, y2, v2);
    y = MK(y2); v = M8(v2); y2 = MK(y); v2 = M8(v);
    put4<35, 53, 36, 54>(row, y, v, y2, v2);
    y = MK(y2); v = M8(v2); y2 = MK(y); v2 = M8(v);
    put4<37, 55, 38, 56>(row, y, v, y2, v2);
    y = MK(y2);
    *(LAS unsigned*)(L + row + 39 * UVS_ROW + lane * 4) = pack2(y.x, y.y);
    STAMPC(29);
#undef M1
#undef M8
#undef MK
  BAR();
  STAMP(3);
  v4u gx0, gx1, gs0, gs1, gs2, gs3, gm, gn;
  unsigned gc0, gc1;
  const int xgo = OFF_XG + id * 8192, xco = OFF_XC + id * 4;
  v4u fv[2][8], fu[2][8];
  if (w < 3) {
    LAS unsigned char* ut = ub + w * 16 * UVS_ROW;
#pragma unroll
    for (int s = 0; s < 8; ++s)
#pragma unroll
      for (int c = 0; c < 2; ++c) { fv[c][s] = lds128(ub + c * UVS_CH + 41 * UVS_ROW + s * 64); fu[c][s] = lds128(ut + c * UVS_CH + s * 64); SB(); }
    f32x4 a0 = {}, a1 = {};
#pragma unroll
    for (int s = 0; s < 8; ++s) { a0 = mfma16(fv[0][s], fu[0][s], a0); SB(); a1 = mfma16(fv[1][s], fu[1][s], a1); SB(); }
    *(LAS unsigned*)ktw[0] = pack2(a0[0] + bf_lo(dsk), a1[0] + bf_hi(dsk));
#pragma unroll
    for (int r = 1; r < 4; ++r) *(LAS unsigned*)ktw[r] = pack2(a0[r], a1[r]);
  }
  SB();
#define LD1(i, so) "buffer_load_dwordx4 %" #i ", %10, %14, " so " offen sc1\n\t"
  asm volatile("buffer_load_dwordx4 %6, %11, %14, 0 offen sc1\n\tbuffer_load_dwordx4 %7, %12, %14, 0 offen sc1\n\t"
               LD1(0, "%15") LD1(1, "%16") "buffer_load_dword %8, %13, %14, %21 offen sc1\n\tbuffer_load_dword %9, %13, %14, %22 offen sc1\n\t"
               LD1(2, "%17") LD1(3, "%18") LD1(4, "%19") LD1(5, "%20")
               : "=&v"(gx0), "=&v"(gx1), "=&v"(gs0), "=&v"(gs1), "=&v"(gs2), "=&v"(gs3), "=&v"(gm), "=&v"(gn), "=&v"(gc0), "=&v"(gc1)
               : "v"(tid * 16), "v"(gmo), "v"(gmo2), "v"(xcr), "s"(rs), "s"(xgo), "s"(xgo + 4096),
                 "s"(OFF_SG), "s"(OFF_SG + 8192), "s"(OFF_SG + 4096), "s"(OFF_SG + 12288), "s"(xco), "s"(xco + 32768) : "memory");
#undef LD1
  SB();
  BAR();
  {
    unsigned k[11];
#pragma unroll
    for (int i = 0; i < 11; ++i) k[i] = *(LAS unsigned*)koff[i];
    SB();
    stg(rs, (tid >> 3) * 32768 + (tid & 7) * 16, OFF_Z + id * 128, v4u{POISON, POISON, POISON, POISON});
    stg(rs, (tid >> 3) * 32768 + (tid & 7) * 16, OFF_Z + id * 128 + 32 * 32768, v4u{POISON, POISON, POISON, POISON});
    SB();
    if (w < 2) *(LAS v4u*)zp = v4u{0u, 0u, 0u, 0u};
#pragma unroll
    for (int c = 0; c < 2; ++c) {
      unsigned e[5], o[5];
#pragma unroll
      for (int i = 0; i < 5; ++i) {
        o[i] = __builtin_amdgcn_perm(k[2 * i + 1], k[2 * i], c ? 0x07060302u : 0x05040100u);
        e[i] = __builtin_amdgcn_perm(k[2 * i + 2], k[2 * i + 1], c ? 0x07060302u : 0x05040100u);
      }
      LAS unsigned char* d = L + L_KC + c * 4 * KC_STRIDE + 32 + tid * 16;
      *(LAS v4u*)(d) = v4u{e[1], e[2], e[3], e[4]};
      *(LAS v4u*)(d + KC_STRIDE) = v4u{o[1], o[2], o[3], o[4]};
      *(LAS v4u*)(d + 2 * KC_STRIDE) = v4u{e[0], e[1], e[2], e[3]};
      *(LAS v4u*)(d + 3 * KC_STRIDE) = v4u{o[0], o[1], o[2], o[3]};
    }
  }
  SB();
  STAMP(4);
  {
    asm volatile("s_waitcnt vmcnt(2)" : "+v"(gx0), "+v"(gx1), "+v"(gs0), "+v"(gs1), "+v"(gs2), "+v"(gs3), "+v"(gm), "+v"(gn), "+v"(gc0), "+v"(gc1) :: "memory");
#define XBAD() (red_step<2>(red_step<1>(red_step<0>(sum8(gc0 + gc1, gx0, gx1)))) ^ (2u * tagA))
#define TB(a, c) tagbad(tagbad(0u, a, tagA, tagB), c, tagA, tagB)
    if (__builtin_expect(__any((XBAD() | TB(gm, gn) | TB(gs0, gs1) | TB(gs2, gs3)) != 0u), 0)) {
      _Pragma("nounroll") for (unsigned spin = 0; spin < SPIN_CAP; ++spin) {
        if (spin) PAUSE();
        COUNT(20);
        if (XBAD()) { reload(gx0, rs, tid * 16, xgo); reload(gx1, rs, tid * 16, xgo + 4096); reload1(gc0, rs, xcr, xco); reload1(gc1, rs, xcr, xco + 32768); }
        if (TB(gs0, gs1)) { reload(gs0, rs, tid * 16, OFF_SG); reload(gs1, rs, tid * 16, OFF_SG + 8192); }
        if (TB(gs2, gs3)) { reload(gs2, rs, tid * 16, OFF_SG + 4096); reload(gs3, rs, tid * 16, OFF_SG + 12288); }
        if (TB(gm, gn)) { reload(gm, rs, gmo, 0); reload(gn, rs, gmo2, 0); }
        if (!__any((XBAD() | TB(gm, gn) | TB(gs0, gs1) | TB(gs2, gs3)) != 0u)) break;
      }
    }
#undef XBAD
#undef TB
  }
  STAMP(5);
  W1P(0, 0, lrw, 0); W1P(1, 1, lrw, 0);
  v4u mtp;
  {
    float r0 = u2f(gm.x), r1 = u2f(gm.z), r2 = u2f(gn.x), r3 = u2f(gn.z);
#define RSTEP(c) r0 = dpp_add<c>(r0); PIN(r0); r1 = dpp_add<c>(r1); PIN(r1); r2 = dpp_add<c>(r2); PIN(r2); r3 = dpp_add<c>(r3); PIN(r3)
    RSTEP(0xB1); RSTEP(0x4E); RSTEP(0x141); RSTEP(0x140);
#undef RSTEP
    const int i0 = (int)pack2(r0, r1), i1 = (int)pack2(r2, r3);
    unsigned md[6];
#pragma unroll
    for (int k = 0; k < 6; ++k) md[k] = (unsigned)__builtin_amdgcn_readlane(k < 4 ? i0 : i1, 16 * (k & 3));
#define MOD(k) f32x2{bf_lo(md[k]), bf_hi(md[k])}
    const f32x2 one = {1.0f, 1.0f}, sr = one + MOD(4), cr = one + MOD(1);
    unsigned sp = pack2(sr.x, sr.y);
    const unsigned cp = pack2(cr.x, cr.y);
    sp |= ((sp & 0x7FFFu) ? 0u : 0x3080u) | ((sp & 0x7FFF0000u) ? 0u : 0x30800000u);
    const f32x2 S = {bf_lo(sp), bf_hi(sp)}, G = MOD(2) * S, A = f32x2{bf_lo(cp), bf_hi(cp)} * G, B = MOD(0) * G;
#undef MOD
    mtp = v4u{sel(lane, md[5], sp), tagA, md[3], tagB};
    auto rows4 = [&](const v4u& gx, const v4u& ga, const v4u& gb_, int i) __attribute__((always_inline)) {
      const f32x2 ma = {bf_lo(ga.x), bf_lo(ga.z)}, mb = {bf_lo(gb_.x), bf_lo(gb_.z)};
      const f32x2 ra_ = {__builtin_amdgcn_rcpf(bf_hi(ga.x)), __builtin_amdgcn_rcpf(bf_hi(ga.z))}, rb_ = {__builtin_amdgcn_rcpf(bf_hi(gb_.x)), __builtin_amdgcn_rcpf(bf_hi(gb_.z))};
      const f32x2 x0 = {bf_lo(gx.x), bf_lo(gx.y)}, x1 = {bf_lo(gx.z), bf_lo(gx.w)}, x2 = {bf_hi(gx.x), bf_hi(gx.y)}, x3 = {bf_hi(gx.z), bf_hi(gx.w)};
      const f32x2 h0 = __builtin_elementwise_fma((x0 - ma) * ra_, f32x2{A.x, A.x}, f32x2{B.x, B.x});
      const f32x2 h1 = __builtin_elementwise_fma((x1 - mb) * rb_, f32x2{A.x, A.x}, f32x2{B.x, B.x});
      const f32x2 h2 = __builtin_elementwise_fma((x2 - ma) * ra_, f32x2{A.y, A.y}, f32x2{B.y, B.y});
      const f32x2 h3 = __builtin_elementwise_fma((x3 - mb) * rb_, f32x2{A.y, A.y}, f32x2{B.y, B.y});
      *(LAS v2u*)(L + L_H + 480 + tid * 8 + i * 2048) = v2u{pack2(h0.x, h0.y), pack2(h1.x, h1.y)};
      *(LAS v2u*)(L + L_H + H_STRIDE + 480 + tid * 8 + i * 2048) = v2u{pack2(h2.x, h2.y), pack2(h3.x, h3.y)};
      const f32x2 y0 = x0 * f32x2{S.x, S.x}, y1 = x1 * f32x2{S.x, S.x}, y2 = x2 * f32x2{S.y, S.y}, y3 = x3 * f32x2{S.y, S.y};
      *(LAS f32x4*)(xbw + i * 2048) = f32x4{y0.x, y0.y, y1.x, y1.y};
      *(LAS f32x4*)(xbw + 8192 + i * 2048) = f32x4{y2.x, y2.y, y3.x, y3.y};
    };
    W1P(2, 2, lrw, 0); W1P(3, 3, lrw, 0);
    rows4(gx0, gs0, gs1, 0);
    W1P(4, 0, lrw, 4096); W1P(5, 1, lrw, 0); W1P(6, 2, lrw, 0); W1P(7, 3, lrw, 0);
    rows4(gx1, gs2, gs3, 1);
    W1P(8, 0, lrw, 8192); W1P(9, 1, lrw, 0);
  }
  asm volatile("s_waitcnt vmcnt(10)" : "+v"(wt) :: "memory");
  BAR();
  const unsigned tb = now();
  STAMP(6);

  v4u w1f[32], w2f[28][2], mt0, mt1, sl0, sl1;
  auto wave_late = [&]() __attribute__((always_inline)) {
    constexpr int ctl[5] = {0x4E, 0x124, 0x128, 0x142, 0x143}, rm[5] = {0xF, 0xF, 0xF, 0xA, 0xC};
    int v = max((int)(mt0.z - tb), (int)(mt1.z - tb));
    static_for<0, 5>([&](auto Ic) __attribute__((always_inline)) { v = max(v, __builtin_amdgcn_update_dpp(v, v, ctl[Ic.v], rm[Ic.v], 0xF, false)); });
    return min(__builtin_amdgcn_readlane(v, 63), 300);
  };
  auto send = [&]() __attribute__((always_inline)) {
    STAMPB(26);
    asm volatile("buffer_load_dwordx4 %0, %2, %3, %4 offen sc1\n\tbuffer_load_dwordx4 %1, %2, %3, %4 offen offset:1024 sc1"
                 :: "v"(sl0), "v"(sl1), "v"(l16), "s"(rs), "s"(slo) : "memory");
    static_for<0, 8>([&](auto Nc) __attribute__((always_inline)) {
      if constexpr ((Nc.v & 3) == 0) grp<Nc.v * 1024, Nc.v * 1024 - 65536>(so, xl, xs2);
      xfer<Nc.v & 3, true>(l16, rs, so);
    });
  };
  {
    const bool rh = w & 1;
    constexpr ConvOrder O = conv_order(), OL = conv_order_last();
    v2u A[4][8][2];
    v4u B[4][8];
    f32x4 acc[8], keep[4];
#pragma unroll
    for (int u = 0; u < 8; ++u) {
      A[0][u][0] = trd(ka + 512 * u + 8);
      A[0][u][1] = trd(ka + 512 * u);
      B[0][7 - u] = lds128(hb + 64 * (8 * (7 - u) + 3));
    }
    SB();
    if (lane) mtp.z = tb;
    stg(rs, tid < 2 ? tid * 16 : OOB, OFF_MT + id * 64, mtp);
    SB();
    auto hand_over = [&](auto Pc) __attribute__((always_inline)) {
      constexpr int i = Pc.v;
      PIN(acc[2 * i]); PIN(acc[2 * i + 1]);
      const f32x4 ev = acc[2 * i], od = acc[2 * i + 1];
      const f32x4 snd = rh ? ev : od, own = rh ? od : ev;
      *(LAS f32x4*)(exa + i * 1024) = snd;
#pragma unroll
      for (int c = 0; c < 4; ++c) { float t = keep[i][c] + own[c]; PIN(t); keep[i][c] = t; }
    };
    auto put_z = [&](auto Pc) __attribute__((always_inline)) {
      constexpr int i = Pc.v;
      const auto s0 = __builtin_amdgcn_permlane16_swap(pack2(keep[2 * i][0], keep[2 * i][1]), pack2(keep[2 * i + 1][0], keep[2 * i + 1][1]), false, false);
      const auto s1 = __builtin_amdgcn_permlane16_swap(pack2(keep[2 * i][2], keep[2 * i][3]), pack2(keep[2 * i + 1][2], keep[2 * i + 1][3]), false, false);
      stg(rs, za, zs + i * 65536, v4u{guard(s0[0]), guard(s1[0]), guard(s0[1]), guard(s1[1])});
    };
    static_for<0, 144>([&](auto Kc) __attribute__((always_inline)) {
      constexpr int K = Kc.v, r = K / 36, k = K % 36, t = r < 3 ? O.t[k] : OL.t[k], u = r < 3 ? O.u[k] : OL.u[k];
      if constexpr (k == 0 && r > 0) asm volatile("" :: "v"(B[r][7]));
      mfma16_v<r == 0 && u == 0>(acc[t], v4u{A[r][u][0].x, A[r][u][0].y, A[r][u][1].x, A[r][u][1].y}, B[r][t - u]);
      SB();
      if constexpr (K == 26) { WAITV(10); __builtin_amdgcn_s_barrier(); FRESH(); }
      if constexpr (K >= 26 && K < 36) W1PULL(K - 26, K - 26);
      if constexpr (K == 40) { asm volatile("" :: "a"(w1f[9])); __builtin_amdgcn_s_barrier(); FRESH(); }
      if constexpr (K == 66) { WAITV(8); __builtin_amdgcn_s_barrier(); FRESH(); }
      if constexpr (K >= 66 && K < 78) W1PULL(K - 56, K - 56);
      if constexpr (K == 82) { asm volatile("" :: "a"(w1f[21])); __builtin_amdgcn_s_barrier(); FRESH(); }
      if constexpr (K == 108) { WAITV(11); __builtin_amdgcn_s_barrier(); FRESH(); }
      if constexpr (K >= 108 && K < 116) W1PULL(K - 84, K - 108);
      if constexpr (K == 116) w1f[22] = trd2(w1x, w1x + 512);
      if constexpr (K == 117) w1f[23] = trd2(w1y, w1y + 512);
      if constexpr (K >= 60 && K < 64) keep[K - 60] = *(LAS f32x4*)(xbr + (K - 60) * 1024);
      constexpr int n = k - (r < 2 ? 0 : 6);
      if constexpr (r < 3 && n >= 0 && n < 24) {
        if constexpr (n % 3 == 2) B[r + 1][n / 3] = lds128(hb + 64 * (8 * (n / 3) - r + 2));
        else A[r + 1][n / 3][n % 3] = trd(ka + 512 * (n / 3) + 64 * (r + 1) + 8 - 8 * (n % 3));
      }
      if constexpr (K == 108 + 10) hand_over(IC<0>{});
      if constexpr (K == 108 + 19) hand_over(IC<1>{});
      if constexpr (K == 108 + 30) hand_over(IC<2>{});
      if constexpr (K < 18 && K % 3 == 1) W1P(10 + K / 3, (2 + K / 3) & 3, lrw, 12288);
      if constexpr (K >= 18 && K < 36 && K % 3 == 1) W1P(10 + K / 3, (K / 3 - 6) & 3, lsp, (K / 3 - 6) * 1024);
      if constexpr (K == 37) W1P(22, 0, lrx, 0);
      if constexpr (K == 41) W1P(23, 0, lto, 0);
      if constexpr (K >= 45 && K < 77 && K % 4 == 1) W1P(24 + (K - 45) / 4, ((K - 45) / 4) & 3, lrw, (K - 45) / 4 * 1024);
      if constexpr (K >= 77 && K < 101 && K % 3 == 2) W2P((K - 77) / 3, ((K - 77) / 3) & 3, lrw, 8192 + (K - 77) / 3 * 1024);
      if constexpr (K >= 101 && K < 119 && K % 3 == 2) W2P((K - 77) / 3, ((K - 101) / 3) & 3, lsp, (K - 101) / 3 * 1024);
      if constexpr (K == MTK)
        asm volatile("buffer_load_dwordx4 %0, %2, %3, 0 offen sc1\n\tbuffer_load_dwordx4 %1, %2, %3, 0 offen offset:2048 sc1"
                     : "=&v"(mt0), "=&v"(mt1) : "v"(mto), "s"(rs) : "memory");
      SB();
    });
    STAMP(7);
    asm volatile("s_nop 7" : "+v"(acc[6]), "+v"(acc[7]));
    hand_over(IC<3>{});
    BAR();
#pragma unroll
    for (int i = 0; i < 4; ++i) keep[i] += *(LAS f32x4*)(eya + i * 1024);
    put_z(IC<0>{});
    put_z(IC<1>{});
    SB();
  }
  STAMP(8);

  static_assert(OFF_MT == 0, "table offset");
  const unsigned tc = now();
  asm volatile("s_waitcnt vmcnt(%2)" : "+v"(mt0), "+v"(mt1) : "n"(2) : "memory");
  if (__builtin_expect(__any(tagbad(tagbad(0u, mt0, tagA, tagB), mt1, tagA, tagB) != 0u), 0)) {
    _Pragma("nounroll") for (unsigned spin = 0; spin < SPIN_CAP; ++spin) {
      FRESH();
      COUNT(21);
      if (tagbad(0u, mt0, tagA, tagB)) mt0 = ldg(rs, mto, OFF_MT);
      if (tagbad(0u, mt1, tagA, tagB)) mt1 = ldg(rs, mto, OFF_MT + 2048);
      if (!__any(tagbad(tagbad(0u, mt0, tagA, tagB), mt1, tagA, tagB) != 0u)) break;
      PAUSE();
    }
  }
  *(LAS unsigned*)(mtw) = mt0.x; *(LAS unsigned*)(mtw + 128) = mt1.x; *(LAS unsigned*)(mtw + 1024) = mt0.z; *(LAS unsigned*)(mtw + 1152) = mt1.z;
  int wait = wave_late() + ZLAG;
  *(LAS int*)(L + L_ETA + w * 4) = wait;
  v4u hq[56], rc[4], res[2], sx[8];
  u64 nanv = 0;
  const int c0 = 7 * w + rot, c3 = c0 - 7 * (rot >> 2), c4 = c0 - 7 * ((rot + 1) >> 2);
  const int hb0 = OFF_HID + (rp * 4 + rg) * 114688, kb0 = OFF_CK + (rp * 4 + rg) * 4096 + w * 896, ckv = lane < 56 ? l16 : OOB;
#define CHUNK(n) (((n) % 28 / 4 < 3 ? c0 : (n) % 28 / 4 == 3 ? c3 : c4) + (n) % 28 / 4)
#define HFRAG(n) ldg(rs, l16 + ((n) & 3) * 1024, hb0 + CHUNK(n) * 4096 + ((n) / 28) * (2 * 114688))
#define HLOAD(n) hq[n] = HFRAG(n)
  const int hoff = OFF_HID + (rp * 4 * 112 + ct * 8 + 2 * w) * 1024;
  const int cko = OFF_CK + rp * 16384 + (ct * 4 + w) * 64;
#define CKPUT(sum, u) __builtin_amdgcn_raw_buffer_store_b32(ckey - red_step<1>(red_step<0>(sum)), rs, ckw, cko + ((u) ^ par) * 4096, SC1)
  const int sxo = OFF_SX + (rp * 4 + w) * 8192;
  int hv0, hv1, ckw;
  unsigned ckey;
  LAS unsigned char *xr[2], *rr, *rr2, *str, *stw, *w2a, *w2b;
  LAS unsigned char *w2sa = pa + (lsp - lrw - 8192), *w2sb = pb + (lsp - lrw - 8192);
  LAS unsigned char* uw = L + L_UVT + hf * 512 + (32 * w + (lane & 31)) * 4;
  LAS unsigned char* uvp = L + L_UVT + (32 * w + 4 * hf) * 4;
  int sxw = sel(lane & 8, OOB, (p4 * 8 + hf * 4 + blk * 2 + ((lane >> 2) & 1)) * 16);
  xr[0] = L + L_XR + tra;
  PIN(w2sa); PIN(w2sb); PIN(uw); PIN(uvp); PIN(sxw); PIN(xr[0]);
  BAR();
  STAMP(9);
  f32x4 uu[4], vv[4];
  {
    const v4u lt = lds128(L + L_ETA);
    const unsigned j0 = *(LAS u16*)(L + L_TS + (si * 32 + (lane >> 2)) * 2), j1 = *(LAS u16*)(L + L_TS + (si * 32 + 16 + (lane >> 2)) * 2);
    v4u qa[4];
#pragma unroll
    for (int i = 0; i < 4; ++i) qa[i] = lds128(qr + i * 256);
    SB();
    WAITV(2);
    asm volatile("" : "=v"(sl0), "=v"(sl1));
    wait = __builtin_amdgcn_readfirstlane(max(max((int)lt.x, (int)lt.y), max((int)lt.z, (int)lt.w)));
    bool sent = false;
    f32x4 cu[4];
    static_for<0, 32>([&](auto Sc) __attribute__((always_inline)) {
      constexpr int s = Sc.v;
      if constexpr (s == 0 || s == 16) {
        if (!sent && (int)(now() - tc) >= wait) { sent = true; send(); }
        SB();
      }
      mfma4_va<(s < 2), s & 7>(cu[2 * (s & 1)], cu[2 * (s & 1) + 1], qa[s >> 3], w1f[s]);
      if constexpr (s < 28 && (s & 1)) {
        constexpr int n = s >> 1;
        LAS unsigned char* src = (n < 8 ? ((n & 1) ? pb : pa) : ((n & 1) ? w2sb : w2sa)) + ((n >> 1) & 3) * 2048;
        w2f[n >> 1][n & 1] = trd2(src, src + 1024);
      }
      SB();
    });
    float i0 = __builtin_amdgcn_rcpf(bf_lo(j0)), i1 = __builtin_amdgcn_rcpf(bf_lo(j1));
    PIN(i0); PIN(i1);
    asm volatile("s_nop 3" : "+v"(cu[0]), "+v"(cu[1]), "+v"(cu[2]), "+v"(cu[3]));
    float un_ = (cu[0][0] + cu[1][0]) + (cu[2][0] + cu[3][0]);
    swap_add32(un_, -LOG2E * ((cu[0][1] + cu[1][1]) + (cu[2][1] + cu[3][1])));
    *(LAS float*)uw = un_;
    SB();
    if (!sent) {
      while ((int)(now() - tc) < wait) { }
      send();
    }
    static_for<0, 8>([&](auto Nc) __attribute__((always_inline)) {
      if constexpr ((Nc.v & 3) == 0) grp<32768 + Nc.v * 1024, Nc.v * 1024 - 65536>(so, xl, xs3);
      xfer<Nc.v & 3, true>(l16, rs, so);
    });
    STAMPB(27);
    asm volatile("s_waitcnt vmcnt(16)" : "+v"(sl0), "+v"(sl1) :: "memory");
    STAMPB(28);
    if (__builtin_expect(__any(max4(max4(0u, sl0), sl1) == POISON), 0)) {
      _Pragma("nounroll") for (unsigned spin = 0; spin < SPIN_CAP; ++spin) {
        FRESH();
        if (spin) PAUSE();
        COUNT(22);
        sl0 = ldg(rs, l16, slo);
        sl1 = ldg(rs, l16 + 1024, slo);
        if (!__any(max4(max4(0u, sl0), sl1) == POISON)) break;
      }
    }
    float sm[8], sq[8];
#pragma unroll
    for (int i = 0; i < 4; ++i) {
      const f32x2 a = f32x2{bf_lo(sl0[i]), bf_hi(sl0[i])} * f32x2{i0, i0}, c = f32x2{bf_lo(sl1[i]), bf_hi(sl1[i])} * f32x2{i1, i1};
      const f32x2 s2 = a + c, q2 = __builtin_elementwise_fma(a, a, c * c);
      sm[2 * i] = s2.x; sm[2 * i + 1] = s2.y; sq[2 * i] = q2.x; sq[2 * i + 1] = q2.y;
    }
#pragma unroll
    for (int i = 0; i < 4; ++i) { swap_add32(sm[i], sm[i + 4]); swap_add32(sq[i], sq[i + 4]); }
#pragma unroll
    for (int i = 0; i < 2; ++i) { swap_add16(sm[i], sm[i + 2]); swap_add16(sq[i], sq[i + 2]); }
#pragma unroll
    for (int i = 0; i < 2; ++i) { sm[i] = dpp_add<0x124>(dpp_add<0x128>(sm[i])); sq[i] = dpp_add<0x124>(dpp_add<0x128>(sq[i])); }
    const bool od = lane & 4;
    stg(rs, sxw, OFF_SX + ((rp * 4 + w) * 16 + si) * 512,
        v4u{f2u(od ? sm[1] : sm[0]), tagA, f2u(od ? sq[1] : sq[0]), tagB});
    SB();
  }
  STAMPB(29);
  asm volatile("" :: "a"(w2f[6][1]));
  W2P(14, 0, lsp, 0); W2P(15, 1, lsp, 0); W2P(16, 2, lsp, 0); W2P(17, 3, lsp, 0); W2P(18, 0, lsp, 4096); W2P(19, 1, lsp, 0);
  WAITV(15);
  STAMP(10);
  BAR();
  STAMP(11);

#define XA(t) (xr[(t) >> 6] + ((t) & 63) * 1024)
#define XF(t) trd2(XA(t), XA(t) + 256)
#define XEN
#define REDO(p) \
  if (XEN __builtin_expect(nanv != 0, 0)) { \
    unsigned m0s; \
    asm volatile("s_mov_b32 %0, m0" : "=s"(m0s)); \
    for (unsigned spin = 0; spin < SPIN_CAP / 16; ++spin) { \
      COUNT(22); \
      _Pragma("nounroll") for (int i = 0; i < 8; ++i) dma_x4(xg + ((p) ^ par) * 32768 + i * 4096, l16, L_XR + (p) * 32768 + i * 4096); \
      WAITV(0); \
      _Pragma("unroll") for (int s = 0; s < 32; ++s) { \
        const v4u t = XF(32 * (p) + s); \
        if (s == 0) mfma_av<true>(acc[(p) % 3], w1f[s], t); else mfma_av<false>(acc[(p) % 3], w1f[s], t); \
        SB(); \
      } \
      mfma_done(acc[(p) % 3]); \
      if (!__any(!(acc[(p) % 3][0] == acc[(p) % 3][0]))) break; \
    } \
    set_m0(m0s); \
  }
  f32x16 acc[3];
  f32x2 ec[4];
  float ep[48], ee[48], ey[48], tl[16];
  unsigned cs = 0, sbad = 0;
  float ssm = 0.f, ssq = 0.f;
  v4u x[128], eo;
#pragma unroll
  for (int t = 0; t < 4; ++t) x[t] = XF(t);
  SB();
  auto eslot = [&](auto Sc) __attribute__((always_inline)) {
    static_for<0, 48>([&](auto Vc) __attribute__((always_inline)) {
      constexpr int V = Vc.v, k = V & 15, st = Sc.v - epi_slot(V);
      if constexpr (st == 6 && (k & 1)) {
        unsigned o = pack2(ey[V - 1], ey[V]);
        PIN(o);
        eo[(k >> 1) & 3] = o;
        if constexpr (k == 7) { stg(rs, hv0, hoff + ((V >> 4) ^ par) * 114688, eo); cs = sum4(0u, eo); PIN(cs); }
        if constexpr (k == 15) { stg(rs, hv1, hoff + ((V >> 4) ^ par) * 114688, eo); CKPUT(sum4(cs, eo), V >> 4); }
      }
      if constexpr (st == 5) { ey[V] = ep[V] * ee[V]; PIN(ey[V]); }
      if constexpr (st == 4) { ee[V] = __builtin_amdgcn_rcpf(ee[V]); PIN(ee[V]); }
      if constexpr (st == 3) { ee[V] = 1.0f + ee[V]; PIN(ee[V]); }
      if constexpr (st == 2) { ee[V] = __builtin_amdgcn_exp2f(ep[V]); PIN(ee[V]); }
      if constexpr (st == 1) { ep[V] = __builtin_fmaf(ec[V >> 4].x, acc[(V >> 4) % 3][k], ep[V]); PIN(ep[V]); }
      if constexpr (st == 0) { ep[V] = __builtin_fmaf(ec[V >> 4].y, uu[k >> 2][k & 3], vv[k >> 2][k & 3]); PIN(ep[V]); }
    });
    SB();
  };
  static_for<0, 64>([&](auto Jc) __attribute__((always_inline)) {
    constexpr int J = Jc.v, p = J >> 4, j = J & 15;
    asm volatile("" :: "v"(x[2 * J + 1]));
    mfma_av<j == 0>(acc[p % 3], w1f[2 * j], x[2 * J]);
    if constexpr (J < SXJ || (J >= SXJ + 4 && J < 20)) {
      constexpr int n = J < SXJ ? J : J - 4;
      if constexpr ((n & 3) == 0) grp<65536 + (n >> 3) * 32768 + (n & 4) * 1024, (n & 4) * 1024>(so, xl, n < 8 ? xs2 : xs3);
      xfer<n & 3, true>(l16, rs, so);
    }
    if constexpr (J >= SXJ && J < SXJ + 4)
      asm volatile("buffer_load_dwordx4 %0, %2, %3, %4 offen offset:%5 sc1\n\tbuffer_load_dwordx4 %1, %2, %3, %4 offen offset:%6 sc1"
                   : "=&v"(sx[2 * (J - SXJ)]), "=&v"(sx[2 * (J - SXJ) + 1])
                   : "v"(l16), "s"(rs), "s"(sxo + ((J - SXJ) & 2) * 2048), "n"(((J - SXJ) & 1) * 2048), "n"(((J - SXJ) & 1) * 2048 + 1024) : "memory");
    if constexpr (J == 43) asm volatile("" :: "a"(w2f[13][1]));
    if constexpr (J >= 20 && J < 28) W2P(J, J & 3, xl, (J - 20) * 1024);
    if constexpr (J >= 28 && J < 34) W2P(J, J & 3, lsp, (J - 28) * 1024);
    if constexpr (J >= 35 && J < 43) W2P(J - 1, (J - 35) & 3, xl, 32768 + (J - 35) * 1024);
    if constexpr (J >= 43 && J < 51) W2P(J - 1, (J - 43) & 3, xl, (J - 43) * 1024);
    static_for<hc_upto(J), hc_upto(J + 1)>([&](auto Nc) __attribute__((always_inline)) { HLOAD(Nc.v); });
    SB();
    constexpr bool sym = epi_pair(J) || J < 19 || J > 60;
    if constexpr (J < 62) { x[2 * J + 4] = XF(2 * J + 4); if constexpr (!sym) x[2 * J + 5] = XF(2 * J + 5); }
    if constexpr (J >= 38 && J < 42) w2f[10 + J - 38][0] = trd2(w2a + (J - 38) * 2048, w2a + (J - 38) * 2048 + 1024);
    SB();
    if constexpr (epi_pair(J)) eslot(IC<2 * J>{});
    mfma_av<false>(acc[p % 3], w1f[2 * j + 1], x[2 * J + 1]);
    if constexpr (sym && J < 62) x[2 * J + 5] = XF(2 * J + 5);
    if constexpr (J >= 38 && J < 42) w2f[10 + J - 38][1] = trd2(w2b + (J - 38) * 2048, w2b + (J - 38) * 2048 + 1024);
    if constexpr (J >= 19 && J < 25) w2f[7 + ((J - 19) >> 1)][(J - 19) & 1] = trd2((((J - 19) & 1) ? w2sb : w2sa) + ((J - 19) >> 1) * 2048, (((J - 19) & 1) ? w2sb : w2sa) + ((J - 19) >> 1) * 2048 + 1024);
    SB();
    if constexpr (J == 10) { waitv<c_between(-1, 10)>(); __builtin_amdgcn_s_barrier(); FRESH(); }
    if constexpr (J == 18) waitv<c_between(7, 18)>();
    if constexpr (J == 34) waitv<c_between(19, 34)>();
    if constexpr (J == 37) waitv<c_between(27, 37)>();
    if constexpr (J == 1) { xr[1] = xr[0] + 65536; rr = xr[0] + (4 * dt + w) * 1024; rr2 = rr + 65536; PIN(xr[1]); PIN(rr); PIN(rr2); }
    if constexpr (J == 2) { hv0 = dt == 7 ? OOB : l16; hv1 = dt == 7 ? OOB : l16 + 1024; PIN(hv0); PIN(hv1); }
    if constexpr (J == 3) { ckw = (p4 || dt == 7) ? OOB : lane; ckey = lane ? 0u : tagA; PIN(ckw); PIN(ckey); }
    if constexpr (J == 4) { str = L + L_STAT + (lane & 31) * 8; stw = str + (w ^ par) * 256; PIN(str); PIN(stw); }
    if constexpr (J == 5) { w2a = pa + (xl - lrw - 8192); w2b = pb + (xl - lrw - 8192); PIN(w2a); PIN(w2b); }
    if constexpr (J >= 6 && J < 10) { uu[J - 6] = *(LAS f32x4*)(uvp + (J - 6) * 32); vv[J - 6] = *(LAS f32x4*)(uvp + 512 + (J - 6) * 32); }
    if constexpr (J == EO - 4 || J == EO - 3) {
      constexpr int i = 4 * (J - EO + 4);
      asm volatile("s_waitcnt vmcnt(%4)" : "+v"(sx[i]), "+v"(sx[i + 1]), "+v"(sx[i + 2]), "+v"(sx[i + 3]) : "n"(c_between(SXJ + 1 + i / 2, J)) : "memory");
      unsigned b0 = tagbad(0u, sx[i], tagA, tagB), b1 = tagbad(0u, sx[i + 1], tagA, tagB);
      float m0 = u2f(sx[i].x) + u2f(sx[i + 1].x), m1 = u2f(sx[i + 2].x) + u2f(sx[i + 3].x);
      PIN(m0); PIN(m1);
      float q0 = u2f(sx[i].z) + u2f(sx[i + 1].z), q1 = u2f(sx[i + 2].z) + u2f(sx[i + 3].z);
      PIN(q0); PIN(q1);
      sbad |= tagbad(b0, sx[i + 2], tagA, tagB) | tagbad(b1, sx[i + 3], tagA, tagB);
      ssm += m0 + m1; PIN(ssm);
      ssq += q0 + q1; PIN(ssq);
      PIN(sbad);
      SB();
    }
    if constexpr (J == EO - 2) {
      if (__builtin_expect(__any(sbad != 0u), 0)) {
        _Pragma("nounroll") for (unsigned spin = 0; spin < SPIN_CAP; ++spin) {
          FRESH();
          if (spin) PAUSE();
          COUNT(23);
          sbad = 0; ssm = 0.f; ssq = 0.f;
#pragma unroll
          for (int i = 0; i < 8; ++i) sx[i] = ldg(rs, l16 + (i & 3) * 1024, sxo + (i >> 2) * 4096);
#pragma unroll
          for (int i = 0; i < 8; i += 4) {
            sbad = tagbad(tagbad(tagbad(tagbad(sbad, sx[i], tagA, tagB), sx[i + 1], tagA, tagB), sx[i + 2], tagA, tagB), sx[i + 3], tagA, tagB);
            ssm += (u2f(sx[i].x) + u2f(sx[i + 1].x)) + (u2f(sx[i + 2].x) + u2f(sx[i + 3].x));
            ssq += (u2f(sx[i].z) + u2f(sx[i + 1].z)) + (u2f(sx[i + 2].z) + u2f(sx[i + 3].z));
          }
          if (!__any(sbad != 0u)) break;
        }
      }
      const float mean = swap32_add(ssm) * (1.0f / 512.0f);
      const float ms = __builtin_fmaf(swap32_add(ssq), 1.0f / 512.0f, 1e-5f);
      const float a = -LOG2E * __builtin_amdgcn_rsqf(fmaxf(__builtin_fmaf(-mean, mean, ms), 1e-5f));
      *(LAS f32x2*)(stw) = f32x2{a, -a * mean};
      SB();
    }
    if constexpr (J == EO - 1) {
      asm volatile("s_waitcnt lgkmcnt(4)" ::: "memory");
      __builtin_amdgcn_s_barrier();
      FRESH();
#pragma unroll
      for (int i = 0; i < 4; ++i) ec[i] = *(LAS f32x2*)(str + i * 256);
      SB();
    }
    if constexpr (!epi_pair(J)) eslot(IC<2 * J>{});
    eslot(IC<2 * J + 1>{});
    if constexpr (J == 16) asm volatile("s_nop 4");
    if constexpr (J >= 61) {
#pragma unroll
      for (int k = (J - 61) * 6; k < (J == 63 ? 16 : (J - 60) * 6); ++k) { tl[k] = __builtin_fmaf(ec[3].y, uu[k >> 2][k & 3], vv[k >> 2][k & 3]); PIN(tl[k]); }
      SB();
    }
    if constexpr (j == 0 && p >= 1) { PIN(acc[(p - 1) % 3]); nanv = __builtin_amdgcn_ballot_w64(!(acc[(p - 1) % 3][0] == acc[(p - 1) % 3][0])); asm volatile("" : "+s"(nanv)); }
    if constexpr (j == 1 && p >= 1) {
      if constexpr (p == 1) REDO(0)
      if constexpr (p == 2) REDO(1)
      if constexpr (p == 3) REDO(2)
      rc[p - 1] = trd2((p == 3 ? rr2 : rr) + ((p - 1) & 1) * 32768, (p == 3 ? rr2 : rr) + ((p - 1) & 1) * 32768 + 256);
    }
    if constexpr (j == 2 && p >= 1) {
      asm volatile("s_waitcnt lgkmcnt(4)" ::: "memory");
      __builtin_amdgcn_s_barrier();
      FRESH();
    }
    SB();
    if constexpr (J == 15) STAMP(16);
    if constexpr (J == 31) STAMP(17);
    if constexpr (J == 47) STAMP(18);
  });
  STAMP(12);
  static_assert(EO <= 33 && SXJ >= 8 && SXJ <= 16 && MTK > 116 && hc_upto(64) <= 24 && hc_upto(57) <= 20 && epi_slot(31) < epi_slot(32) && epi_slot(16) + 1 >= 2 * 34 && epi_slot(32) + 1 >= 2 * 50 && epi_slot(47) + 6 < 128, "schedule");
  static_for<hc_upto(64), hc_upto(64) + HT0>([&](auto Ic) __attribute__((always_inline)) { HLOAD(Ic.v); });
  SB();
  asm volatile("s_nop 7" : "+v"(acc[0]));
  nanv = __builtin_amdgcn_ballot_w64(!(acc[0][0] == acc[0][0]));
  REDO(3)
#pragma unroll
  for (int h = 0; h < 2; ++h) {
    v4u o;
#pragma unroll
    for (int i = 0; i < 4; ++i) {
      const int k = 8 * h + 2 * i;
      const f32x2 t = {tl[k], tl[k + 1]};
      const f32x2 pp = __builtin_elementwise_fma(f32x2{ec[3].x, ec[3].x}, f32x2{acc[0][k], acc[0][k + 1]}, t);
      const f32x2 d = f32x2{__builtin_amdgcn_exp2f(pp.x), __builtin_amdgcn_exp2f(pp.y)} + f32x2{1.0f, 1.0f};
      const f32x2 y = pp * f32x2{__builtin_amdgcn_rcpf(d.x), __builtin_amdgcn_rcpf(d.y)};
      o[i] = pack2(y.x, y.y);
    }
    PIN(o);
    if (h == 0) static_for<hc_upto(64) + HT0, hc_upto(64) + HT0 + HT1>([&](auto Ic) __attribute__((always_inline)) { HLOAD(Ic.v); });
    stg(rs, h ? hv1 : hv0, hoff + (3 ^ par) * 114688, o);
    if (h) CKPUT(sum4(cs, o), 3); else cs = sum4(0u, o);
    SB();
  }
  static_for<hc_upto(64) + HT0 + HT1, 28>([&](auto Ic) __attribute__((always_inline)) { HLOAD(Ic.v); });
  rc[3] = trd2(rr2 + 32768, rr2 + 32768 + 256);
  SB();
  STAMP(13);

  f32x16 d[2][2];
  LAS unsigned char* redw = L + L_XR + w * 8192 + l16;
  LAS unsigned char* redr = L + L_XR + w * 2048 + l16;
#define REDW(un, q) *(LAS f32x4*)(redw + (un) * 32768 + (q) * 1024) = \
    f32x4{d[un][(q) >> 2][4 * ((q) & 3)], d[un][(q) >> 2][4 * ((q) & 3) + 1], d[un][(q) >> 2][4 * ((q) & 3) + 2], d[un][(q) >> 2][4 * ((q) & 3) + 3]}
  LAS unsigned char *w2ua = w2a + 65536, *w2ub = w2b + 65536;
  PIN(w2ua); PIN(w2ub);
#define CHECK(un) \
  if (__builtin_expect((unsigned)__builtin_amdgcn_readlane((int)m[un], 63) != 14u * tagA, 0)) { \
    _Pragma("nounroll") for (unsigned spin = 0; spin < SPIN_CAP; ++spin) { \
      FRESH(); \
      COUNT(24 + (un)); \
      v4u qq[28]; \
      const v4u kk = ldg(rs, ckv, kb0 + (un) * 8192); \
      static_for<0, 12>([&](auto Nc) __attribute__((always_inline)) { qq[Nc.v] = HFRAG(28 * (un) + Nc.v); }); \
      SB(); \
      unsigned t = sum4(0u, kk); \
      static_for<0, 7>([&](auto Cc) __attribute__((always_inline)) { \
        constexpr int c = Cc.v; \
        const v4u* q = &qq[4 * c]; \
        if constexpr (c + 3 < 7) { _Pragma("unroll") for (int i = 0; i < 4; ++i) qq[4 * c + 12 + i] = ldg(rs, l16 + i * 1024, hb0 + CHUNK(4 * c + 12) * 4096 + (un) * (2 * 114688)); } \
        SB(); \
        t = sum8(sum8(t, q[0], q[1]), q[2], q[3]); \
        mfma_av<c == 0>(d[un][0], w2f[4 * c][0], q[0]); mfma_av<c == 0>(d[un][1], w2f[4 * c][1], q[0]); \
        _Pragma("unroll") for (int i = 1; i < 4; ++i) { mfma_av<false>(d[un][0], w2f[4 * c + i][0], q[i]); mfma_av<false>(d[un][1], w2f[4 * c + i][1], q[i]); } \
        SB(); \
      }); \
      mfma_done(d[un][1]); \
      PIN(d[un][0]); \
      if ((unsigned)__builtin_amdgcn_readlane((int)red_all(t), 63) == 14u * tagA) break; \
      PAUSE(); \
    } \
    if (un) { _Pragma("unroll") for (int q = 0; q < 4; ++q) REDW(1, q); } \
  }
  unsigned m[2] = {0u, 0u};
  v4u gbv, scv, ck[2];
  f32x4 pr[8];
  float gb[8], rx[2][8];
  unsigned orow = (rp * 64 + ((lane & 31) >> 3) * 16 + rg * 8 + (lane & 7)) * 1024 + dt * 128 + w * 32 + hf * 16;
  static_for<0, 112>([&](auto Xc) __attribute__((always_inline)) {
    constexpr int X = Xc.v, I = X < 100 ? X >> 1 : 50 + (X - 100) % 6, e = X < 100 ? X & 1 : (X - 100) / 6, un = I / 28;
    mfma_av<I % 28 == 0>(d[un][e], w2f[I % 28][e], hq[I]);
    SB();
    if constexpr (X < 100 && e == 0) {
      if constexpr (I < 3) PULL(14 + I, w2sa, w2sb, I * 2048);
      if constexpr (I >= 3 && I < 7) PULL(14 + I, w2a, w2b, 32768 + (I - 3) * 2048);
      if constexpr (I >= 7 && I < 11) PULL(14 + I, w2a, w2b, (I - 7) * 2048);
      if constexpr (I == 21) waitv<hl_upto(21) - 28 + 1>();
      if constexpr (I >= 21 && I < 24) PULL(4 + I, w2ua, w2ub, (I - 21) * 2048);
      if constexpr (I == 28) acc4(m[0], ck[0]);
      if constexpr (I >= 29 && I < 35) { m[0] = red_step<I - 29>(m[0]); PIN(m[0]); }
      if constexpr (I == 35) CHECK(0)
      if constexpr (I >= 35 && I < 43) REDW(0, I - 35);
    }
    if constexpr (X < 100 && e == 1) {
      if constexpr (I < 6) W2P(50 + I, I & 3, xl, 65536 + I * 1024);
      static_for<hl_upto(I), hl_upto(I + 1)>([&](auto Nc) __attribute__((always_inline)) { HLOAD(Nc.v); });
      if constexpr (I == 6) ck[0] = ldg(rs, ckv, kb0);
      if constexpr (I == HL1 + 16 / HLR) ck[1] = ldg(rs, ckv, kb0 + 8192);
    }
    if constexpr (X < 100 ? e == 1 : e == 0) acc4(m[un], hq[I]);
    if constexpr (X == 105) acc4(m[1], ck[1]);
    if constexpr (X >= 106) { m[1] = red_step<X - 106>(m[1]); PIN(m[1]); }
    if constexpr (X == 23 || X == 24) { const v4u ev = rc[2 * (X - 23)], od = rc[2 * (X - 23) + 1]; res[X - 23] = (rg ^ par) ? od : ev; PIN(res[X - 23]); }
    if constexpr (X == 22) { gbv = lds128(L + L_TG + (64 * dt + 16 * w + 8 * hf) * 2); scv = lds128(L + L_TS + (64 * dt + 16 * w + 8 * hf) * 2); PIN(orow); }
    if constexpr (X >= 26 && X < 42 && (X & 1) == 0) {
      constexpr int q = (X - 26) >> 1;
      const float inv = __builtin_amdgcn_rcpf((q & 1) ? bf_hi(scv[q >> 1]) : bf_lo(scv[q >> 1]));
      gb[q] = -LN2 * ((q & 1) ? bf_hi(gbv[q >> 1]) : bf_lo(gbv[q >> 1]));
      rx[0][q] = inv * ((q & 1) ? bf_hi(res[0][q >> 1]) : bf_lo(res[0][q >> 1]));
      rx[1][q] = inv * ((q & 1) ? bf_hi(res[1][q >> 1]) : bf_lo(res[1][q >> 1]));
      PIN(gb[q]); PIN(rx[0][q]); PIN(rx[1][q]);
    }
    if constexpr (X == 86) BAR();
    if constexpr (X == 86 || X == 88) {
#pragma unroll
      for (int k = X - 86; k < X - 84; ++k) { pr[2 * k] = *(LAS f32x4*)(redr + k * 8192); pr[2 * k + 1] = *(LAS f32x4*)(redr + k * 8192 + 1024); }
    }
    if constexpr (X >= 91 && X < 99) {
      constexpr int q = X - 91;
      float t = (pr[q >> 2][q & 3] + pr[2 + (q >> 2)][q & 3]);
      PIN(t);
      float t2_ = (pr[4 + (q >> 2)][q & 3] + pr[6 + (q >> 2)][q & 3]);
      PIN(t2_);
      rx[0][q] = __builtin_fmaf(gb[q], t + t2_, rx[0][q]);
      PIN(rx[0][q]);
    }
    if constexpr (X == 99)
      *(GAS v4u*)((GAS unsigned char*)out + orow) = v4u{pack2(rx[0][0], rx[0][1]), pack2(rx[0][2], rx[0][3]), pack2(rx[0][4], rx[0][5]), pack2(rx[0][6], rx[0][7])};
    if constexpr (X == 84) __builtin_amdgcn_raw_buffer_store_b32(epoch + 1u, rs, (b | tid) ? OOB : 0, OFF_EP, SC1);
    if constexpr (X == 108) asm volatile("s_nop 3" : "+v"(d[1][0]));
    if constexpr (X >= 108) REDW(1, X - 108);
    SB();
    if constexpr (X == 55) STAMP(14);
  });
  STAMP(19);
  asm volatile("s_nop 7" : "+v"(d[1][1]));
  CHECK(1)
#pragma unroll
  for (int q = 4; q < 8; ++q) REDW(1, q);
  BAR();
  {
#pragma unroll
    for (int k = 0; k < 8; ++k) pr[k] = *(LAS f32x4*)(redr + (k >> 1) * 8192 + 32768 + (k & 1) * 1024);
    SB();
    const f32x4 s0 = (pr[0] + pr[2]) + (pr[4] + pr[6]), s1 = (pr[1] + pr[3]) + (pr[5] + pr[7]);
    float y[8];
#pragma unroll
    for (int q = 0; q < 8; ++q) y[q] = __builtin_fmaf(gb[q], q < 4 ? s0[q & 3] : s1[q & 3], rx[1][q]);
    *(GAS v4u*)((GAS unsigned char*)(out + 1048576) + orow) = v4u{pack2(y[0], y[1]), pack2(y[2], y[3]), pack2(y[4], y[5]), pack2(y[6], y[7])};
  }
  STAMP(15);
}

extern "C" void kernel_launch(void* const* d_in, const int* in_sizes, int n_in, void* d_out, int out_size, void* d_ws, size_t ws_size,
                              hipStream_t stream) {
  (void)in_sizes; (void)n_in; (void)out_size; (void)ws_size;
  (void)hipFuncSetAttribute((const void*)s4_block_kernel, hipFuncAttributeMaxDynamicSharedMemorySize, LDS_BYTES);
  s4_block_kernel<<<256, 256, LDS_BYTES, stream>>>((cptr)d_in[0], (cptr)d_in[1], (cptr)d_in[2], (cptr)d_in[3], (cptr)d_in[4], (cptr)d_in[5], (cptr)d_in[6],
                                                   (cptr)d_in[7], (cptr)d_in[8], (cptr)d_in[10], (cptr)d_in[11], (cptr)d_in[12], (cptr)d_in[13],
                                                   (unsigned char*)d_out, (unsigned char*)d_ws, (cptr)d_in[9]);
}
```

```cpp
#include <hip/hip_runtime.h>

#define GAS __attribute__((address_space(1)))
#define LAS __attribute__((address_space(3)))
#define DI __device__ __forceinline__
typedef unsigned v4u __attribute__((ext_vector_type(4)));
typedef unsigned v2u __attribute__((ext_vector_type(2)));
typedef float f32x2 __attribute__((ext_vector_type(2)));
typedef float f32x4 __attribute__((ext_vector_type(4)));
typedef float f32x16 __attribute__((ext_vector_type(16)));
typedef __bf16 bf16x2 __attribute__((ext_vector_type(2)));
typedef __bf16 bf16x8 __attribute__((ext_vector_type(8)));
typedef short s16x4 __attribute__((ext_vector_type(4)));
typedef unsigned short u16;
typedef unsigned long long u64;

constexpr int SET_BASE = 0;
constexpr int OFF_EP = 49152;
constexpr int OFF_MT = 0;
constexpr int OFF_SG = 32768;
constexpr int OFF_MG = 65536;
constexpr int OFF_SX = 524288;
constexpr int OFF_XC = 458752;
constexpr int OFF_XG = 1 << 20;
constexpr int OFF_Z = OFF_XG + (2 << 20);
constexpr int OFF_HID = OFF_Z + 2097152;
constexpr int OFF_CK = OFF_HID + 7340032;
constexpr int SET_BYTES = OFF_CK + 262144;
static_assert(OFF_MG + 393216 <= OFF_XC && OFF_XC + 65536 <= OFF_SX, "set size");

constexpr int L_TS = 0, L_TB = 1024, L_TG = 2048;
constexpr int L_UVT = 3072;
constexpr int L_M = 4096;
constexpr int L_UVS = L_M;
constexpr int UVS_ROW = 544, UVS_CH = 57 * UVS_ROW;
constexpr int L_KC = L_M;
constexpr int KC_STRIDE = 4160;
constexpr int L_H = L_KC + 8 * KC_STRIDE;
constexpr int H_STRIDE = 4576;
constexpr int L_XB = L_H + 2 * H_STRIDE;
constexpr int L_RX = L_XB + 16384;
constexpr int L_ETA = L_RX + 4096;
constexpr int L_R = L_M + 65536;
constexpr int L_TOP = L_M + 131072 + 24576;
constexpr int L_KT = L_TOP + 16;
constexpr int L_STAT = L_TOP;
constexpr int L_XR = L_M;
constexpr int L_SP = L_M + 131072;
constexpr int LDS_BYTES = 163840;
static_assert(L_TOP + 4096 <= LDS_BYTES && L_ETA + 16 <= L_R && L_UVS + 2 * UVS_CH <= L_R && L_SP + 24576 <= L_TOP, "LDS map");
static_assert(L_H % 16 == 0 && L_XB % 16 == 0 && L_RX % 16 == 0 && L_KT % 16 == 0, "alignment");

#define ZLAG 23
#define MTK 112
#define SXJ 16
#define EOV 30
#define HC0 55
#define HC1 61
#define HL0 9
#define HL1 21
#define HLR 1
#define HT0 2
#define HT1 6
constexpr int EO = EOV;
constexpr int CODE_BYTES = 35072;
constexpr int SC1 = 16;
constexpr int OOB = 0x7FFFFF00;
constexpr unsigned SPIN_CAP = 1u << 15;
constexpr unsigned POISON = 0xFFFFFFFFu;
constexpr float LOG2E = 1.4426950408889634f, LN2 = 0.6931471805599453f, INV2PI = 0.15915494309189535f;

#define SB() __builtin_amdgcn_sched_barrier(0)
#define WAITV(n) asm volatile("s_waitcnt vmcnt(" #n ")" ::: "memory")
#define PIN(x) asm volatile("" : "+v"(x))
#define FRESH() asm volatile("" ::: "memory")
#define PAUSE() asm volatile("s_sleep 4" ::: "memory")
#define BAR() do { asm volatile("s_waitcnt lgkmcnt(0)" ::: "memory"); __builtin_amdgcn_s_barrier(); asm volatile("" ::: "memory"); } while (0)
#define STAMP(i)
#define SARG
#define SPASS
#define COUNT(i)

#define STAMPC(i)
#define STAMPA(i)
#define STAMPB(i) STAMP(i)
DI float u2f(unsigned u) { return __builtin_bit_cast(float, u); }
DI unsigned f2u(float f) { return __builtin_bit_cast(unsigned, f); }
DI float dot2(unsigned u, float c) { const bf16x2 v = __builtin_bit_cast(bf16x2, u); return __builtin_amdgcn_fdot2_f32_bf16(v, v, c, false); }
DI float bf_lo(unsigned u) { return __builtin_bit_cast(float, u << 16); }
DI float bf_hi(unsigned u) { return __builtin_bit_cast(float, u & 0xFFFF0000u); }
DI unsigned pack2(float a, float b) { f32x2 v = {a, b}; return __builtin_bit_cast(unsigned, __builtin_convertvector(v, bf16x2)); }
DI float rbf(float a) { return bf_lo(pack2(a, a)); }
DI unsigned sel(bool c, unsigned a, unsigned b) { asm("" : "+v"(a), "+v"(b)); return c ? a : b; }
DI unsigned guard(unsigned u) { return u < 0xFFFFFFFEu ? u : 0xFFFFFFFEu; }
DI unsigned max4(unsigned m, v4u v) {
  asm("v_max3_u32 %0, %0, %1, %2\n\tv_max3_u32 %0, %0, %3, %4" : "+v"(m) : "v"(v.x), "v"(v.y), "v"(v.z), "v"(v.w));
  return m;
}
DI f32x4 mfma16(v4u a, v4u b, f32x4 c) { return __builtin_amdgcn_mfma_f32_16x16x32_bf16(__builtin_bit_cast(bf16x8, a), __builtin_bit_cast(bf16x8, b), c, 0, 0, 0); }
DI f32x16 mfma32(v4u a, v4u b, f32x16 c) { return __builtin_amdgcn_mfma_f32_32x32x16_bf16(__builtin_bit_cast(bf16x8, a), __builtin_bit_cast(bf16x8, b), c, 0, 0, 0); }
template <bool FIRST> DI void mfma_av(f32x16& acc, const v4u& a, const v4u& b) {
  if (FIRST) asm volatile("v_mfma_f32_32x32x16_bf16 %0, %1, %2, 0" : "=&v"(acc) : "a"(a), "v"(b));
  else asm volatile("v_mfma_f32_32x32x16_bf16 %0, %1, %2, %0" : "+v"(acc) : "a"(a), "v"(b));
}
template <bool FIRST, int BLK> DI void mfma4_va(f32x4& c0, f32x4& c1, const v4u& a, const v4u& b) {
  const v2u al = {a.x, a.y}, ah = {a.z, a.w}, bl = {b.x, b.y}, bh = {b.z, b.w};
  if (FIRST) asm volatile("v_mfma_f32_4x4x4_16b_bf16 %0, %2, %4, 0 cbsz:3 abid:%6\n\tv_mfma_f32_4x4x4_16b_bf16 %1, %3, %5, 0 cbsz:3 abid:%6"
                          : "=&v"(c0), "=&v"(c1) : "v"(al), "v"(ah), "a"(bl), "a"(bh), "n"(BLK));
  else asm volatile("v_mfma_f32_4x4x4_16b_bf16 %0, %2, %4, %0 cbsz:3 abid:%6\n\tv_mfma_f32_4x4x4_16b_bf16 %1, %3, %5, %1 cbsz:3 abid:%6"
                    : "+v"(c0), "+v"(c1) : "v"(al), "v"(ah), "a"(bl), "a"(bh), "n"(BLK));
}
template <bool FIRST> DI void mfma16_v(f32x4& acc, const v4u& a, const v4u& b) {
  if (FIRST) asm volatile("v_mfma_f32_16x16x32_bf16 %0, %1, %2, 0" : "=&v"(acc) : "v"(a), "v"(b));
  else asm volatile("v_mfma_f32_16x16x32_bf16 %0, %1, %2, %0" : "+v"(acc) : "v"(a), "v"(b));
}
DI void mfma_done(f32x16& acc) { asm volatile("s_nop 15" : "+v"(acc)); }
DI v2u trd(LAS unsigned char* p) { return __builtin_bit_cast(v2u, __builtin_amdgcn_ds_read_tr16_b64_v4i16((LAS s16x4*)p)); }
DI v4u trd2(LAS unsigned char* p0, LAS unsigned char* p1) { const v2u a = trd(p0), b = trd(p1); return v4u{a.x, a.y, b.x, b.y}; }
DI v4u lds128(LAS unsigned char* p) { return *(LAS v4u*)p; }
DI v4u ldg(__amdgpu_buffer_rsrc_t r, int voff, int soff) { return __builtin_amdgcn_raw_buffer_load_b128(r, voff, soff, SC1); }
DI void stg(__amdgpu_buffer_rsrc_t r, int voff, int soff, v4u v) {
  __builtin_amdgcn_raw_buffer_store_b128(v, r, voff, soff, SC1);
  asm volatile("s_nop 1" :: "v"(v) : "memory");
}
template <int CTRL> DI float dpp_add(float v) {
  return v + __builtin_bit_cast(float, __builtin_amdgcn_update_dpp(0, __builtin_bit_cast(int, v), CTRL, 0xF, 0xF, true));
}
DI float swap16_add(float v) { const unsigned u = __builtin_bit_cast(unsigned, v); const auto r = __builtin_amdgcn_permlane16_swap(u, u, false, false);
  return u2f(r[0]) + u2f(r[1]); }
DI float swap32_add(float v) { const unsigned u = __builtin_bit_cast(unsigned, v); const auto r = __builtin_amdgcn_permlane32_swap(u, u, false, false);
  return u2f(r[0]) + u2f(r[1]); }
DI float quad_sum(float v) { v = dpp_add<0xB1>(v); return dpp_add<0x4E>(v); }
DI float row_sum(float v) { return dpp_add<0x140>(dpp_add<0x141>(quad_sum(v))); }
DI float wave_sum(float v) { return swap32_add(swap16_add(row_sum(v))); }
DI f32x2 cmul(f32x2 a, f32x2 e, f32x2 ep) { const f32x2 t = f32x2{a.x, a.x} * e; return __builtin_elementwise_fma(f32x2{a.y, a.y}, ep, t); }
DI f32x2 cexp(float re2, float rev) {
  const float m = __builtin_amdgcn_exp2f(re2), f = __builtin_amdgcn_fractf(rev);
  return f32x2{m * __builtin_amdgcn_cosf(f), m * __builtin_amdgcn_sinf(f)};
}
template <int LI, int SI> DI void grp(int& so, unsigned lbase, int sbase) {
  asm volatile("s_add_i32 m0, %1, %3\n\ts_add_i32 %0, %2, %4" : "=s"(so) : "s"(lbase), "s"(sbase), "n"(LI), "n"(SI) : "scc", "memory");
}
template <int K, bool COH> DI void xfer(unsigned v, __amdgpu_buffer_rsrc_t r, int so) {
  if (COH) asm volatile("buffer_load_dwordx4 %0, %1, %2 offen offset:%3 sc1 lds" :: "v"(v), "s"(r), "s"(so), "n"(K * 1024) : "memory");
  else asm volatile("buffer_load_dwordx4 %0, %1, %2 offen offset:%3 lds" :: "v"(v), "s"(r), "s"(so), "n"(K * 1024) : "memory");
}
DI void set_m0(unsigned lds_dst) { asm volatile("s_mov_b32 m0, %0\n\ts_nop 0" :: "s"(lds_dst) : "memory"); }
DI void dma_x4(const unsigned char* sbase, unsigned v, unsigned lds_dst) {
  asm volatile("s_mov_b32 m0, %2\n\ts_nop 0\n\tglobal_load_lds_dwordx4 %0, %1 sc1\n\tglobal_load_lds_dwordx4 %0, %1 offset:1024 sc1\n\t"
               "global_load_lds_dwordx4 %0, %1 offset:2048 sc1\n\tglobal_load_lds_dwordx4 %0, %1 offset:3072 sc1"
               :: "v"(v), "s"(sbase), "s"(lds_dst) : "memory");
}
template <int I> struct IC { static constexpr int v = I; };
template <int I, int N, class F> DI void static_for(F&& f) { if constexpr (I < N) { f(IC<I>{}); static_for<I + 1, N>(f); } }
DI unsigned now() { u64 t; asm volatile("s_memrealtime %0\n\ts_waitcnt lgkmcnt(0)" : "=s"(t) :: "memory"); return (unsigned)t; }
DI void reload(v4u& d, __amdgpu_buffer_rsrc_t r, int voff, int soff) { d = ldg(r, voff, soff); FRESH(); }
DI void reload1(unsigned& d, __amdgpu_buffer_rsrc_t r, int voff, int soff) { d = __builtin_amdgcn_raw_buffer_load_b32(r, voff, soff, SC1); FRESH(); }
template <int N> DI void waitv() { asm volatile("s_waitcnt vmcnt(%0)" :: "n"(N) : "memory"); }
struct ConvOrder { int t[36], u[36]; };
constexpr ConvOrder conv_order() {
  ConvOrder o = {};
  int done[8] = {}, last = -1;
  for (int k = 0; k < 36; ++k) {
    int best = -1;
    for (int t = 0; t < 8; ++t)
      if (t != last && done[t] <= t && (best < 0 || t - done[t] > best - done[best])) best = t;
    o.t[k] = best; o.u[k] = done[best]++; last = best;
  }
  return o;
}
constexpr ConvOrder conv_order_last() {
  ConvOrder o = {};
  int k = 0;
  for (int p = 0; p < 4; ++p)
    for (int u = 0; u <= 2 * p + 1; ++u) {
      o.t[k] = 2 * p + 1; o.u[k] = u; ++k;
      if (u <= 2 * p) { o.t[k] = 2 * p; o.u[k] = u; ++k; }
    }
  return o;
}
constexpr int epi_slot(int v) { return v < 32 ? 2 * EO + v : 2 * 50 - 1 + (v - 32); }
constexpr bool epi_pair(int j) { return (j >= EO && j < EO + 19) || (j >= 50 && j < 61); }
constexpr int hc_upto(int j) { return (j < HC0 ? 0 : j - HC0) + (j < HC1 ? 0 : j - HC1); }
constexpr int hl_upto(int i) { return i < HL0 ? 28 : i < HL0 + 12 ? 28 + (i - HL0) : i < HL1 ? 40 : i < HL1 + 16 / HLR ? 40 + HLR * (i - HL1) : 56; }
constexpr int c_ops(int j) {
  return (j >= SXJ && j < SXJ + 4 ? 2 : j < 34 || (j >= 35 && j < 51) ? 1 : 0) + hc_upto(j + 1) - hc_upto(j) +
         (j == EO + 6 || j == EO + 14 || j == 56 ? 1 : j == EO + 10 || j == EO + 18 || j == 60 ? 2 : 0);
}
constexpr int c_between(int a, int b) { int n = 0; for (int j = a + 1; j < b; ++j) n += c_ops(j); return n; }
DI unsigned tagbad(unsigned bad, v4u g, unsigned tagA, unsigned tagB) {
  return __builtin_amdgcn_bitop3_b32(__builtin_amdgcn_bitop3_b32(bad, g.y, tagA, 0xF6), g.w, tagB, 0xF6);
}
template <int R0, int R1, int R2, int R3> DI void put4(unsigned base, f32x2 a, f32x2 b, f32x2 c, f32x2 d) {
  asm volatile("s_mov_b32 m0, %4\n\ts_nop 0\n\tds_write_addtid_b32 %0 offset:%5\n\tds_write_addtid_b32 %1 offset:%6\n\t"
               "ds_write_addtid_b32 %2 offset:%7\n\tds_write_addtid_b32 %3 offset:%8"
               :: "v"(pack2(a.x, a.y)), "v"(pack2(b.x, b.y)), "v"(pack2(c.x, c.y)), "v"(pack2(d.x, d.y)), "s"(base),
                  "n"(R0 * UVS_ROW), "n"(R1 * UVS_ROW), "n"(R2 * UVS_ROW), "n"(R3 * UVS_ROW) : "memory");
}
DI unsigned sum4(unsigned m, v4u v) {
  asm("v_add3_u32 %0, %0, %1, %2\n\tv_add3_u32 %0, %0, %3, %4" : "+v"(m) : "v"(v.x), "v"(v.y), "v"(v.z), "v"(v.w));
  return m;
}
DI void acc4(unsigned& m, v4u v) {
  asm volatile("v_add3_u32 %0, %0, %1, %2\n\tv_add3_u32 %0, %0, %3, %4" : "+v"(m) : "v"(v.x), "v"(v.y), "v"(v.z), "v"(v.w));
}
DI unsigned sum8(unsigned m, v4u a, v4u b) {
  asm("v_add3_u32 %0, %0, %1, %2\n\tv_add3_u32 %0, %0, %3, %4\n\tv_add3_u32 %0, %0, %5, %6\n\tv_add3_u32 %0, %0, %7, %8"
      : "+v"(m) : "v"(a.x), "v"(a.y), "v"(a.z), "v"(a.w), "v"(b.x), "v"(b.y), "v"(b.z), "v"(b.w));
  return m;
}
template <int I> DI unsigned red_step(unsigned v) {
  constexpr int ctrl[6] = {0xB1, 0x4E, 0x141, 0x140, 0x142, 0x143}, rows[6] = {0xF, 0xF, 0xF, 0xF, 0xA, 0xC};
  return v + (unsigned)__builtin_amdgcn_update_dpp(0, (int)v, ctrl[I], rows[I], 0xF, true);
}
DI unsigned red_all(unsigned v) { return red_step<5>(red_step<4>(red_step<3>(red_step<2>(red_step<1>(red_step<0>(v)))))); }
DI void swap_add32(float& a, float b) { const auto r = __builtin_amdgcn_permlane32_swap(f2u(a), f2u(b), false, false); a = u2f(r[0]) + u2f(r[1]); }
DI void swap_add16(float& a, float b) { const auto r = __builtin_amdgcn_permlane16_swap(f2u(a), f2u(b), false, false); a = u2f(r[0]) + u2f(r[1]); }

typedef const unsigned char* cptr;
typedef unsigned v16u __attribute__((ext_vector_type(16)));
DI cptr mkptr(unsigned lo, unsigned hi) { return (cptr)(((u64)hi << 32) | lo); }

__global__ void __launch_bounds__(256, 1)
s4_block_kernel(cptr, cptr, cptr, cptr, cptr, cptr, cptr, cptr, cptr, cptr, cptr, cptr, cptr, unsigned char*, unsigned char*, cptr) {
  STAMP(0);
  v16u ka0, ka1;
  asm volatile("s_load_dwordx16 %0, %2, 0x0\n\ts_load_dwordx16 %1, %2, 0x40" : "=&s"(ka0), "=&s"(ka1) : "s"(__builtin_amdgcn_kernarg_segment_ptr()));
  SB();
  LAS unsigned char* const L = (LAS unsigned char*)16u - 16;
  const int tid = threadIdx.x, lane = tid & 63, w = __builtin_amdgcn_readfirstlane(tid >> 6);
  const int b = blockIdx.x, xcd = b & 7, slot = b >> 3, id = xcd * 32 + slot;
  const int ks = (id >> 6) * 4 + w, cc = id & 63;
  const int rp = 4 * (xcd >> 1) + (slot >> 3), rg = xcd & 1, dt = slot & 7, si = 8 * rg + dt;
  const int ct = 7 * rg + min(dt, 6);
  const int rot = (0x0340 >> (4 * (2 * (w & 1) + rg))) & 7;

  const unsigned char* pcode;
  asm volatile("s_getpc_b64 %0" : "=s"(pcode));
  unsigned warm;
  asm volatile("global_load_dword %0, %1, %2" : "=v"(warm) : "v"(min((((slot & 7) * 4 + w) * 9 + min(lane, 8)) * 128, CODE_BYTES)), "s"(pcode) : "memory");
  const int g16 = lane >> 4, q4 = (lane & 15) >> 2, p4 = lane & 3, hf = lane >> 5, blk = (lane >> 4) & 1, l16 = lane * 16;
  const unsigned lrw = L_R + w * 16384;
  unsigned lsp = L_SP + w * 6144;
  unsigned co = (ks * 32 + (lane & 31)) * 2, xo = (id >> 2) * 32768 + (id & 3) * 256 + (tid & 7) * 4096 + (tid >> 3) * 8, ro = id * 8192 + w * 2048 + hf * 1024 + (lane & 31) * 16, po = id * 512 + tid * 2;
  PIN(co); PIN(xo); PIN(ro); PIN(po);
  set_m0(lrw);
  SB();
  asm volatile("s_waitcnt lgkmcnt(0)" : "+s"(ka0), "+s"(ka1));
  const cptr xin = mkptr(ka0[0], ka0[1]), pAre = mkptr(ka0[2], ka0[3]), pAim = mkptr(ka0[4], ka0[5]), pBre = mkptr(ka0[6], ka0[7]);
  const cptr pBim = mkptr(ka0[8], ka0[9]), pCre = mkptr(ka0[10], ka0[11]), pCim = mkptr(ka0[12], ka0[13]), pldt = mkptr(ka0[14], ka0[15]);
  const cptr pD = mkptr(ka1[0], ka1[1]), w1 = mkptr(ka1[2], ka1[3]), w2 = mkptr(ka1[4], ka1[5]), pc = mkptr(ka1[6], ka1[7]);
  const cptr wmod = mkptr(ka1[8], ka1[9]), out = mkptr(ka1[10], ka1[11]), ws = mkptr(ka1[12], ka1[13]);
  unsigned uldt, uD;
  asm volatile("s_load_dword %0, %2, %4\n\ts_load_dword %1, %3, %4" : "=&s"(uldt), "=&s"(uD) : "s"(pldt), "s"(pD), "s"(id * 4));
  unsigned uAre, uAim, uBre, uBim, uCre, uCim;
  asm volatile("global_load_ushort %0, %6, %7\n\tglobal_load_ushort %1, %6, %8\n\tglobal_load_ushort %2, %6, %9\n\t"
               "global_load_ushort %3, %6, %10\n\tglobal_load_ushort %4, %6, %11\n\tglobal_load_ushort %5, %6, %12"
               : "=&v"(uAre), "=&v"(uAim), "=&v"(uBre), "=&v"(uBim), "=&v"(uCre), "=&v"(uCim)
               : "v"(po), "s"(pAre), "s"(pAim), "s"(pBre), "s"(pBim), "s"(pCre), "s"(pCim) : "memory");
  unsigned epoch_v, cval;
  v2u xq[4];
  v4u ra, rb;
  asm volatile("global_load_dword %0, %8, %12 sc1\n\tglobal_load_dwordx2 %1, %9, %13\n\tglobal_load_dwordx2 %2, %9, %13 offset:1024\n\t"
               "global_load_dwordx2 %3, %9, %13 offset:2048\n\tglobal_load_dwordx2 %4, %9, %13 offset:3072\n\t"
               "global_load_dwordx4 %5, %10, %13\n\tglobal_load_dwordx4 %6, %10, %13 offset:512\n\tglobal_load_ushort %7, %11, %14"
               : "=&v"(epoch_v), "=&v"(xq[0]), "=&v"(xq[1]), "=&v"(xq[2]), "=&v"(xq[3]), "=&v"(ra), "=&v"(rb), "=&v"(cval)
               : "v"(OFF_EP), "v"(xo), "v"(ro), "v"(co), "s"(ws), "s"(xin), "s"(pc) : "memory");
  unsigned wmo[3];
#pragma unroll
  for (int i = 0; i < 3; ++i) { const unsigned idx = lane + 64 * i; wmo[i] = (ks * 32 * 6144 + cc * 96 - i * 1024) + idx / 6 * (6144 - 96) + idx * 16; }
  asm volatile("global_load_lds_dwordx4 %0, %3\n\tglobal_load_lds_dwordx4 %1, %3 offset:1024\n\tglobal_load_lds_dwordx4 %2, %3 offset:2048"
               :: "v"(wmo[0]), "v"(wmo[1]), "v"(wmo[2]), "s"(wmod) : "memory");
  const unsigned char* setp = ws + SET_BASE;
  const __amdgpu_buffer_rsrc_t rs = __builtin_amdgcn_make_buffer_rsrc((void*)setp, 0, SET_BYTES, 0x00020000);
  SB();
  STAMPA(26);
  unsigned wto = id * 7168 + (w >> 1) * 3584 + min(lane, 27) * 128;
  PIN(wto);
  unsigned w1v[4], w2v[4];
#pragma unroll
  for (int i = 0; i < 4; ++i) {
    w1v[i] = (lane >> 3) * 3584 + (((lane & 7) ^ (blk << 2)) * 16) + i * (57344 - 1024);
    w2v[i] = (lane >> 3) * 1024 + (((lane & 7) ^ (blk << 2)) * 16) + i * (8192 - 1024);
    PIN(w1v[i]); PIN(w2v[i]);
  }
  int w1b = ct * 256 + (w >> 1) * 128 + (w & 1) * (8 * 3584) + ((dt + 1) >> 3 << 24), w2lo = (448 * w + 64 * rot) * 1024 + dt * 128;
  int w2c3 = w2lo - (rot >> 2) * (448 * 1024), w2hi = w2lo - ((rot + 1) >> 2) * (448 * 1024), so = 0;
  unsigned lrx = L_RX + w * 1024, lto = L_TOP + w * 1024;
  int zs = OFF_Z + (2 * id + (w >> 1)) * 64;
  int za = ((((w & 1) + 2 * (g16 & 1)) * 4 + q4) * 4 + (g16 >> 1)) * 32768 + p4 * 16;
  int tra = (hf * 8 + q4) * 64 + blk * 32 + p4 * 8;
  const int wsrc = (w & 2) + hf;
  int w1t = q4 * 128 + (((w & 1) ^ (q4 >> 1)) * 64) + blk * 32 + p4 * 8;
  LAS unsigned char *w1a = L + L_R + wsrc * 16384 + w1t, *w1s = L + L_SP + wsrc * 6144 + w1t;
  LAS unsigned char *w1x = L + L_RX + wsrc * 1024 + w1t, *w1y = L + L_TOP + wsrc * 1024 + w1t;
#define W1PULL(step, n) w1f[step] = trd2(((n) < 16 ? w1a : w1s) + ((n) & 15) * 1024, ((n) < 16 ? w1a : w1s) + ((n) & 15) * 1024 + 512)
  LAS unsigned char* ka = L + L_KC + ((w >> 1) * 4 + q4) * KC_STRIDE + 2 * (32 - 8 * g16 + 4 * p4) - 8 + 256 * (w & 1);
  LAS unsigned char* hb = L + L_H + (w >> 1) * H_STRIDE + (lane & 15) * 32 + g16 * 16 + 256 - 256 * (w & 1);
  LAS unsigned char* zp = L + sel(tid < 64, L_KC + ((lane >> 1) & 7) * KC_STRIDE + (lane & 1) * 16, L_H + hf * H_STRIDE + min(lane & 31, 29) * 16);
  LAS unsigned char* ub = L + L_UVS + (lane & 15) * UVS_ROW + g16 * 16;
  int gmo = OFF_MG + (g16 * 256 + id) * 256 + (lane & 15) * 16, gmo2 = OFF_MG + ((4 + blk) * 256 + id) * 256 + (lane & 15) * 16;
  int mgo = sel(lane < 48, (lane >> 1) * 256 + (lane & 1) * 8, OOB), sgo = sel(lane & 31, OOB, hf * 8);
  LAS unsigned char* xbw = L + L_XB + ((w & 1) * 4 + (w >> 1)) * 1024 + l16;
  int xcw = sel(lane & 7, OOB, tid), xcr = sel(lane & 7, OOB, (tid >> 3) * 1024), xgw = (tid >> 3) * 16384 + (tid & 7) * 16;
  PIN(xcw); PIN(xcr); PIN(xgw);
  LAS unsigned char *exa = L + L_XB + w * 4096 + l16, *eya = L + L_XB + (w ^ 1) * 4096 + l16;
  LAS unsigned char* xbr = L + L_XB + w * 4096 + ((lane & 15) * 4 + g16) * 16;
  PIN(xbw); PIN(xbr); PIN(exa); PIN(eya);
  int mto = (w * 64 + (lane >> 1)) * 64 + (lane & 1) * 16;
  LAS unsigned char* mtw = L + (lane & 1) * L_TG + (w * 64 + (lane >> 1)) * 4;
  PIN(mto); PIN(mtw);
  PIN(za); PIN(tra); PIN(w1a); PIN(w1s); PIN(w1x); PIN(w1y); PIN(ka); PIN(hb); PIN(zp); PIN(ub); PIN(gmo); PIN(gmo2); PIN(mgo); PIN(sgo);
  asm volatile("" : "+s"(w1b), "+s"(w2lo), "+s"(w2c3), "+s"(w2hi), "+s"(lsp), "+s"(lrx), "+s"(lto), "+s"(zs));
#define W1P(n, k, lbase, li) do { if constexpr ((k) == 0) grp<(li), (n) * 57344>(so, lbase, w1b); xfer<(k), false>(w1v[k], rw1, so); } while (0)
#define W2P(n, k, lbase, li) do { if constexpr ((k) == 0) grp<(li), (n) * 8192>(so, lbase, (n) < 24 ? w2lo : (n) < 32 ? w2c3 : w2hi); xfer<(k), false>(w2v[k], rw2, so); } while (0)
  SB();
  LAS unsigned char* koff[12];
  {
    float fj = (float)(8 * tid - 3);
    PIN(fj);
    const unsigned lin = (unsigned)(8 * tid - 3 + 0x4300 - 128) * 0x10001u + 0x10000u;
#pragma unroll
    for (int i = 0; i < 12; i += 2) {
      const unsigned r = sel(tid < 32, lin + i * 0x10001u, pack2(fj + (float)i, fj + (float)(i + 1)));
      koff[i] = L + ((r & 0xFFFFu) * 4 + (L_KT + (128 - 0x4300) * 4));
      koff[i + 1] = L + ((r >> 16) * 4 + (L_KT + (128 - 0x4300) * 4));
      PIN(koff[i]);
      if (i < 10) PIN(koff[i + 1]);
    }
  }
  *(LAS v4u*)(L + L_KT - 16) = v4u{0u, 0u, 0u, 0u};
  LAS unsigned char* ktw[4];
  {
    const int row = w * 16 + (lane & 15), sg = max((row >> 3) - 1, 0), rr_ = row - 8 * sg;
    const int idx0 = 128 * sg + ((rr_ >> (3 - sg)) << (7 - sg)) + (rr_ & ((8 >> sg) - 1)) + 1, st = 8 >> sg;
#pragma unroll
    for (int r = 0; r < 4; ++r) {
      const int idx = sel(row < 40, idx0 + st * (4 * g16 + r), tid == 136 && r == 0 ? 0 : 641 + lane);
      ktw[r] = L + L_KT + idx * 4;
      PIN(ktw[r]);
    }
  }
  const int par = (0x1C >> (ct % 7)) & 1;
  const unsigned char* xg = setp + OFF_Z + rp * 131072;
  unsigned xl = L_XR + w * 8192;
  int xs2 = OFF_Z + rp * 131072 + w * 8192 + (2 ^ par) * 32768, xs3 = xs2 + 32768 - 65536 * par;
  asm volatile("" : "+s"(xl), "+s"(xs2), "+s"(xs3));
  int slo = OFF_Z + (rp * 4 + w) * 32768 + si * 2048;
  asm volatile("" : "+s"(slo));
  const int w2t = (4 * hf + q4) * 128 + blk * 32 + (((p4 & 1) << 1) | (p4 >> 1)) * 8;
  LAS unsigned char* const w2a0 = L + w2t + (q4 >> 1) * 64;
  LAS unsigned char* const w2b0 = L + w2t + ((q4 >> 1) ^ 1) * 64;
  LAS unsigned char *pa = w2a0 + lrw + 8192, *pb = w2b0 + lrw + 8192;
  LAS unsigned char* qr = L + (p4 ? L_TB : L_TS) + hf * 16 + ((lane >> 2) & 7) * 32;
  PIN(pa); PIN(pb); PIN(qr);
#define PULL(step, pa, pb, off) do { w2f[step][0] = trd2((pa) + (off), (pa) + (off) + 1024); w2f[step][1] = trd2((pb) + (off), (pb) + (off) + 1024); } while (0)
  const __amdgpu_buffer_rsrc_t rw1 = __builtin_amdgcn_make_buffer_rsrc((void*)w1, 0, 1835008, 0x00020000);
  const __amdgpu_buffer_rsrc_t rw2 = __builtin_amdgcn_make_buffer_rsrc((void*)w2, 0, 1835008, 0x00020000);
  SB();
  STAMPA(27);
  asm volatile("s_waitcnt lgkmcnt(0)" : "+s"(uldt), "+s"(uD));
  asm volatile("s_waitcnt vmcnt(11)" : "+v"(uAre), "+v"(uAim), "+v"(uBre), "+v"(uBim), "+v"(uCre), "+v"(uCim), "+v"(warm));
    const float dtv = rbf(__builtin_amdgcn_exp2f(LOG2E * (w >> 1 ? bf_hi(uldt) : bf_lo(uldt))));
    const float are = dtv * bf_lo(uAre) * LOG2E, aim = dtv * bf_lo(uAim) * INV2PI;
    const float br = dtv * bf_lo(uBre), bi = dtv * bf_lo(uBim), cr = bf_lo(uCre), ci = bf_lo(uCim);
    const f32x2 wv = {cr * br - ci * bi, cr * bi + ci * br};
    const unsigned row = L_UVS + (w >> 1) * UVS_CH + (w & 1) * 256;
    const f32x2 e1 = cexp(are, aim), e8 = cexp(8.f * are, -8.f * aim), e128 = cexp(128.f * are, 128.f * aim);
    const f32x2 e1p = {-e1.y, e1.x}, e8p = {-e8.y, e8.x}, e128p = {-e128.y, e128.x};
    f32x2 z[8], v, v2, y, y2;
#define M1(a) cmul(a, e1, e1p)
#define M8(a) cmul(a, e8, e8p)
#define MK(a) cmul(a, e128, e128p)
    z[0] = M1(wv);
    put4<40, 41, 42, 0>(row, wv, f32x2{1.f, 0.f}, e8, z[0]);
    v = M8(e8); z[1] = M1(z[0]); v2 = M8(v); z[2] = M1(z[1]);
    put4<43, 1, 44, 2>(row, v, z[1], v2, z[2]);
  SB();
  asm volatile("s_waitcnt vmcnt(6)" : "+v"(epoch_v), "+v"(xq[0]), "+v"(xq[1]), "+v"(xq[2]), "+v"(xq[3]));
  STAMPA(29);
  const unsigned epoch = __builtin_amdgcn_readfirstlane(epoch_v);
  const unsigned tagA = epoch * 0x9E3779B1u + 0x7F4A7C15u, tagB = ~tagA;
  {
    const v4u xc0 = {xq[0].x, xq[1].x, xq[2].x, xq[3].x}, xc1 = {xq[0].y, xq[1].y, xq[2].y, xq[3].y};
    stg(rs, xgw, OFF_XG + (id & 3) * 524288 + (id >> 2) * 128, xc0);
    stg(rs, xgw, OFF_XG + (id & 3) * 524288 + (id >> 2) * 128 + 8192, xc1);
    SB();
    STAMP(1);
    asm volatile("s_waitcnt vmcnt(5)" : "+v"(ra), "+v"(rb), "+v"(cval));
    const float c = bf_lo(cval);
    const float s = c * __builtin_amdgcn_rcpf(1.0f + __builtin_amdgcn_exp2f(-LOG2E * c));
    *(LAS u16*)(L + lrw + 3072 + (lane & 31) * 2) = (u16)pack2(s, s);
    f32x2 fa[8];
#pragma unroll
    for (int i = 0; i < 4; ++i) { fa[i] = f32x2{bf_lo(ra[i]), bf_hi(ra[i])}; fa[4 + i] = f32x2{bf_lo(rb[i]), bf_hi(rb[i])}; }
    const f32x2 s2 = ((fa[0] + fa[1]) + (fa[2] + fa[3])) + ((fa[4] + fa[5]) + (fa[6] + fa[7]));
    float mean = swap16_add(row_sum(s2.x + s2.y)) * (1.0f / 512.0f);
    const unsigned k0 = red_step<2>(red_step<1>(red_step<0>(sum4(0u, xc0)))), k1 = red_step<2>(red_step<1>(red_step<0>(sum4(0u, xc1))));
    __builtin_amdgcn_raw_buffer_store_b64(v2u{tagA - k0, tagA - k1}, rs, xcw, OFF_XC + id * 256, SC1);
    PIN(mean);
    SB();
    f32x2 q2 = {0.f, 0.f}, q3 = {0.f, 0.f};
#pragma unroll
    for (int i = 0; i < 8; i += 2) {
      const f32x2 d0 = fa[i] - f32x2{mean, mean}, d1 = fa[i + 1] - f32x2{mean, mean};
      q2 = __builtin_elementwise_fma(d0, d0, q2);
      q3 = __builtin_elementwise_fma(d1, d1, q3);
    }
    q2 += q3;
    float qs = q2.x + q2.y;
    PIN(qs);
    WAITV(3);
    LAS unsigned char* a = L + lrw + (8 * g16 + q4) * 96 + p4 * 8;
    const v4u sc = lds128(L + lrw + 3072 + g16 * 16);
    const v4u m0 = trd2(a, a + 384), m1 = trd2(a + 32, a + 416), m2 = trd2(a + 64, a + 448);
    SB();
    const float var = rbf(rbf(swap16_add(row_sum(qs)) * (1.0f / 512.0f)) + 1e-5f);
    unsigned st = pack2(mean, __builtin_amdgcn_sqrtf(var));
    PIN(st);
    SB();
    const f32x4 c0 = mfma16(sc, m0, f32x4{}), c1 = mfma16(sc, m1, f32x4{}), c2 = mfma16(sc, m2, f32x4{});
    SB();
    __builtin_amdgcn_raw_buffer_store_b64(v2u{st, hf ? tagB : tagA}, rs, sgo, OFF_SG + ((w & 1) * 512 + id * 2 + (w >> 1)) * 16, SC1);
    __builtin_amdgcn_raw_buffer_store_b64(v2u{sel(lane < 16, f2u(c0[0]), sel(lane < 32, f2u(c1[0]), f2u(c2[0]))), (lane & 1) ? tagB : tagA}, rs, mgo,
                                          OFF_MG + cc * 24 * 256 + ks * 16, SC1);
    SB();
  }
  unsigned wt;
  SB();
  STAMP(2);
  unsigned dsk = tid == 136 ? uD : 0u;
  PIN(dsk);

    asm volatile("global_load_dword %0, %1, %2" : "=v"(wt) : "v"(wto), "s"(w1 + (w & 1) * (w2 - w1)) : "memory");
    v = M8(v2); z[3] = M1(z[2]); v2 = M8(v); z[4] = M1(z[3]);
    put4<45, 3, 46, 4>(row, v, z[3], v2, z[4]);
    v = M8(v2); z[5] = M1(z[4]); v2 = M8(v); z[6] = M1(z[5]);
    put4<47, 5, 48, 6>(row, v, z[5], v2, z[6]);
    z[7] = M1(z[6]); z[0] = MK(z[0]); z[1] = MK(z[1]); z[2] = MK(z[2]);
    put4<7, 8, 9, 10>(row, z[7], z[0], z[1], z[2]);
    z[3] = MK(z[3]); z[4] = MK(z[4]); z[5] = MK(z[5]); z[6] = MK(z[6]);
    put4<11, 12, 13, 14>(row, z[3], z[4], z[5], z[6]);
    z[7] = MK(z[7]); z[1] = MK(z[1]); z[3] = MK(z[3]); z[5] = MK(z[5]);
    put4<15, 16, 17, 18>(row, z[7], z[1], z[3], z[5]);
    z[7] = MK(z[7]); z[1] = MK(z[1]); z[3] = MK(z[3]); z[5] = MK(z[5]);
    put4<19, 20, 21, 22>(row, z[7], z[1], z[3], z[5]);
    y = MK(z[7]); z[3] = MK(z[3]); y2 = MK(y); z[4] = MK(z[3]);
    put4<23, 24, 25, 26>(row, y, z[3], y2, z[4]);
    y = MK(y2); z[3] = MK(z[4]); y2 = MK(y); z[4] = MK(z[3]);
    put4<27, 28, 29, 30>(row, y, z[3], y2, z[4]);
    y = MK(y2); v = M8(v2); y2 = MK(y); v2 = M8(v);
    put4<31, 49, 32, 50>(row, y, v, y2, v2);
    y = MK(y2); v = M8(v2); y2 = MK(y); v2 = M8(v);
    put4<33, 51, 34, 52>(row, y, v, y2, v2);
    y = MK(y2); v = M8(v2); y2 = MK(y); v2 = M8(v);
    put4<35, 53, 36, 54>(row, y, v, y2, v2);
    y = MK(y2); v = M8(v2); y2 = MK(y); v2 = M8(v);
    put4<37, 55, 38, 56>(row, y, v, y2, v2);
    y = MK(y2);
    *(LAS unsigned*)(L + row + 39 * UVS_ROW + lane * 4) = pack2(y.x, y.y);
    STAMPC(29);
#undef M1
#undef M8
#undef MK
  BAR();
  STAMP(3);
  v4u gx0, gx1, gs0, gs1, gs2, gs3, gm, gn;
  unsigned gc0, gc1;
  const int xgo = OFF_XG + id * 8192, xco = OFF_XC + id * 4;
  v4u fv[2][8], fu[2][8];
  if (w < 3) {
    LAS unsigned char* ut = ub + w * 16 * UVS_ROW;
#pragma unroll
    for (int s = 0; s < 8; ++s)
#pragma unroll
      for (int c = 0; c < 2; ++c) { fv[c][s] = lds128(ub + c * UVS_CH + 41 * UVS_ROW + s * 64); fu[c][s] = lds128(ut + c * UVS_CH + s * 64); SB(); }
    f32x4 a0 = {}, a1 = {};
#pragma unroll
    for (int s = 0; s < 8; ++s) { a0 = mfma16(fv[0][s], fu[0][s], a0); SB(); a1 = mfma16(fv[1][s], fu[1][s], a1); SB(); }
    *(LAS unsigned*)ktw[0] = pack2(a0[0] + bf_lo(dsk), a1[0] + bf_hi(dsk));
#pragma unroll
    for (int r = 1; r < 4; ++r) *(LAS unsigned*)ktw[r] = pack2(a0[r], a1[r]);
  }
  SB();
#define LD1(i, so) "buffer_load_dwordx4 %" #i ", %10, %14, " so " offen sc1\n\t"
  asm volatile("buffer_load_dwordx4 %6, %11, %14, 0 offen sc1\n\tbuffer_load_dwordx4 %7, %12, %14, 0 offen sc1\n\t"
               LD1(0, "%15") LD1(1, "%16") "buffer_load_dword %8, %13, %14, %21 offen sc1\n\tbuffer_load_dword %9, %13, %14, %22 offen sc1\n\t"
               LD1(2, "%17") LD1(3, "%18") LD1(4, "%19") LD1(5, "%20")
               : "=&v"(gx0), "=&v"(gx1), "=&v"(gs0), "=&v"(gs1), "=&v"(gs2), "=&v"(gs3), "=&v"(gm), "=&v"(gn), "=&v"(gc0), "=&v"(gc1)
               : "v"(tid * 16), "v"(gmo), "v"(gmo2), "v"(xcr), "s"(rs), "s"(xgo), "s"(xgo + 4096),
                 "s"(OFF_SG), "s"(OFF_SG + 8192), "s"(OFF_SG + 4096), "s"(OFF_SG + 12288), "s"(xco), "s"(xco + 32768) : "memory");
#undef LD1
  SB();
  BAR();
  {
    unsigned k[11];
#pragma unroll
    for (int i = 0; i < 11; ++i) k[i] = *(LAS unsigned*)koff[i];
    SB();
    stg(rs, (tid >> 3) * 32768 + (tid & 7) * 16, OFF_Z + id * 128, v4u{POISON, POISON, POISON, POISON});
    stg(rs, (tid >> 3) * 32768 + (tid & 7) * 16, OFF_Z + id * 128 + 32 * 32768, v4u{POISON, POISON, POISON, POISON});
    SB();
    if (w < 2) *(LAS v4u*)zp = v4u{0u, 0u, 0u, 0u};
#pragma unroll
    for (int c = 0; c < 2; ++c) {
      unsigned e[5], o[5];
#pragma unroll
      for (int i = 0; i < 5; ++i) {
        o[i] = __builtin_amdgcn_perm(k[2 * i + 1], k[2 * i], c ? 0x07060302u : 0x05040100u);
        e[i] = __builtin_amdgcn_perm(k[2 * i + 2], k[2 * i + 1], c ? 0x07060302u : 0x05040100u);
      }
      LAS unsigned char* d = L + L_KC + c * 4 * KC_STRIDE + 32 + tid * 16;
      *(LAS v4u*)(d) = v4u{e[1], e[2], e[3], e[4]};
      *(LAS v4u*)(d + KC_STRIDE) = v4u{o[1], o[2], o[3], o[4]};
      *(LAS v4u*)(d + 2 * KC_STRIDE) = v4u{e[0], e[1], e[2], e[3]};
      *(LAS v4u*)(d + 3 * KC_STRIDE) = v4u{o[0], o[1], o[2], o[3]};
    }
  }
    W1P(0, 0, lrw, 0); W1P(1, 1, lrw, 0); W1P(2, 2, lrw, 0); W1P(3, 3, lrw, 0);
    W1P(4, 0, lrw, 4096); W1P(5, 1, lrw, 0); W1P(6, 2, lrw, 0); W1P(7, 3, lrw, 0);
  SB();
  STAMP(4);
  {
    asm volatile("s_waitcnt vmcnt(10)" : "+v"(gx0), "+v"(gx1), "+v"(gs0), "+v"(gs1), "+v"(gs2), "+v"(gs3), "+v"(gm), "+v"(gn), "+v"(gc0), "+v"(gc1) :: "memory");
#define XBAD() (red_step<2>(red_step<1>(red_step<0>(sum8(gc0 + gc1, gx0, gx1)))) ^ (2u * tagA))
#define TB(a, c) tagbad(tagbad(0u, a, tagA, tagB), c, tagA, tagB)
    if (__builtin_expect(__any((XBAD() | TB(gm, gn) | TB(gs0, gs1) | TB(gs2, gs3)) != 0u), 0)) {
      _Pragma("nounroll") for (unsigned spin = 0; spin < SPIN_CAP; ++spin) {
        if (spin) PAUSE();
        COUNT(20);
        if (XBAD()) { reload(gx0, rs, tid * 16, xgo); reload(gx1, rs, tid * 16, xgo + 4096); reload1(gc0, rs, xcr, xco); reload1(gc1, rs, xcr, xco + 32768); }
        if (TB(gs0, gs1)) { reload(gs0, rs, tid * 16, OFF_SG); reload(gs1, rs, tid * 16, OFF_SG + 8192); }
        if (TB(gs2, gs3)) { reload(gs2, rs, tid * 16, OFF_SG + 4096); reload(gs3, rs, tid * 16, OFF_SG + 12288); }
        if (TB(gm, gn)) { reload(gm, rs, gmo, 0); reload(gn, rs, gmo2, 0); }
        if (!__any((XBAD() | TB(gm, gn) | TB(gs0, gs1) | TB(gs2, gs3)) != 0u)) break;
      }
    }
#undef XBAD
#undef TB
  }
  STAMP(5);
  W1P(8, 0, lrw, 8192); W1P(9, 1, lrw, 0);
  v4u mtp;
  {
    float r0 = u2f(gm.x), r1 = u2f(gm.z), r2 = u2f(gn.x), r3 = u2f(gn.z);
#define RSTEP(c) r0 = dpp_add<c>(r0); PIN(r0); r1 = dpp_add<c>(r1); PIN(r1); r2 = dpp_add<c>(r2); PIN(r2); r3 = dpp_add<c>(r3); PIN(r3)
    RSTEP(0xB1); RSTEP(0x4E); RSTEP(0x141); RSTEP(0x140);
#undef RSTEP
    const int i0 = (int)pack2(r0, r1), i1 = (int)pack2(r2, r3);
    unsigned md[6];
#pragma unroll
    for (int k = 0; k < 6; ++k) md[k] = (unsigned)__builtin_amdgcn_readlane(k < 4 ? i0 : i1, 16 * (k & 3));
#define MOD(k) f32x2{bf_lo(md[k]), bf_hi(md[k])}
    const f32x2 one = {1.0f, 1.0f}, sr = one + MOD(4), cr = one + MOD(1);
    unsigned sp = pack2(sr.x, sr.y);
    const unsigned cp = pack2(cr.x, cr.y);
    sp |= ((sp & 0x7FFFu) ? 0u : 0x3080u) | ((sp & 0x7FFF0000u) ? 0u : 0x30800000u);
    const f32x2 S = {bf_lo(sp), bf_hi(sp)}, G = MOD(2) * S, A = f32x2{bf_lo(cp), bf_hi(cp)} * G, B = MOD(0) * G;
#undef MOD
    mtp = v4u{sel(lane, md[5], sp), tagA, md[3], tagB};
    auto rows4 = [&](const v4u& gx, const v4u& ga, const v4u& gb_, int i) __attribute__((always_inline)) {
      const f32x2 ma = {bf_lo(ga.x), bf_lo(ga.z)}, mb = {bf_lo(gb_.x), bf_lo(gb_.z)};
      const f32x2 ra_ = {__builtin_amdgcn_rcpf(bf_hi(ga.x)), __builtin_amdgcn_rcpf(bf_hi(ga.z))}, rb_ = {__builtin_amdgcn_rcpf(bf_hi(gb_.x)), __builtin_amdgcn_rcpf(bf_hi(gb_.z))};
      const f32x2 x0 = {bf_lo(gx.x), bf_lo(gx.y)}, x1 = {bf_lo(gx.z), bf_lo(gx.w)}, x2 = {bf_hi(gx.x), bf_hi(gx.y)}, x3 = {bf_hi(gx.z), bf_hi(gx.w)};
      const f32x2 h0 = __builtin_elementwise_fma((x0 - ma) * ra_, f32x2{A.x, A.x}, f32x2{B.x, B.x});
      const f32x2 h1 = __builtin_elementwise_fma((x1 - mb) * rb_, f32x2{A.x, A.x}, f32x2{B.x, B.x});
      const f32x2 h2 = __builtin_elementwise_fma((x2 - ma) * ra_, f32x2{A.y, A.y}, f32x2{B.y, B.y});
      const f32x2 h3 = __builtin_elementwise_fma((x3 - mb) * rb_, f32x2{A.y, A.y}, f32x2{B.y, B.y});
      *(LAS v2u*)(L + L_H + 480 + tid * 8 + i * 2048) = v2u{pack2(h0.x, h0.y), pack2(h1.x, h1.y)};
      *(LAS v2u*)(L + L_H + H_STRIDE + 480 + tid * 8 + i * 2048) = v2u{pack2(h2.x, h2.y), pack2(h3.x, h3.y)};
      const f32x2 y0 = x0 * f32x2{S.x, S.x}, y1 = x1 * f32x2{S.x, S.x}, y2 = x2 * f32x2{S.y, S.y}, y3 = x3 * f32x2{S.y, S.y};
      *(LAS f32x4*)(xbw + i * 2048) = f32x4{y0.x, y0.y, y1.x, y1.y};
      *(LAS f32x4*)(xbw + 8192 + i * 2048) = f32x4{y2.x, y2.y, y3.x, y3.y};
    };
    W1P(10, 2, lrw, 0); W1P(11, 3, lrw, 0);
    rows4(gx0, gs0, gs1, 0);
    W1P(12, 0, lrw, 12288); W1P(13, 1, lrw, 0); W1P(14, 2, lrw, 0); W1P(15, 3, lrw, 0);
    rows4(gx1, gs2, gs3, 1);
    W1P(16, 0, lsp, 0); W1P(17, 1, lsp, 0);
  }
  asm volatile("s_waitcnt vmcnt(18)" : "+v"(wt) :: "memory");
  BAR();
  const unsigned tb = now();
  STAMP(6);

  v4u w1f[32], w2f[28][2], mt0, mt1, sl0, sl1;
  auto wave_late = [&]() __attribute__((always_inline)) {
    constexpr int ctl[5] = {0x4E, 0x124, 0x128, 0x142, 0x143}, rm[5] = {0xF, 0xF, 0xF, 0xA, 0xC};
    int v = max((int)(mt0.z - tb), (int)(mt1.z - tb));
    static_for<0, 5>([&](auto Ic) __attribute__((always_inline)) { v = max(v, __builtin_amdgcn_update_dpp(v, v, ctl[Ic.v], rm[Ic.v], 0xF, false)); });
    return min(__builtin_amdgcn_readlane(v, 63), 300);
  };
  auto send = [&]() __attribute__((always_inline)) {
    STAMPB(26);
    asm volatile("buffer_load_dwordx4 %0, %2, %3, %4 offen sc1\n\tbuffer_load_dwordx4 %1, %2, %3, %4 offen offset:1024 sc1"
                 :: "v"(sl0), "v"(sl1), "v"(l16), "s"(rs), "s"(slo) : "memory");
    static_for<0, 8>([&](auto Nc) __attribute__((always_inline)) {
      if constexpr ((Nc.v & 3) == 0) grp<Nc.v * 1024, Nc.v * 1024 - 65536>(so, xl, xs2);
      xfer<Nc.v & 3, true>(l16, rs, so);
    });
  };
  {
    const bool rh = w & 1;
    constexpr ConvOrder O = conv_order(), OL = conv_order_last();
    v2u A[4][8][2];
    v4u B[4][8];
    f32x4 acc[8], keep[4];
#pragma unroll
    for (int u = 0; u < 8; ++u) {
      A[0][u][0] = trd(ka + 512 * u + 8);
      A[0][u][1] = trd(ka + 512 * u);
      B[0][7 - u] = lds128(hb + 64 * (8 * (7 - u) + 3));
    }
    SB();
    if (lane) mtp.z = tb;
    stg(rs, tid < 2 ? tid * 16 : OOB, OFF_MT + id * 64, mtp);
    SB();
    auto hand_over = [&](auto Pc) __attribute__((always_inline)) {
      constexpr int i = Pc.v;
      PIN(acc[2 * i]); PIN(acc[2 * i + 1]);
      const f32x4 ev = acc[2 * i], od = acc[2 * i + 1];
      const f32x4 snd = rh ? ev : od, own = rh ? od : ev;
      *(LAS f32x4*)(exa + i * 1024) = snd;
#pragma unroll
      for (int c = 0; c < 4; ++c) { float t = keep[i][c] + own[c]; PIN(t); keep[i][c] = t; }
    };
    auto put_z = [&](auto Pc) __attribute__((always_inline)) {
      constexpr int i = Pc.v;
      const auto s0 = __builtin_amdgcn_permlane16_swap(pack2(keep[2 * i][0], keep[2 * i][1]), pack2(keep[2 * i + 1][0], keep[2 * i + 1][1]), false, false);
      const auto s1 = __builtin_amdgcn_permlane16_swap(pack2(keep[2 * i][2], keep[2 * i][3]), pack2(keep[2 * i + 1][2], keep[2 * i + 1][3]), false, false);
      stg(rs, za, zs + i * 65536, v4u{guard(s0[0]), guard(s1[0]), guard(s0[1]), guard(s1[1])});
    };
    static_for<0, 144>([&](auto Kc) __attribute__((always_inline)) {
      constexpr int K = Kc.v, r = K / 36, k = K % 36, t = r < 3 ? O.t[k] : OL.t[k], u = r < 3 ? O.u[k] : OL.u[k];
      if constexpr (k == 0 && r > 0) asm volatile("" :: "v"(B[r][7]));
      mfma16_v<r == 0 && u == 0>(acc[t], v4u{A[r][u][0].x, A[r][u][0].y, A[r][u][1].x, A[r][u][1].y}, B[r][t - u]);
      SB();
      if constexpr (K == 24) { WAITV(13); __builtin_amdgcn_s_barrier(); FRESH(); }
      if constexpr (K >= 24 && K < 36) W1PULL(K - 24, K - 24);
      if constexpr (K == 40) { asm volatile("" :: "a"(w1f[11])); __builtin_amdgcn_s_barrier(); FRESH(); }
      if constexpr (K == 60) { WAITV(9); __builtin_amdgcn_s_barrier(); FRESH(); }
      if constexpr (K >= 60 && K < 70) W1PULL(K - 48, K - 48);
      if constexpr (K == 76) { asm volatile("" :: "a"(w1f[21])); __builtin_amdgcn_s_barrier(); FRESH(); }
      if constexpr (K == 101) { WAITV(9); __builtin_amdgcn_s_barrier(); FRESH(); }
      if constexpr (K >= 101 && K < 109) W1PULL(K - 77, K - 101);
      if constexpr (K == 109) w1f[22] = trd2(w1x, w1x + 512);
      if constexpr (K == 110) w1f[23] = trd2(w1y, w1y + 512);
      if constexpr (r == 2 && k < 4) keep[k] = *(LAS f32x4*)(xbr + k * 1024);
      constexpr int n = k - (r < 2 ? 0 : 4);
      if constexpr (r < 3 && n >= 0 && n < 24) {
        if constexpr (n % 3 == 2) B[r + 1][n / 3] = lds128(hb + 64 * (8 * (n / 3) - r + 2));
        else A[r + 1][n / 3][n % 3] = trd(ka + 512 * (n / 3) + 64 * (r + 1) + 8 - 8 * (n % 3));
      }
      if constexpr (K == 108 + 10) hand_over(IC<0>{});
      if constexpr (K == 108 + 19) hand_over(IC<1>{});
      if constexpr (K == 108 + 30) hand_over(IC<2>{});
      if constexpr (K < 14 && K % 4 == 1) W1P(18 + K / 4, (2 + K / 4) & 3, lsp, 4096);
      if constexpr (K == 17) W1P(22, 0, lrx, 0);
      if constexpr (K == 21) W1P(23, 0, lto, 0);
      if constexpr (K >= 41 && K < 65 && K % 3 == 2) W1P(24 + (K - 41) / 3, ((K - 41) / 3) & 3, lrw, (K - 41) / 3 * 1024);
      if constexpr (K >= 66 && K < 98 && K % 4 == 2) W2P((K - 66) / 4, ((K - 66) / 4) & 3, lrw, 8192 + (K - 66) / 4 * 1024);
      if constexpr (K >= 98 && K < 122 && K % 4 == 2) W2P((K - 66) / 4, ((K - 98) / 4) & 3, lsp, (K - 98) / 4 * 1024);
      if constexpr (K == MTK)
        asm volatile("buffer_load_dwordx4 %0, %2, %3, 0 offen sc1\n\tbuffer_load_dwordx4 %1, %2, %3, 0 offen offset:2048 sc1"
                     : "=&v"(mt0), "=&v"(mt1) : "v"(mto), "s"(rs) : "memory");
      SB();
    });
    STAMP(7);
    asm volatile("s_nop 7" : "+v"(acc[6]), "+v"(acc[7]));
    hand_over(IC<3>{});
    BAR();
#pragma unroll
    for (int i = 0; i < 4; ++i) keep[i] += *(LAS f32x4*)(eya + i * 1024);
    put_z(IC<0>{});
    put_z(IC<1>{});
    SB();
  }
  STAMP(8);

  static_assert(OFF_MT == 0, "table offset");
  const unsigned tc = now();
  asm volatile("s_waitcnt vmcnt(%2)" : "+v"(mt0), "+v"(mt1) : "n"(2 + (MTK < 118 ? (121 - MTK) / 4 : 0)) : "memory");
  if (__builtin_expect(__any(tagbad(tagbad(0u, mt0, tagA, tagB), mt1, tagA, tagB) != 0u), 0)) {
    _Pragma("nounroll") for (unsigned spin = 0; spin < SPIN_CAP; ++spin) {
      FRESH();
      COUNT(21);
      if (tagbad(0u, mt0, tagA, tagB)) mt0 = ldg(rs, mto, OFF_MT);
      if (tagbad(0u, mt1, tagA, tagB)) mt1 = ldg(rs, mto, OFF_MT + 2048);
      if (!__any(tagbad(tagbad(0u, mt0, tagA, tagB), mt1, tagA, tagB) != 0u)) break;
      PAUSE();
    }
  }
  *(LAS unsigned*)(mtw) = mt0.x; *(LAS unsigned*)(mtw + 128) = mt1.x; *(LAS unsigned*)(mtw + 1024) = mt0.z; *(LAS unsigned*)(mtw + 1152) = mt1.z;
  int wait = wave_late() + ZLAG;
  *(LAS int*)(L + L_ETA + w * 4) = wait;
  v4u hq[56], rc[4], res[2], sx[8];
  u64 nanv = 0;
  const int c0 = 7 * w + rot, c3 = c0 - 7 * (rot >> 2), c4 = c0 - 7 * ((rot + 1) >> 2);
  const int hb0 = OFF_HID + (rp * 4 + rg) * 114688, kb0 = OFF_CK + (rp * 4 + rg) * 4096 + w * 896, ckv = lane < 56 ? l16 : OOB;
#define CHUNK(n) (((n) % 28 / 4 < 3 ? c0 : (n) % 28 / 4 == 3 ? c3 : c4) + (n) % 28 / 4)
#define HFRAG(n) ldg(rs, l16 + ((n) & 3) * 1024, hb0 + CHUNK(n) * 4096 + ((n) / 28) * (2 * 114688))
#define HLOAD(n) hq[n] = HFRAG(n)
  const int hoff = OFF_HID + (rp * 4 * 112 + ct * 8 + 2 * w) * 1024;
  const int cko = OFF_CK + rp * 16384 + (ct * 4 + w) * 64;
#define CKPUT(sum, u) __builtin_amdgcn_raw_buffer_store_b32(ckey - red_step<1>(red_step<0>(sum)), rs, ckw, cko + ((u) ^ par) * 4096, SC1)
  const int sxo = OFF_SX + (rp * 4 + w) * 8192;
  int hv0, hv1, ckw;
  unsigned ckey;
  LAS unsigned char *xr[2], *rr, *rr2, *str, *stw, *w2a, *w2b;
  LAS unsigned char *w2sa = pa + (lsp - lrw - 8192), *w2sb = pb + (lsp - lrw - 8192);
  LAS unsigned char* uw = L + L_UVT + hf * 512 + (32 * w + (lane & 31)) * 4;
  LAS unsigned char* uvp = L + L_UVT + (32 * w + 4 * hf) * 4;
  int sxw = sel(lane & 8, OOB, (p4 * 8 + hf * 4 + blk * 2 + ((lane >> 2) & 1)) * 16);
  xr[0] = L + L_XR + tra;
  PIN(w2sa); PIN(w2sb); PIN(uw); PIN(uvp); PIN(sxw); PIN(xr[0]);
  BAR();
  STAMP(9);
  f32x4 uu[4], vv[4];
  {
    const v4u lt = lds128(L + L_ETA);
    const unsigned j0 = *(LAS u16*)(L + L_TS + (si * 32 + (lane >> 2)) * 2), j1 = *(LAS u16*)(L + L_TS + (si * 32 + 16 + (lane >> 2)) * 2);
    v4u qa[4];
#pragma unroll
    for (int i = 0; i < 4; ++i) qa[i] = lds128(qr + i * 256);
    SB();
    WAITV(2);
    asm volatile("" : "=v"(sl0), "=v"(sl1));
    wait = __builtin_amdgcn_readfirstlane(max(max((int)lt.x, (int)lt.y), max((int)lt.z, (int)lt.w)));
    bool sent = false;
    f32x4 cu[4];
    static_for<0, 32>([&](auto Sc) __attribute__((always_inline)) {
      constexpr int s = Sc.v;
      if constexpr (s == 0 || s == 16) {
        if (!sent && (int)(now() - tc) >= wait) { sent = true; send(); }
        SB();
      }
      mfma4_va<(s < 2), s & 7>(cu[2 * (s & 1)], cu[2 * (s & 1) + 1], qa[s >> 3], w1f[s]);
      if constexpr (s < 28 && (s & 1)) {
        constexpr int n = s >> 1;
        LAS unsigned char* src = (n < 8 ? ((n & 1) ? pb : pa) : ((n & 1) ? w2sb : w2sa)) + ((n >> 1) & 3) * 2048;
        w2f[n >> 1][n & 1] = trd2(src, src + 1024);
      }
      SB();
    });
    float i0 = __builtin_amdgcn_rcpf(bf_lo(j0)), i1 = __builtin_amdgcn_rcpf(bf_lo(j1));
    PIN(i0); PIN(i1);
    asm volatile("s_nop 3" : "+v"(cu[0]), "+v"(cu[1]), "+v"(cu[2]), "+v"(cu[3]));
    float un_ = (cu[0][0] + cu[1][0]) + (cu[2][0] + cu[3][0]);
    swap_add32(un_, -LOG2E * ((cu[0][1] + cu[1][1]) + (cu[2][1] + cu[3][1])));
    *(LAS float*)uw = un_;
    SB();
    if (!sent) {
      while ((int)(now() - tc) < wait) { }
      send();
    }
    static_for<0, 8>([&](auto Nc) __attribute__((always_inline)) {
      if constexpr ((Nc.v & 3) == 0) grp<32768 + Nc.v * 1024, Nc.v * 1024 - 65536>(so, xl, xs3);
      xfer<Nc.v & 3, true>(l16, rs, so);
    });
    STAMPB(27);
    asm volatile("s_waitcnt vmcnt(16)" : "+v"(sl0), "+v"(sl1) :: "memory");
    STAMPB(28);
    if (__builtin_expect(__any(max4(max4(0u, sl0), sl1) == POISON), 0)) {
      _Pragma("nounroll") for (unsigned spin = 0; spin < SPIN_CAP; ++spin) {
        FRESH();
        if (spin) PAUSE();
        COUNT(22);
        sl0 = ldg(rs, l16, slo);
        sl1 = ldg(rs, l16 + 1024, slo);
        if (!__any(max4(max4(0u, sl0), sl1) == POISON)) break;
      }
    }
    float sm[8], sq[8];
#pragma unroll
    for (int i = 0; i < 4; ++i) {
      const f32x2 a = f32x2{bf_lo(sl0[i]), bf_hi(sl0[i])} * f32x2{i0, i0}, c = f32x2{bf_lo(sl1[i]), bf_hi(sl1[i])} * f32x2{i1, i1};
      const f32x2 s2 = a + c, q2 = __builtin_elementwise_fma(a, a, c * c);
      sm[2 * i] = s2.x; sm[2 * i + 1] = s2.y; sq[2 * i] = q2.x; sq[2 * i + 1] = q2.y;
    }
#pragma unroll
    for (int i = 0; i < 4; ++i) { swap_add32(sm[i], sm[i + 4]); swap_add32(sq[i], sq[i + 4]); }
#pragma unroll
    for (int i = 0; i < 2; ++i) { swap_add16(sm[i], sm[i + 2]); swap_add16(sq[i], sq[i + 2]); }
#pragma unroll
    for (int i = 0; i < 2; ++i) { sm[i] = dpp_add<0x124>(dpp_add<0x128>(sm[i])); sq[i] = dpp_add<0x124>(dpp_add<0x128>(sq[i])); }
    const bool od = lane & 4;
    stg(rs, sxw, OFF_SX + ((rp * 4 + w) * 16 + si) * 512,
        v4u{f2u(od ? sm[1] : sm[0]), tagA, f2u(od ? sq[1] : sq[0]), tagB});
    SB();
  }
  STAMPB(29);
  asm volatile("" :: "a"(w2f[6][1]));
  W2P(14, 0, lsp, 0); W2P(15, 1, lsp, 0); W2P(16, 2, lsp, 0); W2P(17, 3, lsp, 0); W2P(18, 0, lsp, 4096); W2P(19, 1, lsp, 0);
  WAITV(15);
  STAMP(10);
  BAR();
  STAMP(11);

#define XA(t) (xr[(t) >> 6] + ((t) & 63) * 1024)
#define XF(t) trd2(XA(t), XA(t) + 256)
#define XEN
#define REDO(p) \
  if (XEN __builtin_expect(nanv != 0, 0)) { \
    unsigned m0s; \
    asm volatile("s_mov_b32 %0, m0" : "=s"(m0s)); \
    for (unsigned spin = 0; spin < SPIN_CAP / 16; ++spin) { \
      COUNT(22); \
      _Pragma("nounroll") for (int i = 0; i < 8; ++i) dma_x4(xg + ((p) ^ par) * 32768 + i * 4096, l16, L_XR + (p) * 32768 + i * 4096); \
      WAITV(0); \
      _Pragma("unroll") for (int s = 0; s < 32; ++s) { \
        const v4u t = XF(32 * (p) + s); \
        if (s == 0) mfma_av<true>(acc[(p) % 3], w1f[s], t); else mfma_av<false>(acc[(p) % 3], w1f[s], t); \
        SB(); \
      } \
      mfma_done(acc[(p) % 3]); \
      if (!__any(!(acc[(p) % 3][0] == acc[(p) % 3][0]))) break; \
    } \
    set_m0(m0s); \
  }
  f32x16 acc[3];
  f32x2 ec[4];
  float ep[48], ee[48], ey[48], tl[16];
  unsigned cs = 0, sbad = 0;
  float ssm = 0.f, ssq = 0.f;
  v4u x[128], eo;
#pragma unroll
  for (int t = 0; t < 4; ++t) x[t] = XF(t);
  SB();
  auto eslot = [&](auto Sc) __attribute__((always_inline)) {
    static_for<0, 48>([&](auto Vc) __attribute__((always_inline)) {
      constexpr int V = Vc.v, k = V & 15, st = Sc.v - epi_slot(V);
      if constexpr (st == 6 && (k & 1)) {
        unsigned o = pack2(ey[V - 1], ey[V]);
        PIN(o);
        eo[(k >> 1) & 3] = o;
        if constexpr (k == 7) { stg(rs, hv0, hoff + ((V >> 4) ^ par) * 114688, eo); cs = sum4(0u, eo); PIN(cs); }
        if constexpr (k == 15) { stg(rs, hv1, hoff + ((V >> 4) ^ par) * 114688, eo); CKPUT(sum4(cs, eo), V >> 4); }
      }
      if constexpr (st == 5) { ey[V] = ep[V] * ee[V]; PIN(ey[V]); }
      if constexpr (st == 4) { ee[V] = __builtin_amdgcn_rcpf(ee[V]); PIN(ee[V]); }
      if constexpr (st == 3) { ee[V] = 1.0f + ee[V]; PIN(ee[V]); }
      if constexpr (st == 2) { ee[V] = __builtin_amdgcn_exp2f(ep[V]); PIN(ee[V]); }
      if constexpr (st == 1) { ep[V] = __builtin_fmaf(ec[V >> 4].x, acc[(V >> 4) % 3][k], ep[V]); PIN(ep[V]); }
      if constexpr (st == 0) { ep[V] = __builtin_fmaf(ec[V >> 4].y, uu[k >> 2][k & 3], vv[k >> 2][k & 3]); PIN(ep[V]); }
    });
    SB();
  };
  static_for<0, 64>([&](auto Jc) __attribute__((always_inline)) {
    constexpr int J = Jc.v, p = J >> 4, j = J & 15;
    asm volatile("" :: "v"(x[2 * J + 1]));
    mfma_av<j == 0>(acc[p % 3], w1f[2 * j], x[2 * J]);
    if constexpr (J < SXJ || (J >= SXJ + 4 && J < 20)) {
      constexpr int n = J < SXJ ? J : J - 4;
      if constexpr ((n & 3) == 0) grp<65536 + (n >> 3) * 32768 + (n & 4) * 1024, (n & 4) * 1024>(so, xl, n < 8 ? xs2 : xs3);
      xfer<n & 3, true>(l16, rs, so);
    }
    if constexpr (J >= SXJ && J < SXJ + 4)
      asm volatile("buffer_load_dwordx4 %0, %2, %3, %4 offen offset:%5 sc1\n\tbuffer_load_dwordx4 %1, %2, %3, %4 offen offset:%6 sc1"
                   : "=&v"(sx[2 * (J - SXJ)]), "=&v"(sx[2 * (J - SXJ) + 1])
                   : "v"(l16), "s"(rs), "s"(sxo + ((J - SXJ) & 2) * 2048), "n"(((J - SXJ) & 1) * 2048), "n"(((J - SXJ) & 1) * 2048 + 1024) : "memory");
    if constexpr (J == 43) asm volatile("" :: "a"(w2f[13][1]));
    if constexpr (J >= 20 && J < 28) W2P(J, J & 3, xl, (J - 20) * 1024);
    if constexpr (J >= 28 && J < 34) W2P(J, J & 3, lsp, (J - 28) * 1024);
    if constexpr (J >= 35 && J < 43) W2P(J - 1, (J - 35) & 3, xl, 32768 + (J - 35) * 1024);
    if constexpr (J >= 43 && J < 51) W2P(J - 1, (J - 43) & 3, xl, (J - 43) * 1024);
    static_for<hc_upto(J), hc_upto(J + 1)>([&](auto Nc) __attribute__((always_inline)) { HLOAD(Nc.v); });
    SB();
    constexpr bool sym = epi_pair(J) || J < 19 || J > 60;
    if constexpr (J < 62) { x[2 * J + 4] = XF(2 * J + 4); if constexpr (!sym) x[2 * J + 5] = XF(2 * J + 5); }
    if constexpr (J >= 38 && J < 42) w2f[10 + J - 38][0] = trd2(w2a + (J - 38) * 2048, w2a + (J - 38) * 2048 + 1024);
    SB();
    if constexpr (epi_pair(J)) eslot(IC<2 * J>{});
    mfma_av<false>(acc[p % 3], w1f[2 * j + 1], x[2 * J + 1]);
    if constexpr (sym && J < 62) x[2 * J + 5] = XF(2 * J + 5);
    if constexpr (J >= 38 && J < 42) w2f[10 + J - 38][1] = trd2(w2b + (J - 38) * 2048, w2b + (J - 38) * 2048 + 1024);
    if constexpr (J >= 19 && J < 25) w2f[7 + ((J - 19) >> 1)][(J - 19) & 1] = trd2((((J - 19) & 1) ? w2sb : w2sa) + ((J - 19) >> 1) * 2048, (((J - 19) & 1) ? w2sb : w2sa) + ((J - 19) >> 1) * 2048 + 1024);
    SB();
    if constexpr (J == 10) { waitv<c_between(-1, 10)>(); __builtin_amdgcn_s_barrier(); FRESH(); }
    if constexpr (J == 18) waitv<c_between(7, 18)>();
    if constexpr (J == 34) waitv<c_between(19, 34)>();
    if constexpr (J == 37) waitv<c_between(27, 37)>();
    if constexpr (J == 1) { xr[1] = xr[0] + 65536; rr = xr[0] + (4 * dt + w) * 1024; rr2 = rr + 65536; PIN(xr[1]); PIN(rr); PIN(rr2); }
    if constexpr (J == 2) { hv0 = dt == 7 ? OOB : l16; hv1 = dt == 7 ? OOB : l16 + 1024; PIN(hv0); PIN(hv1); }
    if constexpr (J == 3) { ckw = (p4 || dt == 7) ? OOB : lane; ckey = lane ? 0u : tagA; PIN(ckw); PIN(ckey); }
    if constexpr (J == 4) { str = L + L_STAT + (lane & 31) * 8; stw = str + (w ^ par) * 256; PIN(str); PIN(stw); }
    if constexpr (J == 5) { w2a = pa + (xl - lrw - 8192); w2b = pb + (xl - lrw - 8192); PIN(w2a); PIN(w2b); }
    if constexpr (J >= 6 && J < 10) { uu[J - 6] = *(LAS f32x4*)(uvp + (J - 6) * 32); vv[J - 6] = *(LAS f32x4*)(uvp + 512 + (J - 6) * 32); }
    if constexpr (J == EO - 4 || J == EO - 3) {
      constexpr int i = 4 * (J - EO + 4);
      asm volatile("s_waitcnt vmcnt(%4)" : "+v"(sx[i]), "+v"(sx[i + 1]), "+v"(sx[i + 2]), "+v"(sx[i + 3]) : "n"(c_between(SXJ + 1 + i / 2, J)) : "memory");
      unsigned b0 = tagbad(0u, sx[i], tagA, tagB), b1 = tagbad(0u, sx[i + 1], tagA, tagB);
      float m0 = u2f(sx[i].x) + u2f(sx[i + 1].x), m1 = u2f(sx[i + 2].x) + u2f(sx[i + 3].x);
      PIN(m0); PIN(m1);
      float q0 = u2f(sx[i].z) + u2f(sx[i + 1].z), q1 = u2f(sx[i + 2].z) + u2f(sx[i + 3].z);
      PIN(q0); PIN(q1);
      sbad |= tagbad(b0, sx[i + 2], tagA, tagB) | tagbad(b1, sx[i + 3], tagA, tagB);
      ssm += m0 + m1; PIN(ssm);
      ssq += q0 + q1; PIN(ssq);
      PIN(sbad);
      SB();
    }
    if constexpr (J == EO - 2) {
      if (__builtin_expect(__any(sbad != 0u), 0)) {
        _Pragma("nounroll") for (unsigned spin = 0; spin < SPIN_CAP; ++spin) {
          FRESH();
          if (spin) PAUSE();
          COUNT(23);
          sbad = 0; ssm = 0.f; ssq = 0.f;
#pragma unroll
          for (int i = 0; i < 8; ++i) sx[i] = ldg(rs, l16 + (i & 3) * 1024, sxo + (i >> 2) * 4096);
#pragma unroll
          for (int i = 0; i < 8; i += 4) {
            sbad = tagbad(tagbad(tagbad(tagbad(sbad, sx[i], tagA, tagB), sx[i + 1], tagA, tagB), sx[i + 2], tagA, tagB), sx[i + 3], tagA, tagB);
            ssm += (u2f(sx[i].x) + u2f(sx[i + 1].x)) + (u2f(sx[i + 2].x) + u2f(sx[i + 3].x));
            ssq += (u2f(sx[i].z) + u2f(sx[i + 1].z)) + (u2f(sx[i + 2].z) + u2f(sx[i + 3].z));
          }
          if (!__any(sbad != 0u)) break;
        }
      }
      const float mean = swap32_add(ssm) * (1.0f / 512.0f);
      const float ms = __builtin_fmaf(swap32_add(ssq), 1.0f / 512.0f, 1e-5f);
      const float a = -LOG2E * __builtin_amdgcn_rsqf(fmaxf(__builtin_fmaf(-mean, mean, ms), 1e-5f));
      *(LAS f32x2*)(stw) = f32x2{a, -a * mean};
      SB();
    }
    if constexpr (J == EO - 1) {
      asm volatile("s_waitcnt lgkmcnt(4)" ::: "memory");
      __builtin_amdgcn_s_barrier();
      FRESH();
#pragma unroll
      for (int i = 0; i < 4; ++i) ec[i] = *(LAS f32x2*)(str + i * 256);
      SB();
    }
    if constexpr (!epi_pair(J)) eslot(IC<2 * J>{});
    eslot(IC<2 * J + 1>{});
    if constexpr (J == 16) asm volatile("s_nop 4");
    if constexpr (J >= 61) {
#pragma unroll
      for (int k = (J - 61) * 6; k < (J == 63 ? 16 : (J - 60) * 6); ++k) { tl[k] = __builtin_fmaf(ec[3].y, uu[k >> 2][k & 3], vv[k >> 2][k & 3]); PIN(tl[k]); }
      SB();
    }
    if constexpr (j == 0 && p >= 1) { PIN(acc[(p - 1) % 3]); nanv = __builtin_amdgcn_ballot_w64(!(acc[(p - 1) % 3][0] == acc[(p - 1) % 3][0])); asm volatile("" : "+s"(nanv)); }
    if constexpr (j == 1 && p >= 1) {
      if constexpr (p == 1) REDO(0)
      if constexpr (p == 2) REDO(1)
      if constexpr (p == 3) REDO(2)
      rc[p - 1] = trd2((p == 3 ? rr2 : rr) + ((p - 1) & 1) * 32768, (p == 3 ? rr2 : rr) + ((p - 1) & 1) * 32768 + 256);
    }
    if constexpr (j == 2 && p >= 1) {
      asm volatile("s_waitcnt lgkmcnt(4)" ::: "memory");
      __builtin_amdgcn_s_barrier();
      FRESH();
    }
    SB();
    if constexpr (J == 15) STAMP(16);
    if constexpr (J == 31) STAMP(17);
    if constexpr (J == 47) STAMP(18);
  });
  STAMP(12);
  static_assert(EO <= 33 && SXJ >= 8 && SXJ <= 16 && MTK > 101 && hc_upto(64) <= 24 && hc_upto(57) <= 20 && epi_slot(31) < epi_slot(32) && epi_slot(16) + 1 >= 2 * 34 && epi_slot(32) + 1 >= 2 * 50 && epi_slot(47) + 6 < 128, "schedule");
  static_for<hc_upto(64), hc_upto(64) + HT0>([&](auto Ic) __attribute__((always_inline)) { HLOAD(Ic.v); });
  SB();
  asm volatile("s_nop 7" : "+v"(acc[0]));
  nanv = __builtin_amdgcn_ballot_w64(!(acc[0][0] == acc[0][0]));
  REDO(3)
#pragma unroll
  for (int h = 0; h < 2; ++h) {
    v4u o;
#pragma unroll
    for (int i = 0; i < 4; ++i) {
      const int k = 8 * h + 2 * i;
      const f32x2 t = {tl[k], tl[k + 1]};
      const f32x2 pp = __builtin_elementwise_fma(f32x2{ec[3].x, ec[3].x}, f32x2{acc[0][k], acc[0][k + 1]}, t);
      const f32x2 d = f32x2{__builtin_amdgcn_exp2f(pp.x), __builtin_amdgcn_exp2f(pp.y)} + f32x2{1.0f, 1.0f};
      const f32x2 y = pp * f32x2{__builtin_amdgcn_rcpf(d.x), __builtin_amdgcn_rcpf(d.y)};
      o[i] = pack2(y.x, y.y);
    }
    PIN(o);
    if (h == 0) static_for<hc_upto(64) + HT0, hc_upto(64) + HT0 + HT1>([&](auto Ic) __attribute__((always_inline)) { HLOAD(Ic.v); });
    stg(rs, h ? hv1 : hv0, hoff + (3 ^ par) * 114688, o);
    if (h) CKPUT(sum4(cs, o), 3); else cs = sum4(0u, o);
    SB();
  }
  static_for<hc_upto(64) + HT0 + HT1, 28>([&](auto Ic) __attribute__((always_inline)) { HLOAD(Ic.v); });
  rc[3] = trd2(rr2 + 32768, rr2 + 32768 + 256);
  SB();
  STAMP(13);

  f32x16 d[2][2];
  LAS unsigned char* redw = L + L_XR + w * 8192 + l16;
  LAS unsigned char* redr = L + L_XR + w * 2048 + l16;
#define REDW(un, q) *(LAS f32x4*)(redw + (un) * 32768 + (q) * 1024) = \
    f32x4{d[un][(q) >> 2][4 * ((q) & 3)], d[un][(q) >> 2][4 * ((q) & 3) + 1], d[un][(q) >> 2][4 * ((q) & 3) + 2], d[un][(q) >> 2][4 * ((q) & 3) + 3]}
  LAS unsigned char *w2ua = w2a + 65536, *w2ub = w2b + 65536;
  PIN(w2ua); PIN(w2ub);
#define CHECK(un) \
  if (__builtin_expect((unsigned)__builtin_amdgcn_readlane((int)m[un], 63) != 14u * tagA, 0)) { \
    _Pragma("nounroll") for (unsigned spin = 0; spin < SPIN_CAP; ++spin) { \
      FRESH(); \
      COUNT(24 + (un)); \
      v4u qq[28]; \
      const v4u kk = ldg(rs, ckv, kb0 + (un) * 8192); \
      static_for<0, 12>([&](auto Nc) __attribute__((always_inline)) { qq[Nc.v] = HFRAG(28 * (un) + Nc.v); }); \
      SB(); \
      unsigned t = sum4(0u, kk); \
      static_for<0, 7>([&](auto Cc) __attribute__((always_inline)) { \
        constexpr int c = Cc.v; \
        const v4u* q = &qq[4 * c]; \
        if constexpr (c + 3 < 7) { _Pragma("unroll") for (int i = 0; i < 4; ++i) qq[4 * c + 12 + i] = ldg(rs, l16 + i * 1024, hb0 + CHUNK(4 * c + 12) * 4096 + (un) * (2 * 114688)); } \
        SB(); \
        t = sum8(sum8(t, q[0], q[1]), q[2], q[3]); \
        mfma_av<c == 0>(d[un][0], w2f[4 * c][0], q[0]); mfma_av<c == 0>(d[un][1], w2f[4 * c][1], q[0]); \
        _Pragma("unroll") for (int i = 1; i < 4; ++i) { mfma_av<false>(d[un][0], w2f[4 * c + i][0], q[i]); mfma_av<false>(d[un][1], w2f[4 * c + i][1], q[i]); } \
        SB(); \
      }); \
      mfma_done(d[un][1]); \
      PIN(d[un][0]); \
      if ((unsigned)__builtin_amdgcn_readlane((int)red_all(t), 63) == 14u * tagA) break; \
      PAUSE(); \
    } \
    if (un) { _Pragma("unroll") for (int q = 0; q < 4; ++q) REDW(1, q); } \
  }
  unsigned m[2] = {0u, 0u};
  v4u gbv, scv, ck[2];
  f32x4 pr[8];
  float gb[8], rx[2][8];
  unsigned orow = (rp * 64 + ((lane & 31) >> 3) * 16 + rg * 8 + (lane & 7)) * 1024 + dt * 128 + w * 32 + hf * 16;
  static_for<0, 112>([&](auto Xc) __attribute__((always_inline)) {
    constexpr int X = Xc.v, I = X < 100 ? X >> 1 : 50 + (X - 100) % 6, e = X < 100 ? X & 1 : (X - 100) / 6, un = I / 28;
    mfma_av<I % 28 == 0>(d[un][e], w2f[I % 28][e], hq[I]);
    SB();
    if constexpr (X < 100 && e == 0) {
      if constexpr (I < 3) PULL(14 + I, w2sa, w2sb, I * 2048);
      if constexpr (I >= 3 && I < 7) PULL(14 + I, w2a, w2b, 32768 + (I - 3) * 2048);
      if constexpr (I >= 7 && I < 11) PULL(14 + I, w2a, w2b, (I - 7) * 2048);
      if constexpr (I == 21) waitv<hl_upto(21) - 28 + 1>();
      if constexpr (I >= 21 && I < 24) PULL(4 + I, w2ua, w2ub, (I - 21) * 2048);
      if constexpr (I == 28) acc4(m[0], ck[0]);
      if constexpr (I >= 29 && I < 35) { m[0] = red_step<I - 29>(m[0]); PIN(m[0]); }
      if constexpr (I == 35) CHECK(0)
      if constexpr (I >= 35 && I < 43) REDW(0, I - 35);
    }
    if constexpr (X < 100 && e == 1) {
      if constexpr (I < 6) W2P(50 + I, I & 3, xl, 65536 + I * 1024);
      static_for<hl_upto(I), hl_upto(I + 1)>([&](auto Nc) __attribute__((always_inline)) { HLOAD(Nc.v); });
      if constexpr (I == 6) ck[0] = ldg(rs, ckv, kb0);
      if constexpr (I == HL1 + 16 / HLR) ck[1] = ldg(rs, ckv, kb0 + 8192);
    }
    if constexpr (X < 100 ? e == 1 : e == 0) acc4(m[un], hq[I]);
    if constexpr (X == 105) acc4(m[1], ck[1]);
    if constexpr (X >= 106) { m[1] = red_step<X - 106>(m[1]); PIN(m[1]); }
    if constexpr (X == 23 || X == 24) { const v4u ev = rc[2 * (X - 23)], od = rc[2 * (X - 23) + 1]; res[X - 23] = (rg ^ par) ? od : ev; PIN(res[X - 23]); }
    if constexpr (X == 22) { gbv = lds128(L + L_TG + (64 * dt + 16 * w + 8 * hf) * 2); scv = lds128(L + L_TS + (64 * dt + 16 * w + 8 * hf) * 2); PIN(orow); }
    if constexpr (X >= 26 && X < 42 && (X & 1) == 0) {
      constexpr int q = (X - 26) >> 1;
      const float inv = __builtin_amdgcn_rcpf((q & 1) ? bf_hi(scv[q >> 1]) : bf_lo(scv[q >> 1]));
      gb[q] = -LN2 * ((q & 1) ? bf_hi(gbv[q >> 1]) : bf_lo(gbv[q >> 1]));
      rx[0][q] = inv * ((q & 1) ? bf_hi(res[0][q >> 1]) : bf_lo(res[0][q >> 1]));
      rx[1][q] = inv * ((q & 1) ? bf_hi(res[1][q >> 1]) : bf_lo(res[1][q >> 1]));
      PIN(gb[q]); PIN(rx[0][q]); PIN(rx[1][q]);
    }
    if constexpr (X == 86) BAR();
    if constexpr (X == 86 || X == 88) {
#pragma unroll
      for (int k = X - 86; k < X - 84; ++k) { pr[2 * k] = *(LAS f32x4*)(redr + k * 8192); pr[2 * k + 1] = *(LAS f32x4*)(redr + k * 8192 + 1024); }
    }
    if constexpr (X >= 91 && X < 99) {
      constexpr int q = X - 91;
      float t = (pr[q >> 2][q & 3] + pr[2 + (q >> 2)][q & 3]);
      PIN(t);
      float t2_ = (pr[4 + (q >> 2)][q & 3] + pr[6 + (q >> 2)][q & 3]);
      PIN(t2_);
      rx[0][q] = __builtin_fmaf(gb[q], t + t2_, rx[0][q]);
      PIN(rx[0][q]);
    }
    if constexpr (X == 99)
      *(GAS v4u*)((GAS unsigned char*)out + orow) = v4u{pack2(rx[0][0], rx[0][1]), pack2(rx[0][2], rx[0][3]), pack2(rx[0][4], rx[0][5]), pack2(rx[0][6], rx[0][7])};
    if constexpr (X == 84) __builtin_amdgcn_raw_buffer_store_b32(epoch + 1u, rs, (b | tid) ? OOB : 0, OFF_EP, SC1);
    if constexpr (X == 108) asm volatile("s_nop 3" : "+v"(d[1][0]));
    if constexpr (X >= 108) REDW(1, X - 108);
    SB();
    if constexpr (X == 55) STAMP(14);
  });
  STAMP(19);
  asm volatile("s_nop 7" : "+v"(d[1][1]));
  CHECK(1)
#pragma unroll
  for (int q = 4; q < 8; ++q) REDW(1, q);
  BAR();
  {
#pragma unroll
    for (int k = 0; k < 8; ++k) pr[k] = *(LAS f32x4*)(redr + (k >> 1) * 8192 + 32768 + (k & 1) * 1024);
    SB();
    const f32x4 s0 = (pr[0] + pr[2]) + (pr[4] + pr[6]), s1 = (pr[1] + pr[3]) + (pr[5] + pr[7]);
    float y[8];
#pragma unroll
    for (int q = 0; q < 8; ++q) y[q] = __builtin_fmaf(gb[q], q < 4 ? s0[q & 3] : s1[q & 3], rx[1][q]);
    *(GAS v4u*)((GAS unsigned char*)(out + 1048576) + orow) = v4u{pack2(y[0], y[1]), pack2(y[2], y[3]), pack2(y[4], y[5]), pack2(y[6], y[7])};
  }
  STAMP(15);
}

extern "C" void kernel_launch(void* const* d_in, const int* in_sizes, int n_in, void* d_out, int out_size, void* d_ws, size_t ws_size,
                              hipStream_t stream) {
  (void)in_sizes; (void)n_in; (void)out_size; (void)ws_size;
  (void)hipFuncSetAttribute((const void*)s4_block_kernel, hipFuncAttributeMaxDynamicSharedMemorySize, LDS_BYTES);
  s4_block_kernel<<<256, 256, LDS_BYTES, stream>>>((cptr)d_in[0], (cptr)d_in[1], (cptr)d_in[2], (cptr)d_in[3], (cptr)d_in[4], (cptr)d_in[5], (cptr)d_in[6],
                                                   (cptr)d_in[7], (cptr)d_in[8], (cptr)d_in[10], (cptr)d_in[11], (cptr)d_in[12], (cptr)d_in[13],
                                                   (unsigned char*)d_out, (unsigned char*)d_ws, (cptr)d_in[9]);
}
```
